```python
import torch

def _softmax(x, axis=-1):
    return torch.softmax(x, dim=axis)
def _gelu(x):
    return torch.nn.functional.gelu(x, approximate="tanh")
def _silu(x):
    return torch.nn.functional.silu(x)
def _sigmoid(x):
    return torch.sigmoid(x)
def _layernorm(x, axis=-1, eps=1e-5):
    m = x.mean(dim=axis, keepdim=True)
    v = x.var(dim=axis, keepdim=True, unbiased=False)
    return (x - m) / torch.sqrt(v + eps)
def _scatter_add(tgt, idx, src, axis=0):
    return tgt.clone().index_add_(axis, idx, src)
def _segment_sum(src, seg, n):
    out = torch.zeros((n,) + src.shape[1:], dtype=src.dtype)
    return out.index_add_(0, seg, src)
def _pad(x, pw):
    flat = tuple(v for pair in reversed(pw) for v in pair)
    return torch.nn.functional.pad(x, flat)

kernel_name = 'standalone__mha_block_cached__near_ridge'

def setup_inputs():

    _w = (lambda a: (lambda t: t.to(torch.bfloat16) if t.is_floating_point() else t)(torch.as_tensor(a)))
    B, Sq, D = 16, 112, 640
    H, Sk, Dh = 32, 512, 192
    return {
        "x": _w((lambda *s: torch.randn(*s, dtype=torch.bfloat16) * 0.8)(B, Sq, D)),
        "k_cache": _w((lambda *s: torch.randn(*s, dtype=torch.bfloat16) * 0.8)(B, H, Sk, Dh)),
        "v_cache": _w((lambda *s: torch.randn(*s, dtype=torch.bfloat16) * 0.05)(B, H, Sk, Dh)),
        "wq": _w((lambda *s: torch.randn(*s, dtype=torch.bfloat16) * 0.8)(D, H * Dh) * (D ** -0.5)),
        "wo": _w((lambda *s: torch.randn(*s, dtype=torch.bfloat16) * 0.05)(H * Dh, D) * ((H * Dh) ** -0.5)),
    }

def reference(x, k_cache, v_cache, wq, wo):
    B, Sq, D = x.shape
    H, Dh = 32, 192
    q = (lambda x, *d: x.permute(*d))((x @ wq).reshape(B, Sq, H, Dh), 0, 2, 1, 3)
    scores = torch.einsum("bhqd,bhkd->bhqk", q, k_cache) * (Dh ** -0.5)
    p = _softmax(scores, axis=-1)
    o = torch.einsum("bhqk,bhkd->bhqd", p, v_cache)
    return (lambda x, *d: x.permute(*d))(o, 0, 2, 1, 3).reshape(B, Sq, H * Dh) @ wo
```

```cpp
#include <hip/hip_runtime.h>
#include <hip/hip_bf16.h>
typedef short bf16x8 __attribute__((ext_vector_type(8)));
typedef short s16x4 __attribute__((ext_vector_type(4)));
typedef float f32x4 __attribute__((ext_vector_type(4)));
typedef float f32x2 __attribute__((ext_vector_type(2)));
typedef __bf16 bf16x2 __attribute__((ext_vector_type(2)));
typedef unsigned u32x4 __attribute__((ext_vector_type(4)));
typedef unsigned u32x2 __attribute__((ext_vector_type(2)));
#define DI __device__ __forceinline__
#define LDSP(T, p) ((__attribute__((address_space(3))) T*)(p))
#define KV_AUX 0
#define W_AUX 0
constexpr int NB = 16, SQ = 112, DM = 640, NH = 32, SK = 512, DH = 192;
DI unsigned cvtpk(float lo, float hi) { f32x2 v = {lo, hi}; bf16x2 b = __builtin_convertvector(v, bf16x2); return __builtin_bit_cast(unsigned, b); }
DI f32x4 mfma(bf16x8 a, bf16x8 b, f32x4 c) { return __builtin_amdgcn_mfma_f32_16x16x32_bf16(a, b, c, 0, 0, 0); }
DI int sigK(int r) { return (r >> 1) & 7; }
DI int sigV(int r) { return ((r >> 1) & 3) << 1; }
DI int sigW(int r) { return (((r >> 1) & 1) | (((r >> 3) & 1) << 1)) << 1; }
template <int ROWB, int SIG, int AUX> DI void stage(const char* gbase, size_t gstride, char* lds, int wid, int lane) {
  #pragma unroll
  for (int i = 0; i < 6; ++i) {
    int p = wid * 6 + i;
    int byte = p * 1024 + lane * 16;
    int r = byte / ROWB, cp = (byte % ROWB) >> 4;
    int sg = SIG == 0 ? sigK(r) : SIG == 1 ? sigV(r) : sigW(r);
    int c = (cp & ~7) | ((cp & 7) ^ sg);
    __builtin_amdgcn_global_load_lds((const unsigned*)(gbase + r * gstride + c * 16), LDSP(unsigned, lds + p * 1024), 16, 0, AUX);
  }
}
template <int ROWB, int SIG> DI int ldsoff(int r, int c) {
  int sg = SIG == 0 ? sigK(r) : SIG == 1 ? sigV(r) : sigW(r);
  return r * ROWB + (((c & ~7) | ((c & 7) ^ sg)) << 4);
}
DI s16x4 trread(char* lds, int off) { return __builtin_amdgcn_ds_read_tr16_b64_v4i16(LDSP(s16x4, lds + off)); }
DI bf16x8 cat(s16x4 a, s16x4 b) { return __builtin_shufflevector(a, b, 0, 1, 2, 3, 4, 5, 6, 7); }

__global__ __launch_bounds__(256, 1) void fused_k(const char* __restrict__ X, const char* __restrict__ K, const char* __restrict__ V, const char* __restrict__ WQ, const char* __restrict__ WO, char* __restrict__ part) {
  __shared__ __attribute__((aligned(16))) char lds[49152];
  const int tid = threadIdx.x, lane = tid & 63, wid = __builtin_amdgcn_readfirstlane(tid >> 6);
  const int xcd = blockIdx.x & 7, j = blockIdx.x >> 3, b = j >> 1, hp = xcd * 2 + (j & 1);
  const int q16 = lane & 15, g = lane >> 4, qq = q16 >> 2, pp = q16 & 3;
  char* priv = part + (64u << 20) + (size_t)blockIdx.x * 163840 + tid * 8;
  #pragma unroll 1
  for (int hh = 0; hh < 2; ++hh) {
    const int h = hp * 2 + hh;
    f32x4 qa[12][2];
    #pragma unroll
    for (int dt = 0; dt < 12; ++dt) { qa[dt][0] = f32x4{0, 0, 0, 0}; qa[dt][1] = f32x4{0, 0, 0, 0}; }
    #pragma unroll 1
    for (int ku = 0; ku < 10; ++ku) {
      __syncthreads();
      stage<384, 2, W_AUX>(WQ + ((size_t)(ku * 64) * (NH * DH) + h * DH) * 2, (size_t)NH * DH * 2, lds, wid, lane);
      bf16x8 xf[2][2];
      #pragma unroll
      for (int qt = 0; qt < 2; ++qt) { int row = min(wid * 32 + qt * 16 + q16, SQ - 1);
        #pragma unroll
        for (int ks = 0; ks < 2; ++ks) xf[qt][ks] = *(const bf16x8*)(X + ((size_t)(b * SQ + row) * DM + ku * 64 + ks * 32 + g * 8) * 2); }
      __syncthreads();
      #pragma unroll
      for (int ks = 0; ks < 2; ++ks)
        #pragma unroll
        for (int dt = 0; dt < 12; ++dt) {
          int r0 = 32 * ks + 8 * g + qq;
          bf16x8 a = cat(trread(lds, ldsoff<384, 2>(r0, 2 * dt + (pp >> 1)) + 8 * (pp & 1)), trread(lds, ldsoff<384, 2>(r0 + 4, 2 * dt + (pp >> 1)) + 8 * (pp & 1)));
          qa[dt][0] = mfma(a, xf[0][ks], qa[dt][0]); qa[dt][1] = mfma(a, xf[1][ks], qa[dt][1]);
        }
    }
    const float C = 1.4426950408889634f * 0.07216878364870322f;
    bf16x8 qf[6][2];
    #pragma unroll
    for (int s = 0; s < 6; ++s)
      #pragma unroll
      for (int qt = 0; qt < 2; ++qt) {
        unsigned x0 = cvtpk(qa[2 * s][qt][0] * C, qa[2 * s][qt][1] * C), x1 = cvtpk(qa[2 * s][qt][2] * C, qa[2 * s][qt][3] * C);
        unsigned y0 = cvtpk(qa[2 * s + 1][qt][0] * C, qa[2 * s + 1][qt][1] * C), y1 = cvtpk(qa[2 * s + 1][qt][2] * C, qa[2 * s + 1][qt][3] * C);
        auto a0 = __builtin_amdgcn_permlane32_swap(x0, y0, false, false); auto a1 = __builtin_amdgcn_permlane32_swap(x1, y1, false, false);
        auto b0 = __builtin_amdgcn_permlane16_swap(a0[0], a0[1], false, false); auto b1 = __builtin_amdgcn_permlane16_swap(a1[0], a1[1], false, false);
        u32x4 w = {b0[0], b1[0], b0[1], b1[1]};
        qf[s][qt] = __builtin_bit_cast(bf16x8, w);
      }
    f32x4 oa[12][2];
    #pragma unroll
    for (int dt = 0; dt < 12; ++dt) { oa[dt][0] = f32x4{0, 0, 0, 0}; oa[dt][1] = f32x4{0, 0, 0, 0}; }
    float m[2] = {-1e30f, -1e30f}, l[2] = {0.f, 0.f};
    const char* Kh = K + (size_t)(b * NH + h) * SK * DH * 2; const char* Vh = V + (size_t)(b * NH + h) * SK * DH * 2;
    #pragma unroll 1
    for (int t = 0; t < 8; ++t) {
      __syncthreads();
      stage<384, 0, KV_AUX>(Kh + t * 24576, 384, lds, wid, lane);
      stage<384, 1, KV_AUX>(Vh + t * 24576, 384, lds + 24576, wid, lane);
      __syncthreads();
      f32x4 sa[4][2];
      #pragma unroll
      for (int u = 0; u < 4; ++u) { sa[u][0] = f32x4{0, 0, 0, 0}; sa[u][1] = f32x4{0, 0, 0, 0};
        #pragma unroll
        for (int s = 0; s < 6; ++s) { bf16x8 a = *(const bf16x8*)(lds + ldsoff<384, 0>(16 * u + q16, 4 * s + g));
          sa[u][0] = mfma(a, qf[s][0], sa[u][0]); sa[u][1] = mfma(a, qf[s][1], sa[u][1]); } }
      bf16x8 pf[2][2];
      #pragma unroll
      for (int qt = 0; qt < 2; ++qt) {
        float mx = sa[0][qt][0];
        #pragma unroll
        for (int u = 0; u < 4; ++u) for (int e = 0; e < 4; ++e) mx = fmaxf(mx, sa[u][qt][e]);
        mx = fmaxf(mx, __shfl_xor(mx, 16)); mx = fmaxf(mx, __shfl_xor(mx, 32));
        float mn = fmaxf(m[qt], mx), al = __builtin_amdgcn_exp2f(m[qt] - mn); m[qt] = mn;
        float ps = 0.f;
        #pragma unroll
        for (int u = 0; u < 4; ++u) for (int e = 0; e < 4; ++e) { sa[u][qt][e] = __builtin_amdgcn_exp2f(sa[u][qt][e] - mn); ps += sa[u][qt][e]; }
        l[qt] = l[qt] * al + ps;
        #pragma unroll
        for (int dt = 0; dt < 12; ++dt) oa[dt][qt] *= al;
        #pragma unroll
        for (int kk = 0; kk < 2; ++kk) { u32x4 w = {cvtpk(sa[2 * kk][qt][0], sa[2 * kk][qt][1]), cvtpk(sa[2 * kk][qt][2], sa[2 * kk][qt][3]), cvtpk(sa[2 * kk + 1][qt][0], sa[2 * kk + 1][qt][1]), cvtpk(sa[2 * kk + 1][qt][2], sa[2 * kk + 1][qt][3])};
          pf[kk][qt] = __builtin_bit_cast(bf16x8, w); }
      }
      #pragma unroll
      for (int kk = 0; kk < 2; ++kk)
        #pragma unroll
        for (int dt = 0; dt < 12; ++dt) {
          int r0 = 32 * kk + 4 * g + qq;
          bf16x8 a = cat(trread(lds + 24576, ldsoff<384, 1>(r0, 2 * dt + (pp >> 1)) + 8 * (pp & 1)), trread(lds + 24576, ldsoff<384, 1>(r0 + 16, 2 * dt + (pp >> 1)) + 8 * (pp & 1)));
          oa[dt][0] = mfma(a, pf[kk][0], oa[dt][0]); oa[dt][1] = mfma(a, pf[kk][1], oa[dt][1]);
        }
    }
    bf16x8 of[6][2];
    #pragma unroll
    for (int qt = 0; qt < 2; ++qt) {
      float ls = l[qt]; ls += __shfl_xor(ls, 16); ls += __shfl_xor(ls, 32); float il = 1.f / ls;
      #pragma unroll
      for (int s = 0; s < 6; ++s) { u32x4 w = {cvtpk(oa[2 * s][qt][0] * il, oa[2 * s][qt][1] * il), cvtpk(oa[2 * s][qt][2] * il, oa[2 * s][qt][3] * il), cvtpk(oa[2 * s + 1][qt][0] * il, oa[2 * s + 1][qt][1] * il), cvtpk(oa[2 * s + 1][qt][2] * il, oa[2 * s + 1][qt][3] * il)};
        of[s][qt] = __builtin_bit_cast(bf16x8, w); }
    }
    #pragma unroll 1
    for (int cu = 0; cu < 10; ++cu) {
      u32x2 parked[4][2];
      __syncthreads();
      stage<128, 1, W_AUX>(WO + ((size_t)(h * DH) * DM + cu * 64) * 2, (size_t)DM * 2, lds, wid, lane);
      __syncthreads();
      f32x4 ya[4][2];
      #pragma unroll
      for (int ct = 0; ct < 4; ++ct) {
        #pragma unroll
        for (int qt = 0; qt < 2; ++qt) {
          if (hh == 0) ya[ct][qt] = f32x4{0, 0, 0, 0};
          else { u32x2 pk = *(u32x2*)(priv + ((cu * 4 + ct) * 2 + qt) * 2048); ya[ct][qt] = f32x4{__uint_as_float(pk[0] << 16), __uint_as_float(pk[0] & 0xffff0000u), __uint_as_float(pk[1] << 16), __uint_as_float(pk[1] & 0xffff0000u)}; }
        }
        #pragma unroll
        for (int s = 0; s < 6; ++s) {
          int r0 = 32 * s + 4 * g + qq;
          bf16x8 a = cat(trread(lds, ldsoff<128, 1>(r0, 2 * ct + (pp >> 1)) + 8 * (pp & 1)), trread(lds, ldsoff<128, 1>(r0 + 16, 2 * ct + (pp >> 1)) + 8 * (pp & 1)));
          ya[ct][0] = mfma(a, of[s][0], ya[ct][0]); ya[ct][1] = mfma(a, of[s][1], ya[ct][1]);
        }
        #pragma unroll
        for (int qt = 0; qt < 2; ++qt) { parked[ct][qt] = u32x2{cvtpk(ya[ct][qt][0], ya[ct][qt][1]), cvtpk(ya[ct][qt][2], ya[ct][qt][3])}; if (hh == 0) *(u32x2*)(priv + ((cu * 4 + ct) * 2 + qt) * 2048) = parked[ct][qt]; }
      }
      if (hh == 1) {
        #pragma unroll
        for (int qt = 0; qt < 2; ++qt) { const int qtile = wid * 2 + qt;
          if (qtile < 7) {
            #pragma unroll
            for (int cp = 0; cp < 2; ++cp) { u32x4 w = {parked[2 * cp][qt][0], parked[2 * cp][qt][1], parked[2 * cp + 1][qt][0], parked[2 * cp + 1][qt][1]};
              *(u32x4*)(part + ((((size_t)(b * 7 + qtile) * 10 + cu) * 2 + cp) * 16 + hp) * 1024 + lane * 16) = w; }
          } }
      }
    }
  }
}
__global__ __launch_bounds__(256) void reduce_k(const char* __restrict__ part, char* __restrict__ out) {
  const int lane = threadIdx.x & 63, unit = blockIdx.x * 4 + (threadIdx.x >> 6);
  const char* p = part + (size_t)unit * 16384 + lane * 16;
  u32x4 v[16];
  #pragma unroll
  for (int i = 0; i < 16; ++i) v[i] = __builtin_nontemporal_load((const u32x4*)(p + i * 1024));
  float a[8] = {0, 0, 0, 0, 0, 0, 0, 0};
  #pragma unroll
  for (int i = 0; i < 16; ++i)
    #pragma unroll
    for (int e = 0; e < 4; ++e) { a[2 * e] += __uint_as_float(v[i][e] << 16); a[2 * e + 1] += __uint_as_float(v[i][e] & 0xffff0000u); }
  const int cp = unit & 1, cu = (unit >> 1) % 10, bq = unit / 20, qtile = bq % 7, b = bq / 7;
  const int row = b * SQ + qtile * 16 + (lane & 15), col = cu * 64 + cp * 32 + 4 * (lane >> 4);
  *(u32x2*)(out + ((size_t)row * DM + col) * 2) = u32x2{cvtpk(a[0], a[1]), cvtpk(a[2], a[3])};
  *(u32x2*)(out + ((size_t)row * DM + col + 16) * 2) = u32x2{cvtpk(a[4], a[5]), cvtpk(a[6], a[7])};
}
extern "C" void kernel_launch(void* const* d_in, const int* in_sizes, int n_in, void* d_out, int out_size, void* d_ws, size_t ws_size, hipStream_t stream) {
  fused_k<<<256, 256, 0, stream>>>((const char*)d_in[0], (const char*)d_in[1], (const char*)d_in[2], (const char*)d_in[3], (const char*)d_in[4], (char*)d_ws);
  reduce_k<<<560, 256, 0, stream>>>((const char*)d_ws, (char*)d_out);
}
```

```cpp
#include <hip/hip_runtime.h>
#include <hip/hip_bf16.h>
typedef short bf16x8 __attribute__((ext_vector_type(8)));
typedef short s16x4 __attribute__((ext_vector_type(4)));
typedef float f32x4 __attribute__((ext_vector_type(4)));
typedef float f32x2 __attribute__((ext_vector_type(2)));
typedef __bf16 bf16x2 __attribute__((ext_vector_type(2)));
typedef unsigned u32x4 __attribute__((ext_vector_type(4)));
typedef unsigned u32x2 __attribute__((ext_vector_type(2)));
#define DI __device__ __forceinline__
#define LDSP(T, p) ((__attribute__((address_space(3))) T*)(p))
#define KV_AUX 2
#define W_AUX 2
constexpr int NB = 16, SQ = 112, DM = 640, NH = 32, SK = 512, DH = 192;
DI unsigned cvtpk(float lo, float hi) { f32x2 v = {lo, hi}; bf16x2 b = __builtin_convertvector(v, bf16x2); return __builtin_bit_cast(unsigned, b); }
DI f32x4 mfma(bf16x8 a, bf16x8 b, f32x4 c) { return __builtin_amdgcn_mfma_f32_16x16x32_bf16(a, b, c, 0, 0, 0); }
DI int sigK(int r) { return (r >> 1) & 7; }
DI int sigV(int r) { return ((r >> 1) & 3) << 1; }
DI int sigW(int r) { return (((r >> 1) & 1) | (((r >> 3) & 1) << 1)) << 1; }
template <int ROWB, int SIG, int AUX> DI void stage(const char* gbase, size_t gstride, char* lds, int wid, int lane) {
  #pragma unroll
  for (int i = 0; i < 6; ++i) {
    int p = wid * 6 + i;
    int byte = p * 1024 + lane * 16;
    int r = byte / ROWB, cp = (byte % ROWB) >> 4;
    int sg = SIG == 0 ? sigK(r) : SIG == 1 ? sigV(r) : sigW(r);
    int c = (cp & ~7) | ((cp & 7) ^ sg);
    __builtin_amdgcn_global_load_lds((const unsigned*)(gbase + r * gstride + c * 16), LDSP(unsigned, lds + p * 1024), 16, 0, AUX);
  }
}
template <int ROWB, int SIG> DI int ldsoff(int r, int c) {
  int sg = SIG == 0 ? sigK(r) : SIG == 1 ? sigV(r) : sigW(r);
  return r * ROWB + (((c & ~7) | ((c & 7) ^ sg)) << 4);
}
DI s16x4 trread(char* lds, int off) { return __builtin_amdgcn_ds_read_tr16_b64_v4i16(LDSP(s16x4, lds + off)); }
DI bf16x8 cat(s16x4 a, s16x4 b) { return __builtin_shufflevector(a, b, 0, 1, 2, 3, 4, 5, 6, 7); }

__global__ __launch_bounds__(256, 1) void fused_k(const char* __restrict__ X, const char* __restrict__ K, const char* __restrict__ V, const char* __restrict__ WQ, const char* __restrict__ WO, char* __restrict__ part) {
  __shared__ __attribute__((aligned(16))) char lds[49152];
  const int tid = threadIdx.x, lane = tid & 63, wid = __builtin_amdgcn_readfirstlane(tid >> 6);
  const int xcd = blockIdx.x & 7, j = blockIdx.x >> 3, b = j >> 1, hp = xcd * 2 + (j & 1);
  const int q16 = lane & 15, g = lane >> 4, qq = q16 >> 2, pp = q16 & 3;
  char* priv = part + (64u << 20) + (size_t)blockIdx.x * 163840 + tid * 8;
  #pragma unroll 1
  for (int hh = 0; hh < 2; ++hh) {
    const int h = hp * 2 + hh;
    f32x4 qa[12][2];
    #pragma unroll
    for (int dt = 0; dt < 12; ++dt) { qa[dt][0] = f32x4{0, 0, 0, 0}; qa[dt][1] = f32x4{0, 0, 0, 0}; }
    #pragma unroll 1
    for (int ku = 0; ku < 10; ++ku) {
      __syncthreads();
      stage<384, 2, W_AUX>(WQ + ((size_t)(ku * 64) * (NH * DH) + h * DH) * 2, (size_t)NH * DH * 2, lds, wid, lane);
      bf16x8 xf[2][2];
      #pragma unroll
      for (int qt = 0; qt < 2; ++qt) { int row = min(wid * 32 + qt * 16 + q16, SQ - 1);
        #pragma unroll
        for (int ks = 0; ks < 2; ++ks) xf[qt][ks] = *(const bf16x8*)(X + ((size_t)(b * SQ + row) * DM + ku * 64 + ks * 32 + g * 8) * 2); }
      __syncthreads();
      #pragma unroll
      for (int ks = 0; ks < 2; ++ks)
        #pragma unroll
        for (int dt = 0; dt < 12; ++dt) {
          int r0 = 32 * ks + 8 * g + qq;
          bf16x8 a = cat(trread(lds, ldsoff<384, 2>(r0, 2 * dt + (pp >> 1)) + 8 * (pp & 1)), trread(lds, ldsoff<384, 2>(r0 + 4, 2 * dt + (pp >> 1)) + 8 * (pp & 1)));
          qa[dt][0] = mfma(a, xf[0][ks], qa[dt][0]); qa[dt][1] = mfma(a, xf[1][ks], qa[dt][1]);
        }
    }
    const float C = 1.4426950408889634f * 0.07216878364870322f;
    bf16x8 qf[6][2];
    #pragma unroll
    for (int s = 0; s < 6; ++s)
      #pragma unroll
      for (int qt = 0; qt < 2; ++qt) {
        unsigned x0 = cvtpk(qa[2 * s][qt][0] * C, qa[2 * s][qt][1] * C), x1 = cvtpk(qa[2 * s][qt][2] * C, qa[2 * s][qt][3] * C);
        unsigned y0 = cvtpk(qa[2 * s + 1][qt][0] * C, qa[2 * s + 1][qt][1] * C), y1 = cvtpk(qa[2 * s + 1][qt][2] * C, qa[2 * s + 1][qt][3] * C);
        auto a0 = __builtin_amdgcn_permlane32_swap(x0, y0, false, false); auto a1 = __builtin_amdgcn_permlane32_swap(x1, y1, false, false);
        auto b0 = __builtin_amdgcn_permlane16_swap(a0[0], a0[1], false, false); auto b1 = __builtin_amdgcn_permlane16_swap(a1[0], a1[1], false, false);
        u32x4 w = {b0[0], b1[0], b0[1], b1[1]};
        qf[s][qt] = __builtin_bit_cast(bf16x8, w);
      }
    f32x4 oa[12][2];
    #pragma unroll
    for (int dt = 0; dt < 12; ++dt) { oa[dt][0] = f32x4{0, 0, 0, 0}; oa[dt][1] = f32x4{0, 0, 0, 0}; }
    float m[2] = {-1e30f, -1e30f}, l[2] = {0.f, 0.f};
    const char* Kh = K + (size_t)(b * NH + h) * SK * DH * 2; const char* Vh = V + (size_t)(b * NH + h) * SK * DH * 2;
    #pragma unroll 1
    for (int t = 0; t < 8; ++t) {
      __syncthreads();
      stage<384, 0, KV_AUX>(Kh + t * 24576, 384, lds, wid, lane);
      stage<384, 1, KV_AUX>(Vh + t * 24576, 384, lds + 24576, wid, lane);
      __syncthreads();
      f32x4 sa[4][2];
      #pragma unroll
      for (int u = 0; u < 4; ++u) { sa[u][0] = f32x4{0, 0, 0, 0}; sa[u][1] = f32x4{0, 0, 0, 0};
        #pragma unroll
        for (int s = 0; s < 6; ++s) { bf16x8 a = *(const bf16x8*)(lds + ldsoff<384, 0>(16 * u + q16, 4 * s + g));
          sa[u][0] = mfma(a, qf[s][0], sa[u][0]); sa[u][1] = mfma(a, qf[s][1], sa[u][1]); } }
      bf16x8 pf[2][2];
      #pragma unroll
      for (int qt = 0; qt < 2; ++qt) {
        float mx = sa[0][qt][0];
        #pragma unroll
        for (int u = 0; u < 4; ++u) for (int e = 0; e < 4; ++e) mx = fmaxf(mx, sa[u][qt][e]);
        mx = fmaxf(mx, __shfl_xor(mx, 16)); mx = fmaxf(mx, __shfl_xor(mx, 32));
        float mn = fmaxf(m[qt], mx), al = __builtin_amdgcn_exp2f(m[qt] - mn); m[qt] = mn;
        float ps = 0.f;
        #pragma unroll
        for (int u = 0; u < 4; ++u) for (int e = 0; e < 4; ++e) { sa[u][qt][e] = __builtin_amdgcn_exp2f(sa[u][qt][e] - mn); ps += sa[u][qt][e]; }
        l[qt] = l[qt] * al + ps;
        #pragma unroll
        for (int dt = 0; dt < 12; ++dt) oa[dt][qt] *= al;
        #pragma unroll
        for (int kk = 0; kk < 2; ++kk) { u32x4 w = {cvtpk(sa[2 * kk][qt][0], sa[2 * kk][qt][1]), cvtpk(sa[2 * kk][qt][2], sa[2 * kk][qt][3]), cvtpk(sa[2 * kk + 1][qt][0], sa[2 * kk + 1][qt][1]), cvtpk(sa[2 * kk + 1][qt][2], sa[2 * kk + 1][qt][3])};
          pf[kk][qt] = __builtin_bit_cast(bf16x8, w); }
      }
      #pragma unroll
      for (int kk = 0; kk < 2; ++kk)
        #pragma unroll
        for (int dt = 0; dt < 12; ++dt) {
          int r0 = 32 * kk + 4 * g + qq;
          bf16x8 a = cat(trread(lds + 24576, ldsoff<384, 1>(r0, 2 * dt + (pp >> 1)) + 8 * (pp & 1)), trread(lds + 24576, ldsoff<384, 1>(r0 + 16, 2 * dt + (pp >> 1)) + 8 * (pp & 1)));
          oa[dt][0] = mfma(a, pf[kk][0], oa[dt][0]); oa[dt][1] = mfma(a, pf[kk][1], oa[dt][1]);
        }
    }
    bf16x8 of[6][2];
    #pragma unroll
    for (int qt = 0; qt < 2; ++qt) {
      float ls = l[qt]; ls += __shfl_xor(ls, 16); ls += __shfl_xor(ls, 32); float il = 1.f / ls;
      #pragma unroll
      for (int s = 0; s < 6; ++s) { u32x4 w = {cvtpk(oa[2 * s][qt][0] * il, oa[2 * s][qt][1] * il), cvtpk(oa[2 * s][qt][2] * il, oa[2 * s][qt][3] * il), cvtpk(oa[2 * s + 1][qt][0] * il, oa[2 * s + 1][qt][1] * il), cvtpk(oa[2 * s + 1][qt][2] * il, oa[2 * s + 1][qt][3] * il)};
        of[s][qt] = __builtin_bit_cast(bf16x8, w); }
    }
    #pragma unroll 1
    for (int cu = 0; cu < 10; ++cu) {
      u32x2 parked[4][2];
      __syncthreads();
      stage<128, 1, W_AUX>(WO + ((size_t)(h * DH) * DM + cu * 64) * 2, (size_t)DM * 2, lds, wid, lane);
      __syncthreads();
      f32x4 ya[4][2];
      #pragma unroll
      for (int ct = 0; ct < 4; ++ct) {
        #pragma unroll
        for (int qt = 0; qt < 2; ++qt) {
          if (hh == 0) ya[ct][qt] = f32x4{0, 0, 0, 0};
          else { u32x2 pk = *(u32x2*)(priv + ((cu * 4 + ct) * 2 + qt) * 2048); ya[ct][qt] = f32x4{__uint_as_float(pk[0] << 16), __uint_as_float(pk[0] & 0xffff0000u), __uint_as_float(pk[1] << 16), __uint_as_float(pk[1] & 0xffff0000u)}; }
        }
        #pragma unroll
        for (int s = 0; s < 6; ++s) {
          int r0 = 32 * s + 4 * g + qq;
          bf16x8 a = cat(trread(lds, ldsoff<128, 1>(r0, 2 * ct + (pp >> 1)) + 8 * (pp & 1)), trread(lds, ldsoff<128, 1>(r0 + 16, 2 * ct + (pp >> 1)) + 8 * (pp & 1)));
          ya[ct][0] = mfma(a, of[s][0], ya[ct][0]); ya[ct][1] = mfma(a, of[s][1], ya[ct][1]);
        }
        #pragma unroll
        for (int qt = 0; qt < 2; ++qt) { parked[ct][qt] = u32x2{cvtpk(ya[ct][qt][0], ya[ct][qt][1]), cvtpk(ya[ct][qt][2], ya[ct][qt][3])}; if (hh == 0) *(u32x2*)(priv + ((cu * 4 + ct) * 2 + qt) * 2048) = parked[ct][qt]; }
      }
      if (hh == 1) {
        #pragma unroll
        for (int qt = 0; qt < 2; ++qt) { const int qtile = wid * 2 + qt;
          if (qtile < 7) {
            #pragma unroll
            for (int cp = 0; cp < 2; ++cp) { u32x4 w = {parked[2 * cp][qt][0], parked[2 * cp][qt][1], parked[2 * cp + 1][qt][0], parked[2 * cp + 1][qt][1]};
              *(u32x4*)(part + ((((size_t)(b * 7 + qtile) * 10 + cu) * 2 + cp) * 16 + hp) * 1024 + lane * 16) = w; }
          } }
      }
    }
  }
}
__global__ __launch_bounds__(256) void reduce_k(const char* __restrict__ part, char* __restrict__ out) {
  const int lane = threadIdx.x & 63, unit = blockIdx.x * 4 + (threadIdx.x >> 6);
  const char* p = part + (size_t)unit * 16384 + lane * 16;
  u32x4 v[16];
  #pragma unroll
  for (int i = 0; i < 16; ++i) v[i] = __builtin_nontemporal_load((const u32x4*)(p + i * 1024));
  float a[8] = {0, 0, 0, 0, 0, 0, 0, 0};
  #pragma unroll
  for (int i = 0; i < 16; ++i)
    #pragma unroll
    for (int e = 0; e < 4; ++e) { a[2 * e] += __uint_as_float(v[i][e] << 16); a[2 * e + 1] += __uint_as_float(v[i][e] & 0xffff0000u); }
  const int cp = unit & 1, cu = (unit >> 1) % 10, bq = unit / 20, qtile = bq % 7, b = bq / 7;
  const int row = b * SQ + qtile * 16 + (lane & 15), col = cu * 64 + cp * 32 + 4 * (lane >> 4);
  *(u32x2*)(out + ((size_t)row * DM + col) * 2) = u32x2{cvtpk(a[0], a[1]), cvtpk(a[2], a[3])};
  *(u32x2*)(out + ((size_t)row * DM + col + 16) * 2) = u32x2{cvtpk(a[4], a[5]), cvtpk(a[6], a[7])};
}

#define PROBE_AUX 2
constexpr int TILEB = 24576;
__global__ __launch_bounds__(256,1) void dma_stream(const char* __restrict__ K, const char* __restrict__ V) {
  __shared__ __attribute__((aligned(16))) char lds[2 * TILEB];
  const int tid = threadIdx.x, lane = tid & 63;
  const int wid = __builtin_amdgcn_readfirstlane(tid >> 6);
  unsigned long long t0 = __builtin_amdgcn_s_memrealtime();
  for (int t = 0; t < 32; ++t) {
    int pair = blockIdx.x + 256 * (t >> 4);
    const char* base = ((t & 1) ? V : K) + (size_t)pair * 196608 + (size_t)((t & 15) >> 1) * TILEB;
    #pragma unroll
    for (int p = 0; p < 6; ++p) {
      const char* src = base + (wid * 6 + p) * 1024 + lane * 16;
      asm volatile("s_waitcnt vmcnt(11)" ::: "memory");
      __builtin_amdgcn_global_load_lds((const unsigned*)src, LDSP(unsigned, lds + (t & 1) * TILEB + (wid * 6 + p) * 1024), 16, 0, PROBE_AUX);
    }
  }
  asm volatile("s_waitcnt vmcnt(0)" ::: "memory");
  unsigned long long t1 = __builtin_amdgcn_s_memrealtime();
  unsigned long long tend = t0 + (t1 - t0) * 8;
  while (__builtin_amdgcn_s_memrealtime() < tend) __builtin_amdgcn_s_sleep(2);
}
extern "C" void kernel_launch(void* const* d_in, const int* in_sizes, int n_in, void* d_out, int out_size, void* d_ws, size_t ws_size, hipStream_t stream) {
  dma_stream<<<256, 256, 0, stream>>>((const char*)d_in[1], (const char*)d_in[2]);
  fused_k<<<256, 256, 0, stream>>>((const char*)d_in[0], (const char*)d_in[1], (const char*)d_in[2], (const char*)d_in[3], (const char*)d_in[4], (char*)d_ws);
  reduce_k<<<560, 256, 0, stream>>>((const char*)d_ws, (char*)d_out);
}
```

```cpp
#include <hip/hip_runtime.h>
#include <hip/hip_bf16.h>
typedef unsigned u32x4 __attribute__((ext_vector_type(4)));
typedef unsigned u32x2 __attribute__((ext_vector_type(2)));
typedef unsigned u32x16 __attribute__((ext_vector_type(16)));
typedef unsigned u32x8 __attribute__((ext_vector_type(8)));
typedef float f32x2 __attribute__((ext_vector_type(2)));
typedef __bf16 bf16x2 __attribute__((ext_vector_type(2)));
#define MAP_B(blk) ((blk) >> 4)
#define MAP_HP(blk) (((blk) & 7u) * 2u + (((blk) >> 3) & 1u))
constexpr unsigned WS_F = 0, WS_N = 512u << 10, WS_PART = 1u << 20, WS_DBG = 60u << 20, OOB = 0x40000000u;
__device__ __forceinline__ u32x4 mk_srd(const void* p, unsigned bytes) {
  unsigned long long a = (unsigned long long)p;
  return u32x4{(unsigned)a, (unsigned)(a >> 32) & 0xffffu, bytes, 0x20000u};
}
__device__ __forceinline__ unsigned sig(unsigned r) { return ((r >> 1) & 3u) << 1; }

__global__ __launch_bounds__(256, 1) void mha_fused(const char* __restrict__ X, const char* __restrict__ K, const char* __restrict__ V,
                                                    const char* __restrict__ WQ, const char* __restrict__ WO, char* __restrict__ ws, char* __restrict__ out) {
  __shared__ __attribute__((aligned(16))) char lds[163840];
  unsigned blk = blockIdx.x, w = __builtin_amdgcn_readfirstlane(threadIdx.x >> 6), l = threadIdx.x & 63u;
  u32x4 tq; unsigned wm;
  const unsigned rotk128 = (((blk >> 4) + (blk & 7u) + 5u * ((blk >> 3) & 1u)) % 10u) * 128u;
  u32x8 s1, v1;
  {
    const unsigned b = MAP_B(blk), h0 = MAP_HP(blk) * 2u;
    s1[0] = 6144u * w; s1[1] = (32u * (w >> 1) + 4u * (w & 1u)) * 12288u; s1[2] = 48u * w * 1280u; s1[3] = 4096u * w;
    s1[4] = rotk128; s1[5] = rotk128 == 1152u ? 0u : rotk128 + 128u; s1[6] = 0; s1[7] = 0;
    v1[0] = 0;
#pragma unroll
    for (unsigned i = 0; i < 3; ++i) { const unsigned n = 64u * i + l, r = n / 24u, cp = n - 24u * r; v1[1 + i] = (8u * (r >> 2) + (r & 3u)) * 12288u + 16u * (cp ^ sig(r)) - 1024u * i; }
#pragma unroll
    for (unsigned i = 0; i < 4; ++i) v1[4 + i] = (32u * w + 8u * i + (l >> 3)) * 1280u + 16u * ((l & 7u) ^ sig(l >> 3)) - 1024u * i;
    asm volatile(R"ASM(
.macro M d,a,b,c
v_mfma_f32_16x16x32_bf16 \d,\a,\b,\c
.endm
.macro T d,a,o
ds_read_b64_tr_b16 \d,\a offset:\o
.endm
.macro W n
s_waitcnt lgkmcnt(\n)
.endm
s_add_u32 m0, s43, 147456
s_nop 0
buffer_load_dwordx4 v236, s[24:27], s44 offen lds
buffer_load_dwordx4 v237, s[24:27], s44 offen offset:1024 lds
buffer_load_dwordx4 v238, s[24:27], s44 offen offset:2048 lds
buffer_load_dwordx4 v239, s[24:27], s44 offen offset:3072 lds
s_mul_i32 s77, s44, 6144
s_add_u32 s77, s77, s41
s_add_u32 s78, s77, 0
s_add_u32 m0, s40, 0
s_nop 0
buffer_load_dwordx4 v233, s[16:19], s78 offen lds
buffer_load_dwordx4 v234, s[16:19], s78 offen offset:1024 lds
buffer_load_dwordx4 v235, s[16:19], s78 offen offset:2048 lds
s_add_u32 s78, s77, 196608
s_add_u32 m0, s40, 3072
s_nop 0
buffer_load_dwordx4 v233, s[16:19], s78 offen lds
buffer_load_dwordx4 v234, s[16:19], s78 offen offset:1024 lds
buffer_load_dwordx4 v235, s[16:19], s78 offen offset:2048 lds

)ASM"
      : "={s[88:91]}"(tq), "={v253}"(wm), "+s"(blk), "+s"(w), "+v"(l)
      : "{s[16:19]}"(mk_srd(WQ + h0 * 384u, 7864320u - h0 * 384u)), "{s[24:27]}"(mk_srd(X + b * 143360u, 143360u)), "{s[40:47]}"(s1),
        "{s63}"((unsigned)(size_t)lds), "{v[232:239]}"(v1), "{v240}"(16u * l)
      : "memory", "scc", "s77", "s78", "s80", "s81");
  }
  const unsigned xcd = blk & 7u, j = blk >> 3, b = MAP_B(blk), hp = MAP_HP(blk), h0 = hp * 2u;
  const u32x4 sK = mk_srd(K + (size_t)(b * 32u + h0) * 196608u, 393216u), sV = mk_srd(V + (size_t)(b * 32u + h0) * 196608u, 393216u);
  const u32x4 sWQ = mk_srd(WQ + h0 * 384u, 7864320u - h0 * 384u);
  const u32x4 sWO = mk_srd(WO + h0 * 245760u, 491520u);
  const u32x4 sX = mk_srd(X + b * 143360u, 143360u), sWS = mk_srd(ws, 64u << 20), sOut = mk_srd(out, 2293760u);
  const unsigned pos0 = 0 ? 0u : hp * 1024u;
  const char* p0 = ws + WS_PART + (size_t)(b * 140u + 4u * w) * 16384u + pos0;
  const u32x4 sP0 = mk_srd(p0, 2293760u - 4u * w * 16384u - pos0), sP1 = mk_srd(p0 + 32768u, w == 3u ? 0u : 2293760u - (4u * w + 2u) * 16384u - pos0);
  u32x16 sa, sb;
  sa[0] = s1[0]; sa[1] = s1[1]; sa[2] = s1[2]; sa[3] = s1[3];
  const unsigned rot = b % 10u;
  unsigned kk = rotk128, cc = rot * 128u; u32x4 sc;
#pragma unroll
  for (int i = 0; i < 10; ++i) {
    sa[4 + i] = kk; kk = kk == 1152u ? 0u : kk + 128u;
    if (i < 6) sb[10 + i] = cc; else sc[i - 6] = cc;
    cc = cc == 1152u ? 0u : cc + 128u;
  }
  u32x8 st;
#pragma unroll
  for (unsigned i = 0; i < 8; ++i) st[i] = ((i + (j + xcd)) & 7u) * 24576u + s1[0];
  const unsigned me = hp * 4u + w, loc = me & 7u, s0 = (4u - (b & 3u)) & 3u;
  unsigned un[3] = {loc != 3u ? me - (hp >> 1) - (loc > 3u ? 1u : 0u) : ~0u, loc != 7u ? 56u + me - (hp >> 1) : ~0u,
                    w == 3u ? 112u + hp : (w == 0u && (hp & 3u) != s0) ? 128u + hp - (hp > s0 ? (hp - s0 + 3u) >> 2 : 0u) : ~0u}, ub[3], ob[3];
#pragma unroll
  for (int k = 0; k < 3; ++k) {
    const unsigned n = un[k], jj = n / 14u, r = n - jj * 14u; unsigned c = rot + jj; c = c >= 10u ? c - 10u : c;
    ub[k] = n != ~0u ? WS_PART + (b * 140u + n) * 16384u : OOB;
    ob[k] = n != ~0u ? b * 143360u + (r >> 1) * 20480u + c * 128u + (r & 1u) * 64u : OOB;
  }
  sa[14] = ub[0]; sa[15] = ub[1]; sb[0] = ub[2]; sb[1] = ob[0]; sb[2] = ob[1]; sb[3] = ob[2];
  sb[4] = __float_as_uint(8.0f); sb[5] = WS_DBG + (blk * 4u + w) * 1024u; sb[6] = 245760u + (b * 4u + w) * 3840u;
  sb[7] = (w == 0u ? j : 31u) * 8192u;
  sb[8] = __float_as_uint(1.4426950408889634f * 0.07216878364870322f); sb[9] = sb[8];
  u32x16 vc;
  const unsigned g = l >> 4, q16 = l & 15u, qq = q16 >> 2, p = l & 3u, row = 4u * g + qq;
  vc[0] = row * 384u + 16u * sig(row) + 16u * (p >> 1) + 8u * (p & 1u);
  vc[1] = q16 * 384u + 16u * (g ^ sig(q16));
  vc[2] = row * 128u + 16u * sig(row) + 16u * (p >> 1) + 8u * (p & 1u);
#pragma unroll
  for (unsigned i = 0; i < 3; ++i) {
    vc[3 + i] = 16u * (l ^ sig((64u * i + l) / 24u));
    vc[9 + i] = v1[1 + i];
    vc[6 + i] = (l >> 3) * 1280u + 16u * ((l & 7u) ^ sig(l >> 3)) + 9216u * i;
  }
#pragma unroll
  for (unsigned i = 0; i < 4; ++i) vc[12 + i] = v1[4 + i];
  u32x4 vd;
  vd[0] = 16u * l; vd[1] = 147456u + 4096u * w + q16 * 128u + 16u * (g ^ sig(q16)); vd[2] = vd[1] ^ 64u;
  const unsigned pn = 14u * (l >> 2) + 2u * (2u * w + ((l >> 1) & 1u)) + (l & 1u);
  const bool pv = l < 40u && !(w == 3u && (l & 2u));
  unsigned rme, rk;
  if (pn < 56u) { const unsigned x = pn / 7u, e = pn - 7u * x; rme = 8u * x + e + (e >= 3u ? 1u : 0u); rk = 0; }
  else if (pn < 112u) { const unsigned x = (pn - 56u) / 7u; rme = pn - 56u + x; rk = 1; }
  else if (pn < 128u) { rme = (pn - 112u) * 4u + 3u; rk = 2; }
  else { const unsigned q = pn - 128u, x = q / 3u, e = q - 3u * x; rme = (4u * x + e + (e >= s0 ? 1u : 0u)) * 4u; rk = 2; }
  vd[3] = pv ? WS_F + (b * 140u + pn) * 64u + hp * 4u + (0 ? (((hp - (rme >> 2)) & 15u) << 20) + (((rme & 3u) * 3u + rk) << 24) : 0u) : OOB + (0 ? 1u << 20 : 0u);
  const unsigned cn = g == 0u ? un[0] : g == 1u ? un[1] : g == 2u ? un[2] : ~0u;
  u32x8 ve;
  ve[0] = cn != ~0u ? WS_F + (b * 140u + cn) * 64u + q16 * 4u : OOB;
  ve[1] = (cn != ~0u && q16 == 0u) ? WS_N + (b * 140u + cn) * 4u : OOB;
  ve[2] = cn != ~0u ? 0xffffu : 0u; ve[3] = l & 48u; ve[4] = q16;
  ve[5] = pv ? WS_N + (b * 140u + pn) * 4u : OOB; ve[6] = 0; ve[7] = 0;
  asm volatile(R"ASM(
s_getpc_b64 s[80:81]
Lmha_pc:
v_lshl_add_u32 v253, v240, 3, s63
v_min_u32 v253, Lmha_end-Lmha_pc-4, v253
global_load_dword v253, v253, s[80:81]
s_add_u32 m0, s43, 122880
s_nop 0
buffer_load_dwordx4 v236, s[24:27], s45 offen lds
buffer_load_dwordx4 v237, s[24:27], s45 offen offset:1024 lds
buffer_load_dwordx4 v238, s[24:27], s45 offen offset:2048 lds
buffer_load_dwordx4 v239, s[24:27], s45 offen offset:3072 lds
s_mul_i32 s77, s45, 6144
s_add_u32 s77, s77, s41
s_add_u32 s78, s77, 0
s_add_u32 m0, s40, 24576
s_nop 0
buffer_load_dwordx4 v233, s[16:19], s78 offen lds
buffer_load_dwordx4 v234, s[16:19], s78 offen offset:1024 lds
buffer_load_dwordx4 v235, s[16:19], s78 offen offset:2048 lds
s_add_u32 s78, s77, 196608
s_add_u32 m0, s40, 27648
s_nop 0
buffer_load_dwordx4 v233, s[16:19], s78 offen lds
buffer_load_dwordx4 v234, s[16:19], s78 offen offset:1024 lds
buffer_load_dwordx4 v235, s[16:19], s78 offen offset:2048 lds
s_cmp_eq_u32 s43, 12288
s_cbranch_scc1 LW3_%=
v_mov_b32 v244, 0x3f803f80
v_mov_b32 v245, 0x3f803f80
v_mov_b32 v246, 0x3f803f80
v_mov_b32 v247, 0x3f803f80
s_waitcnt vmcnt(17)
ds_read_b128 a[144:147], v241
ds_read_b128 a[152:155], v241 offset:2048
ds_read_b128 a[148:151], v242
ds_read_b128 a[156:159], v242 offset:2048
v_add_u32 v216, 0, v224
v_xor_b32 v217, 32, v216
v_xor_b32 v218, 64, v216
v_xor_b32 v219, 96, v216
s_waitcnt vmcnt(11)
s_barrier
T v[152:153],v216,0
T v[154:155],v216,6144
W 0
M v[0:3],v[152:155],a[144:147],0
T v[156:157],v216,12288
T v[158:159],v216,18432
W 0
M v[0:3],v[156:159],a[148:151],v[0:3]
T v[160:161],v217,0
T v[162:163],v217,6144
M v[4:7],v[156:159],a[156:159],0
T v[164:165],v217,12288
T v[166:167],v217,18432
M v[4:7],v[152:155],a[152:155],v[4:7]
T v[168:169],v218,0
T v[170:171],v218,6144
W 4
M v[8:11],v[160:163],a[144:147],0
T v[172:173],v218,12288
T v[174:175],v218,18432
s_add_u32 m0, s43, 147456
W 4
M v[8:11],v[164:167],a[148:151],v[8:11]
T v[176:177],v219,0
T v[178:179],v219,6144
M v[12:15],v[164:167],a[156:159],0
T v[180:181],v219,12288
T v[182:183],v219,18432
buffer_load_dwordx4 v236, s[24:27], s46 offen lds
M v[12:15],v[160:163],a[152:155],v[12:15]
T v[152:153],v216,128
W 7
M v[16:19],v[168:171],a[144:147],0
T v[154:155],v216,6272
W 6
M v[16:19],v[172:175],a[148:151],v[16:19]
T v[156:157],v216,12416
T v[158:159],v216,18560
buffer_load_dwordx4 v237, s[24:27], s46 offen offset:1024 lds
M v[20:23],v[172:175],a[156:159],0
T v[160:161],v217,128
W 7
M v[20:23],v[168:171],a[152:155],v[20:23]
T v[162:163],v217,6272
W 6
M v[24:27],v[176:179],a[144:147],0
T v[164:165],v217,12416
buffer_load_dwordx4 v238, s[24:27], s46 offen offset:2048 lds
M v[24:27],v[180:183],a[148:151],v[24:27]
T v[166:167],v217,18560
W 6
M v[28:31],v[180:183],a[156:159],0
T v[168:169],v218,128
W 5
M v[28:31],v[176:179],a[152:155],v[28:31]
T v[170:171],v218,6272
buffer_load_dwordx4 v239, s[24:27], s46 offen offset:3072 lds
s_mul_i32 s77, s46, 6144
M v[32:35],v[152:155],a[144:147],0
T v[172:173],v218,12416
s_add_u32 s77, s77, s41
M v[32:35],v[156:159],a[148:151],v[32:35]
T v[174:175],v218,18560
s_add_u32 s78, s77, 0
M v[36:39],v[156:159],a[156:159],0
T v[176:177],v219,128
s_add_u32 m0, s40, 49152
M v[36:39],v[152:155],a[152:155],v[36:39]
T v[178:179],v219,6272
W 5
M v[40:43],v[160:163],a[144:147],0
T v[180:181],v219,12416
buffer_load_dwordx4 v233, s[16:19], s78 offen lds
M v[40:43],v[164:167],a[148:151],v[40:43]
T v[182:183],v219,18560
s_waitcnt vmcnt(11)
M v[44:47],v[164:167],a[156:159],0
T v[152:153],v216,256
v_add_u32 v220, 24576, v224
M v[44:47],v[160:163],a[152:155],v[44:47]
T v[154:155],v216,6400
v_add_u32 v250, -24576, v241
v_add_u32 v251, -24576, v242
ds_read_b128 a[160:163], v250
W 6
M v[48:51],v[168:171],a[144:147],0
T v[156:157],v216,12544
buffer_load_dwordx4 v234, s[16:19], s78 offen offset:1024 lds
M v[48:51],v[172:175],a[148:151],v[48:51]
T v[158:159],v216,18688
ds_read_b128 a[168:171], v250 offset:2048
ds_read_b128 a[164:167], v251
M v[52:55],v[172:175],a[156:159],0
T v[160:161],v217,256
ds_read_b128 a[172:175], v251 offset:2048
M v[52:55],v[168:171],a[152:155],v[52:55]
T v[162:163],v217,6400
buffer_load_dwordx4 v235, s[16:19], s78 offen offset:2048 lds
s_mul_i32 s77, s47, 6144
W 9
M v[56:59],v[176:179],a[144:147],0
T v[164:165],v217,12544
v_xor_b32 v221, 32, v220
M v[56:59],v[180:183],a[148:151],v[56:59]
T v[166:167],v217,18688
s_add_u32 s77, s77, s41
M v[60:63],v[180:183],a[156:159],0
T v[168:169],v218,256
v_xor_b32 v222, 64, v220
M v[60:63],v[176:179],a[152:155],v[60:63]
T v[170:171],v218,6400
s_add_u32 s78, s77, 0
W 5
M v[64:67],v[152:155],a[144:147],0
T v[172:173],v218,12544
v_xor_b32 v223, 96, v220
M v[64:67],v[156:159],a[148:151],v[64:67]
T v[174:175],v218,18688
s_add_u32 m0, s40, 73728
M v[68:71],v[156:159],a[156:159],0
T v[176:177],v219,256
buffer_load_dwordx4 v233, s[16:19], s78 offen lds
M v[68:71],v[152:155],a[152:155],v[68:71]
T v[178:179],v219,6400
W 5
M v[72:75],v[160:163],a[144:147],0
T v[180:181],v219,12544
M v[72:75],v[164:167],a[148:151],v[72:75]
T v[182:183],v219,18688
buffer_load_dwordx4 v234, s[16:19], s78 offen offset:1024 lds
M v[76:79],v[164:167],a[156:159],0
s_waitcnt vmcnt(9)
s_barrier
T v[152:153],v220,0
M v[76:79],v[160:163],a[152:155],v[76:79]
T v[154:155],v220,6144
W 5
M v[80:83],v[168:171],a[144:147],0
T v[156:157],v220,12288
buffer_load_dwordx4 v235, s[16:19], s78 offen offset:2048 lds
s_add_u32 m0, s43, 122880
M v[80:83],v[172:175],a[148:151],v[80:83]
T v[158:159],v220,18432
W 5
M v[84:87],v[172:175],a[156:159],0
T v[160:161],v221,0
M v[84:87],v[168:171],a[152:155],v[84:87]
T v[162:163],v221,6144
buffer_load_dwordx4 v236, s[24:27], s47 offen lds
M v[88:91],v[176:179],a[144:147],0
T v[164:165],v221,12288
W 5
M v[88:91],v[180:183],a[148:151],v[88:91]
T v[166:167],v221,18432
M v[92:95],v[180:183],a[156:159],0
T v[168:169],v222,0
buffer_load_dwordx4 v237, s[24:27], s47 offen offset:1024 lds
M v[92:95],v[176:179],a[152:155],v[92:95]
T v[170:171],v222,6144
W 5
buffer_load_dwordx4 v238, s[24:27], s47 offen offset:2048 lds
buffer_load_dwordx4 v239, s[24:27], s47 offen offset:3072 lds
M v[0:3],v[152:155],a[160:163],v[0:3]
T v[172:173],v222,12288
T v[174:175],v222,18432
M v[0:3],v[156:159],a[164:167],v[0:3]
T v[176:177],v223,0
W 6
M v[4:7],v[156:159],a[172:175],v[4:7]
T v[178:179],v223,6144
M v[4:7],v[152:155],a[168:171],v[4:7]
T v[180:181],v223,12288
T v[182:183],v223,18432
M v[8:11],v[160:163],a[160:163],v[8:11]
T v[152:153],v220,128
W 7
M v[8:11],v[164:167],a[164:167],v[8:11]
T v[154:155],v220,6272
W 6
M v[12:15],v[164:167],a[172:175],v[12:15]
T v[156:157],v220,12416
M v[12:15],v[160:163],a[168:171],v[12:15]
T v[158:159],v220,18560
W 6
M v[16:19],v[168:171],a[160:163],v[16:19]
T v[160:161],v221,128
W 5
M v[16:19],v[172:175],a[164:167],v[16:19]
T v[162:163],v221,6272
M v[20:23],v[172:175],a[172:175],v[20:23]
T v[164:165],v221,12416
W 5
M v[20:23],v[168:171],a[168:171],v[20:23]
T v[166:167],v221,18560
M v[24:27],v[176:179],a[160:163],v[24:27]
T v[168:169],v222,128
W 5
M v[24:27],v[180:183],a[164:167],v[24:27]
T v[170:171],v222,6272
M v[28:31],v[180:183],a[172:175],v[28:31]
T v[172:173],v222,12416
W 5
M v[28:31],v[176:179],a[168:171],v[28:31]
T v[174:175],v222,18560
M v[32:35],v[152:155],a[160:163],v[32:35]
T v[176:177],v223,128
W 5
M v[32:35],v[156:159],a[164:167],v[32:35]
T v[178:179],v223,6272
v_add_u32 v216, 49152, v224
M v[36:39],v[156:159],a[172:175],v[36:39]
T v[180:181],v223,12416
v_xor_b32 v217, 32, v216
M v[36:39],v[152:155],a[168:171],v[36:39]
T v[182:183],v223,18560
v_xor_b32 v218, 64, v216
M v[40:43],v[160:163],a[160:163],v[40:43]
T v[152:153],v220,256
v_xor_b32 v219, 96, v216
M v[40:43],v[164:167],a[164:167],v[40:43]
T v[154:155],v220,6400
s_waitcnt vmcnt(10)
M v[44:47],v[164:167],a[172:175],v[44:47]
T v[156:157],v220,12544
ds_read_b128 a[176:179], v241
M v[44:47],v[160:163],a[168:171],v[44:47]
T v[158:159],v220,18688
W 6
M v[48:51],v[168:171],a[160:163],v[48:51]
T v[160:161],v221,256
ds_read_b128 a[184:187], v241 offset:2048
M v[48:51],v[172:175],a[164:167],v[48:51]
T v[162:163],v221,6400
ds_read_b128 a[180:183], v242
M v[52:55],v[172:175],a[172:175],v[52:55]
T v[164:165],v221,12544
ds_read_b128 a[188:191], v242 offset:2048
M v[52:55],v[168:171],a[168:171],v[52:55]
T v[166:167],v221,18688
W 1
M v[56:59],v[176:179],a[160:163],v[56:59]
T v[168:169],v222,256
s_add_u32 m0, s43, 147456
M v[56:59],v[180:183],a[164:167],v[56:59]
T v[170:171],v222,6400
buffer_load_dwordx4 v236, s[24:27], s48 offen lds
M v[60:63],v[180:183],a[172:175],v[60:63]
T v[172:173],v222,12544
M v[60:63],v[176:179],a[168:171],v[60:63]
T v[174:175],v222,18688
M v[64:67],v[152:155],a[160:163],v[64:67]
T v[176:177],v223,256
buffer_load_dwordx4 v237, s[24:27], s48 offen offset:1024 lds
M v[64:67],v[156:159],a[164:167],v[64:67]
T v[178:179],v223,6400
W 5
M v[68:71],v[156:159],a[172:175],v[68:71]
T v[180:181],v223,12544
M v[68:71],v[152:155],a[168:171],v[68:71]
T v[182:183],v223,18688
buffer_load_dwordx4 v238, s[24:27], s48 offen offset:2048 lds
M v[72:75],v[160:163],a[160:163],v[72:75]
s_waitcnt vmcnt(10)
s_barrier
T v[152:153],v216,0
M v[72:75],v[164:167],a[164:167],v[72:75]
T v[154:155],v216,6144
W 5
M v[76:79],v[164:167],a[172:175],v[76:79]
T v[156:157],v216,12288
buffer_load_dwordx4 v239, s[24:27], s48 offen offset:3072 lds
M v[76:79],v[160:163],a[168:171],v[76:79]
T v[158:159],v216,18432
W 5
M v[80:83],v[168:171],a[160:163],v[80:83]
T v[160:161],v217,0
M v[80:83],v[172:175],a[164:167],v[80:83]
T v[162:163],v217,6144
W 5
M v[84:87],v[172:175],a[172:175],v[84:87]
T v[164:165],v217,12288
M v[84:87],v[168:171],a[168:171],v[84:87]
T v[166:167],v217,18432
W 5
M v[88:91],v[176:179],a[160:163],v[88:91]
T v[168:169],v218,0
M v[88:91],v[180:183],a[164:167],v[88:91]
T v[170:171],v218,6144
W 5
M v[92:95],v[180:183],a[172:175],v[92:95]
T v[172:173],v218,12288
M v[92:95],v[176:179],a[168:171],v[92:95]
T v[174:175],v218,18432
W 5
M v[0:3],v[152:155],a[176:179],v[0:3]
T v[176:177],v219,0
s_mul_i32 s77, s48, 6144
M v[0:3],v[156:159],a[180:183],v[0:3]
T v[178:179],v219,6144
s_add_u32 s77, s77, s41
M v[4:7],v[156:159],a[188:191],v[4:7]
T v[180:181],v219,12288
s_add_u32 s78, s77, 0
M v[4:7],v[152:155],a[184:187],v[4:7]
T v[182:183],v219,18432
s_add_u32 m0, s40, 98304
M v[8:11],v[160:163],a[176:179],v[8:11]
T v[152:153],v216,128
W 5
M v[8:11],v[164:167],a[180:183],v[8:11]
T v[154:155],v216,6272
buffer_load_dwordx4 v233, s[16:19], s78 offen lds
M v[12:15],v[164:167],a[188:191],v[12:15]
T v[156:157],v216,12416
v_mov_b32 v213, v104
M v[12:15],v[160:163],a[184:187],v[12:15]
T v[158:159],v216,18560
v_mov_b32 v214, v109
M v[16:19],v[168:171],a[176:179],v[16:19]
T v[160:161],v217,128
buffer_load_dwordx4 v234, s[16:19], s78 offen offset:1024 lds
M v[16:19],v[172:175],a[180:183],v[16:19]
T v[162:163],v217,6272
buffer_load_dword v110, v213, s[28:31], 0 offen sc1
M v[20:23],v[172:175],a[188:191],v[20:23]
T v[164:165],v217,12416
W 5
M v[20:23],v[168:171],a[184:187],v[20:23]
T v[166:167],v217,18560
buffer_load_dwordx4 v235, s[16:19], s78 offen offset:2048 lds
M v[24:27],v[176:179],a[176:179],v[24:27]
T v[168:169],v218,128
W 5
M v[24:27],v[180:183],a[180:183],v[24:27]
T v[170:171],v218,6272
M v[28:31],v[180:183],a[188:191],v[28:31]
T v[172:173],v218,12416
W 5
M v[28:31],v[176:179],a[184:187],v[28:31]
T v[174:175],v218,18560
M v[32:35],v[152:155],a[176:179],v[32:35]
T v[176:177],v219,128
W 5
M v[32:35],v[156:159],a[180:183],v[32:35]
T v[178:179],v219,6272
v_add_u32 v220, 73728, v224
M v[36:39],v[156:159],a[188:191],v[36:39]
T v[180:181],v219,12416
v_xor_b32 v221, 32, v220
M v[36:39],v[152:155],a[184:187],v[36:39]
T v[182:183],v219,18560
v_xor_b32 v222, 64, v220
M v[40:43],v[160:163],a[176:179],v[40:43]
T v[152:153],v216,256
v_xor_b32 v223, 96, v220
M v[40:43],v[164:167],a[180:183],v[40:43]
T v[154:155],v216,6400
s_waitcnt vmcnt(8)
M v[44:47],v[164:167],a[188:191],v[44:47]
T v[156:157],v216,12544
W 5
M v[44:47],v[160:163],a[184:187],v[44:47]
T v[158:159],v216,18688
v_add_u32 v250, -24576, v241
v_add_u32 v251, -24576, v242
ds_read_b128 a[192:195], v250
M v[48:51],v[168:171],a[176:179],v[48:51]
T v[160:161],v217,256
ds_read_b128 a[200:203], v250 offset:2048
M v[48:51],v[172:175],a[180:183],v[48:51]
T v[162:163],v217,6400
ds_read_b128 a[196:199], v251
M v[52:55],v[172:175],a[188:191],v[52:55]
T v[164:165],v217,12544
ds_read_b128 a[204:207], v251 offset:2048
M v[52:55],v[168:171],a[184:187],v[52:55]
T v[166:167],v217,18688
W 1
M v[56:59],v[176:179],a[176:179],v[56:59]
T v[168:169],v218,256
s_add_u32 m0, s43, 122880
M v[56:59],v[180:183],a[180:183],v[56:59]
T v[170:171],v218,6400
buffer_load_dwordx4 v236, s[24:27], s49 offen lds
M v[60:63],v[180:183],a[188:191],v[60:63]
T v[172:173],v218,12544
M v[60:63],v[176:179],a[184:187],v[60:63]
T v[174:175],v218,18688
M v[64:67],v[152:155],a[176:179],v[64:67]
T v[176:177],v219,256
buffer_load_dwordx4 v237, s[24:27], s49 offen offset:1024 lds
M v[64:67],v[156:159],a[180:183],v[64:67]
T v[178:179],v219,6400
W 5
M v[68:71],v[156:159],a[188:191],v[68:71]
T v[180:181],v219,12544
M v[68:71],v[152:155],a[184:187],v[68:71]
T v[182:183],v219,18688
buffer_load_dwordx4 v238, s[24:27], s49 offen offset:2048 lds
M v[72:75],v[160:163],a[176:179],v[72:75]
s_barrier
T v[152:153],v220,0
M v[72:75],v[164:167],a[180:183],v[72:75]
T v[154:155],v220,6144
W 5
M v[76:79],v[164:167],a[188:191],v[76:79]
T v[156:157],v220,12288
buffer_load_dwordx4 v239, s[24:27], s49 offen offset:3072 lds
M v[76:79],v[160:163],a[184:187],v[76:79]
T v[158:159],v220,18432
W 5
M v[80:83],v[168:171],a[176:179],v[80:83]
T v[160:161],v221,0
M v[80:83],v[172:175],a[180:183],v[80:83]
T v[162:163],v221,6144
W 5
M v[84:87],v[172:175],a[188:191],v[84:87]
T v[164:165],v221,12288
M v[84:87],v[168:171],a[184:187],v[84:87]
T v[166:167],v221,18432
W 5
M v[88:91],v[176:179],a[176:179],v[88:91]
T v[168:169],v222,0
M v[88:91],v[180:183],a[180:183],v[88:91]
T v[170:171],v222,6144
W 5
M v[92:95],v[180:183],a[188:191],v[92:95]
T v[172:173],v222,12288
M v[92:95],v[176:179],a[184:187],v[92:95]
T v[174:175],v222,18432
W 5
M v[0:3],v[152:155],a[192:195],v[0:3]
T v[176:177],v223,0
s_mul_i32 s77, s49, 6144
M v[0:3],v[156:159],a[196:199],v[0:3]
T v[178:179],v223,6144
s_add_u32 s77, s77, s41
M v[4:7],v[156:159],a[204:207],v[4:7]
T v[180:181],v223,12288
s_add_u32 s78, s77, 0
M v[4:7],v[152:155],a[200:203],v[4:7]
T v[182:183],v223,18432
s_add_u32 m0, s40, 0
M v[8:11],v[160:163],a[192:195],v[8:11]
T v[152:153],v220,128
W 5
M v[8:11],v[164:167],a[196:199],v[8:11]
T v[154:155],v220,6272
buffer_load_dwordx4 v233, s[16:19], s78 offen lds
M v[12:15],v[164:167],a[204:207],v[12:15]
T v[156:157],v220,12416
W 5
M v[12:15],v[160:163],a[200:203],v[12:15]
T v[158:159],v220,18560
M v[16:19],v[168:171],a[192:195],v[16:19]
T v[160:161],v221,128
buffer_load_dwordx4 v234, s[16:19], s78 offen offset:1024 lds
M v[16:19],v[172:175],a[196:199],v[16:19]
T v[162:163],v221,6272
W 5
M v[20:23],v[172:175],a[204:207],v[20:23]
T v[164:165],v221,12416
M v[20:23],v[168:171],a[200:203],v[20:23]
T v[166:167],v221,18560
buffer_load_dwordx4 v235, s[16:19], s78 offen offset:2048 lds
M v[24:27],v[176:179],a[192:195],v[24:27]
T v[168:169],v222,128
W 5
M v[24:27],v[180:183],a[196:199],v[24:27]
T v[170:171],v222,6272
M v[28:31],v[180:183],a[204:207],v[28:31]
T v[172:173],v222,12416
W 5
M v[28:31],v[176:179],a[200:203],v[28:31]
T v[174:175],v222,18560
M v[32:35],v[152:155],a[192:195],v[32:35]
T v[176:177],v223,128
W 5
M v[32:35],v[156:159],a[196:199],v[32:35]
T v[178:179],v223,6272
v_add_u32 v216, 98304, v224
M v[36:39],v[156:159],a[204:207],v[36:39]
T v[180:181],v223,12416
v_xor_b32 v217, 32, v216
M v[36:39],v[152:155],a[200:203],v[36:39]
T v[182:183],v223,18560
v_xor_b32 v218, 64, v216
M v[40:43],v[160:163],a[192:195],v[40:43]
T v[152:153],v220,256
v_xor_b32 v219, 96, v216
M v[40:43],v[164:167],a[196:199],v[40:43]
T v[154:155],v220,6400
s_waitcnt vmcnt(11)
M v[44:47],v[164:167],a[204:207],v[44:47]
T v[156:157],v220,12544
ds_read_b128 a[208:211], v241
M v[44:47],v[160:163],a[200:203],v[44:47]
T v[158:159],v220,18688
W 6
M v[48:51],v[168:171],a[192:195],v[48:51]
T v[160:161],v221,256
ds_read_b128 a[216:219], v241 offset:2048
M v[48:51],v[172:175],a[196:199],v[48:51]
T v[162:163],v221,6400
ds_read_b128 a[212:215], v242
M v[52:55],v[172:175],a[204:207],v[52:55]
T v[164:165],v221,12544
ds_read_b128 a[220:223], v242 offset:2048
M v[52:55],v[168:171],a[200:203],v[52:55]
T v[166:167],v221,18688
W 1
M v[56:59],v[176:179],a[192:195],v[56:59]
T v[168:169],v222,256
s_add_u32 m0, s43, 147456
M v[56:59],v[180:183],a[196:199],v[56:59]
T v[170:171],v222,6400
buffer_load_dwordx4 v236, s[24:27], s50 offen lds
M v[60:63],v[180:183],a[204:207],v[60:63]
T v[172:173],v222,12544
M v[60:63],v[176:179],a[200:203],v[60:63]
T v[174:175],v222,18688
M v[64:67],v[152:155],a[192:195],v[64:67]
T v[176:177],v223,256
buffer_load_dwordx4 v237, s[24:27], s50 offen offset:1024 lds
M v[64:67],v[156:159],a[196:199],v[64:67]
T v[178:179],v223,6400
W 5
M v[68:71],v[156:159],a[204:207],v[68:71]
T v[180:181],v223,12544
M v[68:71],v[152:155],a[200:203],v[68:71]
T v[182:183],v223,18688
buffer_load_dwordx4 v238, s[24:27], s50 offen offset:2048 lds
M v[72:75],v[160:163],a[192:195],v[72:75]
s_waitcnt vmcnt(10)
s_barrier
T v[152:153],v216,0
M v[72:75],v[164:167],a[196:199],v[72:75]
T v[154:155],v216,6144
W 5
M v[76:79],v[164:167],a[204:207],v[76:79]
T v[156:157],v216,12288
buffer_load_dwordx4 v239, s[24:27], s50 offen offset:3072 lds
M v[76:79],v[160:163],a[200:203],v[76:79]
T v[158:159],v216,18432
W 5
M v[80:83],v[168:171],a[192:195],v[80:83]
T v[160:161],v217,0
M v[80:83],v[172:175],a[196:199],v[80:83]
T v[162:163],v217,6144
W 5
M v[84:87],v[172:175],a[204:207],v[84:87]
T v[164:165],v217,12288
M v[84:87],v[168:171],a[200:203],v[84:87]
T v[166:167],v217,18432
W 5
M v[88:91],v[176:179],a[192:195],v[88:91]
T v[168:169],v218,0
M v[88:91],v[180:183],a[196:199],v[88:91]
T v[170:171],v218,6144
W 5
M v[92:95],v[180:183],a[204:207],v[92:95]
T v[172:173],v218,12288
M v[92:95],v[176:179],a[200:203],v[92:95]
T v[174:175],v218,18432
W 5
M v[0:3],v[152:155],a[208:211],v[0:3]
T v[176:177],v219,0
s_mul_i32 s77, s50, 6144
M v[0:3],v[156:159],a[212:215],v[0:3]
T v[178:179],v219,6144
s_add_u32 s77, s77, s41
M v[4:7],v[156:159],a[220:223],v[4:7]
T v[180:181],v219,12288
s_add_u32 s78, s77, 0
M v[4:7],v[152:155],a[216:219],v[4:7]
T v[182:183],v219,18432
s_add_u32 m0, s40, 24576
M v[8:11],v[160:163],a[208:211],v[8:11]
T v[152:153],v216,128
W 5
M v[8:11],v[164:167],a[212:215],v[8:11]
T v[154:155],v216,6272
buffer_load_dwordx4 v233, s[16:19], s78 offen lds
M v[12:15],v[164:167],a[220:223],v[12:15]
T v[156:157],v216,12416
W 5
M v[12:15],v[160:163],a[216:219],v[12:15]
T v[158:159],v216,18560
M v[16:19],v[168:171],a[208:211],v[16:19]
T v[160:161],v217,128
buffer_load_dwordx4 v234, s[16:19], s78 offen offset:1024 lds
M v[16:19],v[172:175],a[212:215],v[16:19]
T v[162:163],v217,6272
W 5
M v[20:23],v[172:175],a[220:223],v[20:23]
T v[164:165],v217,12416
M v[20:23],v[168:171],a[216:219],v[20:23]
T v[166:167],v217,18560
buffer_load_dwordx4 v235, s[16:19], s78 offen offset:2048 lds
M v[24:27],v[176:179],a[208:211],v[24:27]
T v[168:169],v218,128
W 5
M v[24:27],v[180:183],a[212:215],v[24:27]
T v[170:171],v218,6272
M v[28:31],v[180:183],a[220:223],v[28:31]
T v[172:173],v218,12416
W 5
M v[28:31],v[176:179],a[216:219],v[28:31]
T v[174:175],v218,18560
M v[32:35],v[152:155],a[208:211],v[32:35]
T v[176:177],v219,128
W 5
M v[32:35],v[156:159],a[212:215],v[32:35]
T v[178:179],v219,6272
v_add_u32 v220, 0, v224
M v[36:39],v[156:159],a[220:223],v[36:39]
T v[180:181],v219,12416
v_xor_b32 v221, 32, v220
M v[36:39],v[152:155],a[216:219],v[36:39]
T v[182:183],v219,18560
v_xor_b32 v222, 64, v220
M v[40:43],v[160:163],a[208:211],v[40:43]
T v[152:153],v216,256
v_xor_b32 v223, 96, v220
M v[40:43],v[164:167],a[212:215],v[40:43]
T v[154:155],v216,6400
s_waitcnt vmcnt(10)
M v[44:47],v[164:167],a[220:223],v[44:47]
T v[156:157],v216,12544
W 5
M v[44:47],v[160:163],a[216:219],v[44:47]
T v[158:159],v216,18688
v_add_u32 v250, -24576, v241
v_add_u32 v251, -24576, v242
ds_read_b128 a[224:227], v250
M v[48:51],v[168:171],a[208:211],v[48:51]
T v[160:161],v217,256
ds_read_b128 a[232:235], v250 offset:2048
M v[48:51],v[172:175],a[212:215],v[48:51]
T v[162:163],v217,6400
ds_read_b128 a[228:231], v251
M v[52:55],v[172:175],a[220:223],v[52:55]
T v[164:165],v217,12544
ds_read_b128 a[236:239], v251 offset:2048
M v[52:55],v[168:171],a[216:219],v[52:55]
T v[166:167],v217,18688
W 1
M v[56:59],v[176:179],a[208:211],v[56:59]
T v[168:169],v218,256
s_add_u32 m0, s43, 122880
M v[56:59],v[180:183],a[212:215],v[56:59]
T v[170:171],v218,6400
buffer_load_dwordx4 v236, s[24:27], s51 offen lds
M v[60:63],v[180:183],a[220:223],v[60:63]
T v[172:173],v218,12544
M v[60:63],v[176:179],a[216:219],v[60:63]
T v[174:175],v218,18688
M v[64:67],v[152:155],a[208:211],v[64:67]
T v[176:177],v219,256
buffer_load_dwordx4 v237, s[24:27], s51 offen offset:1024 lds
M v[64:67],v[156:159],a[212:215],v[64:67]
T v[178:179],v219,6400
W 5
M v[68:71],v[156:159],a[220:223],v[68:71]
T v[180:181],v219,12544
M v[68:71],v[152:155],a[216:219],v[68:71]
T v[182:183],v219,18688
buffer_load_dwordx4 v238, s[24:27], s51 offen offset:2048 lds
M v[72:75],v[160:163],a[208:211],v[72:75]
s_waitcnt vmcnt(10)
s_barrier
T v[152:153],v220,0
M v[72:75],v[164:167],a[212:215],v[72:75]
T v[154:155],v220,6144
W 5
M v[76:79],v[164:167],a[220:223],v[76:79]
T v[156:157],v220,12288
buffer_load_dwordx4 v239, s[24:27], s51 offen offset:3072 lds
M v[76:79],v[160:163],a[216:219],v[76:79]
T v[158:159],v220,18432
W 5
M v[80:83],v[168:171],a[208:211],v[80:83]
T v[160:161],v221,0
M v[80:83],v[172:175],a[212:215],v[80:83]
T v[162:163],v221,6144
W 5
M v[84:87],v[172:175],a[220:223],v[84:87]
T v[164:165],v221,12288
M v[84:87],v[168:171],a[216:219],v[84:87]
T v[166:167],v221,18432
W 5
M v[88:91],v[176:179],a[208:211],v[88:91]
T v[168:169],v222,0
M v[88:91],v[180:183],a[212:215],v[88:91]
T v[170:171],v222,6144
W 5
M v[92:95],v[180:183],a[220:223],v[92:95]
T v[172:173],v222,12288
M v[92:95],v[176:179],a[216:219],v[92:95]
T v[174:175],v222,18432
W 5
M v[0:3],v[152:155],a[224:227],v[0:3]
T v[176:177],v223,0
s_mul_i32 s77, s51, 6144
M v[0:3],v[156:159],a[228:231],v[0:3]
T v[178:179],v223,6144
s_add_u32 s77, s77, s41
M v[4:7],v[156:159],a[236:239],v[4:7]
T v[180:181],v223,12288
s_add_u32 s78, s77, 0
M v[4:7],v[152:155],a[232:235],v[4:7]
T v[182:183],v223,18432
s_add_u32 m0, s40, 49152
M v[8:11],v[160:163],a[224:227],v[8:11]
T v[152:153],v220,128
W 5
M v[8:11],v[164:167],a[228:231],v[8:11]
T v[154:155],v220,6272
buffer_load_dwordx4 v233, s[16:19], s78 offen lds
M v[12:15],v[164:167],a[236:239],v[12:15]
T v[156:157],v220,12416
W 5
M v[12:15],v[160:163],a[232:235],v[12:15]
T v[158:159],v220,18560
M v[16:19],v[168:171],a[224:227],v[16:19]
T v[160:161],v221,128
buffer_load_dwordx4 v234, s[16:19], s78 offen offset:1024 lds
M v[16:19],v[172:175],a[228:231],v[16:19]
T v[162:163],v221,6272
W 5
M v[20:23],v[172:175],a[236:239],v[20:23]
T v[164:165],v221,12416
M v[20:23],v[168:171],a[232:235],v[20:23]
T v[166:167],v221,18560
buffer_load_dwordx4 v235, s[16:19], s78 offen offset:2048 lds
M v[24:27],v[176:179],a[224:227],v[24:27]
T v[168:169],v222,128
W 5
M v[24:27],v[180:183],a[228:231],v[24:27]
T v[170:171],v222,6272
M v[28:31],v[180:183],a[236:239],v[28:31]
T v[172:173],v222,12416
W 5
M v[28:31],v[176:179],a[232:235],v[28:31]
T v[174:175],v222,18560
M v[32:35],v[152:155],a[224:227],v[32:35]
T v[176:177],v223,128
W 5
M v[32:35],v[156:159],a[228:231],v[32:35]
T v[178:179],v223,6272
v_add_u32 v216, 24576, v224
M v[36:39],v[156:159],a[236:239],v[36:39]
T v[180:181],v223,12416
v_xor_b32 v217, 32, v216
M v[36:39],v[152:155],a[232:235],v[36:39]
T v[182:183],v223,18560
v_xor_b32 v218, 64, v216
M v[40:43],v[160:163],a[224:227],v[40:43]
T v[152:153],v220,256
v_xor_b32 v219, 96, v216
M v[40:43],v[164:167],a[228:231],v[40:43]
T v[154:155],v220,6400
s_waitcnt vmcnt(10)
M v[44:47],v[164:167],a[236:239],v[44:47]
T v[156:157],v220,12544
ds_read_b128 a[240:243], v241
M v[44:47],v[160:163],a[232:235],v[44:47]
T v[158:159],v220,18688
W 6
M v[48:51],v[168:171],a[224:227],v[48:51]
T v[160:161],v221,256
ds_read_b128 a[248:251], v241 offset:2048
M v[48:51],v[172:175],a[228:231],v[48:51]
T v[162:163],v221,6400
ds_read_b128 a[244:247], v242
M v[52:55],v[172:175],a[236:239],v[52:55]
T v[164:165],v221,12544
ds_read_b128 a[252:255], v242 offset:2048
M v[52:55],v[168:171],a[232:235],v[52:55]
T v[166:167],v221,18688
W 1
M v[56:59],v[176:179],a[224:227],v[56:59]
T v[168:169],v222,256
s_add_u32 m0, s43, 147456
M v[56:59],v[180:183],a[228:231],v[56:59]
T v[170:171],v222,6400
buffer_load_dwordx4 v236, s[24:27], s52 offen lds
M v[60:63],v[180:183],a[236:239],v[60:63]
T v[172:173],v222,12544
M v[60:63],v[176:179],a[232:235],v[60:63]
T v[174:175],v222,18688
M v[64:67],v[152:155],a[224:227],v[64:67]
T v[176:177],v223,256
buffer_load_dwordx4 v237, s[24:27], s52 offen offset:1024 lds
M v[64:67],v[156:159],a[228:231],v[64:67]
T v[178:179],v223,6400
W 5
M v[68:71],v[156:159],a[236:239],v[68:71]
T v[180:181],v223,12544
M v[68:71],v[152:155],a[232:235],v[68:71]
T v[182:183],v223,18688
buffer_load_dwordx4 v238, s[24:27], s52 offen offset:2048 lds
M v[72:75],v[160:163],a[224:227],v[72:75]
s_waitcnt vmcnt(10)
s_barrier
T v[152:153],v216,0
M v[72:75],v[164:167],a[228:231],v[72:75]
T v[154:155],v216,6144
W 5
M v[76:79],v[164:167],a[236:239],v[76:79]
T v[156:157],v216,12288
buffer_load_dwordx4 v239, s[24:27], s52 offen offset:3072 lds
M v[76:79],v[160:163],a[232:235],v[76:79]
T v[158:159],v216,18432
W 5
M v[80:83],v[168:171],a[224:227],v[80:83]
T v[160:161],v217,0
M v[80:83],v[172:175],a[228:231],v[80:83]
T v[162:163],v217,6144
W 5
M v[84:87],v[172:175],a[236:239],v[84:87]
T v[164:165],v217,12288
M v[84:87],v[168:171],a[232:235],v[84:87]
T v[166:167],v217,18432
W 5
M v[88:91],v[176:179],a[224:227],v[88:91]
T v[168:169],v218,0
M v[88:91],v[180:183],a[228:231],v[88:91]
T v[170:171],v218,6144
W 5
M v[92:95],v[180:183],a[236:239],v[92:95]
T v[172:173],v218,12288
M v[92:95],v[176:179],a[232:235],v[92:95]
T v[174:175],v218,18432
W 5
M v[0:3],v[152:155],a[240:243],v[0:3]
T v[176:177],v219,0
s_mul_i32 s77, s52, 6144
M v[0:3],v[156:159],a[244:247],v[0:3]
T v[178:179],v219,6144
s_add_u32 s77, s77, s41
M v[4:7],v[156:159],a[252:255],v[4:7]
T v[180:181],v219,12288
s_add_u32 s78, s77, 0
M v[4:7],v[152:155],a[248:251],v[4:7]
T v[182:183],v219,18432
s_add_u32 m0, s40, 73728
M v[8:11],v[160:163],a[240:243],v[8:11]
T v[152:153],v216,128
W 5
M v[8:11],v[164:167],a[244:247],v[8:11]
T v[154:155],v216,6272
buffer_load_dwordx4 v233, s[16:19], s78 offen lds
M v[12:15],v[164:167],a[252:255],v[12:15]
T v[156:157],v216,12416
W 5
M v[12:15],v[160:163],a[248:251],v[12:15]
T v[158:159],v216,18560
M v[16:19],v[168:171],a[240:243],v[16:19]
T v[160:161],v217,128
buffer_load_dwordx4 v234, s[16:19], s78 offen offset:1024 lds
M v[16:19],v[172:175],a[244:247],v[16:19]
T v[162:163],v217,6272
W 5
M v[20:23],v[172:175],a[252:255],v[20:23]
T v[164:165],v217,12416
M v[20:23],v[168:171],a[248:251],v[20:23]
T v[166:167],v217,18560
buffer_load_dwordx4 v235, s[16:19], s78 offen offset:2048 lds
M v[24:27],v[176:179],a[240:243],v[24:27]
T v[168:169],v218,128
W 5
M v[24:27],v[180:183],a[244:247],v[24:27]
T v[170:171],v218,6272
M v[28:31],v[180:183],a[252:255],v[28:31]
T v[172:173],v218,12416
W 5
M v[28:31],v[176:179],a[248:251],v[28:31]
T v[174:175],v218,18560
M v[32:35],v[152:155],a[240:243],v[32:35]
T v[176:177],v219,128
W 5
M v[32:35],v[156:159],a[244:247],v[32:35]
T v[178:179],v219,6272
v_add_u32 v220, 49152, v224
M v[36:39],v[156:159],a[252:255],v[36:39]
T v[180:181],v219,12416
v_xor_b32 v221, 32, v220
M v[36:39],v[152:155],a[248:251],v[36:39]
T v[182:183],v219,18560
v_xor_b32 v222, 64, v220
M v[40:43],v[160:163],a[240:243],v[40:43]
T v[152:153],v216,256
v_xor_b32 v223, 96, v220
M v[40:43],v[164:167],a[244:247],v[40:43]
T v[154:155],v216,6400
s_waitcnt vmcnt(10)
M v[44:47],v[164:167],a[252:255],v[44:47]
T v[156:157],v216,12544
W 5
M v[44:47],v[160:163],a[248:251],v[44:47]
T v[158:159],v216,18688
v_add_u32 v250, -24576, v241
v_add_u32 v251, -24576, v242
ds_read_b128 v[184:187], v250
M v[48:51],v[168:171],a[240:243],v[48:51]
T v[160:161],v217,256
ds_read_b128 v[192:195], v250 offset:2048
M v[48:51],v[172:175],a[244:247],v[48:51]
T v[162:163],v217,6400
ds_read_b128 v[188:191], v251
M v[52:55],v[172:175],a[252:255],v[52:55]
T v[164:165],v217,12544
ds_read_b128 v[196:199], v251 offset:2048
M v[52:55],v[168:171],a[248:251],v[52:55]
T v[166:167],v217,18688
W 1
M v[56:59],v[176:179],a[240:243],v[56:59]
T v[168:169],v218,256
s_add_u32 m0, s43, 122880
M v[56:59],v[180:183],a[244:247],v[56:59]
T v[170:171],v218,6400
buffer_load_dwordx4 v236, s[24:27], s53 offen lds
M v[60:63],v[180:183],a[252:255],v[60:63]
T v[172:173],v218,12544
M v[60:63],v[176:179],a[248:251],v[60:63]
T v[174:175],v218,18688
M v[64:67],v[152:155],a[240:243],v[64:67]
T v[176:177],v219,256
buffer_load_dwordx4 v237, s[24:27], s53 offen offset:1024 lds
M v[64:67],v[156:159],a[244:247],v[64:67]
T v[178:179],v219,6400
W 5
M v[68:71],v[156:159],a[252:255],v[68:71]
T v[180:181],v219,12544
M v[68:71],v[152:155],a[248:251],v[68:71]
T v[182:183],v219,18688
buffer_load_dwordx4 v238, s[24:27], s53 offen offset:2048 lds
M v[72:75],v[160:163],a[240:243],v[72:75]
s_waitcnt vmcnt(10)
s_barrier
T v[152:153],v220,0
M v[72:75],v[164:167],a[244:247],v[72:75]
T v[154:155],v220,6144
W 5
M v[76:79],v[164:167],a[252:255],v[76:79]
T v[156:157],v220,12288
buffer_load_dwordx4 v239, s[24:27], s53 offen offset:3072 lds
M v[76:79],v[160:163],a[248:251],v[76:79]
T v[158:159],v220,18432
W 5
M v[80:83],v[168:171],a[240:243],v[80:83]
T v[160:161],v221,0
M v[80:83],v[172:175],a[244:247],v[80:83]
T v[162:163],v221,6144
W 5
M v[84:87],v[172:175],a[252:255],v[84:87]
T v[164:165],v221,12288
M v[84:87],v[168:171],a[248:251],v[84:87]
T v[166:167],v221,18432
W 5
M v[88:91],v[176:179],a[240:243],v[88:91]
T v[168:169],v222,0
M v[88:91],v[180:183],a[244:247],v[88:91]
T v[170:171],v222,6144
W 5
M v[92:95],v[180:183],a[252:255],v[92:95]
T v[172:173],v222,12288
M v[92:95],v[176:179],a[248:251],v[92:95]
T v[174:175],v222,18432
W 5
M v[0:3],v[152:155],v[184:187],v[0:3]
T v[176:177],v223,0
s_mul_i32 s77, s53, 6144
M v[0:3],v[156:159],v[188:191],v[0:3]
T v[178:179],v223,6144
s_add_u32 s77, s77, s41
M v[4:7],v[156:159],v[196:199],v[4:7]
T v[180:181],v223,12288
s_add_u32 s78, s77, 0
M v[4:7],v[152:155],v[192:195],v[4:7]
T v[182:183],v223,18432
s_add_u32 m0, s40, 98304
M v[8:11],v[160:163],v[184:187],v[8:11]
T v[152:153],v220,128
W 5
M v[8:11],v[164:167],v[188:191],v[8:11]
T v[154:155],v220,6272
buffer_load_dwordx4 v233, s[16:19], s78 offen lds
M v[12:15],v[164:167],v[196:199],v[12:15]
T v[156:157],v220,12416
W 5
M v[12:15],v[160:163],v[192:195],v[12:15]
T v[158:159],v220,18560
M v[16:19],v[168:171],v[184:187],v[16:19]
T v[160:161],v221,128
buffer_load_dwordx4 v234, s[16:19], s78 offen offset:1024 lds
M v[16:19],v[172:175],v[188:191],v[16:19]
T v[162:163],v221,6272
W 5
M v[20:23],v[172:175],v[196:199],v[20:23]
T v[164:165],v221,12416
M v[20:23],v[168:171],v[192:195],v[20:23]
T v[166:167],v221,18560
buffer_load_dwordx4 v235, s[16:19], s78 offen offset:2048 lds
M v[24:27],v[176:179],v[184:187],v[24:27]
T v[168:169],v222,128
s_add_u32 s79, s0, 0
M v[24:27],v[180:183],v[188:191],v[24:27]
T v[170:171],v222,6272
s_add_u32 m0, s40, 0
M v[28:31],v[180:183],v[196:199],v[28:31]
T v[172:173],v222,12416
buffer_load_dwordx4 v227, s[8:11], s79 offen nt lds
M v[28:31],v[176:179],v[192:195],v[28:31]
T v[174:175],v222,18560
W 5
M v[32:35],v[152:155],v[184:187],v[32:35]
T v[176:177],v223,128
M v[32:35],v[156:159],v[188:191],v[32:35]
T v[178:179],v223,6272
buffer_load_dwordx4 v228, s[8:11], s79 offen offset:1024 nt lds
M v[36:39],v[156:159],v[196:199],v[36:39]
T v[180:181],v223,12416
v_add_u32 v216, 73728, v224
M v[36:39],v[152:155],v[192:195],v[36:39]
T v[182:183],v223,18560
v_xor_b32 v217, 32, v216
M v[40:43],v[160:163],v[184:187],v[40:43]
T v[152:153],v220,256
W 5
M v[40:43],v[164:167],v[188:191],v[40:43]
T v[154:155],v220,6400
s_waitcnt vmcnt(12)
M v[44:47],v[164:167],v[196:199],v[44:47]
T v[156:157],v220,12544
ds_read_b128 a[144:147], v241
M v[44:47],v[160:163],v[192:195],v[44:47]
T v[158:159],v220,18688
ds_read_b128 a[152:155], v241 offset:2048
M v[48:51],v[168:171],v[184:187],v[48:51]
T v[160:161],v221,256
ds_read_b128 a[148:151], v242
M v[48:51],v[172:175],v[188:191],v[48:51]
T v[162:163],v221,6400
ds_read_b128 a[156:159], v242 offset:2048
M v[52:55],v[172:175],v[196:199],v[52:55]
v_xor_b32 v218, 64, v216
v_xor_b32 v219, 96, v216
M v[52:55],v[168:171],v[192:195],v[52:55]
buffer_load_dwordx4 v229, s[8:11], s79 offen offset:2048 nt lds
W 6
M v[56:59],v[176:179],v[184:187],v[56:59]
T v[164:165],v221,12544
T v[166:167],v221,18688
M v[56:59],v[180:183],v[188:191],v[56:59]
T v[168:169],v222,256
W 5
M v[60:63],v[180:183],v[196:199],v[60:63]
T v[170:171],v222,6400
W 4
M v[60:63],v[176:179],v[192:195],v[60:63]
T v[172:173],v222,12544
T v[174:175],v222,18688
M v[64:67],v[152:155],v[184:187],v[64:67]
T v[176:177],v223,256
M v[64:67],v[156:159],v[188:191],v[64:67]
T v[178:179],v223,6400
W 6
M v[68:71],v[156:159],v[196:199],v[68:71]
T v[180:181],v223,12544
M v[68:71],v[152:155],v[192:195],v[68:71]
T v[182:183],v223,18688
W 6
M v[72:75],v[160:163],v[184:187],v[72:75]
s_waitcnt vmcnt(10)
s_barrier
T v[152:153],v216,0
M v[72:75],v[164:167],v[188:191],v[72:75]
T v[154:155],v216,6144
s_mul_i32 s77, s52, 6144
M v[76:79],v[164:167],v[196:199],v[76:79]
T v[156:157],v216,12288
s_add_u32 s77, s77, s41
M v[76:79],v[160:163],v[192:195],v[76:79]
T v[158:159],v216,18432
s_add_u32 s78, s77, 384
M v[80:83],v[168:171],v[184:187],v[80:83]
T v[160:161],v217,0
W 5
M v[80:83],v[172:175],v[188:191],v[80:83]
T v[162:163],v217,6144
s_add_u32 m0, s40, 24576
M v[84:87],v[172:175],v[196:199],v[84:87]
T v[164:165],v217,12288
buffer_load_dwordx4 v233, s[16:19], s78 offen lds
M v[84:87],v[168:171],v[192:195],v[84:87]
T v[166:167],v217,18432
W 5
M v[88:91],v[176:179],v[184:187],v[88:91]
T v[168:169],v218,0
M v[88:91],v[180:183],v[188:191],v[88:91]
T v[170:171],v218,6144
buffer_load_dwordx4 v234, s[16:19], s78 offen offset:1024 lds
M v[92:95],v[180:183],v[196:199],v[92:95]
T v[172:173],v218,12288
W 5
M v[92:95],v[176:179],v[192:195],v[92:95]
T v[174:175],v218,18432
M v[0:3],v[152:155],a[144:147],v[0:3]
T v[176:177],v219,0
buffer_load_dwordx4 v235, s[16:19], s78 offen offset:2048 lds
M v[0:3],v[156:159],a[148:151],v[0:3]
T v[178:179],v219,6144
v_lshrrev_b32 v110, v108, v110
M v[4:7],v[156:159],a[156:159],v[4:7]
T v[180:181],v219,12288
v_and_b32 v110, 1, v110
M v[4:7],v[152:155],a[152:155],v[4:7]
T v[182:183],v219,18432
v_cmp_eq_u32 s[74:75], 0, v110
M v[8:11],v[160:163],a[144:147],v[8:11]
T v[152:153],v216,128
W 5
M v[8:11],v[164:167],a[148:151],v[8:11]
T v[154:155],v216,6272
v_lshrrev_b64 v[110:111], v107, s[74:75]
M v[12:15],v[164:167],a[156:159],v[12:15]
T v[156:157],v216,12416
v_and_b32 v212, v106, v110
M v[12:15],v[160:163],a[152:155],v[12:15]
T v[158:159],v216,18560
buffer_store_dword v212, v105, s[28:31], 0 offen sc1
M v[16:19],v[168:171],a[144:147],v[16:19]
T v[160:161],v217,128
W 5
M v[16:19],v[172:175],a[148:151],v[16:19]
T v[162:163],v217,6272
M v[20:23],v[172:175],a[156:159],v[20:23]
T v[164:165],v217,12416
W 5
M v[20:23],v[168:171],a[152:155],v[20:23]
T v[166:167],v217,18560
M v[24:27],v[176:179],a[144:147],v[24:27]
T v[168:169],v218,128
W 5
M v[24:27],v[180:183],a[148:151],v[24:27]
T v[170:171],v218,6272
M v[28:31],v[180:183],a[156:159],v[28:31]
T v[172:173],v218,12416
W 5
M v[28:31],v[176:179],a[152:155],v[28:31]
T v[174:175],v218,18560
M v[32:35],v[152:155],a[144:147],v[32:35]
T v[176:177],v219,128
W 5
M v[32:35],v[156:159],a[148:151],v[32:35]
T v[178:179],v219,6272
v_add_u32 v220, 98304, v224
M v[36:39],v[156:159],a[156:159],v[36:39]
T v[180:181],v219,12416
v_xor_b32 v221, 32, v220
M v[36:39],v[152:155],a[152:155],v[36:39]
T v[182:183],v219,18560
v_xor_b32 v222, 64, v220
M v[40:43],v[160:163],a[144:147],v[40:43]
T v[152:153],v216,256
v_xor_b32 v223, 96, v220
M v[40:43],v[164:167],a[148:151],v[40:43]
T v[154:155],v216,6400
s_waitcnt vmcnt(10)
M v[44:47],v[164:167],a[156:159],v[44:47]
T v[156:157],v216,12544
W 5
M v[44:47],v[160:163],a[152:155],v[44:47]
T v[158:159],v216,18688
v_add_u32 v250, -24576, v241
v_add_u32 v251, -24576, v242
ds_read_b128 a[160:163], v250
M v[48:51],v[168:171],a[144:147],v[48:51]
T v[160:161],v217,256
ds_read_b128 a[168:171], v250 offset:2048
M v[48:51],v[172:175],a[148:151],v[48:51]
T v[162:163],v217,6400
ds_read_b128 a[164:167], v251
M v[52:55],v[172:175],a[156:159],v[52:55]
T v[164:165],v217,12544
ds_read_b128 a[172:175], v251 offset:2048
M v[52:55],v[168:171],a[152:155],v[52:55]
T v[166:167],v217,18688
W 9
M v[56:59],v[176:179],a[144:147],v[56:59]
T v[168:169],v218,256
W 8
M v[56:59],v[180:183],a[148:151],v[56:59]
T v[170:171],v218,6400
W 7
M v[60:63],v[180:183],a[156:159],v[60:63]
T v[172:173],v218,12544
W 6
M v[60:63],v[176:179],a[152:155],v[60:63]
T v[174:175],v218,18688
W 5
M v[64:67],v[152:155],a[144:147],v[64:67]
T v[176:177],v219,256
M v[64:67],v[156:159],a[148:151],v[64:67]
T v[178:179],v219,6400
W 5
M v[68:71],v[156:159],a[156:159],v[68:71]
T v[180:181],v219,12544
M v[68:71],v[152:155],a[152:155],v[68:71]
T v[182:183],v219,18688
W 5
M v[72:75],v[160:163],a[144:147],v[72:75]
s_waitcnt vmcnt(7)
s_barrier
T v[152:153],v220,0
M v[72:75],v[164:167],a[148:151],v[72:75]
T v[154:155],v220,6144
s_add_u32 s79, s0, 0
M v[76:79],v[164:167],a[156:159],v[76:79]
T v[156:157],v220,12288
s_add_u32 m0, s40, 49152
M v[76:79],v[160:163],a[152:155],v[76:79]
T v[158:159],v220,18432
buffer_load_dwordx4 v227, s[12:15], s79 offen nt lds
M v[80:83],v[168:171],a[144:147],v[80:83]
T v[160:161],v221,0
W 5
M v[80:83],v[172:175],a[148:151],v[80:83]
T v[162:163],v221,6144
M v[84:87],v[172:175],a[156:159],v[84:87]
T v[164:165],v221,12288
buffer_load_dwordx4 v228, s[12:15], s79 offen offset:1024 nt lds
M v[84:87],v[168:171],a[152:155],v[84:87]
T v[166:167],v221,18432
W 5
M v[88:91],v[176:179],a[144:147],v[88:91]
T v[168:169],v222,0
M v[88:91],v[180:183],a[148:151],v[88:91]
T v[170:171],v222,6144
buffer_load_dwordx4 v229, s[12:15], s79 offen offset:2048 nt lds
M v[92:95],v[180:183],a[156:159],v[92:95]
T v[172:173],v222,12288
W 5
M v[92:95],v[176:179],a[152:155],v[92:95]
T v[174:175],v222,18432
M v[0:3],v[152:155],a[160:163],v[0:3]
T v[176:177],v223,0
W 5
M v[0:3],v[156:159],a[164:167],v[0:3]
T v[178:179],v223,6144
M v[4:7],v[156:159],a[172:175],v[4:7]
T v[180:181],v223,12288
W 5
M v[4:7],v[152:155],a[168:171],v[4:7]
T v[182:183],v223,18432
M v[8:11],v[160:163],a[160:163],v[8:11]
T v[152:153],v220,128
W 5
M v[8:11],v[164:167],a[164:167],v[8:11]
T v[154:155],v220,6272
M v[12:15],v[164:167],a[172:175],v[12:15]
T v[156:157],v220,12416
W 5
M v[12:15],v[160:163],a[168:171],v[12:15]
T v[158:159],v220,18560
M v[16:19],v[168:171],a[160:163],v[16:19]
T v[160:161],v221,128
W 5
M v[16:19],v[172:175],a[164:167],v[16:19]
T v[162:163],v221,6272
M v[20:23],v[172:175],a[172:175],v[20:23]
T v[164:165],v221,12416
W 5
M v[20:23],v[168:171],a[168:171],v[20:23]
T v[166:167],v221,18560
v_pk_mul_f32 v[0:1], v[0:1], s[64:65]
M v[24:27],v[176:179],a[160:163],v[24:27]
T v[168:169],v222,128
v_pk_mul_f32 v[8:9], v[8:9], s[64:65]
M v[24:27],v[180:183],a[164:167],v[24:27]
T v[170:171],v222,6272
v_pk_mul_f32 v[2:3], v[2:3], s[64:65]
M v[28:31],v[180:183],a[172:175],v[28:31]
T v[172:173],v222,12416
v_pk_mul_f32 v[10:11], v[10:11], s[64:65]
M v[28:31],v[176:179],a[168:171],v[28:31]
T v[174:175],v222,18560
v_cvt_pk_bf16_f32 v204, v0, v1
M v[32:35],v[152:155],a[160:163],v[32:35]
T v[176:177],v223,128
W 5
M v[32:35],v[156:159],a[164:167],v[32:35]
T v[178:179],v223,6272
v_cvt_pk_bf16_f32 v206, v8, v9
M v[36:39],v[156:159],a[172:175],v[36:39]
T v[180:181],v223,12416
v_cvt_pk_bf16_f32 v205, v2, v3
M v[36:39],v[152:155],a[168:171],v[36:39]
T v[182:183],v223,18560
v_cvt_pk_bf16_f32 v207, v10, v11
M v[40:43],v[160:163],a[160:163],v[40:43]
T v[152:153],v220,256
v_pk_mul_f32 v[4:5], v[4:5], s[64:65]
M v[40:43],v[164:167],a[164:167],v[40:43]
T v[154:155],v220,6400
v_pk_mul_f32 v[12:13], v[12:13], s[64:65]
M v[44:47],v[164:167],a[172:175],v[44:47]
T v[156:157],v220,12544
v_pk_mul_f32 v[6:7], v[6:7], s[64:65]
v_pk_mul_f32 v[14:15], v[14:15], s[64:65]
M v[44:47],v[160:163],a[168:171],v[44:47]
T v[158:159],v220,18688
v_permlane32_swap_b32 v204, v206
v_permlane32_swap_b32 v205, v207
W 5
M v[48:51],v[168:171],a[160:163],v[48:51]
T v[160:161],v221,256
v_permlane16_swap_b32 v204, v206
M v[48:51],v[172:175],a[164:167],v[48:51]
T v[162:163],v221,6400
v_permlane16_swap_b32 v205, v207
v_accvgpr_write_b32 a0, v204
M v[52:55],v[172:175],a[172:175],v[52:55]
T v[164:165],v221,12544
v_accvgpr_write_b32 a1, v205
v_accvgpr_write_b32 a2, v206
M v[52:55],v[168:171],a[168:171],v[52:55]
T v[166:167],v221,18688
v_accvgpr_write_b32 a3, v207
v_cvt_pk_bf16_f32 v208, v4, v5
M v[56:59],v[176:179],a[160:163],v[56:59]
T v[168:169],v222,256
v_cvt_pk_bf16_f32 v210, v12, v13
v_cvt_pk_bf16_f32 v209, v6, v7
W 5
M v[56:59],v[180:183],a[164:167],v[56:59]
T v[170:171],v222,6400
v_cvt_pk_bf16_f32 v211, v14, v15
v_permlane32_swap_b32 v208, v210
M v[60:63],v[180:183],a[172:175],v[60:63]
T v[172:173],v222,12544
v_permlane32_swap_b32 v209, v211
v_permlane16_swap_b32 v208, v210
M v[60:63],v[176:179],a[168:171],v[60:63]
T v[174:175],v222,18688
v_permlane16_swap_b32 v209, v211
v_accvgpr_write_b32 a4, v208
M v[64:67],v[152:155],a[160:163],v[64:67]
T v[176:177],v223,256
v_accvgpr_write_b32 a5, v209
v_accvgpr_write_b32 a6, v210
M v[64:67],v[156:159],a[164:167],v[64:67]
T v[178:179],v223,6400
v_accvgpr_write_b32 a7, v211
v_pk_mul_f32 v[16:17], v[16:17], s[64:65]
v_pk_mul_f32 v[24:25], v[24:25], s[64:65]
M v[68:71],v[156:159],a[172:175],v[68:71]
T v[180:181],v223,12544
v_pk_mul_f32 v[18:19], v[18:19], s[64:65]
v_pk_mul_f32 v[26:27], v[26:27], s[64:65]
M v[68:71],v[152:155],a[168:171],v[68:71]
T v[182:183],v223,18688
v_cvt_pk_bf16_f32 v204, v16, v17
v_cvt_pk_bf16_f32 v206, v24, v25
W 5
M v[72:75],v[160:163],a[160:163],v[72:75]
v_cvt_pk_bf16_f32 v205, v18, v19
v_cvt_pk_bf16_f32 v207, v26, v27
v_pk_mul_f32 v[20:21], v[20:21], s[64:65]
M v[72:75],v[164:167],a[164:167],v[72:75]
v_pk_mul_f32 v[28:29], v[28:29], s[64:65]
v_pk_mul_f32 v[22:23], v[22:23], s[64:65]
M v[76:79],v[164:167],a[172:175],v[76:79]
v_pk_mul_f32 v[30:31], v[30:31], s[64:65]
v_permlane32_swap_b32 v204, v206
v_permlane32_swap_b32 v205, v207
M v[76:79],v[160:163],a[168:171],v[76:79]
v_permlane16_swap_b32 v204, v206
v_permlane16_swap_b32 v205, v207
v_accvgpr_write_b32 a8, v204
M v[80:83],v[168:171],a[160:163],v[80:83]
v_add_u32 v216, 0, v225
v_xor_b32 v217, 64, v216
s_waitcnt vmcnt(7)
s_barrier
ds_read_b128 v[152:155], v216 offset:0
v_accvgpr_write_b32 a9, v205
v_accvgpr_write_b32 a10, v206
W 1
M v[80:83],v[172:175],a[164:167],v[80:83]
ds_read_b128 v[156:159], v216 offset:6144
s_add_u32 s79, s1, 0
s_add_u32 m0, s40, 73728
s_nop 0
buffer_load_dwordx4 v227, s[8:11], s79 offen nt lds
v_accvgpr_write_b32 a11, v207
v_cvt_pk_bf16_f32 v208, v20, v21
v_cvt_pk_bf16_f32 v210, v28, v29
M v[84:87],v[172:175],a[172:175],v[84:87]
ds_read_b128 v[160:163], v216 offset:12288
v_cvt_pk_bf16_f32 v209, v22, v23
v_cvt_pk_bf16_f32 v211, v30, v31
M v[84:87],v[168:171],a[168:171],v[84:87]
ds_read_b128 v[164:167], v216 offset:18432
v_permlane32_swap_b32 v208, v210
v_permlane32_swap_b32 v209, v211
s_nop 0
v_permlane16_swap_b32 v208, v210
M v[88:91],v[176:179],a[160:163],v[88:91]
ds_read_b128 v[168:171], v217 offset:0
v_permlane16_swap_b32 v209, v211
v_accvgpr_write_b32 a12, v208
v_accvgpr_write_b32 a13, v209
M v[88:91],v[180:183],a[164:167],v[88:91]
ds_read_b128 v[172:175], v217 offset:6144
v_accvgpr_write_b32 a14, v210
v_accvgpr_write_b32 a15, v211
v_pk_mul_f32 v[32:33], v[32:33], s[64:65]
M v[92:95],v[180:183],a[172:175],v[92:95]
v_pk_mul_f32 v[40:41], v[40:41], s[64:65]
v_pk_mul_f32 v[34:35], v[34:35], s[64:65]
v_pk_mul_f32 v[42:43], v[42:43], s[64:65]
M v[92:95],v[176:179],a[168:171],v[92:95]
ds_read_b128 v[176:179], v217 offset:12288
v_cvt_pk_bf16_f32 v204, v32, v33
v_cvt_pk_bf16_f32 v206, v40, v41
v_cvt_pk_bf16_f32 v205, v34, v35
W 4
M v[104:107],v[152:155],a[0:3],0
v_cvt_pk_bf16_f32 v207, v42, v43
v_pk_mul_f32 v[36:37], v[36:37], s[64:65]
v_pk_mul_f32 v[44:45], v[44:45], s[64:65]
M v[108:111],v[152:155],a[4:7],0
ds_read_b128 v[180:183], v217 offset:18432
v_pk_mul_f32 v[38:39], v[38:39], s[64:65]
v_pk_mul_f32 v[46:47], v[46:47], s[64:65]
v_permlane32_swap_b32 v204, v206
M v[112:115],v[156:159],a[0:3],0
v_permlane32_swap_b32 v205, v207
v_permlane16_swap_b32 v204, v206
s_nop 0
v_permlane16_swap_b32 v205, v207
M v[116:119],v[156:159],a[4:7],0
ds_read_b128 v[152:155], v216 offset:128
v_accvgpr_write_b32 a16, v204
v_accvgpr_write_b32 a17, v205
v_accvgpr_write_b32 a18, v206
M v[120:123],v[160:163],a[0:3],0
v_accvgpr_write_b32 a19, v207
v_cvt_pk_bf16_f32 v208, v36, v37
v_cvt_pk_bf16_f32 v210, v44, v45
M v[124:127],v[160:163],a[4:7],0
ds_read_b128 v[156:159], v216 offset:6272
v_cvt_pk_bf16_f32 v209, v38, v39
v_cvt_pk_bf16_f32 v211, v46, v47
v_permlane32_swap_b32 v208, v210
W 3
M v[128:131],v[164:167],a[0:3],0
v_permlane32_swap_b32 v209, v211
v_permlane16_swap_b32 v208, v210
s_nop 0
v_permlane16_swap_b32 v209, v211
v_accvgpr_write_b32 a20, v208
M v[132:135],v[164:167],a[4:7],0
ds_read_b128 v[160:163], v216 offset:12416
v_accvgpr_write_b32 a21, v209
v_accvgpr_write_b32 a22, v210
v_accvgpr_write_b32 a23, v211
M v[104:107],v[168:171],a[8:11],v[104:107]
v_pk_mul_f32 v[48:49], v[48:49], s[64:65]
v_pk_mul_f32 v[56:57], v[56:57], s[64:65]
v_pk_mul_f32 v[50:51], v[50:51], s[64:65]
M v[108:111],v[168:171],a[12:15],v[108:111]
ds_read_b128 v[164:167], v216 offset:18560
v_pk_mul_f32 v[58:59], v[58:59], s[64:65]
v_cvt_pk_bf16_f32 v204, v48, v49
v_cvt_pk_bf16_f32 v206, v56, v57
M v[112:115],v[172:175],a[8:11],v[112:115]
v_cvt_pk_bf16_f32 v205, v50, v51
v_cvt_pk_bf16_f32 v207, v58, v59
v_pk_mul_f32 v[52:53], v[52:53], s[64:65]
M v[116:119],v[172:175],a[12:15],v[116:119]
ds_read_b128 v[168:171], v217 offset:128
v_pk_mul_f32 v[60:61], v[60:61], s[64:65]
v_pk_mul_f32 v[54:55], v[54:55], s[64:65]
v_pk_mul_f32 v[62:63], v[62:63], s[64:65]
M v[120:123],v[176:179],a[8:11],v[120:123]
v_permlane32_swap_b32 v204, v206
v_permlane32_swap_b32 v205, v207
s_nop 0
v_permlane16_swap_b32 v204, v206
M v[124:127],v[176:179],a[12:15],v[124:127]
ds_read_b128 v[172:175], v217 offset:6272
v_permlane16_swap_b32 v205, v207
v_accvgpr_write_b32 a24, v204
v_accvgpr_write_b32 a25, v205
s_mul_i32 s77, s53, 6144
W 3
M v[128:131],v[180:183],a[8:11],v[128:131]
buffer_load_dwordx4 v228, s[8:11], s79 offen offset:1024 nt lds
v_accvgpr_write_b32 a26, v206
v_accvgpr_write_b32 a27, v207
v_cvt_pk_bf16_f32 v208, v52, v53
M v[132:135],v[180:183],a[12:15],v[132:135]
ds_read_b128 v[176:179], v217 offset:12416
v_cvt_pk_bf16_f32 v210, v60, v61
v_cvt_pk_bf16_f32 v209, v54, v55
v_cvt_pk_bf16_f32 v211, v62, v63
v_permlane32_swap_b32 v208, v210
M v[104:107],v[152:155],a[16:19],v[104:107]
v_permlane32_swap_b32 v209, v211
v_permlane16_swap_b32 v208, v210
s_nop 0
v_permlane16_swap_b32 v209, v211
M v[108:111],v[152:155],a[20:23],v[108:111]
ds_read_b128 v[180:183], v217 offset:18560
v_accvgpr_write_b32 a28, v208
v_accvgpr_write_b32 a29, v209
v_accvgpr_write_b32 a30, v210
M v[112:115],v[156:159],a[16:19],v[112:115]
v_accvgpr_write_b32 a31, v211
v_pk_mul_f32 v[64:65], v[64:65], s[64:65]
v_pk_mul_f32 v[72:73], v[72:73], s[64:65]
M v[116:119],v[156:159],a[20:23],v[116:119]
ds_read_b128 v[152:155], v216 offset:256
v_pk_mul_f32 v[66:67], v[66:67], s[64:65]
v_pk_mul_f32 v[74:75], v[74:75], s[64:65]
v_cvt_pk_bf16_f32 v204, v64, v65
s_add_u32 s77, s77, s41
M v[120:123],v[160:163],a[16:19],v[120:123]
v_cvt_pk_bf16_f32 v206, v72, v73
v_cvt_pk_bf16_f32 v205, v66, v67
v_cvt_pk_bf16_f32 v207, v74, v75
M v[124:127],v[160:163],a[20:23],v[124:127]
ds_read_b128 v[156:159], v216 offset:6400
v_pk_mul_f32 v[68:69], v[68:69], s[64:65]
v_pk_mul_f32 v[76:77], v[76:77], s[64:65]
v_pk_mul_f32 v[70:71], v[70:71], s[64:65]
W 3
M v[128:131],v[164:167],a[16:19],v[128:131]
v_pk_mul_f32 v[78:79], v[78:79], s[64:65]
v_permlane32_swap_b32 v204, v206
v_permlane32_swap_b32 v205, v207
M v[132:135],v[164:167],a[20:23],v[132:135]
ds_read_b128 v[160:163], v216 offset:12544
v_permlane16_swap_b32 v204, v206
v_permlane16_swap_b32 v205, v207
v_accvgpr_write_b32 a32, v204
M v[104:107],v[168:171],a[24:27],v[104:107]
v_accvgpr_write_b32 a33, v205
v_accvgpr_write_b32 a34, v206
v_accvgpr_write_b32 a35, v207
v_cvt_pk_bf16_f32 v208, v68, v69
M v[108:111],v[168:171],a[28:31],v[108:111]
ds_read_b128 v[164:167], v216 offset:18688
v_cvt_pk_bf16_f32 v210, v76, v77
v_cvt_pk_bf16_f32 v209, v70, v71
v_cvt_pk_bf16_f32 v211, v78, v79
s_add_u32 s78, s77, 384
M v[112:115],v[172:175],a[24:27],v[112:115]
v_permlane32_swap_b32 v208, v210
v_permlane32_swap_b32 v209, v211
s_nop 0
v_permlane16_swap_b32 v208, v210
M v[116:119],v[172:175],a[28:31],v[116:119]
ds_read_b128 v[168:171], v217 offset:256
v_permlane16_swap_b32 v209, v211
v_accvgpr_write_b32 a36, v208
v_accvgpr_write_b32 a37, v209
M v[120:123],v[176:179],a[24:27],v[120:123]
v_accvgpr_write_b32 a38, v210
v_accvgpr_write_b32 a39, v211
v_pk_mul_f32 v[80:81], v[80:81], s[64:65]
M v[124:127],v[176:179],a[28:31],v[124:127]
ds_read_b128 v[172:175], v217 offset:6400
v_pk_mul_f32 v[88:89], v[88:89], s[64:65]
v_pk_mul_f32 v[82:83], v[82:83], s[64:65]
v_pk_mul_f32 v[90:91], v[90:91], s[64:65]
W 3
M v[128:131],v[180:183],a[24:27],v[128:131]
v_cvt_pk_bf16_f32 v204, v80, v81
v_cvt_pk_bf16_f32 v206, v88, v89
v_cvt_pk_bf16_f32 v205, v82, v83
M v[132:135],v[180:183],a[28:31],v[132:135]
ds_read_b128 v[176:179], v217 offset:12544
v_cvt_pk_bf16_f32 v207, v90, v91
v_pk_mul_f32 v[84:85], v[84:85], s[64:65]
v_pk_mul_f32 v[92:93], v[92:93], s[64:65]
s_add_u32 m0, s40, 122880
M v[104:107],v[152:155],a[32:35],v[104:107]
s_add_u32 m0, s40, 73728
v_pk_mul_f32 v[86:87], v[86:87], s[64:65]
v_pk_mul_f32 v[94:95], v[94:95], s[64:65]
v_permlane32_swap_b32 v204, v206
M v[108:111],v[152:155],a[36:39],v[108:111]
ds_read_b128 v[180:183], v217 offset:18688
v_permlane32_swap_b32 v205, v207
v_permlane16_swap_b32 v204, v206
s_nop 0
v_permlane16_swap_b32 v205, v207
v_accvgpr_write_b32 a40, v204
M v[112:115],v[156:159],a[32:35],v[112:115]
buffer_load_dwordx4 v229, s[8:11], s79 offen offset:2048 nt lds
v_accvgpr_write_b32 a41, v205
v_accvgpr_write_b32 a42, v206
v_accvgpr_write_b32 a43, v207
M v[116:119],v[156:159],a[36:39],v[116:119]
v_cvt_pk_bf16_f32 v208, v84, v85
v_cvt_pk_bf16_f32 v210, v92, v93
v_cvt_pk_bf16_f32 v209, v86, v87
M v[120:123],v[160:163],a[32:35],v[120:123]
v_cvt_pk_bf16_f32 v211, v94, v95
v_permlane32_swap_b32 v208, v210
s_nop 0
v_permlane32_swap_b32 v209, v211
s_add_u32 m0, s40, 122880
M v[124:127],v[160:163],a[36:39],v[124:127]
buffer_load_dwordx4 v233, s[16:19], s78 offen lds
v_permlane16_swap_b32 v208, v210
v_permlane16_swap_b32 v209, v211
v_accvgpr_write_b32 a44, v208
W 1
M v[128:131],v[164:167],a[32:35],v[128:131]
v_accvgpr_write_b32 a45, v209
v_accvgpr_write_b32 a46, v210
v_accvgpr_write_b32 a47, v211
M v[132:135],v[164:167],a[36:39],v[132:135]
v_add_u32 v220, 24576, v224
v_xor_b32 v221, 32, v220
M v[104:107],v[168:171],a[40:43],v[104:107]
v_xor_b32 v222, 64, v220
v_xor_b32 v223, 96, v220
s_waitcnt vmcnt(8)
s_barrier
T v[152:153],v220,0
T v[154:155],v220,6144
M v[108:111],v[168:171],a[44:47],v[108:111]
T v[156:157],v220,12288
T v[158:159],v220,18432
M v[112:115],v[172:175],a[40:43],v[112:115]
T v[160:161],v221,0
buffer_load_dwordx4 v234, s[16:19], s78 offen offset:1024 lds
M v[116:119],v[172:175],a[44:47],v[116:119]
T v[162:163],v221,6144
M v[120:123],v[176:179],a[40:43],v[120:123]
T v[164:165],v221,12288
M v[124:127],v[176:179],a[44:47],v[124:127]
T v[166:167],v221,18432
buffer_load_dwordx4 v235, s[16:19], s78 offen offset:2048 lds
W 6
M v[128:131],v[180:183],a[40:43],v[128:131]
T v[168:169],v222,0
v_max3_f32 v248, v104, v105, v106
M v[132:135],v[180:183],a[44:47],v[132:135]
T v[170:171],v222,6144
v_max3_f32 v249, v108, v109, v110
M a[48:51],v[152:155],a[144:147],0
T v[172:173],v222,12288
W 5
M a[48:51],v[156:159],a[148:151],a[48:51]
T v[174:175],v222,18432
v_max_f32 v248, v248, v107
M a[52:55],v[156:159],a[156:159],0
T v[176:177],v223,0
v_max_f32 v249, v249, v111
v_max3_f32 v248, v248, v112, v113
M a[52:55],v[152:155],a[152:155],a[52:55]
T v[178:179],v223,6144
v_max3_f32 v249, v249, v116, v117
v_max3_f32 v248, v248, v114, v115
M a[56:59],v[160:163],a[144:147],0
T v[180:181],v223,12288
v_max3_f32 v249, v249, v118, v119
v_max3_f32 v248, v248, v120, v121
v_max3_f32 v249, v249, v124, v125
W 5
M a[56:59],v[164:167],a[148:151],a[56:59]
T v[182:183],v223,18432
s_add_u32 s79, s1, 0
v_max3_f32 v248, v248, v122, v123
v_max3_f32 v249, v249, v126, v127
v_max3_f32 v248, v248, v128, v129
M a[60:63],v[164:167],a[156:159],0
T v[152:153],v220,128
s_add_u32 m0, s40, 98304
v_max3_f32 v249, v249, v132, v133
v_max3_f32 v248, v248, v130, v131
v_max3_f32 v249, v249, v134, v135
M a[60:63],v[160:163],a[152:155],a[60:63]
T v[154:155],v220,6272
buffer_load_dwordx4 v227, s[12:15], s79 offen nt lds
v_mov_b32 v250, v248
v_mov_b32 v251, v249
M a[64:67],v[168:171],a[144:147],0
T v[156:157],v220,12416
v_permlane32_swap_b32 v248, v250
v_permlane32_swap_b32 v249, v251
W 5
M a[64:67],v[172:175],a[148:151],a[64:67]
T v[158:159],v220,18560
v_max_f32 v248, v248, v250
v_max_f32 v249, v249, v251
M a[68:71],v[172:175],a[156:159],0
T v[160:161],v221,128
v_mov_b32 v250, v248
v_mov_b32 v251, v249
M a[68:71],v[168:171],a[152:155],a[68:71]
T v[162:163],v221,6272
v_permlane16_swap_b32 v248, v250
v_permlane16_swap_b32 v249, v251
M a[72:75],v[176:179],a[144:147],0
T v[164:165],v221,12416
v_max_f32 v248, v248, v250
v_max_f32 v249, v249, v251
W 5
M a[72:75],v[180:183],a[148:151],a[72:75]
T v[166:167],v221,18560
v_sub_f32 v104, v104, v248
v_sub_f32 v105, v105, v248
M a[76:79],v[180:183],a[156:159],0
T v[168:169],v222,128
v_sub_f32 v106, v106, v248
v_sub_f32 v107, v107, v248
M a[76:79],v[176:179],a[152:155],a[76:79]
T v[170:171],v222,6272
v_sub_f32 v108, v108, v249
v_sub_f32 v109, v109, v249
M a[80:83],v[152:155],a[144:147],0
T v[172:173],v222,12416
v_sub_f32 v110, v110, v249
v_sub_f32 v111, v111, v249
W 5
M a[80:83],v[156:159],a[148:151],a[80:83]
T v[174:175],v222,18560
v_sub_f32 v112, v112, v248
v_sub_f32 v113, v113, v248
M a[84:87],v[156:159],a[156:159],0
T v[176:177],v223,128
v_sub_f32 v114, v114, v248
v_sub_f32 v115, v115, v248
M a[84:87],v[152:155],a[152:155],a[84:87]
T v[178:179],v223,6272
v_sub_f32 v116, v116, v249
v_sub_f32 v117, v117, v249
M a[88:91],v[160:163],a[144:147],0
T v[180:181],v223,12416
v_sub_f32 v118, v118, v249
v_sub_f32 v119, v119, v249
W 5
M a[88:91],v[164:167],a[148:151],a[88:91]
T v[182:183],v223,18560
v_exp_f32 v200, v104
M a[92:95],v[164:167],a[156:159],0
T v[152:153],v220,256
v_exp_f32 v201, v105
M a[92:95],v[160:163],a[152:155],a[92:95]
T v[154:155],v220,6400
v_cvt_pk_bf16_f32 v136, v200, v201
v_exp_f32 v202, v106
M a[96:99],v[168:171],a[144:147],0
T v[156:157],v220,12544
v_exp_f32 v203, v107
W 5
M a[96:99],v[172:175],a[148:151],a[96:99]
T v[158:159],v220,18688
v_cvt_pk_bf16_f32 v137, v202, v203
M a[100:103],v[172:175],a[156:159],0
T v[160:161],v221,256
v_exp_f32 v200, v108
M a[100:103],v[168:171],a[152:155],a[100:103]
T v[162:163],v221,6400
buffer_load_dwordx4 v228, s[12:15], s79 offen offset:1024 nt lds
v_exp_f32 v201, v109
M a[104:107],v[176:179],a[144:147],0
T v[164:165],v221,12544
v_exp_f32 v202, v110
W 5
M a[104:107],v[180:183],a[148:151],a[104:107]
T v[166:167],v221,18688
v_cvt_pk_bf16_f32 v140, v200, v201
v_exp_f32 v203, v111
M a[108:111],v[180:183],a[156:159],0
T v[168:169],v222,256
v_cvt_pk_bf16_f32 v141, v202, v203
M a[108:111],v[176:179],a[152:155],a[108:111]
T v[170:171],v222,6400
v_exp_f32 v200, v112
M a[112:115],v[152:155],a[144:147],0
T v[172:173],v222,12544
v_exp_f32 v201, v113
W 5
M a[112:115],v[156:159],a[148:151],a[112:115]
T v[174:175],v222,18688
v_cvt_pk_bf16_f32 v138, v200, v201
v_exp_f32 v202, v114
M a[116:119],v[156:159],a[156:159],0
T v[176:177],v223,256
v_exp_f32 v203, v115
M a[116:119],v[152:155],a[152:155],a[116:119]
T v[178:179],v223,6400
v_cvt_pk_bf16_f32 v139, v202, v203
M a[120:123],v[160:163],a[144:147],0
T v[180:181],v223,12544
v_exp_f32 v200, v116
W 5
M a[120:123],v[164:167],a[148:151],a[120:123]
T v[182:183],v223,18688
v_exp_f32 v201, v117
M a[124:127],v[164:167],a[156:159],0
v_exp_f32 v202, v118
M a[124:127],v[160:163],a[152:155],a[124:127]
v_cvt_pk_bf16_f32 v142, v200, v201
v_sub_f32 v120, v120, v248
M a[128:131],v[168:171],a[144:147],0
v_add_u32 v216, 49152, v224
v_xor_b32 v217, 32, v216
v_xor_b32 v218, 64, v216
v_xor_b32 v219, 96, v216
s_waitcnt vmcnt(8)
s_barrier
T v[152:153],v216,0
T v[154:155],v216,6144
v_exp_f32 v203, v119
W 4
M a[128:131],v[172:175],a[148:151],a[128:131]
T v[156:157],v217,0
v_cvt_pk_bf16_f32 v143, v202, v203
v_sub_f32 v121, v121, v248
M a[132:135],v[172:175],a[156:159],0
T v[158:159],v217,6144
v_sub_f32 v122, v122, v248
v_sub_f32 v123, v123, v248
M a[132:135],v[168:171],a[152:155],a[132:135]
T v[160:161],v218,0
v_sub_f32 v124, v124, v249
v_sub_f32 v125, v125, v249
M a[136:139],v[176:179],a[144:147],0
T v[162:163],v218,6144
v_sub_f32 v126, v126, v249
v_sub_f32 v127, v127, v249
W 6
M a[136:139],v[180:183],a[148:151],a[136:139]
T v[164:165],v219,0
v_sub_f32 v128, v128, v248
v_sub_f32 v129, v129, v248
M a[140:143],v[180:183],a[156:159],0
T v[166:167],v219,6144
v_sub_f32 v130, v130, v248
v_sub_f32 v131, v131, v248
M a[140:143],v[176:179],a[152:155],a[140:143]
T v[168:169],v216,128
v_sub_f32 v132, v132, v249
v_sub_f32 v133, v133, v249
W 5
M v[0:3],v[152:155],v[136:139],0
T v[170:171],v216,6272
buffer_load_dwordx4 v229, s[12:15], s79 offen offset:2048 nt lds
v_sub_f32 v134, v134, v249
v_sub_f32 v135, v135, v249
M v[4:7],v[152:155],v[140:143],0
T v[172:173],v217,128
v_exp_f32 v200, v120
M v[96:99],v[244:247],v[136:139],0
T v[174:175],v217,6272
v_exp_f32 v201, v121
M v[100:103],v[244:247],v[140:143],0
T v[176:177],v218,128
v_cvt_pk_bf16_f32 v144, v200, v201
v_exp_f32 v202, v122
M v[8:11],v[156:159],v[136:139],0
T v[178:179],v218,6272
v_exp_f32 v203, v123
M v[12:15],v[156:159],v[140:143],0
T v[180:181],v219,128
v_exp_f32 v200, v124
W 5
M v[16:19],v[160:163],v[136:139],0
T v[182:183],v219,6272
v_cvt_pk_bf16_f32 v145, v202, v203
M v[20:23],v[160:163],v[140:143],0
T v[152:153],v216,256
v_exp_f32 v201, v125
M v[24:27],v[164:167],v[136:139],0
T v[154:155],v216,6400
v_cvt_pk_bf16_f32 v148, v200, v201
v_exp_f32 v202, v126
M v[28:31],v[164:167],v[140:143],0
T v[156:157],v217,256
v_exp_f32 v203, v127
M v[32:35],v[168:171],v[136:139],0
T v[158:159],v217,6400
v_cvt_pk_bf16_f32 v149, v202, v203
M v[36:39],v[168:171],v[140:143],0
T v[160:161],v218,256
v_exp_f32 v200, v128
W 5
M v[40:43],v[172:175],v[136:139],0
T v[162:163],v218,6400
v_exp_f32 v201, v129
M v[44:47],v[172:175],v[140:143],0
T v[164:165],v219,256
v_cvt_pk_bf16_f32 v146, v200, v201
v_exp_f32 v202, v130
M v[48:51],v[176:179],v[136:139],0
T v[166:167],v219,6400
v_exp_f32 v203, v131
M v[52:55],v[176:179],v[140:143],0
T v[168:169],v216,12288
v_cvt_pk_bf16_f32 v147, v202, v203
M v[56:59],v[180:183],v[136:139],0
T v[170:171],v216,18432
v_exp_f32 v200, v132
M v[60:63],v[180:183],v[140:143],0
T v[172:173],v217,12288
v_exp_f32 v201, v133
W 5
M v[64:67],v[152:155],v[136:139],0
T v[174:175],v217,18432
s_add_u32 s79, s2, 0
v_exp_f32 v202, v134
M v[68:71],v[152:155],v[140:143],0
T v[176:177],v218,12288
s_add_u32 m0, s40, 0
v_cvt_pk_bf16_f32 v150, v200, v201
v_exp_f32 v203, v135
M v[72:75],v[156:159],v[136:139],0
T v[178:179],v218,18432
buffer_load_dwordx4 v227, s[8:11], s79 offen nt lds
v_cvt_pk_bf16_f32 v151, v202, v203
M v[76:79],v[156:159],v[140:143],0
T v[180:181],v219,12288
v_sub_f32 v204, 0, v248
v_sub_f32 v205, 0, v248
M v[80:83],v[160:163],v[136:139],0
T v[182:183],v219,18432
v_sub_f32 v206, 0, v248
v_sub_f32 v207, 0, v248
M v[84:87],v[160:163],v[140:143],0
T v[152:153],v216,12416
v_sub_f32 v208, 0, v249
v_sub_f32 v209, 0, v249
W 5
M v[88:91],v[164:167],v[136:139],0
T v[154:155],v216,18560
v_sub_f32 v210, 0, v249
v_sub_f32 v211, 0, v249
M v[92:95],v[164:167],v[140:143],0
T v[156:157],v217,12416
v_add_u32 v220, 73728, v225
M v[0:3],v[168:171],v[144:147],v[0:3]
T v[158:159],v217,18560
v_xor_b32 v221, 64, v220
M v[4:7],v[168:171],v[148:151],v[4:7]
T v[160:161],v218,12416
W 5
M v[96:99],v[244:247],v[144:147],v[96:99]
T v[162:163],v218,18560
M v[100:103],v[244:247],v[148:151],v[100:103]
T v[164:165],v219,12416
W 5
M v[8:11],v[172:175],v[144:147],v[8:11]
T v[166:167],v219,18560
M v[12:15],v[172:175],v[148:151],v[12:15]
T v[168:169],v216,12544
W 5
M v[16:19],v[176:179],v[144:147],v[16:19]
T v[170:171],v216,18688
M v[20:23],v[176:179],v[148:151],v[20:23]
T v[172:173],v217,12544
W 5
M v[24:27],v[180:183],v[144:147],v[24:27]
T v[174:175],v217,18688
M v[28:31],v[180:183],v[148:151],v[28:31]
T v[176:177],v218,12544
W 5
M v[32:35],v[152:155],v[144:147],v[32:35]
T v[178:179],v218,18688
M v[36:39],v[152:155],v[148:151],v[36:39]
T v[180:181],v219,12544
W 5
M v[40:43],v[156:159],v[144:147],v[40:43]
T v[182:183],v219,18688
M v[44:47],v[156:159],v[148:151],v[44:47]
s_waitcnt vmcnt(7)
s_barrier
ds_read_b128 v[152:155], v220 offset:0
M v[48:51],v[160:163],v[144:147],v[48:51]
ds_read_b128 v[156:159], v221 offset:0
buffer_load_dwordx4 v228, s[8:11], s79 offen offset:1024 nt lds
M v[52:55],v[160:163],v[148:151],v[52:55]
ds_read_b128 v[160:163], v220 offset:128
W 5
M v[56:59],v[164:167],v[144:147],v[56:59]
M v[60:63],v[164:167],v[148:151],v[60:63]
W 3
M v[64:67],v[168:171],v[144:147],v[64:67]
M v[68:71],v[168:171],v[148:151],v[68:71]
ds_read_b128 v[164:167], v221 offset:128
W 2
M v[72:75],v[172:175],v[144:147],v[72:75]
ds_read_b128 v[168:171], v220 offset:256
M v[76:79],v[172:175],v[148:151],v[76:79]
ds_read_b128 v[172:175], v221 offset:256
M v[80:83],v[176:179],v[144:147],v[80:83]
M v[84:87],v[176:179],v[148:151],v[84:87]
M v[88:91],v[180:183],v[144:147],v[88:91]
W 2
M v[92:95],v[180:183],v[148:151],v[92:95]
ds_read_b128 v[176:179], v220 offset:6144
M v[104:107],v[152:155],a[0:3],v[204:207]
ds_read_b128 v[180:183], v221 offset:6144
W 2
M v[104:107],v[156:159],a[8:11],v[104:107]
M v[104:107],v[160:163],a[16:19],v[104:107]
M v[108:111],v[160:163],a[20:23],v[208:211]
M v[108:111],v[156:159],a[12:15],v[108:111]
M v[108:111],v[152:155],a[4:7],v[108:111]
ds_read_b128 v[152:155], v220 offset:6272
W 1
M v[108:111],v[164:167],a[28:31],v[108:111]
ds_read_b128 v[156:159], v221 offset:6272
M v[108:111],v[168:171],a[36:39],v[108:111]
ds_read_b128 v[160:163], v220 offset:6400
M v[108:111],v[172:175],a[44:47],v[108:111]
buffer_load_dwordx4 v229, s[8:11], s79 offen offset:2048 nt lds
s_mul_i32 s77, s46, 6144
M v[104:107],v[172:175],a[40:43],v[104:107]
s_add_u32 s77, s77, s41
s_add_u32 s78, s77, 384
M v[104:107],v[168:171],a[32:35],v[104:107]
s_add_u32 m0, s40, 24576
s_nop 0
buffer_load_dwordx4 v233, s[16:19], s78 offen lds
M v[104:107],v[164:167],a[24:27],v[104:107]
ds_read_b128 v[164:167], v221 offset:6400
W 2
M v[112:115],v[176:179],a[0:3],v[204:207]
ds_read_b128 v[168:171], v220 offset:12288
M v[112:115],v[180:183],a[8:11],v[112:115]
ds_read_b128 v[172:175], v221 offset:12288
buffer_load_dwordx4 v234, s[16:19], s78 offen offset:1024 lds
M v[112:115],v[152:155],a[16:19],v[112:115]
M v[116:119],v[152:155],a[20:23],v[208:211]
v_max3_f32 v248, v104, v105, v106
v_max3_f32 v249, v108, v109, v110
M v[116:119],v[180:183],a[12:15],v[116:119]
buffer_load_dwordx4 v235, s[16:19], s78 offen offset:2048 lds
v_max_f32 v248, v248, v107
M v[116:119],v[176:179],a[4:7],v[116:119]
ds_read_b128 v[176:179], v220 offset:12416
v_max_f32 v249, v249, v111
M v[116:119],v[156:159],a[28:31],v[116:119]
ds_read_b128 v[180:183], v221 offset:12416
W 2
M v[116:119],v[160:163],a[36:39],v[116:119]
v_exp_f32 v200, v104
M v[116:119],v[164:167],a[44:47],v[116:119]
ds_read_b128 v[152:155], v220 offset:12544
v_add_u32 v216, 122880, v224
M v[112:115],v[164:167],a[40:43],v[112:115]
v_exp_f32 v201, v105
M v[112:115],v[160:163],a[32:35],v[112:115]
v_exp_f32 v202, v106
M v[112:115],v[156:159],a[24:27],v[112:115]
ds_read_b128 v[156:159], v221 offset:12544
v_cvt_pk_bf16_f32 v136, v200, v201
M v[120:123],v[168:171],a[0:3],v[204:207]
v_exp_f32 v203, v107
M v[120:123],v[172:175],a[8:11],v[120:123]
ds_read_b128 v[160:163], v220 offset:18432
W 2
M v[120:123],v[176:179],a[16:19],v[120:123]
ds_read_b128 v[164:167], v221 offset:18432
v_cvt_pk_bf16_f32 v137, v202, v203
M v[124:127],v[176:179],a[20:23],v[208:211]
v_exp_f32 v200, v108
M v[124:127],v[172:175],a[12:15],v[124:127]
v_exp_f32 v201, v109
M v[124:127],v[168:171],a[4:7],v[124:127]
ds_read_b128 v[168:171], v220 offset:18560
v_cvt_pk_bf16_f32 v140, v200, v201
M v[124:127],v[180:183],a[28:31],v[124:127]
v_exp_f32 v202, v110
M v[124:127],v[152:155],a[36:39],v[124:127]
ds_read_b128 v[172:175], v221 offset:18560
W 2
M v[124:127],v[156:159],a[44:47],v[124:127]
ds_read_b128 v[176:179], v220 offset:18688
v_max3_f32 v248, v248, v112, v113
M v[120:123],v[156:159],a[40:43],v[120:123]
v_exp_f32 v203, v111
M v[120:123],v[152:155],a[32:35],v[120:123]
v_cvt_pk_bf16_f32 v141, v202, v203
v_max3_f32 v249, v249, v116, v117
M v[120:123],v[180:183],a[24:27],v[120:123]
ds_read_b128 v[180:183], v221 offset:18688
v_xor_b32 v217, 32, v216
M v[128:131],v[160:163],a[0:3],v[204:207]
v_xor_b32 v218, 64, v216
v_xor_b32 v219, 96, v216
M v[128:131],v[164:167],a[8:11],v[128:131]
s_waitcnt vmcnt(9)
s_barrier
T v[152:153],v216,0
W 2
M v[128:131],v[168:171],a[16:19],v[128:131]
T v[154:155],v216,6144
v_max3_f32 v248, v248, v114, v115
M v[132:135],v[168:171],a[20:23],v[208:211]
T v[156:157],v216,12288
v_max3_f32 v249, v249, v118, v119
M v[132:135],v[164:167],a[12:15],v[132:135]
T v[158:159],v216,18432
M v[132:135],v[160:163],a[4:7],v[132:135]
T v[160:161],v217,0
M v[132:135],v[172:175],a[28:31],v[132:135]
T v[162:163],v217,6144
W 5
M v[132:135],v[176:179],a[36:39],v[132:135]
T v[164:165],v217,12288
M v[132:135],v[180:183],a[44:47],v[132:135]
T v[166:167],v217,18432
W 5
M v[128:131],v[180:183],a[40:43],v[128:131]
T v[168:169],v218,0
v_exp_f32 v200, v112
M v[128:131],v[176:179],a[32:35],v[128:131]
T v[170:171],v218,6144
W 5
M v[128:131],v[172:175],a[24:27],v[128:131]
T v[172:173],v218,12288
v_exp_f32 v201, v113
M a[48:51],v[152:155],a[160:163],a[48:51]
T v[174:175],v218,18432
s_add_u32 s79, s2, 0
s_add_u32 m0, s40, 49152
s_nop 0
buffer_load_dwordx4 v227, s[12:15], s79 offen nt lds
M a[48:51],v[156:159],a[164:167],a[48:51]
T v[176:177],v219,0
v_cvt_pk_bf16_f32 v138, v200, v201
M a[52:55],v[156:159],a[172:175],a[52:55]
T v[178:179],v219,6144
W 5
M a[52:55],v[152:155],a[168:171],a[52:55]
T v[180:181],v219,12288
v_exp_f32 v202, v114
M a[56:59],v[160:163],a[160:163],a[56:59]
T v[182:183],v219,18432
W 5
M a[56:59],v[164:167],a[164:167],a[56:59]
T v[152:153],v216,128
v_exp_f32 v203, v115
M a[60:63],v[164:167],a[172:175],a[60:63]
T v[154:155],v216,6272
v_cvt_pk_bf16_f32 v139, v202, v203
M a[60:63],v[160:163],a[168:171],a[60:63]
T v[156:157],v216,12416
W 5
M a[64:67],v[168:171],a[160:163],a[64:67]
T v[158:159],v216,18560
v_exp_f32 v200, v116
M a[64:67],v[172:175],a[164:167],a[64:67]
T v[160:161],v217,128
W 5
M a[68:71],v[172:175],a[172:175],a[68:71]
T v[162:163],v217,6272
M a[68:71],v[168:171],a[168:171],a[68:71]
T v[164:165],v217,12416
v_exp_f32 v201, v117
M a[72:75],v[176:179],a[160:163],a[72:75]
T v[166:167],v217,18560
v_cvt_pk_bf16_f32 v142, v200, v201
M a[72:75],v[180:183],a[164:167],a[72:75]
T v[168:169],v218,128
W 5
M a[76:79],v[180:183],a[172:175],a[76:79]
T v[170:171],v218,6272
v_exp_f32 v202, v118
M a[76:79],v[176:179],a[168:171],a[76:79]
T v[172:173],v218,12416
v_max3_f32 v248, v248, v120, v121
M a[80:83],v[152:155],a[160:163],a[80:83]
T v[174:175],v218,18560
v_max3_f32 v249, v249, v124, v125
M a[80:83],v[156:159],a[164:167],a[80:83]
T v[176:177],v219,128
v_max3_f32 v248, v248, v122, v123
M a[84:87],v[156:159],a[172:175],a[84:87]
T v[178:179],v219,6272
v_max3_f32 v249, v249, v126, v127
M a[84:87],v[152:155],a[168:171],a[84:87]
T v[180:181],v219,12416
W 5
M a[88:91],v[160:163],a[160:163],a[88:91]
T v[182:183],v219,18560
buffer_load_dwordx4 v228, s[12:15], s79 offen offset:1024 nt lds
v_exp_f32 v203, v119
M a[88:91],v[164:167],a[164:167],a[88:91]
T v[152:153],v216,256
v_cvt_pk_bf16_f32 v143, v202, v203
M a[92:95],v[164:167],a[172:175],a[92:95]
T v[154:155],v216,6400
v_max3_f32 v248, v248, v128, v129
M a[92:95],v[160:163],a[168:171],a[92:95]
T v[156:157],v216,12544
v_max3_f32 v249, v249, v132, v133
M a[96:99],v[168:171],a[160:163],a[96:99]
T v[158:159],v216,18688
W 5
M a[96:99],v[172:175],a[164:167],a[96:99]
T v[160:161],v217,256
v_max3_f32 v248, v248, v130, v131
M a[100:103],v[172:175],a[172:175],a[100:103]
T v[162:163],v217,6400
v_max3_f32 v249, v249, v134, v135
M a[100:103],v[168:171],a[168:171],a[100:103]
T v[164:165],v217,12544
v_max_f32 v250, v248, v249
M a[104:107],v[176:179],a[160:163],a[104:107]
T v[166:167],v217,18688
W 5
M a[104:107],v[180:183],a[164:167],a[104:107]
T v[168:169],v218,256
v_cmp_lt_f32 vcc, s60, v250
M a[108:111],v[180:183],a[172:175],a[108:111]
T v[170:171],v218,6400
s_cbranch_vccnz LST0_1_%=
LRT0_1_%=:
M a[108:111],v[176:179],a[168:171],a[108:111]
T v[172:173],v218,12544
v_exp_f32 v200, v120
M a[112:115],v[152:155],a[160:163],a[112:115]
T v[174:175],v218,18688
W 5
M a[112:115],v[156:159],a[164:167],a[112:115]
T v[176:177],v219,256
v_exp_f32 v201, v121
M a[116:119],v[156:159],a[172:175],a[116:119]
T v[178:179],v219,6400
v_cvt_pk_bf16_f32 v144, v200, v201
M a[116:119],v[152:155],a[168:171],a[116:119]
T v[180:181],v219,12544
v_add_u32 v220, 98304, v224
M a[120:123],v[160:163],a[160:163],a[120:123]
T v[182:183],v219,18688
v_exp_f32 v202, v122
W 5
M a[120:123],v[164:167],a[164:167],a[120:123]
v_xor_b32 v221, 32, v220
v_xor_b32 v222, 64, v220
M a[124:127],v[164:167],a[172:175],a[124:127]
v_exp_f32 v203, v123
M a[124:127],v[160:163],a[168:171],a[124:127]
v_xor_b32 v223, 96, v220
v_cvt_pk_bf16_f32 v145, v202, v203
M a[128:131],v[168:171],a[160:163],a[128:131]
s_waitcnt vmcnt(8)
s_barrier
T v[152:153],v220,0
T v[154:155],v220,6144
W 3
M a[128:131],v[172:175],a[164:167],a[128:131]
T v[156:157],v221,0
buffer_load_dwordx4 v229, s[12:15], s79 offen offset:2048 nt lds
M a[132:135],v[172:175],a[172:175],a[132:135]
T v[158:159],v221,6144
v_exp_f32 v200, v124
M a[132:135],v[168:171],a[168:171],a[132:135]
T v[160:161],v222,0
M a[136:139],v[176:179],a[160:163],a[136:139]
T v[162:163],v222,6144
v_exp_f32 v201, v125
W 6
M a[136:139],v[180:183],a[164:167],a[136:139]
T v[164:165],v223,0
v_cvt_pk_bf16_f32 v148, v200, v201
M a[140:143],v[180:183],a[172:175],a[140:143]
T v[166:167],v223,6144
W 5
M a[140:143],v[176:179],a[168:171],a[140:143]
T v[168:169],v220,128
v_exp_f32 v202, v126
M v[0:3],v[152:155],v[136:139],v[0:3]
T v[170:171],v220,6272
W 5
M v[4:7],v[152:155],v[140:143],v[4:7]
T v[172:173],v221,128
v_exp_f32 v203, v127
M v[96:99],v[244:247],v[136:139],v[96:99]
T v[174:175],v221,6272
v_cvt_pk_bf16_f32 v149, v202, v203
M v[100:103],v[244:247],v[140:143],v[100:103]
T v[176:177],v222,128
W 5
M v[8:11],v[156:159],v[136:139],v[8:11]
T v[178:179],v222,6272
v_exp_f32 v200, v128
M v[12:15],v[156:159],v[140:143],v[12:15]
T v[180:181],v223,128
W 5
M v[16:19],v[160:163],v[136:139],v[16:19]
T v[182:183],v223,6272
v_exp_f32 v201, v129
s_add_u32 m0, s43, 147456
M v[20:23],v[160:163],v[140:143],v[20:23]
T v[152:153],v220,256
v_cvt_pk_bf16_f32 v146, v200, v201
M v[24:27],v[164:167],v[136:139],v[24:27]
T v[154:155],v220,6400
W 5
M v[28:31],v[164:167],v[140:143],v[28:31]
T v[156:157],v221,256
buffer_load_dwordx4 v236, s[24:27], s44 offen lds
M v[32:35],v[168:171],v[136:139],v[32:35]
T v[158:159],v221,6400
v_exp_f32 v202, v130
M v[36:39],v[168:171],v[140:143],v[36:39]
T v[160:161],v222,256
s_add_u32 s79, s3, 0
M v[40:43],v[172:175],v[136:139],v[40:43]
T v[162:163],v222,6400
s_add_u32 m0, s40, 73728
v_exp_f32 v203, v131
s_add_u32 m0, s43, 147456
M v[44:47],v[172:175],v[140:143],v[44:47]
T v[164:165],v223,256
W 5
M v[48:51],v[176:179],v[136:139],v[48:51]
T v[166:167],v223,6400
s_add_u32 m0, s40, 73728
v_exp_f32 v200, v132
s_add_u32 m0, s43, 147456
s_add_u32 m0, s40, 73728
M v[52:55],v[176:179],v[140:143],v[52:55]
T v[168:169],v220,12288
buffer_load_dwordx4 v227, s[8:11], s79 offen nt lds
s_add_u32 m0, s43, 147456
M v[56:59],v[180:183],v[136:139],v[56:59]
T v[170:171],v220,18432
buffer_load_dwordx4 v237, s[24:27], s44 offen offset:1024 lds
v_cvt_pk_bf16_f32 v147, v202, v203
M v[60:63],v[180:183],v[140:143],v[60:63]
T v[172:173],v221,12288
v_exp_f32 v201, v133
M v[64:67],v[152:155],v[136:139],v[64:67]
T v[174:175],v221,18432
v_cvt_pk_bf16_f32 v150, v200, v201
M v[68:71],v[152:155],v[140:143],v[68:71]
T v[176:177],v222,12288
buffer_load_dwordx4 v238, s[24:27], s44 offen offset:2048 lds
buffer_load_dwordx4 v239, s[24:27], s44 offen offset:3072 lds
W 5
M v[72:75],v[156:159],v[136:139],v[72:75]
T v[178:179],v222,18432
v_exp_f32 v202, v134
M v[76:79],v[156:159],v[140:143],v[76:79]
T v[180:181],v223,12288
v_add_u32 v216, 0, v225
M v[80:83],v[160:163],v[136:139],v[80:83]
T v[182:183],v223,18432
v_exp_f32 v203, v135
M v[84:87],v[160:163],v[140:143],v[84:87]
T v[152:153],v220,12416
v_cvt_pk_bf16_f32 v151, v202, v203
M v[88:91],v[164:167],v[136:139],v[88:91]
T v[154:155],v220,18560
v_xor_b32 v217, 64, v216
M v[92:95],v[164:167],v[140:143],v[92:95]
T v[156:157],v221,12416
W 5
M v[0:3],v[168:171],v[144:147],v[0:3]
T v[158:159],v221,18560
M v[4:7],v[168:171],v[148:151],v[4:7]
T v[160:161],v222,12416
W 5
M v[96:99],v[244:247],v[144:147],v[96:99]
T v[162:163],v222,18560
M v[100:103],v[244:247],v[148:151],v[100:103]
T v[164:165],v223,12416
W 5
M v[8:11],v[172:175],v[144:147],v[8:11]
T v[166:167],v223,18560
M v[12:15],v[172:175],v[148:151],v[12:15]
T v[168:169],v220,12544
W 5
M v[16:19],v[176:179],v[144:147],v[16:19]
T v[170:171],v220,18688
M v[20:23],v[176:179],v[148:151],v[20:23]
T v[172:173],v221,12544
s_add_u32 m0, s40, 73728
M v[24:27],v[180:183],v[144:147],v[24:27]
T v[174:175],v221,18688
W 5
M v[28:31],v[180:183],v[148:151],v[28:31]
T v[176:177],v222,12544
buffer_load_dwordx4 v228, s[8:11], s79 offen offset:1024 nt lds
M v[32:35],v[152:155],v[144:147],v[32:35]
T v[178:179],v222,18688
W 5
M v[36:39],v[152:155],v[148:151],v[36:39]
T v[180:181],v223,12544
M v[40:43],v[156:159],v[144:147],v[40:43]
T v[182:183],v223,18688
W 5
M v[44:47],v[156:159],v[148:151],v[44:47]
s_waitcnt vmcnt(12)
s_barrier
ds_read_b128 v[152:155], v216 offset:0
M v[48:51],v[160:163],v[144:147],v[48:51]
ds_read_b128 v[156:159], v217 offset:0
W 5
M v[52:55],v[160:163],v[148:151],v[52:55]
ds_read_b128 v[160:163], v216 offset:128
M v[56:59],v[164:167],v[144:147],v[56:59]
W 4
M v[60:63],v[164:167],v[148:151],v[60:63]
M v[64:67],v[168:171],v[144:147],v[64:67]
W 2
M v[68:71],v[168:171],v[148:151],v[68:71]
ds_read_b128 v[164:167], v217 offset:128
M v[72:75],v[172:175],v[144:147],v[72:75]
ds_read_b128 v[168:171], v216 offset:256
W 2
M v[76:79],v[172:175],v[148:151],v[76:79]
ds_read_b128 v[172:175], v217 offset:256
M v[80:83],v[176:179],v[144:147],v[80:83]
M v[84:87],v[176:179],v[148:151],v[84:87]
M v[88:91],v[180:183],v[144:147],v[88:91]
M v[92:95],v[180:183],v[148:151],v[92:95]
ds_read_b128 v[176:179], v216 offset:6144
W 2
M v[104:107],v[152:155],a[0:3],v[204:207]
ds_read_b128 v[180:183], v217 offset:6144
M v[104:107],v[156:159],a[8:11],v[104:107]
M v[104:107],v[160:163],a[16:19],v[104:107]
M v[108:111],v[160:163],a[20:23],v[208:211]
buffer_load_dwordx4 v229, s[8:11], s79 offen offset:2048 nt lds
s_mul_i32 s77, s47, 6144
M v[108:111],v[156:159],a[12:15],v[108:111]
s_add_u32 s77, s77, s41
s_add_u32 s78, s77, 384
M v[108:111],v[152:155],a[4:7],v[108:111]
ds_read_b128 v[152:155], v216 offset:6272
s_add_u32 m0, s40, 122880
M v[108:111],v[164:167],a[28:31],v[108:111]
ds_read_b128 v[156:159], v217 offset:6272
buffer_load_dwordx4 v233, s[16:19], s78 offen lds
M v[108:111],v[168:171],a[36:39],v[108:111]
ds_read_b128 v[160:163], v216 offset:6400
W 3
M v[108:111],v[172:175],a[44:47],v[108:111]
M v[104:107],v[172:175],a[40:43],v[104:107]
buffer_load_dwordx4 v234, s[16:19], s78 offen offset:1024 lds
M v[104:107],v[168:171],a[32:35],v[104:107]
M v[104:107],v[164:167],a[24:27],v[104:107]
ds_read_b128 v[164:167], v217 offset:6400
W 2
M v[112:115],v[176:179],a[0:3],v[204:207]
ds_read_b128 v[168:171], v216 offset:12288
buffer_load_dwordx4 v235, s[16:19], s78 offen offset:2048 lds
M v[112:115],v[180:183],a[8:11],v[112:115]
ds_read_b128 v[172:175], v217 offset:12288
M v[112:115],v[152:155],a[16:19],v[112:115]
M v[116:119],v[152:155],a[20:23],v[208:211]
v_max3_f32 v248, v104, v105, v106
v_max3_f32 v249, v108, v109, v110
M v[116:119],v[180:183],a[12:15],v[116:119]
v_max_f32 v248, v248, v107
v_max_f32 v249, v249, v111
M v[116:119],v[176:179],a[4:7],v[116:119]
ds_read_b128 v[176:179], v216 offset:12416
W 2
M v[116:119],v[156:159],a[28:31],v[116:119]
v_exp_f32 v200, v104
M v[116:119],v[160:163],a[36:39],v[116:119]
ds_read_b128 v[180:183], v217 offset:12416
v_add_u32 v220, 24576, v224
M v[116:119],v[164:167],a[44:47],v[116:119]
ds_read_b128 v[152:155], v216 offset:12544
v_xor_b32 v221, 32, v220
M v[112:115],v[164:167],a[40:43],v[112:115]
v_exp_f32 v201, v105
M v[112:115],v[160:163],a[32:35],v[112:115]
v_exp_f32 v202, v106
M v[112:115],v[156:159],a[24:27],v[112:115]
ds_read_b128 v[156:159], v217 offset:12544
v_cvt_pk_bf16_f32 v136, v200, v201
M v[120:123],v[168:171],a[0:3],v[204:207]
ds_read_b128 v[160:163], v216 offset:18432
W 3
M v[120:123],v[172:175],a[8:11],v[120:123]
v_exp_f32 v203, v107
M v[120:123],v[176:179],a[16:19],v[120:123]
ds_read_b128 v[164:167], v217 offset:18432
v_cvt_pk_bf16_f32 v137, v202, v203
M v[124:127],v[176:179],a[20:23],v[208:211]
v_exp_f32 v200, v108
M v[124:127],v[172:175],a[12:15],v[124:127]
v_exp_f32 v201, v109
M v[124:127],v[168:171],a[4:7],v[124:127]
ds_read_b128 v[168:171], v216 offset:18560
v_cvt_pk_bf16_f32 v140, v200, v201
M v[124:127],v[180:183],a[28:31],v[124:127]
ds_read_b128 v[172:175], v217 offset:18560
W 3
M v[124:127],v[152:155],a[36:39],v[124:127]
v_exp_f32 v202, v110
M v[124:127],v[156:159],a[44:47],v[124:127]
ds_read_b128 v[176:179], v216 offset:18688
v_max3_f32 v248, v248, v112, v113
M v[120:123],v[156:159],a[40:43],v[120:123]
v_exp_f32 v203, v111
M v[120:123],v[152:155],a[32:35],v[120:123]
v_cvt_pk_bf16_f32 v141, v202, v203
v_max3_f32 v249, v249, v116, v117
M v[120:123],v[180:183],a[24:27],v[120:123]
ds_read_b128 v[180:183], v217 offset:18688
v_xor_b32 v222, 64, v220
M v[128:131],v[160:163],a[0:3],v[204:207]
v_xor_b32 v223, 96, v220
W 2
M v[128:131],v[164:167],a[8:11],v[128:131]
s_waitcnt vmcnt(13)
s_barrier
T v[152:153],v220,0
M v[128:131],v[168:171],a[16:19],v[128:131]
T v[154:155],v220,6144
v_max3_f32 v248, v248, v114, v115
M v[132:135],v[168:171],a[20:23],v[208:211]
T v[156:157],v220,12288
v_max3_f32 v249, v249, v118, v119
M v[132:135],v[164:167],a[12:15],v[132:135]
T v[158:159],v220,18432
W 4
M v[132:135],v[160:163],a[4:7],v[132:135]
T v[160:161],v221,0
M v[132:135],v[172:175],a[28:31],v[132:135]
T v[162:163],v221,6144
M v[132:135],v[176:179],a[36:39],v[132:135]
T v[164:165],v221,12288
W 5
M v[132:135],v[180:183],a[44:47],v[132:135]
T v[166:167],v221,18432
M v[128:131],v[180:183],a[40:43],v[128:131]
T v[168:169],v222,0
v_exp_f32 v200, v112
M v[128:131],v[176:179],a[32:35],v[128:131]
T v[170:171],v222,6144
W 5
M v[128:131],v[172:175],a[24:27],v[128:131]
T v[172:173],v222,12288
v_exp_f32 v201, v113
M a[48:51],v[152:155],a[176:179],a[48:51]
T v[174:175],v222,18432
s_add_u32 s79, s3, 0
s_add_u32 m0, s40, 98304
s_nop 0
buffer_load_dwordx4 v227, s[12:15], s79 offen nt lds
M a[48:51],v[156:159],a[180:183],a[48:51]
T v[176:177],v223,0
v_cvt_pk_bf16_f32 v138, v200, v201
M a[52:55],v[156:159],a[188:191],a[52:55]
T v[178:179],v223,6144
W 5
M a[52:55],v[152:155],a[184:187],a[52:55]
T v[180:181],v223,12288
v_exp_f32 v202, v114
M a[56:59],v[160:163],a[176:179],a[56:59]
T v[182:183],v223,18432
W 5
M a[56:59],v[164:167],a[180:183],a[56:59]
T v[152:153],v220,128
v_exp_f32 v203, v115
M a[60:63],v[164:167],a[188:191],a[60:63]
T v[154:155],v220,6272
v_cvt_pk_bf16_f32 v139, v202, v203
M a[60:63],v[160:163],a[184:187],a[60:63]
T v[156:157],v220,12416
W 5
M a[64:67],v[168:171],a[176:179],a[64:67]
T v[158:159],v220,18560
v_exp_f32 v200, v116
M a[64:67],v[172:175],a[180:183],a[64:67]
T v[160:161],v221,128
W 5
M a[68:71],v[172:175],a[188:191],a[68:71]
T v[162:163],v221,6272
M a[68:71],v[168:171],a[184:187],a[68:71]
T v[164:165],v221,12416
v_exp_f32 v201, v117
M a[72:75],v[176:179],a[176:179],a[72:75]
T v[166:167],v221,18560
v_cvt_pk_bf16_f32 v142, v200, v201
M a[72:75],v[180:183],a[180:183],a[72:75]
T v[168:169],v222,128
W 5
M a[76:79],v[180:183],a[188:191],a[76:79]
T v[170:171],v222,6272
v_exp_f32 v202, v118
M a[76:79],v[176:179],a[184:187],a[76:79]
T v[172:173],v222,12416
v_max3_f32 v248, v248, v120, v121
M a[80:83],v[152:155],a[176:179],a[80:83]
T v[174:175],v222,18560
v_max3_f32 v249, v249, v124, v125
M a[80:83],v[156:159],a[180:183],a[80:83]
T v[176:177],v223,128
v_max3_f32 v248, v248, v122, v123
M a[84:87],v[156:159],a[188:191],a[84:87]
T v[178:179],v223,6272
v_max3_f32 v249, v249, v126, v127
M a[84:87],v[152:155],a[184:187],a[84:87]
T v[180:181],v223,12416
W 5
M a[88:91],v[160:163],a[176:179],a[88:91]
T v[182:183],v223,18560
buffer_load_dwordx4 v228, s[12:15], s79 offen offset:1024 nt lds
v_exp_f32 v203, v119
M a[88:91],v[164:167],a[180:183],a[88:91]
T v[152:153],v220,256
v_cvt_pk_bf16_f32 v143, v202, v203
M a[92:95],v[164:167],a[188:191],a[92:95]
T v[154:155],v220,6400
v_max3_f32 v248, v248, v128, v129
M a[92:95],v[160:163],a[184:187],a[92:95]
T v[156:157],v220,12544
v_max3_f32 v249, v249, v132, v133
M a[96:99],v[168:171],a[176:179],a[96:99]
T v[158:159],v220,18688
W 5
M a[96:99],v[172:175],a[180:183],a[96:99]
T v[160:161],v221,256
v_max3_f32 v248, v248, v130, v131
M a[100:103],v[172:175],a[188:191],a[100:103]
T v[162:163],v221,6400
v_max3_f32 v249, v249, v134, v135
M a[100:103],v[168:171],a[184:187],a[100:103]
T v[164:165],v221,12544
v_max_f32 v250, v248, v249
M a[104:107],v[176:179],a[176:179],a[104:107]
T v[166:167],v221,18688
W 5
M a[104:107],v[180:183],a[180:183],a[104:107]
T v[168:169],v222,256
v_cmp_lt_f32 vcc, s60, v250
M a[108:111],v[180:183],a[188:191],a[108:111]
T v[170:171],v222,6400
s_cbranch_vccnz LST0_2_%=
LRT0_2_%=:
M a[108:111],v[176:179],a[184:187],a[108:111]
T v[172:173],v222,12544
v_exp_f32 v200, v120
M a[112:115],v[152:155],a[176:179],a[112:115]
T v[174:175],v222,18688
W 5
M a[112:115],v[156:159],a[180:183],a[112:115]
T v[176:177],v223,256
v_exp_f32 v201, v121
M a[116:119],v[156:159],a[188:191],a[116:119]
T v[178:179],v223,6400
v_cvt_pk_bf16_f32 v144, v200, v201
M a[116:119],v[152:155],a[184:187],a[116:119]
T v[180:181],v223,12544
v_add_u32 v216, 49152, v224
M a[120:123],v[160:163],a[176:179],a[120:123]
T v[182:183],v223,18688
v_exp_f32 v202, v122
W 5
M a[120:123],v[164:167],a[180:183],a[120:123]
v_xor_b32 v217, 32, v216
v_xor_b32 v218, 64, v216
M a[124:127],v[164:167],a[188:191],a[124:127]
v_exp_f32 v203, v123
M a[124:127],v[160:163],a[184:187],a[124:127]
v_xor_b32 v219, 96, v216
v_cvt_pk_bf16_f32 v145, v202, v203
M a[128:131],v[168:171],a[176:179],a[128:131]
s_waitcnt vmcnt(12)
s_barrier
T v[152:153],v216,0
T v[154:155],v216,6144
W 3
M a[128:131],v[172:175],a[180:183],a[128:131]
T v[156:157],v217,0
buffer_load_dwordx4 v229, s[12:15], s79 offen offset:2048 nt lds
M a[132:135],v[172:175],a[188:191],a[132:135]
T v[158:159],v217,6144
v_exp_f32 v200, v124
M a[132:135],v[168:171],a[184:187],a[132:135]
T v[160:161],v218,0
M a[136:139],v[176:179],a[176:179],a[136:139]
T v[162:163],v218,6144
v_exp_f32 v201, v125
W 6
M a[136:139],v[180:183],a[180:183],a[136:139]
T v[164:165],v219,0
v_cvt_pk_bf16_f32 v148, v200, v201
M a[140:143],v[180:183],a[188:191],a[140:143]
T v[166:167],v219,6144
W 5
M a[140:143],v[176:179],a[184:187],a[140:143]
T v[168:169],v216,128
v_exp_f32 v202, v126
M v[0:3],v[152:155],v[136:139],v[0:3]
T v[170:171],v216,6272
s_mul_i32 s77, s48, 6144
M v[4:7],v[152:155],v[140:143],v[4:7]
T v[172:173],v217,128
v_exp_f32 v203, v127
M v[96:99],v[244:247],v[136:139],v[96:99]
T v[174:175],v217,6272
v_cvt_pk_bf16_f32 v149, v202, v203
M v[100:103],v[244:247],v[140:143],v[100:103]
T v[176:177],v218,128
W 5
M v[8:11],v[156:159],v[136:139],v[8:11]
T v[178:179],v218,6272
v_exp_f32 v200, v128
M v[12:15],v[156:159],v[140:143],v[12:15]
T v[180:181],v219,128
s_add_u32 s77, s77, s41
M v[16:19],v[160:163],v[136:139],v[16:19]
T v[182:183],v219,6272
v_exp_f32 v201, v129
M v[20:23],v[160:163],v[140:143],v[20:23]
T v[152:153],v216,256
v_cvt_pk_bf16_f32 v146, v200, v201
M v[24:27],v[164:167],v[136:139],v[24:27]
T v[154:155],v216,6400
s_add_u32 s78, s77, 384
M v[28:31],v[164:167],v[140:143],v[28:31]
T v[156:157],v217,256
W 5
M v[32:35],v[168:171],v[136:139],v[32:35]
T v[158:159],v217,6400
v_exp_f32 v202, v130
M v[36:39],v[168:171],v[140:143],v[36:39]
T v[160:161],v218,256
s_add_u32 m0, s40, 0
M v[40:43],v[172:175],v[136:139],v[40:43]
T v[162:163],v218,6400
v_exp_f32 v203, v131
M v[44:47],v[172:175],v[140:143],v[44:47]
T v[164:165],v219,256
v_cvt_pk_bf16_f32 v147, v202, v203
M v[48:51],v[176:179],v[136:139],v[48:51]
T v[166:167],v219,6400
buffer_load_dwordx4 v233, s[16:19], s78 offen lds
M v[52:55],v[176:179],v[140:143],v[52:55]
T v[168:169],v216,12288
v_exp_f32 v200, v132
W 5
M v[56:59],v[180:183],v[136:139],v[56:59]
T v[170:171],v216,18432
M v[60:63],v[180:183],v[140:143],v[60:63]
T v[172:173],v217,12288
v_exp_f32 v201, v133
M v[64:67],v[152:155],v[136:139],v[64:67]
T v[174:175],v217,18432
v_cvt_pk_bf16_f32 v150, v200, v201
M v[68:71],v[152:155],v[140:143],v[68:71]
T v[176:177],v218,12288
buffer_load_dwordx4 v234, s[16:19], s78 offen offset:1024 lds
M v[72:75],v[156:159],v[136:139],v[72:75]
T v[178:179],v218,18432
v_exp_f32 v202, v134
M v[76:79],v[156:159],v[140:143],v[76:79]
T v[180:181],v219,12288
W 5
M v[80:83],v[160:163],v[136:139],v[80:83]
T v[182:183],v219,18432
buffer_load_dwordx4 v235, s[16:19], s78 offen offset:2048 lds
M v[84:87],v[160:163],v[140:143],v[84:87]
T v[152:153],v216,12416
v_exp_f32 v203, v135
M v[88:91],v[164:167],v[136:139],v[88:91]
T v[154:155],v216,18560
v_cvt_pk_bf16_f32 v151, v202, v203
M v[92:95],v[164:167],v[140:143],v[92:95]
T v[156:157],v217,12416
v_add_u32 v220, 73728, v225
M v[0:3],v[168:171],v[144:147],v[0:3]
T v[158:159],v217,18560
v_xor_b32 v221, 64, v220
M v[4:7],v[168:171],v[148:151],v[4:7]
T v[160:161],v218,12416
W 5
M v[96:99],v[244:247],v[144:147],v[96:99]
T v[162:163],v218,18560
M v[100:103],v[244:247],v[148:151],v[100:103]
T v[164:165],v219,12416
W 5
M v[8:11],v[172:175],v[144:147],v[8:11]
T v[166:167],v219,18560
M v[12:15],v[172:175],v[148:151],v[12:15]
T v[168:169],v216,12544
W 5
M v[16:19],v[176:179],v[144:147],v[16:19]
T v[170:171],v216,18688
M v[20:23],v[176:179],v[148:151],v[20:23]
T v[172:173],v217,12544
W 5
M v[24:27],v[180:183],v[144:147],v[24:27]
T v[174:175],v217,18688
M v[28:31],v[180:183],v[148:151],v[28:31]
T v[176:177],v218,12544
W 5
M v[32:35],v[152:155],v[144:147],v[32:35]
T v[178:179],v218,18688
M v[36:39],v[152:155],v[148:151],v[36:39]
T v[180:181],v219,12544
W 5
M v[40:43],v[156:159],v[144:147],v[40:43]
T v[182:183],v219,18688
M v[44:47],v[156:159],v[148:151],v[44:47]
s_waitcnt vmcnt(9)
s_barrier
ds_read_b128 v[152:155], v220 offset:0
M v[48:51],v[160:163],v[144:147],v[48:51]
ds_read_b128 v[156:159], v221 offset:0
W 5
M v[52:55],v[160:163],v[148:151],v[52:55]
ds_read_b128 v[160:163], v220 offset:128
M v[56:59],v[164:167],v[144:147],v[56:59]
W 4
M v[60:63],v[164:167],v[148:151],v[60:63]
M v[64:67],v[168:171],v[144:147],v[64:67]
W 2
M v[68:71],v[168:171],v[148:151],v[68:71]
ds_read_b128 v[164:167], v221 offset:128
M v[72:75],v[172:175],v[144:147],v[72:75]
ds_read_b128 v[168:171], v220 offset:256
W 2
M v[76:79],v[172:175],v[148:151],v[76:79]
ds_read_b128 v[172:175], v221 offset:256
M v[80:83],v[176:179],v[144:147],v[80:83]
M v[84:87],v[176:179],v[148:151],v[84:87]
M v[88:91],v[180:183],v[144:147],v[88:91]
M v[92:95],v[180:183],v[148:151],v[92:95]
ds_read_b128 v[176:179], v220 offset:6144
W 2
M v[104:107],v[152:155],a[0:3],v[204:207]
ds_read_b128 v[180:183], v221 offset:6144
s_add_u32 s79, s4, 0
s_add_u32 m0, s40, 24576
s_nop 0
buffer_load_dwordx4 v227, s[8:11], s79 offen nt lds
M v[104:107],v[156:159],a[8:11],v[104:107]
M v[104:107],v[160:163],a[16:19],v[104:107]
M v[108:111],v[160:163],a[20:23],v[208:211]
M v[108:111],v[156:159],a[12:15],v[108:111]
W 1
M v[108:111],v[152:155],a[4:7],v[108:111]
ds_read_b128 v[152:155], v220 offset:6272
M v[108:111],v[164:167],a[28:31],v[108:111]
ds_read_b128 v[156:159], v221 offset:6272
M v[108:111],v[168:171],a[36:39],v[108:111]
ds_read_b128 v[160:163], v220 offset:6400
M v[108:111],v[172:175],a[44:47],v[108:111]
M v[104:107],v[172:175],a[40:43],v[104:107]
M v[104:107],v[168:171],a[32:35],v[104:107]
W 2
M v[104:107],v[164:167],a[24:27],v[104:107]
ds_read_b128 v[164:167], v221 offset:6400
M v[112:115],v[176:179],a[0:3],v[204:207]
ds_read_b128 v[168:171], v220 offset:12288
W 2
M v[112:115],v[180:183],a[8:11],v[112:115]
ds_read_b128 v[172:175], v221 offset:12288
M v[112:115],v[152:155],a[16:19],v[112:115]
M v[116:119],v[152:155],a[20:23],v[208:211]
v_max3_f32 v248, v104, v105, v106
v_max3_f32 v249, v108, v109, v110
M v[116:119],v[180:183],a[12:15],v[116:119]
v_max_f32 v248, v248, v107
v_max_f32 v249, v249, v111
M v[116:119],v[176:179],a[4:7],v[116:119]
ds_read_b128 v[176:179], v220 offset:12416
W 2
M v[116:119],v[156:159],a[28:31],v[116:119]
v_exp_f32 v200, v104
M v[116:119],v[160:163],a[36:39],v[116:119]
ds_read_b128 v[180:183], v221 offset:12416
v_add_u32 v216, 122880, v224
M v[116:119],v[164:167],a[44:47],v[116:119]
ds_read_b128 v[152:155], v220 offset:12544
buffer_load_dwordx4 v228, s[8:11], s79 offen offset:1024 nt lds
M v[112:115],v[164:167],a[40:43],v[112:115]
v_exp_f32 v201, v105
M v[112:115],v[160:163],a[32:35],v[112:115]
v_exp_f32 v202, v106
M v[112:115],v[156:159],a[24:27],v[112:115]
ds_read_b128 v[156:159], v221 offset:12544
v_cvt_pk_bf16_f32 v136, v200, v201
M v[120:123],v[168:171],a[0:3],v[204:207]
ds_read_b128 v[160:163], v220 offset:18432
W 3
M v[120:123],v[172:175],a[8:11],v[120:123]
v_exp_f32 v203, v107
M v[120:123],v[176:179],a[16:19],v[120:123]
ds_read_b128 v[164:167], v221 offset:18432
v_cvt_pk_bf16_f32 v137, v202, v203
M v[124:127],v[176:179],a[20:23],v[208:211]
v_exp_f32 v200, v108
M v[124:127],v[172:175],a[12:15],v[124:127]
v_exp_f32 v201, v109
M v[124:127],v[168:171],a[4:7],v[124:127]
ds_read_b128 v[168:171], v220 offset:18560
v_cvt_pk_bf16_f32 v140, v200, v201
M v[124:127],v[180:183],a[28:31],v[124:127]
ds_read_b128 v[172:175], v221 offset:18560
W 3
M v[124:127],v[152:155],a[36:39],v[124:127]
v_exp_f32 v202, v110
M v[124:127],v[156:159],a[44:47],v[124:127]
ds_read_b128 v[176:179], v220 offset:18688
v_max3_f32 v248, v248, v112, v113
M v[120:123],v[156:159],a[40:43],v[120:123]
v_exp_f32 v203, v111
M v[120:123],v[152:155],a[32:35],v[120:123]
v_cvt_pk_bf16_f32 v141, v202, v203
v_max3_f32 v249, v249, v116, v117
M v[120:123],v[180:183],a[24:27],v[120:123]
ds_read_b128 v[180:183], v221 offset:18688
v_xor_b32 v217, 32, v216
M v[128:131],v[160:163],a[0:3],v[204:207]
v_xor_b32 v218, 64, v216
W 2
M v[128:131],v[164:167],a[8:11],v[128:131]
v_xor_b32 v219, 96, v216
v_max3_f32 v248, v248, v114, v115
M v[128:131],v[168:171],a[16:19],v[128:131]
s_waitcnt vmcnt(8)
s_barrier
T v[152:153],v216,0
M v[132:135],v[168:171],a[20:23],v[208:211]
T v[154:155],v216,6144
v_max3_f32 v249, v249, v118, v119
M v[132:135],v[164:167],a[12:15],v[132:135]
T v[156:157],v216,12288
buffer_load_dwordx4 v229, s[8:11], s79 offen offset:2048 nt lds
M v[132:135],v[160:163],a[4:7],v[132:135]
T v[158:159],v216,18432
W 4
M v[132:135],v[172:175],a[28:31],v[132:135]
T v[160:161],v217,0
M v[132:135],v[176:179],a[36:39],v[132:135]
T v[162:163],v217,6144
M v[132:135],v[180:183],a[44:47],v[132:135]
T v[164:165],v217,12288
W 5
M v[128:131],v[180:183],a[40:43],v[128:131]
T v[166:167],v217,18432
v_exp_f32 v200, v112
M v[128:131],v[176:179],a[32:35],v[128:131]
T v[168:169],v218,0
W 5
M v[128:131],v[172:175],a[24:27],v[128:131]
T v[170:171],v218,6144
v_exp_f32 v201, v113
M a[48:51],v[152:155],a[192:195],a[48:51]
T v[172:173],v218,12288
v_cvt_pk_bf16_f32 v138, v200, v201
M a[48:51],v[156:159],a[196:199],a[48:51]
T v[174:175],v218,18432
s_mul_i32 s77, s49, 6144
M a[52:55],v[156:159],a[204:207],a[52:55]
T v[176:177],v219,0
s_add_u32 s77, s77, s41
M a[52:55],v[152:155],a[200:203],a[52:55]
T v[178:179],v219,6144
v_exp_f32 v202, v114
W 5
M a[56:59],v[160:163],a[192:195],a[56:59]
T v[180:181],v219,12288
s_add_u32 s78, s77, 384
M a[56:59],v[164:167],a[196:199],a[56:59]
T v[182:183],v219,18432
v_exp_f32 v203, v115
M a[60:63],v[164:167],a[204:207],a[60:63]
T v[152:153],v216,128
v_cvt_pk_bf16_f32 v139, v202, v203
M a[60:63],v[160:163],a[200:203],a[60:63]
T v[154:155],v216,6272
W 5
M a[64:67],v[168:171],a[192:195],a[64:67]
T v[156:157],v216,12416
v_exp_f32 v200, v116
M a[64:67],v[172:175],a[196:199],a[64:67]
T v[158:159],v216,18560
s_add_u32 m0, s40, 49152
M a[68:71],v[172:175],a[204:207],a[68:71]
T v[160:161],v217,128
buffer_load_dwordx4 v233, s[16:19], s78 offen lds
M a[68:71],v[168:171],a[200:203],a[68:71]
T v[162:163],v217,6272
v_exp_f32 v201, v117
W 5
M a[72:75],v[176:179],a[192:195],a[72:75]
T v[164:165],v217,12416
v_cvt_pk_bf16_f32 v142, v200, v201
M a[72:75],v[180:183],a[196:199],a[72:75]
T v[166:167],v217,18560
buffer_load_dwordx4 v234, s[16:19], s78 offen offset:1024 lds
M a[76:79],v[180:183],a[204:207],a[76:79]
T v[168:169],v218,128
v_exp_f32 v202, v118
M a[76:79],v[176:179],a[200:203],a[76:79]
T v[170:171],v218,6272
W 5
M a[80:83],v[152:155],a[192:195],a[80:83]
T v[172:173],v218,12416
v_max3_f32 v248, v248, v120, v121
M a[80:83],v[156:159],a[196:199],a[80:83]
T v[174:175],v218,18560
v_max3_f32 v249, v249, v124, v125
M a[84:87],v[156:159],a[204:207],a[84:87]
T v[176:177],v219,128
v_max3_f32 v248, v248, v122, v123
M a[84:87],v[152:155],a[200:203],a[84:87]
T v[178:179],v219,6272
v_exp_f32 v203, v119
W 5
M a[88:91],v[160:163],a[192:195],a[88:91]
T v[180:181],v219,12416
v_cvt_pk_bf16_f32 v143, v202, v203
M a[88:91],v[164:167],a[196:199],a[88:91]
T v[182:183],v219,18560
v_max3_f32 v249, v249, v126, v127
M a[92:95],v[164:167],a[204:207],a[92:95]
T v[152:153],v216,256
v_max3_f32 v248, v248, v128, v129
M a[92:95],v[160:163],a[200:203],a[92:95]
T v[154:155],v216,6400
W 5
M a[96:99],v[168:171],a[192:195],a[96:99]
T v[156:157],v216,12544
v_max3_f32 v249, v249, v132, v133
M a[96:99],v[172:175],a[196:199],a[96:99]
T v[158:159],v216,18688
v_max3_f32 v248, v248, v130, v131
M a[100:103],v[172:175],a[204:207],a[100:103]
T v[160:161],v217,256
v_max3_f32 v249, v249, v134, v135
M a[100:103],v[168:171],a[200:203],a[100:103]
T v[162:163],v217,6400
W 5
M a[104:107],v[176:179],a[192:195],a[104:107]
T v[164:165],v217,12544
v_max_f32 v250, v248, v249
M a[104:107],v[180:183],a[196:199],a[104:107]
T v[166:167],v217,18688
v_cmp_lt_f32 vcc, s60, v250
M a[108:111],v[180:183],a[204:207],a[108:111]
T v[168:169],v218,256
s_cbranch_vccnz LST0_3_%=
LRT0_3_%=:
M a[108:111],v[176:179],a[200:203],a[108:111]
T v[170:171],v218,6400
v_exp_f32 v200, v120
W 5
M a[112:115],v[152:155],a[192:195],a[112:115]
T v[172:173],v218,12544
buffer_load_dwordx4 v235, s[16:19], s78 offen offset:2048 lds
M a[112:115],v[156:159],a[196:199],a[112:115]
T v[174:175],v218,18688
v_exp_f32 v201, v121
M a[116:119],v[156:159],a[204:207],a[116:119]
T v[176:177],v219,256
v_cvt_pk_bf16_f32 v144, v200, v201
M a[116:119],v[152:155],a[200:203],a[116:119]
T v[178:179],v219,6400
W 5
M a[120:123],v[160:163],a[192:195],a[120:123]
T v[180:181],v219,12544
v_exp_f32 v202, v122
M a[120:123],v[164:167],a[196:199],a[120:123]
T v[182:183],v219,18688
v_add_u32 v220, 98304, v224
M a[124:127],v[164:167],a[204:207],a[124:127]
v_exp_f32 v203, v123
M a[124:127],v[160:163],a[200:203],a[124:127]
v_xor_b32 v221, 32, v220
W 3
M a[128:131],v[168:171],a[192:195],a[128:131]
v_xor_b32 v222, 64, v220
v_xor_b32 v223, 96, v220
s_waitcnt vmcnt(9)
s_barrier
T v[152:153],v220,0
T v[154:155],v220,6144
v_exp_f32 v200, v124
M a[128:131],v[172:175],a[196:199],a[128:131]
T v[156:157],v221,0
v_cvt_pk_bf16_f32 v145, v202, v203
M a[132:135],v[172:175],a[204:207],a[132:135]
T v[158:159],v221,6144
W 4
M a[132:135],v[168:171],a[200:203],a[132:135]
T v[160:161],v222,0
M a[136:139],v[176:179],a[192:195],a[136:139]
T v[162:163],v222,6144
v_exp_f32 v201, v125
M a[136:139],v[180:183],a[196:199],a[136:139]
T v[164:165],v223,0
v_cvt_pk_bf16_f32 v148, v200, v201
M a[140:143],v[180:183],a[204:207],a[140:143]
T v[166:167],v223,6144
W 5
M a[140:143],v[176:179],a[200:203],a[140:143]
T v[168:169],v220,128
v_exp_f32 v202, v126
M v[0:3],v[152:155],v[136:139],v[0:3]
T v[170:171],v220,6272
s_add_u32 s79, s4, 0
s_add_u32 m0, s40, 73728
s_nop 0
buffer_load_dwordx4 v227, s[12:15], s79 offen nt lds
M v[4:7],v[152:155],v[140:143],v[4:7]
T v[172:173],v221,128
v_exp_f32 v203, v127
M v[96:99],v[244:247],v[136:139],v[96:99]
T v[174:175],v221,6272
v_cvt_pk_bf16_f32 v149, v202, v203
M v[100:103],v[244:247],v[140:143],v[100:103]
T v[176:177],v222,128
W 5
M v[8:11],v[156:159],v[136:139],v[8:11]
T v[178:179],v222,6272
v_exp_f32 v200, v128
M v[12:15],v[156:159],v[140:143],v[12:15]
T v[180:181],v223,128
W 5
M v[16:19],v[160:163],v[136:139],v[16:19]
T v[182:183],v223,6272
v_exp_f32 v201, v129
M v[20:23],v[160:163],v[140:143],v[20:23]
T v[152:153],v220,256
v_cvt_pk_bf16_f32 v146, v200, v201
M v[24:27],v[164:167],v[136:139],v[24:27]
T v[154:155],v220,6400
W 5
M v[28:31],v[164:167],v[140:143],v[28:31]
T v[156:157],v221,256
M v[32:35],v[168:171],v[136:139],v[32:35]
T v[158:159],v221,6400
v_exp_f32 v202, v130
M v[36:39],v[168:171],v[140:143],v[36:39]
T v[160:161],v222,256
W 5
M v[40:43],v[172:175],v[136:139],v[40:43]
T v[162:163],v222,6400
v_exp_f32 v203, v131
M v[44:47],v[172:175],v[140:143],v[44:47]
T v[164:165],v223,256
v_cvt_pk_bf16_f32 v147, v202, v203
M v[48:51],v[176:179],v[136:139],v[48:51]
T v[166:167],v223,6400
W 5
M v[52:55],v[176:179],v[140:143],v[52:55]
T v[168:169],v220,12288
v_exp_f32 v200, v132
M v[56:59],v[180:183],v[136:139],v[56:59]
T v[170:171],v220,18432
W 5
M v[60:63],v[180:183],v[140:143],v[60:63]
T v[172:173],v221,12288
v_exp_f32 v201, v133
M v[64:67],v[152:155],v[136:139],v[64:67]
T v[174:175],v221,18432
v_cvt_pk_bf16_f32 v150, v200, v201
M v[68:71],v[152:155],v[140:143],v[68:71]
T v[176:177],v222,12288
W 5
M v[72:75],v[156:159],v[136:139],v[72:75]
T v[178:179],v222,18432
buffer_load_dwordx4 v228, s[12:15], s79 offen offset:1024 nt lds
v_exp_f32 v202, v134
M v[76:79],v[156:159],v[140:143],v[76:79]
T v[180:181],v223,12288
v_add_u32 v216, 0, v224
M v[80:83],v[160:163],v[136:139],v[80:83]
T v[182:183],v223,18432
v_xor_b32 v217, 32, v216
M v[84:87],v[160:163],v[140:143],v[84:87]
T v[152:153],v220,12416
v_exp_f32 v203, v135
M v[88:91],v[164:167],v[136:139],v[88:91]
T v[154:155],v220,18560
v_cvt_pk_bf16_f32 v151, v202, v203
M v[92:95],v[164:167],v[140:143],v[92:95]
T v[156:157],v221,12416
W 5
M v[0:3],v[168:171],v[144:147],v[0:3]
T v[158:159],v221,18560
v_xor_b32 v218, 64, v216
M v[4:7],v[168:171],v[148:151],v[4:7]
T v[160:161],v222,12416
v_xor_b32 v219, 96, v216
M v[96:99],v[244:247],v[144:147],v[96:99]
T v[162:163],v222,18560
W 5
M v[100:103],v[244:247],v[148:151],v[100:103]
T v[164:165],v223,12416
M v[8:11],v[172:175],v[144:147],v[8:11]
T v[166:167],v223,18560
W 5
M v[12:15],v[172:175],v[148:151],v[12:15]
T v[168:169],v220,12544
ds_read_b128 a[144:147], v241
M v[16:19],v[176:179],v[144:147],v[16:19]
T v[170:171],v220,18688
ds_read_b128 a[152:155], v241 offset:2048
M v[20:23],v[176:179],v[148:151],v[20:23]
T v[172:173],v221,12544
ds_read_b128 a[148:151], v242
M v[24:27],v[180:183],v[144:147],v[24:27]
T v[174:175],v221,18688
ds_read_b128 a[156:159], v242 offset:2048
M v[28:31],v[180:183],v[148:151],v[28:31]
W 8
M v[32:35],v[152:155],v[144:147],v[32:35]
T v[176:177],v222,12544
W 7
M v[36:39],v[152:155],v[148:151],v[36:39]
T v[178:179],v222,18688
W 6
M v[40:43],v[156:159],v[144:147],v[40:43]
T v[180:181],v223,12544
W 5
M v[44:47],v[156:159],v[148:151],v[44:47]
T v[182:183],v223,18688
W 4
M v[48:51],v[160:163],v[144:147],v[48:51]
s_waitcnt vmcnt(8)
s_barrier
T v[152:153],v216,0
M v[52:55],v[160:163],v[148:151],v[52:55]
T v[154:155],v216,6144
buffer_load_dwordx4 v229, s[12:15], s79 offen offset:2048 nt lds
M v[56:59],v[164:167],v[144:147],v[56:59]
T v[156:157],v216,12288
s_add_u32 m0, s43, 147456
M v[60:63],v[164:167],v[148:151],v[60:63]
T v[158:159],v216,18432
buffer_load_dwordx4 v236, s[24:27], s45 offen lds
M v[64:67],v[168:171],v[144:147],v[64:67]
T v[160:161],v217,0
W 5
M v[68:71],v[168:171],v[148:151],v[68:71]
T v[162:163],v217,6144
M v[72:75],v[172:175],v[144:147],v[72:75]
T v[164:165],v217,12288
buffer_load_dwordx4 v237, s[24:27], s45 offen offset:1024 lds
M v[76:79],v[172:175],v[148:151],v[76:79]
T v[166:167],v217,18432
W 5
M v[80:83],v[176:179],v[144:147],v[80:83]
T v[168:169],v218,0
M v[84:87],v[176:179],v[148:151],v[84:87]
T v[170:171],v218,6144
buffer_load_dwordx4 v238, s[24:27], s45 offen offset:2048 lds
M v[88:91],v[180:183],v[144:147],v[88:91]
T v[172:173],v218,12288
W 5
M v[92:95],v[180:183],v[148:151],v[92:95]
T v[174:175],v218,18432
M a[48:51],v[152:155],a[208:211],a[48:51]
T v[176:177],v219,0
W 5
M a[48:51],v[156:159],a[212:215],a[48:51]
T v[178:179],v219,6144
M a[52:55],v[156:159],a[220:223],a[52:55]
T v[180:181],v219,12288
W 5
M a[52:55],v[152:155],a[216:219],a[52:55]
T v[182:183],v219,18432
M a[56:59],v[160:163],a[208:211],a[56:59]
T v[152:153],v216,128
W 5
M a[56:59],v[164:167],a[212:215],a[56:59]
T v[154:155],v216,6272
buffer_load_dwordx4 v239, s[24:27], s45 offen offset:3072 lds
M a[60:63],v[164:167],a[220:223],a[60:63]
T v[156:157],v216,12416
W 5
M a[60:63],v[160:163],a[216:219],a[60:63]
T v[158:159],v216,18560
s_add_u32 s79, s5, 0
M a[64:67],v[168:171],a[208:211],a[64:67]
T v[160:161],v217,128
s_add_u32 m0, s40, 122880
M a[64:67],v[172:175],a[212:215],a[64:67]
T v[162:163],v217,6272
buffer_load_dwordx4 v227, s[8:11], s79 offen nt lds
M a[68:71],v[172:175],a[220:223],a[68:71]
T v[164:165],v217,12416
W 5
M a[68:71],v[168:171],a[216:219],a[68:71]
T v[166:167],v217,18560
M a[72:75],v[176:179],a[208:211],a[72:75]
T v[168:169],v218,128
W 5
M a[72:75],v[180:183],a[212:215],a[72:75]
T v[170:171],v218,6272
M a[76:79],v[180:183],a[220:223],a[76:79]
T v[172:173],v218,12416
W 5
M a[76:79],v[176:179],a[216:219],a[76:79]
T v[174:175],v218,18560
M a[80:83],v[152:155],a[208:211],a[80:83]
T v[176:177],v219,128
W 5
M a[80:83],v[156:159],a[212:215],a[80:83]
T v[178:179],v219,6272
v_add_u32 v220, 24576, v225
M a[84:87],v[156:159],a[220:223],a[84:87]
T v[180:181],v219,12416
v_xor_b32 v221, 64, v220
M a[84:87],v[152:155],a[216:219],a[84:87]
T v[182:183],v219,18560
W 5
M a[88:91],v[160:163],a[208:211],a[88:91]
T v[152:153],v216,256
M a[88:91],v[164:167],a[212:215],a[88:91]
T v[154:155],v216,6400
W 5
M a[92:95],v[164:167],a[220:223],a[92:95]
T v[156:157],v216,12544
M a[92:95],v[160:163],a[216:219],a[92:95]
T v[158:159],v216,18688
W 5
M a[96:99],v[168:171],a[208:211],a[96:99]
T v[160:161],v217,256
M a[96:99],v[172:175],a[212:215],a[96:99]
T v[162:163],v217,6400
W 5
M a[100:103],v[172:175],a[220:223],a[100:103]
T v[164:165],v217,12544
M a[100:103],v[168:171],a[216:219],a[100:103]
T v[166:167],v217,18688
W 5
M a[104:107],v[176:179],a[208:211],a[104:107]
T v[168:169],v218,256
M a[104:107],v[180:183],a[212:215],a[104:107]
T v[170:171],v218,6400
buffer_load_dwordx4 v228, s[8:11], s79 offen offset:1024 nt lds
M a[108:111],v[180:183],a[220:223],a[108:111]
T v[172:173],v218,12544
W 5
M a[108:111],v[176:179],a[216:219],a[108:111]
T v[174:175],v218,18688
M a[112:115],v[152:155],a[208:211],a[112:115]
T v[176:177],v219,256
W 5
M a[112:115],v[156:159],a[212:215],a[112:115]
T v[178:179],v219,6400
M a[116:119],v[156:159],a[220:223],a[116:119]
T v[180:181],v219,12544
W 5
M a[116:119],v[152:155],a[216:219],a[116:119]
T v[182:183],v219,18688
M a[120:123],v[160:163],a[208:211],a[120:123]
s_waitcnt vmcnt(12)
s_barrier
ds_read_b128 v[152:155], v220 offset:0
M a[120:123],v[164:167],a[212:215],a[120:123]
ds_read_b128 v[156:159], v221 offset:0
W 5
M a[124:127],v[164:167],a[220:223],a[124:127]
M a[124:127],v[160:163],a[216:219],a[124:127]
ds_read_b128 v[160:163], v220 offset:128
W 4
M a[128:131],v[168:171],a[208:211],a[128:131]
M a[128:131],v[172:175],a[212:215],a[128:131]
ds_read_b128 v[164:167], v221 offset:128
W 3
M a[132:135],v[172:175],a[220:223],a[132:135]
M a[132:135],v[168:171],a[216:219],a[132:135]
ds_read_b128 v[168:171], v220 offset:256
M a[136:139],v[176:179],a[208:211],a[136:139]
ds_read_b128 v[172:175], v221 offset:256
W 3
M a[136:139],v[180:183],a[212:215],a[136:139]
M a[140:143],v[180:183],a[220:223],a[140:143]
M a[140:143],v[176:179],a[216:219],a[140:143]
ds_read_b128 v[176:179], v220 offset:6144
M v[104:107],v[152:155],a[0:3],v[204:207]
ds_read_b128 v[180:183], v221 offset:6144
W 3
M v[104:107],v[156:159],a[8:11],v[104:107]
buffer_load_dwordx4 v229, s[8:11], s79 offen offset:2048 nt lds
s_mul_i32 s77, s50, 6144
M v[104:107],v[160:163],a[16:19],v[104:107]
s_add_u32 s77, s77, s41
s_add_u32 s78, s77, 384
M v[108:111],v[160:163],a[20:23],v[208:211]
s_add_u32 m0, s40, 98304
s_nop 0
buffer_load_dwordx4 v233, s[16:19], s78 offen lds
M v[108:111],v[156:159],a[12:15],v[108:111]
W 1
M v[108:111],v[152:155],a[4:7],v[108:111]
ds_read_b128 v[152:155], v220 offset:6272
M v[108:111],v[164:167],a[28:31],v[108:111]
ds_read_b128 v[156:159], v221 offset:6272
buffer_load_dwordx4 v234, s[16:19], s78 offen offset:1024 lds
M v[108:111],v[168:171],a[36:39],v[108:111]
ds_read_b128 v[160:163], v220 offset:6400
M v[108:111],v[172:175],a[44:47],v[108:111]
M v[104:107],v[172:175],a[40:43],v[104:107]
buffer_load_dwordx4 v235, s[16:19], s78 offen offset:2048 lds
M v[104:107],v[168:171],a[32:35],v[104:107]
W 2
M v[104:107],v[164:167],a[24:27],v[104:107]
ds_read_b128 v[164:167], v221 offset:6400
M v[112:115],v[176:179],a[0:3],v[204:207]
ds_read_b128 v[168:171], v220 offset:12288
W 2
M v[112:115],v[180:183],a[8:11],v[112:115]
ds_read_b128 v[172:175], v221 offset:12288
M v[112:115],v[152:155],a[16:19],v[112:115]
M v[116:119],v[152:155],a[20:23],v[208:211]
v_max3_f32 v248, v104, v105, v106
v_max3_f32 v249, v108, v109, v110
M v[116:119],v[180:183],a[12:15],v[116:119]
v_max_f32 v248, v248, v107
v_max_f32 v249, v249, v111
M v[116:119],v[176:179],a[4:7],v[116:119]
ds_read_b128 v[176:179], v220 offset:12416
W 2
M v[116:119],v[156:159],a[28:31],v[116:119]
v_exp_f32 v200, v104
M v[116:119],v[160:163],a[36:39],v[116:119]
ds_read_b128 v[180:183], v221 offset:12416
v_add_u32 v216, 49152, v224
M v[116:119],v[164:167],a[44:47],v[116:119]
ds_read_b128 v[152:155], v220 offset:12544
v_xor_b32 v217, 32, v216
M v[112:115],v[164:167],a[40:43],v[112:115]
v_exp_f32 v201, v105
M v[112:115],v[160:163],a[32:35],v[112:115]
v_exp_f32 v202, v106
M v[112:115],v[156:159],a[24:27],v[112:115]
ds_read_b128 v[156:159], v221 offset:12544
v_cvt_pk_bf16_f32 v136, v200, v201
M v[120:123],v[168:171],a[0:3],v[204:207]
ds_read_b128 v[160:163], v220 offset:18432
W 3
M v[120:123],v[172:175],a[8:11],v[120:123]
v_exp_f32 v203, v107
M v[120:123],v[176:179],a[16:19],v[120:123]
ds_read_b128 v[164:167], v221 offset:18432
v_cvt_pk_bf16_f32 v137, v202, v203
M v[124:127],v[176:179],a[20:23],v[208:211]
v_exp_f32 v200, v108
M v[124:127],v[172:175],a[12:15],v[124:127]
v_exp_f32 v201, v109
M v[124:127],v[168:171],a[4:7],v[124:127]
ds_read_b128 v[168:171], v220 offset:18560
v_cvt_pk_bf16_f32 v140, v200, v201
M v[124:127],v[180:183],a[28:31],v[124:127]
ds_read_b128 v[172:175], v221 offset:18560
W 3
M v[124:127],v[152:155],a[36:39],v[124:127]
v_exp_f32 v202, v110
M v[124:127],v[156:159],a[44:47],v[124:127]
ds_read_b128 v[176:179], v220 offset:18688
v_max3_f32 v248, v248, v112, v113
M v[120:123],v[156:159],a[40:43],v[120:123]
v_exp_f32 v203, v111
M v[120:123],v[152:155],a[32:35],v[120:123]
v_cvt_pk_bf16_f32 v141, v202, v203
v_max3_f32 v249, v249, v116, v117
M v[120:123],v[180:183],a[24:27],v[120:123]
ds_read_b128 v[180:183], v221 offset:18688
v_xor_b32 v218, 64, v216
M v[128:131],v[160:163],a[0:3],v[204:207]
v_xor_b32 v219, 96, v216
W 2
M v[128:131],v[164:167],a[8:11],v[128:131]
s_waitcnt vmcnt(13)
s_barrier
T v[152:153],v216,0
M v[128:131],v[168:171],a[16:19],v[128:131]
T v[154:155],v216,6144
v_max3_f32 v248, v248, v114, v115
M v[132:135],v[168:171],a[20:23],v[208:211]
T v[156:157],v216,12288
v_max3_f32 v249, v249, v118, v119
M v[132:135],v[164:167],a[12:15],v[132:135]
T v[158:159],v216,18432
W 4
M v[132:135],v[160:163],a[4:7],v[132:135]
T v[160:161],v217,0
M v[132:135],v[172:175],a[28:31],v[132:135]
T v[162:163],v217,6144
M v[132:135],v[176:179],a[36:39],v[132:135]
T v[164:165],v217,12288
W 5
M v[132:135],v[180:183],a[44:47],v[132:135]
T v[166:167],v217,18432
M v[128:131],v[180:183],a[40:43],v[128:131]
T v[168:169],v218,0
v_exp_f32 v200, v112
M v[128:131],v[176:179],a[32:35],v[128:131]
T v[170:171],v218,6144
W 5
M v[128:131],v[172:175],a[24:27],v[128:131]
T v[172:173],v218,12288
v_exp_f32 v201, v113
M a[48:51],v[152:155],a[224:227],a[48:51]
T v[174:175],v218,18432
s_add_u32 s79, s5, 0
s_add_u32 m0, s40, 0
s_nop 0
buffer_load_dwordx4 v227, s[12:15], s79 offen nt lds
M a[48:51],v[156:159],a[228:231],a[48:51]
T v[176:177],v219,0
v_cvt_pk_bf16_f32 v138, v200, v201
M a[52:55],v[156:159],a[236:239],a[52:55]
T v[178:179],v219,6144
W 5
M a[52:55],v[152:155],a[232:235],a[52:55]
T v[180:181],v219,12288
v_exp_f32 v202, v114
M a[56:59],v[160:163],a[224:227],a[56:59]
T v[182:183],v219,18432
W 5
M a[56:59],v[164:167],a[228:231],a[56:59]
T v[152:153],v216,128
v_exp_f32 v203, v115
M a[60:63],v[164:167],a[236:239],a[60:63]
T v[154:155],v216,6272
v_cvt_pk_bf16_f32 v139, v202, v203
M a[60:63],v[160:163],a[232:235],a[60:63]
T v[156:157],v216,12416
W 5
M a[64:67],v[168:171],a[224:227],a[64:67]
T v[158:159],v216,18560
v_exp_f32 v200, v116
M a[64:67],v[172:175],a[228:231],a[64:67]
T v[160:161],v217,128
W 5
M a[68:71],v[172:175],a[236:239],a[68:71]
T v[162:163],v217,6272
M a[68:71],v[168:171],a[232:235],a[68:71]
T v[164:165],v217,12416
v_exp_f32 v201, v117
M a[72:75],v[176:179],a[224:227],a[72:75]
T v[166:167],v217,18560
v_cvt_pk_bf16_f32 v142, v200, v201
M a[72:75],v[180:183],a[228:231],a[72:75]
T v[168:169],v218,128
W 5
M a[76:79],v[180:183],a[236:239],a[76:79]
T v[170:171],v218,6272
v_exp_f32 v202, v118
M a[76:79],v[176:179],a[232:235],a[76:79]
T v[172:173],v218,12416
v_max3_f32 v248, v248, v120, v121
M a[80:83],v[152:155],a[224:227],a[80:83]
T v[174:175],v218,18560
v_max3_f32 v249, v249, v124, v125
M a[80:83],v[156:159],a[228:231],a[80:83]
T v[176:177],v219,128
v_max3_f32 v248, v248, v122, v123
M a[84:87],v[156:159],a[236:239],a[84:87]
T v[178:179],v219,6272
v_max3_f32 v249, v249, v126, v127
M a[84:87],v[152:155],a[232:235],a[84:87]
T v[180:181],v219,12416
W 5
M a[88:91],v[160:163],a[224:227],a[88:91]
T v[182:183],v219,18560
buffer_load_dwordx4 v228, s[12:15], s79 offen offset:1024 nt lds
v_exp_f32 v203, v119
M a[88:91],v[164:167],a[228:231],a[88:91]
T v[152:153],v216,256
v_cvt_pk_bf16_f32 v143, v202, v203
M a[92:95],v[164:167],a[236:239],a[92:95]
T v[154:155],v216,6400
v_max3_f32 v248, v248, v128, v129
M a[92:95],v[160:163],a[232:235],a[92:95]
T v[156:157],v216,12544
v_max3_f32 v249, v249, v132, v133
M a[96:99],v[168:171],a[224:227],a[96:99]
T v[158:159],v216,18688
W 5
M a[96:99],v[172:175],a[228:231],a[96:99]
T v[160:161],v217,256
v_max3_f32 v248, v248, v130, v131
M a[100:103],v[172:175],a[236:239],a[100:103]
T v[162:163],v217,6400
v_max3_f32 v249, v249, v134, v135
M a[100:103],v[168:171],a[232:235],a[100:103]
T v[164:165],v217,12544
v_max_f32 v250, v248, v249
M a[104:107],v[176:179],a[224:227],a[104:107]
T v[166:167],v217,18688
W 5
M a[104:107],v[180:183],a[228:231],a[104:107]
T v[168:169],v218,256
v_cmp_lt_f32 vcc, s60, v250
M a[108:111],v[180:183],a[236:239],a[108:111]
T v[170:171],v218,6400
s_cbranch_vccnz LST0_4_%=
LRT0_4_%=:
M a[108:111],v[176:179],a[232:235],a[108:111]
T v[172:173],v218,12544
v_exp_f32 v200, v120
M a[112:115],v[152:155],a[224:227],a[112:115]
T v[174:175],v218,18688
W 5
M a[112:115],v[156:159],a[228:231],a[112:115]
T v[176:177],v219,256
v_exp_f32 v201, v121
M a[116:119],v[156:159],a[236:239],a[116:119]
T v[178:179],v219,6400
v_cvt_pk_bf16_f32 v144, v200, v201
M a[116:119],v[152:155],a[232:235],a[116:119]
T v[180:181],v219,12544
v_add_u32 v220, 73728, v224
M a[120:123],v[160:163],a[224:227],a[120:123]
T v[182:183],v219,18688
v_exp_f32 v202, v122
W 5
M a[120:123],v[164:167],a[228:231],a[120:123]
v_xor_b32 v221, 32, v220
v_xor_b32 v222, 64, v220
M a[124:127],v[164:167],a[236:239],a[124:127]
v_exp_f32 v203, v123
M a[124:127],v[160:163],a[232:235],a[124:127]
v_xor_b32 v223, 96, v220
v_cvt_pk_bf16_f32 v145, v202, v203
M a[128:131],v[168:171],a[224:227],a[128:131]
s_waitcnt vmcnt(12)
s_barrier
T v[152:153],v220,0
T v[154:155],v220,6144
W 3
M a[128:131],v[172:175],a[228:231],a[128:131]
T v[156:157],v221,0
buffer_load_dwordx4 v229, s[12:15], s79 offen offset:2048 nt lds
M a[132:135],v[172:175],a[236:239],a[132:135]
T v[158:159],v221,6144
v_exp_f32 v200, v124
M a[132:135],v[168:171],a[232:235],a[132:135]
T v[160:161],v222,0
M a[136:139],v[176:179],a[224:227],a[136:139]
T v[162:163],v222,6144
v_exp_f32 v201, v125
W 6
M a[136:139],v[180:183],a[228:231],a[136:139]
T v[164:165],v223,0
v_cvt_pk_bf16_f32 v148, v200, v201
M a[140:143],v[180:183],a[236:239],a[140:143]
T v[166:167],v223,6144
W 5
M a[140:143],v[176:179],a[232:235],a[140:143]
T v[168:169],v220,128
v_exp_f32 v202, v126
M v[0:3],v[152:155],v[136:139],v[0:3]
T v[170:171],v220,6272
W 5
M v[4:7],v[152:155],v[140:143],v[4:7]
T v[172:173],v221,128
v_exp_f32 v203, v127
M v[96:99],v[244:247],v[136:139],v[96:99]
T v[174:175],v221,6272
v_cvt_pk_bf16_f32 v149, v202, v203
M v[100:103],v[244:247],v[140:143],v[100:103]
T v[176:177],v222,128
W 5
M v[8:11],v[156:159],v[136:139],v[8:11]
T v[178:179],v222,6272
v_exp_f32 v200, v128
M v[12:15],v[156:159],v[140:143],v[12:15]
T v[180:181],v223,128
W 5
M v[16:19],v[160:163],v[136:139],v[16:19]
T v[182:183],v223,6272
v_exp_f32 v201, v129
M v[20:23],v[160:163],v[140:143],v[20:23]
T v[152:153],v220,256
v_cvt_pk_bf16_f32 v146, v200, v201
M v[24:27],v[164:167],v[136:139],v[24:27]
T v[154:155],v220,6400
W 5
M v[28:31],v[164:167],v[140:143],v[28:31]
T v[156:157],v221,256
M v[32:35],v[168:171],v[136:139],v[32:35]
T v[158:159],v221,6400
v_exp_f32 v202, v130
M v[36:39],v[168:171],v[140:143],v[36:39]
T v[160:161],v222,256
s_add_u32 s79, s6, 0
M v[40:43],v[172:175],v[136:139],v[40:43]
T v[162:163],v222,6400
s_add_u32 m0, s40, 24576
v_exp_f32 v203, v131
M v[44:47],v[172:175],v[140:143],v[44:47]
T v[164:165],v223,256
W 5
M v[48:51],v[176:179],v[136:139],v[48:51]
T v[166:167],v223,6400
buffer_load_dwordx4 v227, s[8:11], s79 offen nt lds
v_exp_f32 v200, v132
M v[52:55],v[176:179],v[140:143],v[52:55]
T v[168:169],v220,12288
v_cvt_pk_bf16_f32 v147, v202, v203
M v[56:59],v[180:183],v[136:139],v[56:59]
T v[170:171],v220,18432
W 5
M v[60:63],v[180:183],v[140:143],v[60:63]
T v[172:173],v221,12288
v_exp_f32 v201, v133
M v[64:67],v[152:155],v[136:139],v[64:67]
T v[174:175],v221,18432
v_cvt_pk_bf16_f32 v150, v200, v201
M v[68:71],v[152:155],v[140:143],v[68:71]
T v[176:177],v222,12288
W 5
M v[72:75],v[156:159],v[136:139],v[72:75]
T v[178:179],v222,18432
v_exp_f32 v202, v134
M v[76:79],v[156:159],v[140:143],v[76:79]
T v[180:181],v223,12288
v_add_u32 v216, 122880, v225
M v[80:83],v[160:163],v[136:139],v[80:83]
T v[182:183],v223,18432
v_xor_b32 v217, 64, v216
M v[84:87],v[160:163],v[140:143],v[84:87]
T v[152:153],v220,12416
v_exp_f32 v203, v135
M v[88:91],v[164:167],v[136:139],v[88:91]
T v[154:155],v220,18560
v_cvt_pk_bf16_f32 v151, v202, v203
M v[92:95],v[164:167],v[140:143],v[92:95]
T v[156:157],v221,12416
W 5
M v[0:3],v[168:171],v[144:147],v[0:3]
T v[158:159],v221,18560
M v[4:7],v[168:171],v[148:151],v[4:7]
T v[160:161],v222,12416
W 5
M v[96:99],v[244:247],v[144:147],v[96:99]
T v[162:163],v222,18560
M v[100:103],v[244:247],v[148:151],v[100:103]
T v[164:165],v223,12416
W 5
M v[8:11],v[172:175],v[144:147],v[8:11]
T v[166:167],v223,18560
M v[12:15],v[172:175],v[148:151],v[12:15]
T v[168:169],v220,12544
W 5
M v[16:19],v[176:179],v[144:147],v[16:19]
T v[170:171],v220,18688
M v[20:23],v[176:179],v[148:151],v[20:23]
T v[172:173],v221,12544
W 5
M v[24:27],v[180:183],v[144:147],v[24:27]
T v[174:175],v221,18688
buffer_load_dwordx4 v228, s[8:11], s79 offen offset:1024 nt lds
M v[28:31],v[180:183],v[148:151],v[28:31]
T v[176:177],v222,12544
W 5
M v[32:35],v[152:155],v[144:147],v[32:35]
T v[178:179],v222,18688
M v[36:39],v[152:155],v[148:151],v[36:39]
T v[180:181],v223,12544
W 5
M v[40:43],v[156:159],v[144:147],v[40:43]
T v[182:183],v223,18688
M v[44:47],v[156:159],v[148:151],v[44:47]
s_waitcnt vmcnt(8)
s_barrier
ds_read_b128 v[152:155], v216 offset:0
M v[48:51],v[160:163],v[144:147],v[48:51]
ds_read_b128 v[156:159], v217 offset:0
W 5
M v[52:55],v[160:163],v[148:151],v[52:55]
ds_read_b128 v[160:163], v216 offset:128
M v[56:59],v[164:167],v[144:147],v[56:59]
W 4
M v[60:63],v[164:167],v[148:151],v[60:63]
M v[64:67],v[168:171],v[144:147],v[64:67]
W 2
M v[68:71],v[168:171],v[148:151],v[68:71]
ds_read_b128 v[164:167], v217 offset:128
M v[72:75],v[172:175],v[144:147],v[72:75]
ds_read_b128 v[168:171], v216 offset:256
W 2
M v[76:79],v[172:175],v[148:151],v[76:79]
ds_read_b128 v[172:175], v217 offset:256
M v[80:83],v[176:179],v[144:147],v[80:83]
M v[84:87],v[176:179],v[148:151],v[84:87]
M v[88:91],v[180:183],v[144:147],v[88:91]
M v[92:95],v[180:183],v[148:151],v[92:95]
ds_read_b128 v[176:179], v216 offset:6144
W 2
M v[104:107],v[152:155],a[0:3],v[204:207]
ds_read_b128 v[180:183], v217 offset:6144
M v[104:107],v[156:159],a[8:11],v[104:107]
M v[104:107],v[160:163],a[16:19],v[104:107]
buffer_load_dwordx4 v229, s[8:11], s79 offen offset:2048 nt lds
s_mul_i32 s77, s51, 6144
M v[108:111],v[160:163],a[20:23],v[208:211]
s_add_u32 s77, s77, s41
s_add_u32 s78, s77, 384
M v[108:111],v[156:159],a[12:15],v[108:111]
s_add_u32 m0, s40, 49152
s_nop 0
buffer_load_dwordx4 v233, s[16:19], s78 offen lds
M v[108:111],v[152:155],a[4:7],v[108:111]
ds_read_b128 v[152:155], v216 offset:6272
W 1
M v[108:111],v[164:167],a[28:31],v[108:111]
ds_read_b128 v[156:159], v217 offset:6272
M v[108:111],v[168:171],a[36:39],v[108:111]
ds_read_b128 v[160:163], v216 offset:6400
buffer_load_dwordx4 v234, s[16:19], s78 offen offset:1024 lds
M v[108:111],v[172:175],a[44:47],v[108:111]
M v[104:107],v[172:175],a[40:43],v[104:107]
M v[104:107],v[168:171],a[32:35],v[104:107]
buffer_load_dwordx4 v235, s[16:19], s78 offen offset:2048 lds
M v[104:107],v[164:167],a[24:27],v[104:107]
ds_read_b128 v[164:167], v217 offset:6400
W 2
M v[112:115],v[176:179],a[0:3],v[204:207]
ds_read_b128 v[168:171], v216 offset:12288
M v[112:115],v[180:183],a[8:11],v[112:115]
ds_read_b128 v[172:175], v217 offset:12288
M v[112:115],v[152:155],a[16:19],v[112:115]
M v[116:119],v[152:155],a[20:23],v[208:211]
v_max3_f32 v248, v104, v105, v106
v_max3_f32 v249, v108, v109, v110
M v[116:119],v[180:183],a[12:15],v[116:119]
v_max_f32 v248, v248, v107
v_max_f32 v249, v249, v111
M v[116:119],v[176:179],a[4:7],v[116:119]
ds_read_b128 v[176:179], v216 offset:12416
W 2
M v[116:119],v[156:159],a[28:31],v[116:119]
v_exp_f32 v200, v104
M v[116:119],v[160:163],a[36:39],v[116:119]
ds_read_b128 v[180:183], v217 offset:12416
v_add_u32 v220, 98304, v224
M v[116:119],v[164:167],a[44:47],v[116:119]
ds_read_b128 v[152:155], v216 offset:12544
v_xor_b32 v221, 32, v220
M v[112:115],v[164:167],a[40:43],v[112:115]
v_exp_f32 v201, v105
M v[112:115],v[160:163],a[32:35],v[112:115]
v_exp_f32 v202, v106
M v[112:115],v[156:159],a[24:27],v[112:115]
ds_read_b128 v[156:159], v217 offset:12544
v_cvt_pk_bf16_f32 v136, v200, v201
M v[120:123],v[168:171],a[0:3],v[204:207]
ds_read_b128 v[160:163], v216 offset:18432
W 3
M v[120:123],v[172:175],a[8:11],v[120:123]
v_exp_f32 v203, v107
M v[120:123],v[176:179],a[16:19],v[120:123]
ds_read_b128 v[164:167], v217 offset:18432
v_cvt_pk_bf16_f32 v137, v202, v203
M v[124:127],v[176:179],a[20:23],v[208:211]
v_exp_f32 v200, v108
M v[124:127],v[172:175],a[12:15],v[124:127]
v_exp_f32 v201, v109
M v[124:127],v[168:171],a[4:7],v[124:127]
ds_read_b128 v[168:171], v216 offset:18560
v_cvt_pk_bf16_f32 v140, v200, v201
M v[124:127],v[180:183],a[28:31],v[124:127]
ds_read_b128 v[172:175], v217 offset:18560
W 3
M v[124:127],v[152:155],a[36:39],v[124:127]
v_exp_f32 v202, v110
M v[124:127],v[156:159],a[44:47],v[124:127]
ds_read_b128 v[176:179], v216 offset:18688
v_max3_f32 v248, v248, v112, v113
M v[120:123],v[156:159],a[40:43],v[120:123]
v_exp_f32 v203, v111
M v[120:123],v[152:155],a[32:35],v[120:123]
v_cvt_pk_bf16_f32 v141, v202, v203
v_max3_f32 v249, v249, v116, v117
M v[120:123],v[180:183],a[24:27],v[120:123]
ds_read_b128 v[180:183], v217 offset:18688
v_xor_b32 v222, 64, v220
M v[128:131],v[160:163],a[0:3],v[204:207]
v_xor_b32 v223, 96, v220
W 2
M v[128:131],v[164:167],a[8:11],v[128:131]
s_waitcnt vmcnt(9)
s_barrier
T v[152:153],v220,0
M v[128:131],v[168:171],a[16:19],v[128:131]
T v[154:155],v220,6144
v_max3_f32 v248, v248, v114, v115
M v[132:135],v[168:171],a[20:23],v[208:211]
T v[156:157],v220,12288
v_max3_f32 v249, v249, v118, v119
M v[132:135],v[164:167],a[12:15],v[132:135]
T v[158:159],v220,18432
W 4
M v[132:135],v[160:163],a[4:7],v[132:135]
T v[160:161],v221,0
M v[132:135],v[172:175],a[28:31],v[132:135]
T v[162:163],v221,6144
M v[132:135],v[176:179],a[36:39],v[132:135]
T v[164:165],v221,12288
W 5
M v[132:135],v[180:183],a[44:47],v[132:135]
T v[166:167],v221,18432
M v[128:131],v[180:183],a[40:43],v[128:131]
T v[168:169],v222,0
v_exp_f32 v200, v112
M v[128:131],v[176:179],a[32:35],v[128:131]
T v[170:171],v222,6144
W 5
M v[128:131],v[172:175],a[24:27],v[128:131]
T v[172:173],v222,12288
v_exp_f32 v201, v113
M a[48:51],v[152:155],a[240:243],a[48:51]
T v[174:175],v222,18432
s_add_u32 s79, s6, 0
s_add_u32 m0, s40, 73728
s_nop 0
buffer_load_dwordx4 v227, s[12:15], s79 offen nt lds
M a[48:51],v[156:159],a[244:247],a[48:51]
T v[176:177],v223,0
v_cvt_pk_bf16_f32 v138, v200, v201
M a[52:55],v[156:159],a[252:255],a[52:55]
T v[178:179],v223,6144
W 5
M a[52:55],v[152:155],a[248:251],a[52:55]
T v[180:181],v223,12288
v_exp_f32 v202, v114
M a[56:59],v[160:163],a[240:243],a[56:59]
T v[182:183],v223,18432
W 5
M a[56:59],v[164:167],a[244:247],a[56:59]
T v[152:153],v220,128
v_exp_f32 v203, v115
M a[60:63],v[164:167],a[252:255],a[60:63]
T v[154:155],v220,6272
v_cvt_pk_bf16_f32 v139, v202, v203
M a[60:63],v[160:163],a[248:251],a[60:63]
T v[156:157],v220,12416
W 5
M a[64:67],v[168:171],a[240:243],a[64:67]
T v[158:159],v220,18560
v_exp_f32 v200, v116
M a[64:67],v[172:175],a[244:247],a[64:67]
T v[160:161],v221,128
W 5
M a[68:71],v[172:175],a[252:255],a[68:71]
T v[162:163],v221,6272
M a[68:71],v[168:171],a[248:251],a[68:71]
T v[164:165],v221,12416
v_exp_f32 v201, v117
M a[72:75],v[176:179],a[240:243],a[72:75]
T v[166:167],v221,18560
v_cvt_pk_bf16_f32 v142, v200, v201
M a[72:75],v[180:183],a[244:247],a[72:75]
T v[168:169],v222,128
W 5
M a[76:79],v[180:183],a[252:255],a[76:79]
T v[170:171],v222,6272
v_exp_f32 v202, v118
M a[76:79],v[176:179],a[248:251],a[76:79]
T v[172:173],v222,12416
v_max3_f32 v248, v248, v120, v121
M a[80:83],v[152:155],a[240:243],a[80:83]
T v[174:175],v222,18560
v_max3_f32 v249, v249, v124, v125
M a[80:83],v[156:159],a[244:247],a[80:83]
T v[176:177],v223,128
v_max3_f32 v248, v248, v122, v123
M a[84:87],v[156:159],a[252:255],a[84:87]
T v[178:179],v223,6272
v_max3_f32 v249, v249, v126, v127
M a[84:87],v[152:155],a[248:251],a[84:87]
T v[180:181],v223,12416
W 5
M a[88:91],v[160:163],a[240:243],a[88:91]
T v[182:183],v223,18560
buffer_load_dwordx4 v228, s[12:15], s79 offen offset:1024 nt lds
v_exp_f32 v203, v119
M a[88:91],v[164:167],a[244:247],a[88:91]
T v[152:153],v220,256
v_cvt_pk_bf16_f32 v143, v202, v203
M a[92:95],v[164:167],a[252:255],a[92:95]
T v[154:155],v220,6400
v_max3_f32 v248, v248, v128, v129
M a[92:95],v[160:163],a[248:251],a[92:95]
T v[156:157],v220,12544
v_max3_f32 v249, v249, v132, v133
M a[96:99],v[168:171],a[240:243],a[96:99]
T v[158:159],v220,18688
W 5
M a[96:99],v[172:175],a[244:247],a[96:99]
T v[160:161],v221,256
v_max3_f32 v248, v248, v130, v131
M a[100:103],v[172:175],a[252:255],a[100:103]
T v[162:163],v221,6400
v_max3_f32 v249, v249, v134, v135
M a[100:103],v[168:171],a[248:251],a[100:103]
T v[164:165],v221,12544
v_max_f32 v250, v248, v249
M a[104:107],v[176:179],a[240:243],a[104:107]
T v[166:167],v221,18688
W 5
M a[104:107],v[180:183],a[244:247],a[104:107]
T v[168:169],v222,256
v_cmp_lt_f32 vcc, s60, v250
M a[108:111],v[180:183],a[252:255],a[108:111]
T v[170:171],v222,6400
s_cbranch_vccnz LST0_5_%=
LRT0_5_%=:
M a[108:111],v[176:179],a[248:251],a[108:111]
T v[172:173],v222,12544
v_exp_f32 v200, v120
M a[112:115],v[152:155],a[240:243],a[112:115]
T v[174:175],v222,18688
W 5
M a[112:115],v[156:159],a[244:247],a[112:115]
T v[176:177],v223,256
v_exp_f32 v201, v121
M a[116:119],v[156:159],a[252:255],a[116:119]
T v[178:179],v223,6400
v_cvt_pk_bf16_f32 v144, v200, v201
M a[116:119],v[152:155],a[248:251],a[116:119]
T v[180:181],v223,12544
v_add_u32 v216, 0, v224
M a[120:123],v[160:163],a[240:243],a[120:123]
T v[182:183],v223,18688
v_exp_f32 v202, v122
W 5
M a[120:123],v[164:167],a[244:247],a[120:123]
v_xor_b32 v217, 32, v216
v_xor_b32 v218, 64, v216
M a[124:127],v[164:167],a[252:255],a[124:127]
v_exp_f32 v203, v123
M a[124:127],v[160:163],a[248:251],a[124:127]
v_xor_b32 v219, 96, v216
v_cvt_pk_bf16_f32 v145, v202, v203
M a[128:131],v[168:171],a[240:243],a[128:131]
s_waitcnt vmcnt(8)
s_barrier
T v[152:153],v216,0
T v[154:155],v216,6144
W 3
M a[128:131],v[172:175],a[244:247],a[128:131]
T v[156:157],v217,0
buffer_load_dwordx4 v229, s[12:15], s79 offen offset:2048 nt lds
M a[132:135],v[172:175],a[252:255],a[132:135]
T v[158:159],v217,6144
v_exp_f32 v200, v124
M a[132:135],v[168:171],a[248:251],a[132:135]
T v[160:161],v218,0
M a[136:139],v[176:179],a[240:243],a[136:139]
T v[162:163],v218,6144
v_exp_f32 v201, v125
W 6
M a[136:139],v[180:183],a[244:247],a[136:139]
T v[164:165],v219,0
v_cvt_pk_bf16_f32 v148, v200, v201
M a[140:143],v[180:183],a[252:255],a[140:143]
T v[166:167],v219,6144
W 5
M a[140:143],v[176:179],a[248:251],a[140:143]
T v[168:169],v216,128
v_exp_f32 v202, v126
M v[0:3],v[152:155],v[136:139],v[0:3]
T v[170:171],v216,6272
s_mul_i32 s77, s44, 6144
M v[4:7],v[152:155],v[140:143],v[4:7]
T v[172:173],v217,128
v_exp_f32 v203, v127
M v[96:99],v[244:247],v[136:139],v[96:99]
T v[174:175],v217,6272
v_cvt_pk_bf16_f32 v149, v202, v203
M v[100:103],v[244:247],v[140:143],v[100:103]
T v[176:177],v218,128
W 5
M v[8:11],v[156:159],v[136:139],v[8:11]
T v[178:179],v218,6272
v_exp_f32 v200, v128
M v[12:15],v[156:159],v[140:143],v[12:15]
T v[180:181],v219,128
s_add_u32 s77, s77, s41
M v[16:19],v[160:163],v[136:139],v[16:19]
T v[182:183],v219,6272
v_exp_f32 v201, v129
M v[20:23],v[160:163],v[140:143],v[20:23]
T v[152:153],v216,256
v_cvt_pk_bf16_f32 v146, v200, v201
M v[24:27],v[164:167],v[136:139],v[24:27]
T v[154:155],v216,6400
s_add_u32 s78, s77, 384
M v[28:31],v[164:167],v[140:143],v[28:31]
T v[156:157],v217,256
W 5
M v[32:35],v[168:171],v[136:139],v[32:35]
T v[158:159],v217,6400
v_exp_f32 v202, v130
M v[36:39],v[168:171],v[140:143],v[36:39]
T v[160:161],v218,256
s_add_u32 m0, s40, 122880
M v[40:43],v[172:175],v[136:139],v[40:43]
T v[162:163],v218,6400
v_exp_f32 v203, v131
M v[44:47],v[172:175],v[140:143],v[44:47]
T v[164:165],v219,256
v_cvt_pk_bf16_f32 v147, v202, v203
M v[48:51],v[176:179],v[136:139],v[48:51]
T v[166:167],v219,6400
buffer_load_dwordx4 v233, s[16:19], s78 offen lds
M v[52:55],v[176:179],v[140:143],v[52:55]
T v[168:169],v216,12288
v_exp_f32 v200, v132
W 5
M v[56:59],v[180:183],v[136:139],v[56:59]
T v[170:171],v216,18432
M v[60:63],v[180:183],v[140:143],v[60:63]
T v[172:173],v217,12288
v_exp_f32 v201, v133
M v[64:67],v[152:155],v[136:139],v[64:67]
T v[174:175],v217,18432
v_cvt_pk_bf16_f32 v150, v200, v201
M v[68:71],v[152:155],v[140:143],v[68:71]
T v[176:177],v218,12288
buffer_load_dwordx4 v234, s[16:19], s78 offen offset:1024 lds
M v[72:75],v[156:159],v[136:139],v[72:75]
T v[178:179],v218,18432
v_exp_f32 v202, v134
M v[76:79],v[156:159],v[140:143],v[76:79]
T v[180:181],v219,12288
W 5
M v[80:83],v[160:163],v[136:139],v[80:83]
T v[182:183],v219,18432
buffer_load_dwordx4 v235, s[16:19], s78 offen offset:2048 lds
M v[84:87],v[160:163],v[140:143],v[84:87]
T v[152:153],v216,12416
v_exp_f32 v203, v135
M v[88:91],v[164:167],v[136:139],v[88:91]
T v[154:155],v216,18560
v_cvt_pk_bf16_f32 v151, v202, v203
M v[92:95],v[164:167],v[140:143],v[92:95]
T v[156:157],v217,12416
v_add_u32 v220, 24576, v225
M v[0:3],v[168:171],v[144:147],v[0:3]
T v[158:159],v217,18560
v_xor_b32 v221, 64, v220
M v[4:7],v[168:171],v[148:151],v[4:7]
T v[160:161],v218,12416
W 5
M v[96:99],v[244:247],v[144:147],v[96:99]
T v[162:163],v218,18560
M v[100:103],v[244:247],v[148:151],v[100:103]
T v[164:165],v219,12416
W 5
M v[8:11],v[172:175],v[144:147],v[8:11]
T v[166:167],v219,18560
M v[12:15],v[172:175],v[148:151],v[12:15]
T v[168:169],v216,12544
W 5
M v[16:19],v[176:179],v[144:147],v[16:19]
T v[170:171],v216,18688
M v[20:23],v[176:179],v[148:151],v[20:23]
T v[172:173],v217,12544
W 5
M v[24:27],v[180:183],v[144:147],v[24:27]
T v[174:175],v217,18688
M v[28:31],v[180:183],v[148:151],v[28:31]
T v[176:177],v218,12544
W 5
M v[32:35],v[152:155],v[144:147],v[32:35]
T v[178:179],v218,18688
M v[36:39],v[152:155],v[148:151],v[36:39]
T v[180:181],v219,12544
W 5
M v[40:43],v[156:159],v[144:147],v[40:43]
T v[182:183],v219,18688
M v[44:47],v[156:159],v[148:151],v[44:47]
s_waitcnt vmcnt(9)
s_barrier
ds_read_b128 v[152:155], v220 offset:0
M v[48:51],v[160:163],v[144:147],v[48:51]
ds_read_b128 v[156:159], v221 offset:0
W 5
M v[52:55],v[160:163],v[148:151],v[52:55]
ds_read_b128 v[160:163], v220 offset:128
M v[56:59],v[164:167],v[144:147],v[56:59]
W 4
M v[60:63],v[164:167],v[148:151],v[60:63]
M v[64:67],v[168:171],v[144:147],v[64:67]
W 2
M v[68:71],v[168:171],v[148:151],v[68:71]
ds_read_b128 v[164:167], v221 offset:128
M v[72:75],v[172:175],v[144:147],v[72:75]
ds_read_b128 v[168:171], v220 offset:256
W 2
M v[76:79],v[172:175],v[148:151],v[76:79]
ds_read_b128 v[172:175], v221 offset:256
M v[80:83],v[176:179],v[144:147],v[80:83]
M v[84:87],v[176:179],v[148:151],v[84:87]
M v[88:91],v[180:183],v[144:147],v[88:91]
M v[92:95],v[180:183],v[148:151],v[92:95]
ds_read_b128 v[176:179], v220 offset:6144
W 2
M v[104:107],v[152:155],a[0:3],v[204:207]
ds_read_b128 v[180:183], v221 offset:6144
s_add_u32 s79, s7, 0
s_add_u32 m0, s40, 98304
s_nop 0
buffer_load_dwordx4 v227, s[8:11], s79 offen nt lds
M v[104:107],v[156:159],a[8:11],v[104:107]
M v[104:107],v[160:163],a[16:19],v[104:107]
M v[108:111],v[160:163],a[20:23],v[208:211]
M v[108:111],v[156:159],a[12:15],v[108:111]
W 1
M v[108:111],v[152:155],a[4:7],v[108:111]
ds_read_b128 v[152:155], v220 offset:6272
M v[108:111],v[164:167],a[28:31],v[108:111]
ds_read_b128 v[156:159], v221 offset:6272
M v[108:111],v[168:171],a[36:39],v[108:111]
ds_read_b128 v[160:163], v220 offset:6400
M v[108:111],v[172:175],a[44:47],v[108:111]
M v[104:107],v[172:175],a[40:43],v[104:107]
M v[104:107],v[168:171],a[32:35],v[104:107]
W 2
M v[104:107],v[164:167],a[24:27],v[104:107]
ds_read_b128 v[164:167], v221 offset:6400
M v[112:115],v[176:179],a[0:3],v[204:207]
ds_read_b128 v[168:171], v220 offset:12288
W 2
M v[112:115],v[180:183],a[8:11],v[112:115]
ds_read_b128 v[172:175], v221 offset:12288
M v[112:115],v[152:155],a[16:19],v[112:115]
M v[116:119],v[152:155],a[20:23],v[208:211]
v_max3_f32 v248, v104, v105, v106
v_max3_f32 v249, v108, v109, v110
M v[116:119],v[180:183],a[12:15],v[116:119]
v_max_f32 v248, v248, v107
v_max_f32 v249, v249, v111
M v[116:119],v[176:179],a[4:7],v[116:119]
ds_read_b128 v[176:179], v220 offset:12416
W 2
M v[116:119],v[156:159],a[28:31],v[116:119]
v_exp_f32 v200, v104
M v[116:119],v[160:163],a[36:39],v[116:119]
ds_read_b128 v[180:183], v221 offset:12416
v_add_u32 v216, 49152, v224
M v[116:119],v[164:167],a[44:47],v[116:119]
ds_read_b128 v[152:155], v220 offset:12544
buffer_load_dwordx4 v228, s[8:11], s79 offen offset:1024 nt lds
M v[112:115],v[164:167],a[40:43],v[112:115]
v_exp_f32 v201, v105
M v[112:115],v[160:163],a[32:35],v[112:115]
v_exp_f32 v202, v106
M v[112:115],v[156:159],a[24:27],v[112:115]
ds_read_b128 v[156:159], v221 offset:12544
v_cvt_pk_bf16_f32 v136, v200, v201
M v[120:123],v[168:171],a[0:3],v[204:207]
ds_read_b128 v[160:163], v220 offset:18432
W 3
M v[120:123],v[172:175],a[8:11],v[120:123]
v_exp_f32 v203, v107
M v[120:123],v[176:179],a[16:19],v[120:123]
ds_read_b128 v[164:167], v221 offset:18432
v_cvt_pk_bf16_f32 v137, v202, v203
M v[124:127],v[176:179],a[20:23],v[208:211]
v_exp_f32 v200, v108
M v[124:127],v[172:175],a[12:15],v[124:127]
v_exp_f32 v201, v109
M v[124:127],v[168:171],a[4:7],v[124:127]
ds_read_b128 v[168:171], v220 offset:18560
v_cvt_pk_bf16_f32 v140, v200, v201
M v[124:127],v[180:183],a[28:31],v[124:127]
ds_read_b128 v[172:175], v221 offset:18560
W 3
M v[124:127],v[152:155],a[36:39],v[124:127]
v_exp_f32 v202, v110
M v[124:127],v[156:159],a[44:47],v[124:127]
ds_read_b128 v[176:179], v220 offset:18688
v_max3_f32 v248, v248, v112, v113
M v[120:123],v[156:159],a[40:43],v[120:123]
v_exp_f32 v203, v111
M v[120:123],v[152:155],a[32:35],v[120:123]
v_cvt_pk_bf16_f32 v141, v202, v203
v_max3_f32 v249, v249, v116, v117
M v[120:123],v[180:183],a[24:27],v[120:123]
ds_read_b128 v[180:183], v221 offset:18688
v_xor_b32 v217, 32, v216
M v[128:131],v[160:163],a[0:3],v[204:207]
v_xor_b32 v218, 64, v216
W 2
M v[128:131],v[164:167],a[8:11],v[128:131]
v_xor_b32 v219, 96, v216
v_max3_f32 v248, v248, v114, v115
M v[128:131],v[168:171],a[16:19],v[128:131]
s_waitcnt vmcnt(8)
s_barrier
T v[152:153],v216,0
M v[132:135],v[168:171],a[20:23],v[208:211]
T v[154:155],v216,6144
v_max3_f32 v249, v249, v118, v119
M v[132:135],v[164:167],a[12:15],v[132:135]
T v[156:157],v216,12288
buffer_load_dwordx4 v229, s[8:11], s79 offen offset:2048 nt lds
M v[132:135],v[160:163],a[4:7],v[132:135]
T v[158:159],v216,18432
W 4
M v[132:135],v[172:175],a[28:31],v[132:135]
T v[160:161],v217,0
M v[132:135],v[176:179],a[36:39],v[132:135]
T v[162:163],v217,6144
M v[132:135],v[180:183],a[44:47],v[132:135]
T v[164:165],v217,12288
W 5
M v[128:131],v[180:183],a[40:43],v[128:131]
T v[166:167],v217,18432
v_exp_f32 v200, v112
M v[128:131],v[176:179],a[32:35],v[128:131]
T v[168:169],v218,0
W 5
M v[128:131],v[172:175],a[24:27],v[128:131]
T v[170:171],v218,6144
v_exp_f32 v201, v113
M a[48:51],v[152:155],v[184:187],a[48:51]
T v[172:173],v218,12288
v_cvt_pk_bf16_f32 v138, v200, v201
M a[48:51],v[156:159],v[188:191],a[48:51]
T v[174:175],v218,18432
s_mul_i32 s77, s45, 6144
M a[52:55],v[156:159],v[196:199],a[52:55]
T v[176:177],v219,0
s_add_u32 s77, s77, s41
M a[52:55],v[152:155],v[192:195],a[52:55]
T v[178:179],v219,6144
v_exp_f32 v202, v114
W 5
M a[56:59],v[160:163],v[184:187],a[56:59]
T v[180:181],v219,12288
s_add_u32 s78, s77, 384
M a[56:59],v[164:167],v[188:191],a[56:59]
T v[182:183],v219,18432
v_exp_f32 v203, v115
M a[60:63],v[164:167],v[196:199],a[60:63]
T v[152:153],v216,128
v_cvt_pk_bf16_f32 v139, v202, v203
M a[60:63],v[160:163],v[192:195],a[60:63]
T v[154:155],v216,6272
W 5
M a[64:67],v[168:171],v[184:187],a[64:67]
T v[156:157],v216,12416
v_exp_f32 v200, v116
M a[64:67],v[172:175],v[188:191],a[64:67]
T v[158:159],v216,18560
s_add_u32 m0, s40, 0
M a[68:71],v[172:175],v[196:199],a[68:71]
T v[160:161],v217,128
buffer_load_dwordx4 v233, s[16:19], s78 offen lds
M a[68:71],v[168:171],v[192:195],a[68:71]
T v[162:163],v217,6272
v_exp_f32 v201, v117
W 5
M a[72:75],v[176:179],v[184:187],a[72:75]
T v[164:165],v217,12416
v_cvt_pk_bf16_f32 v142, v200, v201
M a[72:75],v[180:183],v[188:191],a[72:75]
T v[166:167],v217,18560
buffer_load_dwordx4 v234, s[16:19], s78 offen offset:1024 lds
M a[76:79],v[180:183],v[196:199],a[76:79]
T v[168:169],v218,128
v_exp_f32 v202, v118
M a[76:79],v[176:179],v[192:195],a[76:79]
T v[170:171],v218,6272
W 5
M a[80:83],v[152:155],v[184:187],a[80:83]
T v[172:173],v218,12416
v_max3_f32 v248, v248, v120, v121
M a[80:83],v[156:159],v[188:191],a[80:83]
T v[174:175],v218,18560
v_max3_f32 v249, v249, v124, v125
M a[84:87],v[156:159],v[196:199],a[84:87]
T v[176:177],v219,128
v_max3_f32 v248, v248, v122, v123
M a[84:87],v[152:155],v[192:195],a[84:87]
T v[178:179],v219,6272
v_exp_f32 v203, v119
W 5
M a[88:91],v[160:163],v[184:187],a[88:91]
T v[180:181],v219,12416
v_cvt_pk_bf16_f32 v143, v202, v203
M a[88:91],v[164:167],v[188:191],a[88:91]
T v[182:183],v219,18560
v_max3_f32 v249, v249, v126, v127
M a[92:95],v[164:167],v[196:199],a[92:95]
T v[152:153],v216,256
v_max3_f32 v248, v248, v128, v129
M a[92:95],v[160:163],v[192:195],a[92:95]
T v[154:155],v216,6400
W 5
M a[96:99],v[168:171],v[184:187],a[96:99]
T v[156:157],v216,12544
v_max3_f32 v249, v249, v132, v133
M a[96:99],v[172:175],v[188:191],a[96:99]
T v[158:159],v216,18688
v_max3_f32 v248, v248, v130, v131
M a[100:103],v[172:175],v[196:199],a[100:103]
T v[160:161],v217,256
v_max3_f32 v249, v249, v134, v135
M a[100:103],v[168:171],v[192:195],a[100:103]
T v[162:163],v217,6400
W 5
M a[104:107],v[176:179],v[184:187],a[104:107]
T v[164:165],v217,12544
v_max_f32 v250, v248, v249
M a[104:107],v[180:183],v[188:191],a[104:107]
T v[166:167],v217,18688
v_cmp_lt_f32 vcc, s60, v250
M a[108:111],v[180:183],v[196:199],a[108:111]
T v[168:169],v218,256
s_cbranch_vccnz LST0_6_%=
LRT0_6_%=:
M a[108:111],v[176:179],v[192:195],a[108:111]
T v[170:171],v218,6400
v_exp_f32 v200, v120
W 5
M a[112:115],v[152:155],v[184:187],a[112:115]
T v[172:173],v218,12544
buffer_load_dwordx4 v235, s[16:19], s78 offen offset:2048 lds
M a[112:115],v[156:159],v[188:191],a[112:115]
T v[174:175],v218,18688
v_exp_f32 v201, v121
M a[116:119],v[156:159],v[196:199],a[116:119]
T v[176:177],v219,256
v_cvt_pk_bf16_f32 v144, v200, v201
M a[116:119],v[152:155],v[192:195],a[116:119]
T v[178:179],v219,6400
W 5
M a[120:123],v[160:163],v[184:187],a[120:123]
T v[180:181],v219,12544
v_exp_f32 v202, v122
M a[120:123],v[164:167],v[188:191],a[120:123]
T v[182:183],v219,18688
v_add_u32 v220, 73728, v224
M a[124:127],v[164:167],v[196:199],a[124:127]
v_exp_f32 v203, v123
M a[124:127],v[160:163],v[192:195],a[124:127]
v_xor_b32 v221, 32, v220
W 3
M a[128:131],v[168:171],v[184:187],a[128:131]
v_xor_b32 v222, 64, v220
v_xor_b32 v223, 96, v220
s_waitcnt vmcnt(9)
s_barrier
T v[152:153],v220,0
T v[154:155],v220,6144
v_exp_f32 v200, v124
M a[128:131],v[172:175],v[188:191],a[128:131]
T v[156:157],v221,0
v_cvt_pk_bf16_f32 v145, v202, v203
M a[132:135],v[172:175],v[196:199],a[132:135]
T v[158:159],v221,6144
W 4
M a[132:135],v[168:171],v[192:195],a[132:135]
T v[160:161],v222,0
M a[136:139],v[176:179],v[184:187],a[136:139]
T v[162:163],v222,6144
v_exp_f32 v201, v125
M a[136:139],v[180:183],v[188:191],a[136:139]
T v[164:165],v223,0
v_cvt_pk_bf16_f32 v148, v200, v201
M a[140:143],v[180:183],v[196:199],a[140:143]
T v[166:167],v223,6144
W 5
M a[140:143],v[176:179],v[192:195],a[140:143]
T v[168:169],v220,128
v_exp_f32 v202, v126
M v[0:3],v[152:155],v[136:139],v[0:3]
T v[170:171],v220,6272
s_add_u32 s79, s7, 0
s_add_u32 m0, s40, 24576
s_nop 0
buffer_load_dwordx4 v227, s[12:15], s79 offen nt lds
M v[4:7],v[152:155],v[140:143],v[4:7]
T v[172:173],v221,128
v_exp_f32 v203, v127
M v[96:99],v[244:247],v[136:139],v[96:99]
T v[174:175],v221,6272
v_cvt_pk_bf16_f32 v149, v202, v203
M v[100:103],v[244:247],v[140:143],v[100:103]
T v[176:177],v222,128
W 5
M v[8:11],v[156:159],v[136:139],v[8:11]
T v[178:179],v222,6272
v_exp_f32 v200, v128
M v[12:15],v[156:159],v[140:143],v[12:15]
T v[180:181],v223,128
W 5
M v[16:19],v[160:163],v[136:139],v[16:19]
T v[182:183],v223,6272
v_exp_f32 v201, v129
M v[20:23],v[160:163],v[140:143],v[20:23]
T v[152:153],v220,256
v_cvt_pk_bf16_f32 v146, v200, v201
M v[24:27],v[164:167],v[136:139],v[24:27]
T v[154:155],v220,6400
W 5
M v[28:31],v[164:167],v[140:143],v[28:31]
T v[156:157],v221,256
M v[32:35],v[168:171],v[136:139],v[32:35]
T v[158:159],v221,6400
v_exp_f32 v202, v130
M v[36:39],v[168:171],v[140:143],v[36:39]
T v[160:161],v222,256
W 5
M v[40:43],v[172:175],v[136:139],v[40:43]
T v[162:163],v222,6400
v_exp_f32 v203, v131
M v[44:47],v[172:175],v[140:143],v[44:47]
T v[164:165],v223,256
v_cvt_pk_bf16_f32 v147, v202, v203
M v[48:51],v[176:179],v[136:139],v[48:51]
T v[166:167],v223,6400
W 5
M v[52:55],v[176:179],v[140:143],v[52:55]
T v[168:169],v220,12288
v_exp_f32 v200, v132
M v[56:59],v[180:183],v[136:139],v[56:59]
T v[170:171],v220,18432
W 5
M v[60:63],v[180:183],v[140:143],v[60:63]
T v[172:173],v221,12288
v_exp_f32 v201, v133
M v[64:67],v[152:155],v[136:139],v[64:67]
T v[174:175],v221,18432
v_cvt_pk_bf16_f32 v150, v200, v201
M v[68:71],v[152:155],v[140:143],v[68:71]
T v[176:177],v222,12288
W 5
M v[72:75],v[156:159],v[136:139],v[72:75]
T v[178:179],v222,18432
buffer_load_dwordx4 v228, s[12:15], s79 offen offset:1024 nt lds
v_exp_f32 v202, v134
M v[76:79],v[156:159],v[140:143],v[76:79]
T v[180:181],v223,12288
v_add_u32 v216, 122880, v224
M v[80:83],v[160:163],v[136:139],v[80:83]
T v[182:183],v223,18432
v_xor_b32 v217, 32, v216
M v[84:87],v[160:163],v[140:143],v[84:87]
T v[152:153],v220,12416
v_exp_f32 v203, v135
M v[88:91],v[164:167],v[136:139],v[88:91]
T v[154:155],v220,18560
v_cvt_pk_bf16_f32 v151, v202, v203
M v[92:95],v[164:167],v[140:143],v[92:95]
T v[156:157],v221,12416
W 5
M v[0:3],v[168:171],v[144:147],v[0:3]
T v[158:159],v221,18560
v_xor_b32 v218, 64, v216
M v[4:7],v[168:171],v[148:151],v[4:7]
T v[160:161],v222,12416
v_xor_b32 v219, 96, v216
M v[96:99],v[244:247],v[144:147],v[96:99]
T v[162:163],v222,18560
W 5
M v[100:103],v[244:247],v[148:151],v[100:103]
T v[164:165],v223,12416
M v[8:11],v[172:175],v[144:147],v[8:11]
T v[166:167],v223,18560
W 5
M v[12:15],v[172:175],v[148:151],v[12:15]
T v[168:169],v220,12544
ds_read_b128 a[160:163], v241
M v[16:19],v[176:179],v[144:147],v[16:19]
T v[170:171],v220,18688
ds_read_b128 a[168:171], v241 offset:2048
M v[20:23],v[176:179],v[148:151],v[20:23]
T v[172:173],v221,12544
ds_read_b128 a[164:167], v242
M v[24:27],v[180:183],v[144:147],v[24:27]
T v[174:175],v221,18688
ds_read_b128 a[172:175], v242 offset:2048
M v[28:31],v[180:183],v[148:151],v[28:31]
W 8
M v[32:35],v[152:155],v[144:147],v[32:35]
T v[176:177],v222,12544
W 7
M v[36:39],v[152:155],v[148:151],v[36:39]
T v[178:179],v222,18688
W 6
M v[40:43],v[156:159],v[144:147],v[40:43]
T v[180:181],v223,12544
W 5
M v[44:47],v[156:159],v[148:151],v[44:47]
T v[182:183],v223,18688
W 4
M v[48:51],v[160:163],v[144:147],v[48:51]
s_waitcnt vmcnt(8)
s_barrier
T v[152:153],v216,0
M v[52:55],v[160:163],v[148:151],v[52:55]
T v[154:155],v216,6144
buffer_load_dwordx4 v229, s[12:15], s79 offen offset:2048 nt lds
M v[56:59],v[164:167],v[144:147],v[56:59]
T v[156:157],v216,12288
W 5
M v[60:63],v[164:167],v[148:151],v[60:63]
T v[158:159],v216,18432
M v[64:67],v[168:171],v[144:147],v[64:67]
T v[160:161],v217,0
W 5
M v[68:71],v[168:171],v[148:151],v[68:71]
T v[162:163],v217,6144
M v[72:75],v[172:175],v[144:147],v[72:75]
T v[164:165],v217,12288
W 5
M v[76:79],v[172:175],v[148:151],v[76:79]
T v[166:167],v217,18432
M v[80:83],v[176:179],v[144:147],v[80:83]
T v[168:169],v218,0
W 5
M v[84:87],v[176:179],v[148:151],v[84:87]
T v[170:171],v218,6144
M v[88:91],v[180:183],v[144:147],v[88:91]
T v[172:173],v218,12288
W 5
M v[92:95],v[180:183],v[148:151],v[92:95]
T v[174:175],v218,18432
M a[48:51],v[152:155],a[144:147],a[48:51]
T v[176:177],v219,0
W 5
M a[48:51],v[156:159],a[148:151],a[48:51]
T v[178:179],v219,6144
M a[52:55],v[156:159],a[156:159],a[52:55]
T v[180:181],v219,12288
W 5
M a[52:55],v[152:155],a[152:155],a[52:55]
T v[182:183],v219,18432
M a[56:59],v[160:163],a[144:147],a[56:59]
T v[152:153],v216,128
W 5
M a[56:59],v[164:167],a[148:151],a[56:59]
T v[154:155],v216,6272
M a[60:63],v[164:167],a[156:159],a[60:63]
T v[156:157],v216,12416
W 5
M a[60:63],v[160:163],a[152:155],a[60:63]
T v[158:159],v216,18560
s_add_u32 s79, s0, 196608
M a[64:67],v[168:171],a[144:147],a[64:67]
T v[160:161],v217,128
s_add_u32 m0, s40, 49152
M a[64:67],v[172:175],a[148:151],a[64:67]
T v[162:163],v217,6272
buffer_load_dwordx4 v227, s[8:11], s79 offen nt lds
M a[68:71],v[172:175],a[156:159],a[68:71]
T v[164:165],v217,12416
W 5
M a[68:71],v[168:171],a[152:155],a[68:71]
T v[166:167],v217,18560
M a[72:75],v[176:179],a[144:147],a[72:75]
T v[168:169],v218,128
W 5
M a[72:75],v[180:183],a[148:151],a[72:75]
T v[170:171],v218,6272
M a[76:79],v[180:183],a[156:159],a[76:79]
T v[172:173],v218,12416
W 5
M a[76:79],v[176:179],a[152:155],a[76:79]
T v[174:175],v218,18560
M a[80:83],v[152:155],a[144:147],a[80:83]
T v[176:177],v219,128
W 5
M a[80:83],v[156:159],a[148:151],a[80:83]
T v[178:179],v219,6272
v_add_u32 v220, 98304, v225
M a[84:87],v[156:159],a[156:159],a[84:87]
T v[180:181],v219,12416
v_xor_b32 v221, 64, v220
M a[84:87],v[152:155],a[152:155],a[84:87]
T v[182:183],v219,18560
W 5
M a[88:91],v[160:163],a[144:147],a[88:91]
T v[152:153],v216,256
M a[88:91],v[164:167],a[148:151],a[88:91]
T v[154:155],v216,6400
W 5
M a[92:95],v[164:167],a[156:159],a[92:95]
T v[156:157],v216,12544
M a[92:95],v[160:163],a[152:155],a[92:95]
T v[158:159],v216,18688
W 5
M a[96:99],v[168:171],a[144:147],a[96:99]
T v[160:161],v217,256
M a[96:99],v[172:175],a[148:151],a[96:99]
T v[162:163],v217,6400
W 5
M a[100:103],v[172:175],a[156:159],a[100:103]
T v[164:165],v217,12544
M a[100:103],v[168:171],a[152:155],a[100:103]
T v[166:167],v217,18688
W 5
M a[104:107],v[176:179],a[144:147],a[104:107]
T v[168:169],v218,256
M a[104:107],v[180:183],a[148:151],a[104:107]
T v[170:171],v218,6400
buffer_load_dwordx4 v228, s[8:11], s79 offen offset:1024 nt lds
M a[108:111],v[180:183],a[156:159],a[108:111]
T v[172:173],v218,12544
W 5
M a[108:111],v[176:179],a[152:155],a[108:111]
T v[174:175],v218,18688
M a[112:115],v[152:155],a[144:147],a[112:115]
T v[176:177],v219,256
W 5
M a[112:115],v[156:159],a[148:151],a[112:115]
T v[178:179],v219,6400
M a[116:119],v[156:159],a[156:159],a[116:119]
T v[180:181],v219,12544
W 5
M a[116:119],v[152:155],a[152:155],a[116:119]
T v[182:183],v219,18688
M a[120:123],v[160:163],a[144:147],a[120:123]
s_waitcnt vmcnt(8)
s_barrier
ds_read_b128 v[152:155], v220 offset:0
M a[120:123],v[164:167],a[148:151],a[120:123]
ds_read_b128 v[156:159], v221 offset:0
W 5
M a[124:127],v[164:167],a[156:159],a[124:127]
M a[124:127],v[160:163],a[152:155],a[124:127]
ds_read_b128 v[160:163], v220 offset:128
W 4
M a[128:131],v[168:171],a[144:147],a[128:131]
M a[128:131],v[172:175],a[148:151],a[128:131]
ds_read_b128 v[164:167], v221 offset:128
W 3
M a[132:135],v[172:175],a[156:159],a[132:135]
M a[132:135],v[168:171],a[152:155],a[132:135]
ds_read_b128 v[168:171], v220 offset:256
M a[136:139],v[176:179],a[144:147],a[136:139]
ds_read_b128 v[172:175], v221 offset:256
W 3
M a[136:139],v[180:183],a[148:151],a[136:139]
M a[140:143],v[180:183],a[156:159],a[140:143]
M a[140:143],v[176:179],a[152:155],a[140:143]
ds_read_b128 v[176:179], v220 offset:6144
M v[104:107],v[152:155],a[0:3],v[204:207]
ds_read_b128 v[180:183], v221 offset:6144
W 3
M v[104:107],v[156:159],a[8:11],v[104:107]
buffer_load_dwordx4 v229, s[8:11], s79 offen offset:2048 nt lds
s_add_u32 s77, s66, s42
M v[104:107],v[160:163],a[16:19],v[104:107]
s_add_u32 s78, s77, 0
s_add_u32 m0, s40, 73728
M v[108:111],v[160:163],a[20:23],v[208:211]
buffer_load_dwordx4 v230, s[20:23], s78 offen lds
M v[108:111],v[156:159],a[12:15],v[108:111]
W 1
M v[108:111],v[152:155],a[4:7],v[108:111]
ds_read_b128 v[152:155], v220 offset:6272
M v[108:111],v[164:167],a[28:31],v[108:111]
ds_read_b128 v[156:159], v221 offset:6272
buffer_load_dwordx4 v231, s[20:23], s78 offen offset:1024 lds
M v[108:111],v[168:171],a[36:39],v[108:111]
ds_read_b128 v[160:163], v220 offset:6400
M v[108:111],v[172:175],a[44:47],v[108:111]
M v[104:107],v[172:175],a[40:43],v[104:107]
buffer_load_dwordx4 v232, s[20:23], s78 offen offset:2048 lds
M v[104:107],v[168:171],a[32:35],v[104:107]
W 2
M v[104:107],v[164:167],a[24:27],v[104:107]
ds_read_b128 v[164:167], v221 offset:6400
M v[112:115],v[176:179],a[0:3],v[204:207]
ds_read_b128 v[168:171], v220 offset:12288
W 2
M v[112:115],v[180:183],a[8:11],v[112:115]
ds_read_b128 v[172:175], v221 offset:12288
M v[112:115],v[152:155],a[16:19],v[112:115]
M v[116:119],v[152:155],a[20:23],v[208:211]
v_max3_f32 v248, v104, v105, v106
v_max3_f32 v249, v108, v109, v110
M v[116:119],v[180:183],a[12:15],v[116:119]
v_max_f32 v248, v248, v107
v_max_f32 v249, v249, v111
M v[116:119],v[176:179],a[4:7],v[116:119]
ds_read_b128 v[176:179], v220 offset:12416
W 2
M v[116:119],v[156:159],a[28:31],v[116:119]
v_exp_f32 v200, v104
M v[116:119],v[160:163],a[36:39],v[116:119]
ds_read_b128 v[180:183], v221 offset:12416
v_add_u32 v216, 0, v224
M v[116:119],v[164:167],a[44:47],v[116:119]
ds_read_b128 v[152:155], v220 offset:12544
v_xor_b32 v217, 32, v216
M v[112:115],v[164:167],a[40:43],v[112:115]
v_exp_f32 v201, v105
M v[112:115],v[160:163],a[32:35],v[112:115]
v_exp_f32 v202, v106
M v[112:115],v[156:159],a[24:27],v[112:115]
ds_read_b128 v[156:159], v221 offset:12544
v_cvt_pk_bf16_f32 v136, v200, v201
M v[120:123],v[168:171],a[0:3],v[204:207]
ds_read_b128 v[160:163], v220 offset:18432
W 3
M v[120:123],v[172:175],a[8:11],v[120:123]
v_exp_f32 v203, v107
M v[120:123],v[176:179],a[16:19],v[120:123]
ds_read_b128 v[164:167], v221 offset:18432
v_cvt_pk_bf16_f32 v137, v202, v203
M v[124:127],v[176:179],a[20:23],v[208:211]
v_exp_f32 v200, v108
M v[124:127],v[172:175],a[12:15],v[124:127]
v_exp_f32 v201, v109
M v[124:127],v[168:171],a[4:7],v[124:127]
ds_read_b128 v[168:171], v220 offset:18560
v_cvt_pk_bf16_f32 v140, v200, v201
M v[124:127],v[180:183],a[28:31],v[124:127]
ds_read_b128 v[172:175], v221 offset:18560
W 3
M v[124:127],v[152:155],a[36:39],v[124:127]
v_exp_f32 v202, v110
M v[124:127],v[156:159],a[44:47],v[124:127]
ds_read_b128 v[176:179], v220 offset:18688
v_max3_f32 v248, v248, v112, v113
M v[120:123],v[156:159],a[40:43],v[120:123]
v_exp_f32 v203, v111
M v[120:123],v[152:155],a[32:35],v[120:123]
v_cvt_pk_bf16_f32 v141, v202, v203
v_max3_f32 v249, v249, v116, v117
M v[120:123],v[180:183],a[24:27],v[120:123]
ds_read_b128 v[180:183], v221 offset:18688
v_xor_b32 v218, 64, v216
M v[128:131],v[160:163],a[0:3],v[204:207]
v_xor_b32 v219, 96, v216
W 2
M v[128:131],v[164:167],a[8:11],v[128:131]
s_waitcnt vmcnt(9)
s_barrier
T v[152:153],v216,0
M v[128:131],v[168:171],a[16:19],v[128:131]
T v[154:155],v216,6144
v_max3_f32 v248, v248, v114, v115
M v[132:135],v[168:171],a[20:23],v[208:211]
T v[156:157],v216,12288
v_max3_f32 v249, v249, v118, v119
M v[132:135],v[164:167],a[12:15],v[132:135]
T v[158:159],v216,18432
W 4
M v[132:135],v[160:163],a[4:7],v[132:135]
T v[160:161],v217,0
M v[132:135],v[172:175],a[28:31],v[132:135]
T v[162:163],v217,6144
M v[132:135],v[176:179],a[36:39],v[132:135]
T v[164:165],v217,12288
W 5
M v[132:135],v[180:183],a[44:47],v[132:135]
T v[166:167],v217,18432
M v[128:131],v[180:183],a[40:43],v[128:131]
T v[168:169],v218,0
v_exp_f32 v200, v112
M v[128:131],v[176:179],a[32:35],v[128:131]
T v[170:171],v218,6144
W 5
M v[128:131],v[172:175],a[24:27],v[128:131]
T v[172:173],v218,12288
v_exp_f32 v201, v113
M a[48:51],v[152:155],a[160:163],a[48:51]
T v[174:175],v218,18432
s_add_u32 s79, s0, 196608
s_add_u32 m0, s40, 122880
s_nop 0
buffer_load_dwordx4 v227, s[12:15], s79 offen nt lds
M a[48:51],v[156:159],a[164:167],a[48:51]
T v[176:177],v219,0
v_cvt_pk_bf16_f32 v138, v200, v201
M a[52:55],v[156:159],a[172:175],a[52:55]
T v[178:179],v219,6144
W 5
M a[52:55],v[152:155],a[168:171],a[52:55]
T v[180:181],v219,12288
v_exp_f32 v202, v114
M a[56:59],v[160:163],a[160:163],a[56:59]
T v[182:183],v219,18432
W 5
M a[56:59],v[164:167],a[164:167],a[56:59]
T v[152:153],v216,128
v_exp_f32 v203, v115
M a[60:63],v[164:167],a[172:175],a[60:63]
T v[154:155],v216,6272
v_cvt_pk_bf16_f32 v139, v202, v203
M a[60:63],v[160:163],a[168:171],a[60:63]
T v[156:157],v216,12416
W 5
M a[64:67],v[168:171],a[160:163],a[64:67]
T v[158:159],v216,18560
v_exp_f32 v200, v116
M a[64:67],v[172:175],a[164:167],a[64:67]
T v[160:161],v217,128
W 5
M a[68:71],v[172:175],a[172:175],a[68:71]
T v[162:163],v217,6272
M a[68:71],v[168:171],a[168:171],a[68:71]
T v[164:165],v217,12416
v_exp_f32 v201, v117
M a[72:75],v[176:179],a[160:163],a[72:75]
T v[166:167],v217,18560
v_cvt_pk_bf16_f32 v142, v200, v201
M a[72:75],v[180:183],a[164:167],a[72:75]
T v[168:169],v218,128
W 5
M a[76:79],v[180:183],a[172:175],a[76:79]
T v[170:171],v218,6272
v_exp_f32 v202, v118
M a[76:79],v[176:179],a[168:171],a[76:79]
T v[172:173],v218,12416
v_max3_f32 v248, v248, v120, v121
M a[80:83],v[152:155],a[160:163],a[80:83]
T v[174:175],v218,18560
v_max3_f32 v249, v249, v124, v125
M a[80:83],v[156:159],a[164:167],a[80:83]
T v[176:177],v219,128
v_max3_f32 v248, v248, v122, v123
M a[84:87],v[156:159],a[172:175],a[84:87]
T v[178:179],v219,6272
v_max3_f32 v249, v249, v126, v127
M a[84:87],v[152:155],a[168:171],a[84:87]
T v[180:181],v219,12416
W 5
M a[88:91],v[160:163],a[160:163],a[88:91]
T v[182:183],v219,18560
buffer_load_dwordx4 v228, s[12:15], s79 offen offset:1024 nt lds
v_exp_f32 v203, v119
M a[88:91],v[164:167],a[164:167],a[88:91]
T v[152:153],v216,256
v_cvt_pk_bf16_f32 v143, v202, v203
M a[92:95],v[164:167],a[172:175],a[92:95]
T v[154:155],v216,6400
v_max3_f32 v248, v248, v128, v129
M a[92:95],v[160:163],a[168:171],a[92:95]
T v[156:157],v216,12544
v_max3_f32 v249, v249, v132, v133
M a[96:99],v[168:171],a[160:163],a[96:99]
T v[158:159],v216,18688
W 5
M a[96:99],v[172:175],a[164:167],a[96:99]
T v[160:161],v217,256
v_max3_f32 v248, v248, v130, v131
M a[100:103],v[172:175],a[172:175],a[100:103]
T v[162:163],v217,6400
v_max3_f32 v249, v249, v134, v135
M a[100:103],v[168:171],a[168:171],a[100:103]
T v[164:165],v217,12544
v_max_f32 v250, v248, v249
M a[104:107],v[176:179],a[160:163],a[104:107]
T v[166:167],v217,18688
W 5
M a[104:107],v[180:183],a[164:167],a[104:107]
T v[168:169],v218,256
v_cmp_lt_f32 vcc, s60, v250
M a[108:111],v[180:183],a[172:175],a[108:111]
T v[170:171],v218,6400
s_cbranch_vccnz LST0_7_%=
LRT0_7_%=:
M a[108:111],v[176:179],a[168:171],a[108:111]
T v[172:173],v218,12544
v_exp_f32 v200, v120
M a[112:115],v[152:155],a[160:163],a[112:115]
T v[174:175],v218,18688
W 5
M a[112:115],v[156:159],a[164:167],a[112:115]
T v[176:177],v219,256
v_exp_f32 v201, v121
M a[116:119],v[156:159],a[172:175],a[116:119]
T v[178:179],v219,6400
v_cvt_pk_bf16_f32 v144, v200, v201
M a[116:119],v[152:155],a[168:171],a[116:119]
T v[180:181],v219,12544
v_add_u32 v220, 24576, v224
M a[120:123],v[160:163],a[160:163],a[120:123]
T v[182:183],v219,18688
v_exp_f32 v202, v122
W 5
M a[120:123],v[164:167],a[164:167],a[120:123]
v_xor_b32 v221, 32, v220
v_xor_b32 v222, 64, v220
M a[124:127],v[164:167],a[172:175],a[124:127]
v_exp_f32 v203, v123
M a[124:127],v[160:163],a[168:171],a[124:127]
v_xor_b32 v223, 96, v220
v_cvt_pk_bf16_f32 v145, v202, v203
M a[128:131],v[168:171],a[160:163],a[128:131]
s_waitcnt vmcnt(8)
s_barrier
T v[152:153],v220,0
T v[154:155],v220,6144
W 3
M a[128:131],v[172:175],a[164:167],a[128:131]
T v[156:157],v221,0
buffer_load_dwordx4 v229, s[12:15], s79 offen offset:2048 nt lds
M a[132:135],v[172:175],a[172:175],a[132:135]
T v[158:159],v221,6144
v_exp_f32 v200, v124
M a[132:135],v[168:171],a[168:171],a[132:135]
T v[160:161],v222,0
M a[136:139],v[176:179],a[160:163],a[136:139]
T v[162:163],v222,6144
v_exp_f32 v201, v125
W 6
M a[136:139],v[180:183],a[164:167],a[136:139]
T v[164:165],v223,0
v_cvt_pk_bf16_f32 v148, v200, v201
M a[140:143],v[180:183],a[172:175],a[140:143]
T v[166:167],v223,6144
W 5
M a[140:143],v[176:179],a[168:171],a[140:143]
T v[168:169],v220,128
v_exp_f32 v202, v126
M v[0:3],v[152:155],v[136:139],v[0:3]
T v[170:171],v220,6272
W 5
M v[4:7],v[152:155],v[140:143],v[4:7]
T v[172:173],v221,128
v_exp_f32 v203, v127
M v[96:99],v[244:247],v[136:139],v[96:99]
T v[174:175],v221,6272
v_cvt_pk_bf16_f32 v149, v202, v203
M v[100:103],v[244:247],v[140:143],v[100:103]
T v[176:177],v222,128
W 5
M v[8:11],v[156:159],v[136:139],v[8:11]
T v[178:179],v222,6272
v_exp_f32 v200, v128
M v[12:15],v[156:159],v[140:143],v[12:15]
T v[180:181],v223,128
W 5
M v[16:19],v[160:163],v[136:139],v[16:19]
T v[182:183],v223,6272
v_exp_f32 v201, v129
M v[20:23],v[160:163],v[140:143],v[20:23]
T v[152:153],v220,256
v_cvt_pk_bf16_f32 v146, v200, v201
M v[24:27],v[164:167],v[136:139],v[24:27]
T v[154:155],v220,6400
W 5
M v[28:31],v[164:167],v[140:143],v[28:31]
T v[156:157],v221,256
M v[32:35],v[168:171],v[136:139],v[32:35]
T v[158:159],v221,6400
v_exp_f32 v202, v130
M v[36:39],v[168:171],v[140:143],v[36:39]
T v[160:161],v222,256
s_add_u32 s79, s1, 196608
M v[40:43],v[172:175],v[136:139],v[40:43]
T v[162:163],v222,6400
s_add_u32 m0, s40, 98304
v_exp_f32 v203, v131
M v[44:47],v[172:175],v[140:143],v[44:47]
T v[164:165],v223,256
W 5
M v[48:51],v[176:179],v[136:139],v[48:51]
T v[166:167],v223,6400
buffer_load_dwordx4 v227, s[8:11], s79 offen nt lds
v_exp_f32 v200, v132
M v[52:55],v[176:179],v[140:143],v[52:55]
T v[168:169],v220,12288
v_cvt_pk_bf16_f32 v147, v202, v203
M v[56:59],v[180:183],v[136:139],v[56:59]
T v[170:171],v220,18432
W 5
M v[60:63],v[180:183],v[140:143],v[60:63]
T v[172:173],v221,12288
v_exp_f32 v201, v133
v_exp_f32 v202, v134
M v[64:67],v[152:155],v[136:139],v[64:67]
T v[174:175],v221,18432
v_cvt_pk_bf16_f32 v150, v200, v201
v_exp_f32 v203, v135
s_nop 0
v_cvt_pk_bf16_f32 v151, v202, v203
M v[68:71],v[152:155],v[140:143],v[68:71]
T v[176:177],v222,12288
v_accvgpr_read_b32 v184, a48
v_accvgpr_read_b32 v185, a49
v_accvgpr_read_b32 v186, a50
v_accvgpr_read_b32 v187, a51
M v[72:75],v[156:159],v[136:139],v[72:75]
T v[178:179],v222,18432
v_accvgpr_read_b32 v188, a56
v_accvgpr_read_b32 v189, a57
v_accvgpr_read_b32 v190, a58
v_accvgpr_read_b32 v191, a59
M v[76:79],v[156:159],v[140:143],v[76:79]
T v[180:181],v223,12288
v_pk_mul_f32 v[184:185], v[184:185], s[64:65]
v_pk_mul_f32 v[188:189], v[188:189], s[64:65]
v_pk_mul_f32 v[186:187], v[186:187], s[64:65]
v_pk_mul_f32 v[190:191], v[190:191], s[64:65]
W 5
M v[80:83],v[160:163],v[136:139],v[80:83]
T v[182:183],v223,18432
v_cvt_pk_bf16_f32 v204, v184, v185
v_cvt_pk_bf16_f32 v206, v188, v189
v_cvt_pk_bf16_f32 v205, v186, v187
v_cvt_pk_bf16_f32 v207, v190, v191
M v[84:87],v[160:163],v[140:143],v[84:87]
T v[152:153],v220,12416
v_accvgpr_read_b32 v192, a52
v_accvgpr_read_b32 v193, a53
v_accvgpr_read_b32 v194, a54
v_accvgpr_read_b32 v195, a55
M v[88:91],v[164:167],v[136:139],v[88:91]
T v[154:155],v220,18560
v_permlane32_swap_b32 v204, v206
v_permlane32_swap_b32 v205, v207
s_nop 0
v_permlane16_swap_b32 v204, v206
v_permlane16_swap_b32 v205, v207
M v[92:95],v[164:167],v[140:143],v[92:95]
T v[156:157],v221,12416
v_accvgpr_write_b32 a0, v204
v_accvgpr_write_b32 a1, v205
v_accvgpr_write_b32 a2, v206
v_accvgpr_write_b32 a3, v207
M v[0:3],v[168:171],v[144:147],v[0:3]
T v[158:159],v221,18560
v_accvgpr_read_b32 v196, a60
v_accvgpr_read_b32 v197, a61
v_accvgpr_read_b32 v198, a62
v_accvgpr_read_b32 v199, a63
M v[4:7],v[168:171],v[148:151],v[4:7]
T v[160:161],v222,12416
v_pk_mul_f32 v[192:193], v[192:193], s[64:65]
v_pk_mul_f32 v[196:197], v[196:197], s[64:65]
v_pk_mul_f32 v[194:195], v[194:195], s[64:65]
v_pk_mul_f32 v[198:199], v[198:199], s[64:65]
M v[96:99],v[244:247],v[144:147],v[96:99]
T v[162:163],v222,18560
v_cvt_pk_bf16_f32 v208, v192, v193
v_cvt_pk_bf16_f32 v210, v196, v197
v_cvt_pk_bf16_f32 v209, v194, v195
v_cvt_pk_bf16_f32 v211, v198, v199
M v[100:103],v[244:247],v[148:151],v[100:103]
T v[164:165],v223,12416
v_permlane32_swap_b32 v208, v210
v_permlane32_swap_b32 v209, v211
s_nop 0
v_permlane16_swap_b32 v208, v210
v_permlane16_swap_b32 v209, v211
W 5
M v[8:11],v[172:175],v[144:147],v[8:11]
T v[166:167],v223,18560
v_accvgpr_write_b32 a4, v208
v_accvgpr_write_b32 a5, v209
v_accvgpr_write_b32 a6, v210
v_accvgpr_write_b32 a7, v211
M v[12:15],v[172:175],v[148:151],v[12:15]
T v[168:169],v220,12544
v_accvgpr_read_b32 v184, a64
v_accvgpr_read_b32 v185, a65
v_accvgpr_read_b32 v186, a66
v_accvgpr_read_b32 v187, a67
M v[16:19],v[176:179],v[144:147],v[16:19]
T v[170:171],v220,18688
v_accvgpr_read_b32 v188, a72
v_accvgpr_read_b32 v189, a73
v_accvgpr_read_b32 v190, a74
v_accvgpr_read_b32 v191, a75
M v[20:23],v[176:179],v[148:151],v[20:23]
T v[172:173],v221,12544
v_pk_mul_f32 v[184:185], v[184:185], s[64:65]
v_pk_mul_f32 v[188:189], v[188:189], s[64:65]
v_pk_mul_f32 v[186:187], v[186:187], s[64:65]
v_pk_mul_f32 v[190:191], v[190:191], s[64:65]
M v[24:27],v[180:183],v[144:147],v[24:27]
T v[174:175],v221,18688
buffer_load_dwordx4 v228, s[8:11], s79 offen offset:1024 nt lds
v_cvt_pk_bf16_f32 v204, v184, v185
v_cvt_pk_bf16_f32 v206, v188, v189
v_cvt_pk_bf16_f32 v205, v186, v187
v_cvt_pk_bf16_f32 v207, v190, v191
M v[28:31],v[180:183],v[148:151],v[28:31]
T v[176:177],v222,12544
v_accvgpr_read_b32 v192, a68
v_accvgpr_read_b32 v193, a69
v_accvgpr_read_b32 v194, a70
v_accvgpr_read_b32 v195, a71
M v[32:35],v[152:155],v[144:147],v[32:35]
T v[178:179],v222,18688
v_permlane32_swap_b32 v204, v206
v_permlane32_swap_b32 v205, v207
s_nop 0
v_permlane16_swap_b32 v204, v206
v_permlane16_swap_b32 v205, v207
M v[36:39],v[152:155],v[148:151],v[36:39]
T v[180:181],v223,12544
v_accvgpr_write_b32 a8, v204
v_accvgpr_write_b32 a9, v205
v_accvgpr_write_b32 a10, v206
v_accvgpr_write_b32 a11, v207
W 5
M v[40:43],v[156:159],v[144:147],v[40:43]
T v[182:183],v223,18688
v_accvgpr_read_b32 v196, a76
v_accvgpr_read_b32 v197, a77
v_accvgpr_read_b32 v198, a78
v_accvgpr_read_b32 v199, a79
M v[44:47],v[156:159],v[148:151],v[44:47]
v_pk_mul_f32 v[192:193], v[192:193], s[64:65]
v_pk_mul_f32 v[196:197], v[196:197], s[64:65]
v_pk_mul_f32 v[194:195], v[194:195], s[64:65]
v_pk_mul_f32 v[198:199], v[198:199], s[64:65]
M v[48:51],v[160:163],v[144:147],v[48:51]
v_cvt_pk_bf16_f32 v208, v192, v193
v_cvt_pk_bf16_f32 v210, v196, v197
v_cvt_pk_bf16_f32 v209, v194, v195
v_cvt_pk_bf16_f32 v211, v198, v199
M v[52:55],v[160:163],v[148:151],v[52:55]
v_permlane32_swap_b32 v208, v210
v_permlane32_swap_b32 v209, v211
s_nop 0
v_permlane16_swap_b32 v208, v210
v_permlane16_swap_b32 v209, v211
M v[56:59],v[164:167],v[144:147],v[56:59]
v_accvgpr_write_b32 a12, v208
v_accvgpr_write_b32 a13, v209
v_accvgpr_write_b32 a14, v210
v_accvgpr_write_b32 a15, v211
M v[60:63],v[164:167],v[148:151],v[60:63]
v_accvgpr_read_b32 v184, a80
v_accvgpr_read_b32 v185, a81
v_accvgpr_read_b32 v186, a82
v_accvgpr_read_b32 v187, a83
M v[64:67],v[168:171],v[144:147],v[64:67]
v_add_u32 v216, 49152, v225
v_xor_b32 v217, 64, v216
s_waitcnt vmcnt(8)
s_barrier
ds_read_b128 v[152:155], v216 offset:0
v_accvgpr_read_b32 v188, a88
v_accvgpr_read_b32 v189, a89
v_accvgpr_read_b32 v190, a90
v_accvgpr_read_b32 v191, a91
M v[68:71],v[168:171],v[148:151],v[68:71]
ds_read_b128 v[156:159], v216 offset:6144
v_pk_mul_f32 v[184:185], v[184:185], s[64:65]
v_pk_mul_f32 v[188:189], v[188:189], s[64:65]
v_pk_mul_f32 v[186:187], v[186:187], s[64:65]
v_pk_mul_f32 v[190:191], v[190:191], s[64:65]
W 2
M v[72:75],v[172:175],v[144:147],v[72:75]
ds_read_b128 v[160:163], v216 offset:12288
v_cvt_pk_bf16_f32 v204, v184, v185
v_cvt_pk_bf16_f32 v206, v188, v189
v_cvt_pk_bf16_f32 v205, v186, v187
v_cvt_pk_bf16_f32 v207, v190, v191
M v[76:79],v[172:175],v[148:151],v[76:79]
ds_read_b128 v[164:167], v216 offset:18432
v_accvgpr_read_b32 v192, a84
v_accvgpr_read_b32 v193, a85
v_accvgpr_read_b32 v194, a86
v_accvgpr_read_b32 v195, a87
M v[80:83],v[176:179],v[144:147],v[80:83]
ds_read_b128 v[168:171], v217 offset:0
v_permlane32_swap_b32 v204, v206
v_permlane32_swap_b32 v205, v207
s_nop 0
v_permlane16_swap_b32 v204, v206
v_permlane16_swap_b32 v205, v207
M v[84:87],v[176:179],v[148:151],v[84:87]
ds_read_b128 v[172:175], v217 offset:6144
v_accvgpr_write_b32 a16, v204
v_accvgpr_write_b32 a17, v205
v_accvgpr_write_b32 a18, v206
v_accvgpr_write_b32 a19, v207
M v[88:91],v[180:183],v[144:147],v[88:91]
v_accvgpr_read_b32 v196, a92
v_accvgpr_read_b32 v197, a93
v_accvgpr_read_b32 v198, a94
v_accvgpr_read_b32 v199, a95
M v[92:95],v[180:183],v[148:151],v[92:95]
ds_read_b128 v[176:179], v217 offset:12288
v_pk_mul_f32 v[192:193], v[192:193], s[64:65]
v_pk_mul_f32 v[196:197], v[196:197], s[64:65]
v_pk_mul_f32 v[194:195], v[194:195], s[64:65]
v_pk_mul_f32 v[198:199], v[198:199], s[64:65]
W 4
M v[104:107],v[152:155],a[0:3],0
v_cvt_pk_bf16_f32 v208, v192, v193
v_cvt_pk_bf16_f32 v210, v196, v197
v_cvt_pk_bf16_f32 v209, v194, v195
v_cvt_pk_bf16_f32 v211, v198, v199
M v[108:111],v[152:155],a[4:7],0
ds_read_b128 v[180:183], v217 offset:18432
v_permlane32_swap_b32 v208, v210
v_permlane32_swap_b32 v209, v211
s_nop 0
v_permlane16_swap_b32 v208, v210
v_permlane16_swap_b32 v209, v211
M v[112:115],v[156:159],a[0:3],0
buffer_load_dwordx4 v229, s[8:11], s79 offen offset:2048 nt lds
v_accvgpr_write_b32 a20, v208
v_accvgpr_write_b32 a21, v209
v_accvgpr_write_b32 a22, v210
v_accvgpr_write_b32 a23, v211
M v[116:119],v[156:159],a[4:7],0
ds_read_b128 v[152:155], v216 offset:128
v_accvgpr_read_b32 v184, a96
v_accvgpr_read_b32 v185, a97
v_accvgpr_read_b32 v186, a98
v_accvgpr_read_b32 v187, a99
v_accvgpr_read_b32 v188, a104
v_accvgpr_read_b32 v189, a105
v_accvgpr_read_b32 v190, a106
M v[120:123],v[160:163],a[0:3],0
v_accvgpr_read_b32 v191, a107
v_pk_mul_f32 v[184:185], v[184:185], s[64:65]
v_pk_mul_f32 v[188:189], v[188:189], s[64:65]
v_pk_mul_f32 v[186:187], v[186:187], s[64:65]
v_pk_mul_f32 v[190:191], v[190:191], s[64:65]
v_cvt_pk_bf16_f32 v204, v184, v185
M v[124:127],v[160:163],a[4:7],0
ds_read_b128 v[156:159], v216 offset:6272
v_cvt_pk_bf16_f32 v206, v188, v189
v_cvt_pk_bf16_f32 v205, v186, v187
v_cvt_pk_bf16_f32 v207, v190, v191
v_accvgpr_read_b32 v192, a100
v_accvgpr_read_b32 v193, a101
v_accvgpr_read_b32 v194, a102
v_accvgpr_read_b32 v195, a103
W 3
M v[128:131],v[164:167],a[0:3],0
v_permlane32_swap_b32 v204, v206
v_permlane32_swap_b32 v205, v207
s_nop 0
v_permlane16_swap_b32 v204, v206
v_permlane16_swap_b32 v205, v207
v_accvgpr_write_b32 a24, v204
v_accvgpr_write_b32 a25, v205
v_accvgpr_write_b32 a26, v206
M v[132:135],v[164:167],a[4:7],0
ds_read_b128 v[160:163], v216 offset:12416
v_accvgpr_write_b32 a27, v207
v_accvgpr_read_b32 v196, a108
v_accvgpr_read_b32 v197, a109
v_accvgpr_read_b32 v198, a110
v_accvgpr_read_b32 v199, a111
v_pk_mul_f32 v[192:193], v[192:193], s[64:65]
M v[104:107],v[168:171],a[8:11],v[104:107]
v_pk_mul_f32 v[196:197], v[196:197], s[64:65]
v_pk_mul_f32 v[194:195], v[194:195], s[64:65]
v_pk_mul_f32 v[198:199], v[198:199], s[64:65]
v_cvt_pk_bf16_f32 v208, v192, v193
v_cvt_pk_bf16_f32 v210, v196, v197
v_cvt_pk_bf16_f32 v209, v194, v195
v_cvt_pk_bf16_f32 v211, v198, v199
M v[108:111],v[168:171],a[12:15],v[108:111]
ds_read_b128 v[164:167], v216 offset:18560
v_permlane32_swap_b32 v208, v210
v_permlane32_swap_b32 v209, v211
s_nop 0
v_permlane16_swap_b32 v208, v210
v_permlane16_swap_b32 v209, v211
v_accvgpr_write_b32 a28, v208
v_accvgpr_write_b32 a29, v209
M v[112:115],v[172:175],a[8:11],v[112:115]
v_accvgpr_write_b32 a30, v210
v_accvgpr_write_b32 a31, v211
v_accvgpr_read_b32 v184, a112
v_accvgpr_read_b32 v185, a113
v_accvgpr_read_b32 v186, a114
v_accvgpr_read_b32 v187, a115
v_accvgpr_read_b32 v188, a120
M v[116:119],v[172:175],a[12:15],v[116:119]
ds_read_b128 v[168:171], v217 offset:128
v_accvgpr_read_b32 v189, a121
v_accvgpr_read_b32 v190, a122
v_accvgpr_read_b32 v191, a123
v_pk_mul_f32 v[184:185], v[184:185], s[64:65]
v_pk_mul_f32 v[188:189], v[188:189], s[64:65]
v_pk_mul_f32 v[186:187], v[186:187], s[64:65]
v_pk_mul_f32 v[190:191], v[190:191], s[64:65]
M v[120:123],v[176:179],a[8:11],v[120:123]
v_cvt_pk_bf16_f32 v204, v184, v185
v_cvt_pk_bf16_f32 v206, v188, v189
v_cvt_pk_bf16_f32 v205, v186, v187
v_cvt_pk_bf16_f32 v207, v190, v191
v_accvgpr_read_b32 v192, a116
v_accvgpr_read_b32 v193, a117
M v[124:127],v[176:179],a[12:15],v[124:127]
ds_read_b128 v[172:175], v217 offset:6272
v_accvgpr_read_b32 v194, a118
v_accvgpr_read_b32 v195, a119
v_permlane32_swap_b32 v204, v206
v_permlane32_swap_b32 v205, v207
s_nop 0
v_permlane16_swap_b32 v204, v206
v_permlane16_swap_b32 v205, v207
v_accvgpr_write_b32 a32, v204
W 3
M v[128:131],v[180:183],a[8:11],v[128:131]
v_accvgpr_write_b32 a33, v205
v_accvgpr_write_b32 a34, v206
v_accvgpr_write_b32 a35, v207
v_accvgpr_read_b32 v196, a124
v_accvgpr_read_b32 v197, a125
v_accvgpr_read_b32 v198, a126
v_accvgpr_read_b32 v199, a127
M v[132:135],v[180:183],a[12:15],v[132:135]
ds_read_b128 v[176:179], v217 offset:12416
v_pk_mul_f32 v[192:193], v[192:193], s[64:65]
v_pk_mul_f32 v[196:197], v[196:197], s[64:65]
v_pk_mul_f32 v[194:195], v[194:195], s[64:65]
v_pk_mul_f32 v[198:199], v[198:199], s[64:65]
v_cvt_pk_bf16_f32 v208, v192, v193
v_cvt_pk_bf16_f32 v210, v196, v197
s_add_u32 s77, s67, s42
M v[104:107],v[152:155],a[16:19],v[104:107]
v_cvt_pk_bf16_f32 v209, v194, v195
v_cvt_pk_bf16_f32 v211, v198, v199
v_permlane32_swap_b32 v208, v210
s_nop 0
v_permlane32_swap_b32 v209, v211
v_permlane16_swap_b32 v208, v210
s_nop 0
v_permlane16_swap_b32 v209, v211
v_accvgpr_write_b32 a36, v208
M v[108:111],v[152:155],a[20:23],v[108:111]
ds_read_b128 v[180:183], v217 offset:18560
v_accvgpr_write_b32 a37, v209
v_accvgpr_write_b32 a38, v210
v_accvgpr_write_b32 a39, v211
v_accvgpr_read_b32 v184, a128
v_accvgpr_read_b32 v185, a129
v_accvgpr_read_b32 v186, a130
v_accvgpr_read_b32 v187, a131
M v[112:115],v[156:159],a[16:19],v[112:115]
v_accvgpr_read_b32 v188, a136
v_accvgpr_read_b32 v189, a137
v_accvgpr_read_b32 v190, a138
v_accvgpr_read_b32 v191, a139
v_pk_mul_f32 v[184:185], v[184:185], s[64:65]
v_pk_mul_f32 v[188:189], v[188:189], s[64:65]
M v[116:119],v[156:159],a[20:23],v[116:119]
ds_read_b128 v[152:155], v216 offset:256
v_pk_mul_f32 v[186:187], v[186:187], s[64:65]
v_pk_mul_f32 v[190:191], v[190:191], s[64:65]
v_cvt_pk_bf16_f32 v204, v184, v185
v_cvt_pk_bf16_f32 v206, v188, v189
v_cvt_pk_bf16_f32 v205, v186, v187
v_cvt_pk_bf16_f32 v207, v190, v191
v_accvgpr_read_b32 v192, a132
M v[120:123],v[160:163],a[16:19],v[120:123]
v_accvgpr_read_b32 v193, a133
v_accvgpr_read_b32 v194, a134
v_accvgpr_read_b32 v195, a135
v_permlane32_swap_b32 v204, v206
v_permlane32_swap_b32 v205, v207
s_nop 0
v_permlane16_swap_b32 v204, v206
M v[124:127],v[160:163],a[20:23],v[124:127]
ds_read_b128 v[156:159], v216 offset:6400
v_permlane16_swap_b32 v205, v207
v_accvgpr_write_b32 a40, v204
v_accvgpr_write_b32 a41, v205
v_accvgpr_write_b32 a42, v206
v_accvgpr_write_b32 a43, v207
v_accvgpr_read_b32 v196, a140
v_accvgpr_read_b32 v197, a141
W 3
M v[128:131],v[164:167],a[16:19],v[128:131]
v_accvgpr_read_b32 v198, a142
v_accvgpr_read_b32 v199, a143
v_pk_mul_f32 v[192:193], v[192:193], s[64:65]
v_pk_mul_f32 v[196:197], v[196:197], s[64:65]
v_pk_mul_f32 v[194:195], v[194:195], s[64:65]
v_pk_mul_f32 v[198:199], v[198:199], s[64:65]
v_cvt_pk_bf16_f32 v208, v192, v193
s_add_u32 s78, s77, 0
M v[132:135],v[164:167],a[20:23],v[132:135]
ds_read_b128 v[160:163], v216 offset:12544
v_cvt_pk_bf16_f32 v210, v196, v197
v_cvt_pk_bf16_f32 v209, v194, v195
v_cvt_pk_bf16_f32 v211, v198, v199
v_permlane32_swap_b32 v208, v210
s_nop 0
v_permlane32_swap_b32 v209, v211
v_permlane16_swap_b32 v208, v210
M v[104:107],v[168:171],a[24:27],v[104:107]
v_permlane16_swap_b32 v209, v211
v_accvgpr_write_b32 a44, v208
v_accvgpr_write_b32 a45, v209
v_accvgpr_write_b32 a46, v210
v_accvgpr_write_b32 a47, v211
v_rcp_f32 v250, v96
M v[108:111],v[168:171],a[28:31],v[108:111]
v_rcp_f32 v252, v100
v_pk_mul_f32 v[0:1], v[0:1], v[250:251] op_sel_hi:[1,0]
v_pk_mul_f32 v[2:3], v[2:3], v[250:251] op_sel_hi:[1,0]
v_pk_mul_f32 v[8:9], v[8:9], v[250:251] op_sel_hi:[1,0]
v_pk_mul_f32 v[10:11], v[10:11], v[250:251] op_sel_hi:[1,0]
v_cvt_pk_bf16_f32 v136, v0, v1
M v[112:115],v[172:175],a[24:27],v[112:115]
ds_read_b128 v[164:167], v216 offset:18688
v_cvt_pk_bf16_f32 v137, v2, v3
v_cvt_pk_bf16_f32 v138, v8, v9
v_cvt_pk_bf16_f32 v139, v10, v11
v_accvgpr_write_b32 a48, v136
v_accvgpr_write_b32 a49, v137
v_accvgpr_write_b32 a50, v138
M v[116:119],v[172:175],a[28:31],v[116:119]
ds_read_b128 v[168:171], v217 offset:256
v_accvgpr_write_b32 a51, v139
v_pk_mul_f32 v[4:5], v[4:5], v[252:253] op_sel_hi:[1,0]
v_pk_mul_f32 v[6:7], v[6:7], v[252:253] op_sel_hi:[1,0]
v_pk_mul_f32 v[12:13], v[12:13], v[252:253] op_sel_hi:[1,0]
v_pk_mul_f32 v[14:15], v[14:15], v[252:253] op_sel_hi:[1,0]
v_cvt_pk_bf16_f32 v140, v4, v5
v_cvt_pk_bf16_f32 v141, v6, v7
M v[120:123],v[176:179],a[24:27],v[120:123]
v_cvt_pk_bf16_f32 v142, v12, v13
v_cvt_pk_bf16_f32 v143, v14, v15
v_accvgpr_write_b32 a52, v140
v_accvgpr_write_b32 a53, v141
v_accvgpr_write_b32 a54, v142
v_accvgpr_write_b32 a55, v143
v_pk_mul_f32 v[16:17], v[16:17], v[250:251] op_sel_hi:[1,0]
M v[124:127],v[176:179],a[28:31],v[124:127]
ds_read_b128 v[172:175], v217 offset:6400
v_pk_mul_f32 v[18:19], v[18:19], v[250:251] op_sel_hi:[1,0]
v_pk_mul_f32 v[24:25], v[24:25], v[250:251] op_sel_hi:[1,0]
v_pk_mul_f32 v[26:27], v[26:27], v[250:251] op_sel_hi:[1,0]
v_cvt_pk_bf16_f32 v144, v16, v17
v_cvt_pk_bf16_f32 v145, v18, v19
v_cvt_pk_bf16_f32 v146, v24, v25
s_add_u32 m0, s40, 0
W 3
M v[128:131],v[180:183],a[24:27],v[128:131]
buffer_load_dwordx4 v230, s[20:23], s78 offen lds
v_cvt_pk_bf16_f32 v147, v26, v27
v_accvgpr_write_b32 a56, v144
v_accvgpr_write_b32 a57, v145
v_accvgpr_write_b32 a58, v146
v_accvgpr_write_b32 a59, v147
v_pk_mul_f32 v[20:21], v[20:21], v[252:253] op_sel_hi:[1,0]
v_pk_mul_f32 v[22:23], v[22:23], v[252:253] op_sel_hi:[1,0]
M v[132:135],v[180:183],a[28:31],v[132:135]
ds_read_b128 v[176:179], v217 offset:12544
v_pk_mul_f32 v[28:29], v[28:29], v[252:253] op_sel_hi:[1,0]
v_pk_mul_f32 v[30:31], v[30:31], v[252:253] op_sel_hi:[1,0]
v_cvt_pk_bf16_f32 v148, v20, v21
v_cvt_pk_bf16_f32 v149, v22, v23
v_cvt_pk_bf16_f32 v150, v28, v29
v_cvt_pk_bf16_f32 v151, v30, v31
M v[104:107],v[152:155],a[32:35],v[104:107]
v_accvgpr_write_b32 a60, v148
v_accvgpr_write_b32 a61, v149
v_accvgpr_write_b32 a62, v150
v_accvgpr_write_b32 a63, v151
v_pk_mul_f32 v[32:33], v[32:33], v[250:251] op_sel_hi:[1,0]
v_pk_mul_f32 v[34:35], v[34:35], v[250:251] op_sel_hi:[1,0]
v_pk_mul_f32 v[40:41], v[40:41], v[250:251] op_sel_hi:[1,0]
M v[108:111],v[152:155],a[36:39],v[108:111]
ds_read_b128 v[180:183], v217 offset:18688
buffer_load_dwordx4 v231, s[20:23], s78 offen offset:1024 lds
v_pk_mul_f32 v[42:43], v[42:43], v[250:251] op_sel_hi:[1,0]
v_cvt_pk_bf16_f32 v136, v32, v33
v_cvt_pk_bf16_f32 v137, v34, v35
v_cvt_pk_bf16_f32 v138, v40, v41
v_cvt_pk_bf16_f32 v139, v42, v43
v_accvgpr_write_b32 a64, v136
v_accvgpr_write_b32 a65, v137
M v[112:115],v[156:159],a[32:35],v[112:115]
v_accvgpr_write_b32 a66, v138
v_accvgpr_write_b32 a67, v139
v_pk_mul_f32 v[36:37], v[36:37], v[252:253] op_sel_hi:[1,0]
v_pk_mul_f32 v[38:39], v[38:39], v[252:253] op_sel_hi:[1,0]
v_pk_mul_f32 v[44:45], v[44:45], v[252:253] op_sel_hi:[1,0]
v_pk_mul_f32 v[46:47], v[46:47], v[252:253] op_sel_hi:[1,0]
M v[116:119],v[156:159],a[36:39],v[116:119]
v_cvt_pk_bf16_f32 v140, v36, v37
v_cvt_pk_bf16_f32 v141, v38, v39
v_cvt_pk_bf16_f32 v142, v44, v45
v_cvt_pk_bf16_f32 v143, v46, v47
v_accvgpr_write_b32 a68, v140
v_accvgpr_write_b32 a69, v141
v_accvgpr_write_b32 a70, v142
M v[120:123],v[160:163],a[32:35],v[120:123]
v_accvgpr_write_b32 a71, v143
v_pk_mul_f32 v[48:49], v[48:49], v[250:251] op_sel_hi:[1,0]
v_pk_mul_f32 v[50:51], v[50:51], v[250:251] op_sel_hi:[1,0]
v_pk_mul_f32 v[56:57], v[56:57], v[250:251] op_sel_hi:[1,0]
v_pk_mul_f32 v[58:59], v[58:59], v[250:251] op_sel_hi:[1,0]
v_cvt_pk_bf16_f32 v144, v48, v49
v_cvt_pk_bf16_f32 v145, v50, v51
M v[124:127],v[160:163],a[36:39],v[124:127]
v_cvt_pk_bf16_f32 v146, v56, v57
v_cvt_pk_bf16_f32 v147, v58, v59
v_accvgpr_write_b32 a72, v144
v_accvgpr_write_b32 a73, v145
v_accvgpr_write_b32 a74, v146
v_accvgpr_write_b32 a75, v147
W 1
M v[128:131],v[164:167],a[32:35],v[128:131]
v_pk_mul_f32 v[52:53], v[52:53], v[252:253] op_sel_hi:[1,0]
v_pk_mul_f32 v[54:55], v[54:55], v[252:253] op_sel_hi:[1,0]
v_pk_mul_f32 v[60:61], v[60:61], v[252:253] op_sel_hi:[1,0]
v_pk_mul_f32 v[62:63], v[62:63], v[252:253] op_sel_hi:[1,0]
v_cvt_pk_bf16_f32 v148, v52, v53
v_cvt_pk_bf16_f32 v149, v54, v55
v_cvt_pk_bf16_f32 v150, v60, v61
M v[132:135],v[164:167],a[36:39],v[132:135]
v_cvt_pk_bf16_f32 v151, v62, v63
v_accvgpr_write_b32 a76, v148
v_accvgpr_write_b32 a77, v149
v_accvgpr_write_b32 a78, v150
v_accvgpr_write_b32 a79, v151
v_pk_mul_f32 v[64:65], v[64:65], v[250:251] op_sel_hi:[1,0]
v_pk_mul_f32 v[66:67], v[66:67], v[250:251] op_sel_hi:[1,0]
M v[104:107],v[168:171],a[40:43],v[104:107]
v_add_u32 v220, 73728, v226
v_xor_b32 v221, 32, v220
v_xor_b32 v222, 64, v220
v_xor_b32 v223, 96, v220
s_waitcnt vmcnt(8)
s_barrier
T v[152:153],v220,0
T v[154:155],v220,2048
v_pk_mul_f32 v[72:73], v[72:73], v[250:251] op_sel_hi:[1,0]
v_pk_mul_f32 v[74:75], v[74:75], v[250:251] op_sel_hi:[1,0]
v_cvt_pk_bf16_f32 v136, v64, v65
v_cvt_pk_bf16_f32 v137, v66, v67
v_cvt_pk_bf16_f32 v138, v72, v73
v_cvt_pk_bf16_f32 v139, v74, v75
M v[108:111],v[168:171],a[44:47],v[108:111]
T v[156:157],v220,4096
T v[158:159],v220,6144
v_accvgpr_write_b32 a80, v136
v_accvgpr_write_b32 a81, v137
v_accvgpr_write_b32 a82, v138
v_accvgpr_write_b32 a83, v139
v_pk_mul_f32 v[68:69], v[68:69], v[252:253] op_sel_hi:[1,0]
v_pk_mul_f32 v[70:71], v[70:71], v[252:253] op_sel_hi:[1,0]
v_pk_mul_f32 v[76:77], v[76:77], v[252:253] op_sel_hi:[1,0]
M v[112:115],v[172:175],a[40:43],v[112:115]
T v[160:161],v220,8192
T v[162:163],v220,10240
v_pk_mul_f32 v[78:79], v[78:79], v[252:253] op_sel_hi:[1,0]
v_cvt_pk_bf16_f32 v140, v68, v69
v_cvt_pk_bf16_f32 v141, v70, v71
v_cvt_pk_bf16_f32 v142, v76, v77
v_cvt_pk_bf16_f32 v143, v78, v79
v_accvgpr_write_b32 a84, v140
M v[116:119],v[172:175],a[44:47],v[116:119]
T v[164:165],v220,12288
buffer_load_dwordx4 v232, s[20:23], s78 offen offset:2048 lds
v_accvgpr_write_b32 a85, v141
v_accvgpr_write_b32 a86, v142
v_accvgpr_write_b32 a87, v143
v_pk_mul_f32 v[80:81], v[80:81], v[250:251] op_sel_hi:[1,0]
v_pk_mul_f32 v[82:83], v[82:83], v[250:251] op_sel_hi:[1,0]
v_pk_mul_f32 v[88:89], v[88:89], v[250:251] op_sel_hi:[1,0]
v_pk_mul_f32 v[90:91], v[90:91], v[250:251] op_sel_hi:[1,0]
M v[120:123],v[176:179],a[40:43],v[120:123]
T v[166:167],v220,14336
v_cvt_pk_bf16_f32 v144, v80, v81
v_cvt_pk_bf16_f32 v145, v82, v83
v_cvt_pk_bf16_f32 v146, v88, v89
v_cvt_pk_bf16_f32 v147, v90, v91
v_accvgpr_write_b32 a88, v144
v_accvgpr_write_b32 a89, v145
v_accvgpr_write_b32 a90, v146
M v[124:127],v[176:179],a[44:47],v[124:127]
T v[168:169],v220,16384
v_accvgpr_write_b32 a91, v147
v_pk_mul_f32 v[84:85], v[84:85], v[252:253] op_sel_hi:[1,0]
v_pk_mul_f32 v[86:87], v[86:87], v[252:253] op_sel_hi:[1,0]
v_pk_mul_f32 v[92:93], v[92:93], v[252:253] op_sel_hi:[1,0]
v_pk_mul_f32 v[94:95], v[94:95], v[252:253] op_sel_hi:[1,0]
v_cvt_pk_bf16_f32 v148, v84, v85
W 7
M v[128:131],v[180:183],a[40:43],v[128:131]
T v[170:171],v220,18432
v_cvt_pk_bf16_f32 v149, v86, v87
v_cvt_pk_bf16_f32 v150, v92, v93
v_cvt_pk_bf16_f32 v151, v94, v95
v_accvgpr_write_b32 a92, v148
v_accvgpr_write_b32 a93, v149
v_accvgpr_write_b32 a94, v150
v_accvgpr_write_b32 a95, v151
M v[132:135],v[180:183],a[44:47],v[132:135]
T v[172:173],v220,20480
v_max3_f32 v248, v104, v105, v106
M v[184:187],v[152:155],a[48:51],0
T v[174:175],v220,22528
s_add_u32 s79, s1, 196608
s_add_u32 m0, s40, 24576
s_nop 0
buffer_load_dwordx4 v227, s[12:15], s79 offen nt lds
v_max3_f32 v249, v108, v109, v110
W 5
M v[184:187],v[156:159],a[56:59],v[184:187]
T v[176:177],v221,0
v_max_f32 v248, v248, v107
v_max_f32 v249, v249, v111
M v[184:187],v[160:163],a[64:67],v[184:187]
T v[178:179],v221,2048
v_max3_f32 v248, v248, v112, v113
v_max3_f32 v249, v249, v116, v117
M v[188:191],v[160:163],a[68:71],0
T v[180:181],v221,4096
v_max3_f32 v248, v248, v114, v115
v_max3_f32 v249, v249, v118, v119
M v[188:191],v[156:159],a[60:63],v[188:191]
T v[182:183],v221,6144
v_max3_f32 v248, v248, v120, v121
v_max3_f32 v249, v249, v124, v125
v_max3_f32 v248, v248, v122, v123
M v[188:191],v[152:155],a[52:55],v[188:191]
T v[152:153],v221,8192
v_max3_f32 v249, v249, v126, v127
v_max3_f32 v248, v248, v128, v129
W 5
M v[188:191],v[164:167],a[76:79],v[188:191]
T v[154:155],v221,10240
v_max3_f32 v249, v249, v132, v133
v_max3_f32 v248, v248, v130, v131
v_max3_f32 v249, v249, v134, v135
M v[188:191],v[168:171],a[84:87],v[188:191]
T v[156:157],v221,12288
v_mov_b32 v250, v248
v_mov_b32 v251, v249
M v[188:191],v[172:175],a[92:95],v[188:191]
T v[158:159],v221,14336
v_permlane32_swap_b32 v248, v250
v_permlane32_swap_b32 v249, v251
M v[184:187],v[172:175],a[88:91],v[184:187]
T v[160:161],v221,16384
v_max_f32 v248, v248, v250
v_max_f32 v249, v249, v251
M v[184:187],v[168:171],a[80:83],v[184:187]
T v[162:163],v221,18432
v_mov_b32 v250, v248
v_mov_b32 v251, v249
M v[184:187],v[164:167],a[72:75],v[184:187]
T v[164:165],v221,20480
v_permlane16_swap_b32 v248, v250
v_permlane16_swap_b32 v249, v251
W 5
M v[192:195],v[176:179],a[48:51],0
T v[166:167],v221,22528
v_max_f32 v248, v248, v250
v_max_f32 v249, v249, v251
M v[192:195],v[180:183],a[56:59],v[192:195]
T v[168:169],v222,0
v_sub_f32 v104, v104, v248
v_sub_f32 v105, v105, v248
M v[192:195],v[152:155],a[64:67],v[192:195]
T v[170:171],v222,2048
v_sub_f32 v106, v106, v248
v_sub_f32 v107, v107, v248
M v[196:199],v[152:155],a[68:71],0
T v[172:173],v222,4096
v_sub_f32 v108, v108, v249
v_sub_f32 v109, v109, v249
M v[196:199],v[180:183],a[60:63],v[196:199]
T v[174:175],v222,6144
v_sub_f32 v110, v110, v249
v_sub_f32 v111, v111, v249
M v[196:199],v[176:179],a[52:55],v[196:199]
T v[176:177],v222,8192
v_cvt_pk_bf16_f32 v252, v184, v185
v_cvt_pk_bf16_f32 v253, v186, v187
W 5
M v[196:199],v[156:159],a[76:79],v[196:199]
T v[178:179],v222,10240
v_accvgpr_write_b32 a96, v252
v_accvgpr_write_b32 a97, v253
M v[196:199],v[160:163],a[84:87],v[196:199]
T v[180:181],v222,12288
v_cvt_pk_bf16_f32 v252, v188, v189
v_cvt_pk_bf16_f32 v253, v190, v191
v_accvgpr_write_b32 a100, v252
M v[196:199],v[164:167],a[92:95],v[196:199]
T v[182:183],v222,14336
buffer_load_dwordx4 v228, s[12:15], s79 offen offset:1024 nt lds
v_accvgpr_write_b32 a101, v253
v_sub_f32 v112, v112, v248
M v[192:195],v[164:167],a[88:91],v[192:195]
T v[152:153],v222,16384
v_sub_f32 v113, v113, v248
v_sub_f32 v114, v114, v248
M v[192:195],v[160:163],a[80:83],v[192:195]
T v[154:155],v222,18432
v_sub_f32 v115, v115, v248
v_sub_f32 v116, v116, v249
M v[192:195],v[156:159],a[72:75],v[192:195]
T v[156:157],v222,20480
v_sub_f32 v117, v117, v249
v_sub_f32 v118, v118, v249
W 5
M v[184:187],v[168:171],a[48:51],0
T v[158:159],v222,22528
v_sub_f32 v119, v119, v249
v_exp_f32 v200, v104
M v[184:187],v[172:175],a[56:59],v[184:187]
T v[160:161],v223,0
v_exp_f32 v201, v105
M v[184:187],v[176:179],a[64:67],v[184:187]
T v[162:163],v223,2048
v_cvt_pk_bf16_f32 v136, v200, v201
v_exp_f32 v202, v106
M v[188:191],v[176:179],a[68:71],0
T v[164:165],v223,4096
v_exp_f32 v203, v107
M v[188:191],v[172:175],a[60:63],v[188:191]
T v[166:167],v223,6144
v_cvt_pk_bf16_f32 v137, v202, v203
M v[188:191],v[168:171],a[52:55],v[188:191]
T v[168:169],v223,8192
v_cvt_pk_bf16_f32 v252, v192, v193
v_cvt_pk_bf16_f32 v253, v194, v195
W 5
M v[188:191],v[180:183],a[76:79],v[188:191]
T v[170:171],v223,10240
v_accvgpr_write_b32 a98, v252
v_accvgpr_write_b32 a99, v253
M v[188:191],v[152:155],a[84:87],v[188:191]
T v[172:173],v223,12288
v_cvt_pk_bf16_f32 v252, v196, v197
v_cvt_pk_bf16_f32 v253, v198, v199
v_accvgpr_write_b32 a102, v252
M v[188:191],v[156:159],a[92:95],v[188:191]
T v[174:175],v223,14336
v_accvgpr_write_b32 a103, v253
v_exp_f32 v200, v108
M v[184:187],v[156:159],a[88:91],v[184:187]
T v[176:177],v223,16384
v_exp_f32 v201, v109
M v[184:187],v[152:155],a[80:83],v[184:187]
T v[178:179],v223,18432
v_cvt_pk_bf16_f32 v140, v200, v201
v_exp_f32 v202, v110
M v[184:187],v[180:183],a[72:75],v[184:187]
T v[180:181],v223,20480
v_exp_f32 v203, v111
W 5
M v[192:195],v[160:163],a[48:51],0
T v[182:183],v223,22528
v_cvt_pk_bf16_f32 v141, v202, v203
M v[192:195],v[164:167],a[56:59],v[192:195]
v_exp_f32 v200, v112
v_exp_f32 v201, v113
M v[192:195],v[168:171],a[64:67],v[192:195]
v_exp_f32 v202, v114
M v[196:199],v[168:171],a[68:71],0
v_cvt_pk_bf16_f32 v138, v200, v201
v_add_u32 v216, 122880, v224
M v[196:199],v[164:167],a[60:63],v[196:199]
v_xor_b32 v217, 32, v216
v_xor_b32 v218, 64, v216
v_xor_b32 v219, 96, v216
s_waitcnt vmcnt(8)
s_barrier
T v[152:153],v216,0
T v[154:155],v216,6144
v_exp_f32 v203, v115
v_exp_f32 v200, v116
M v[196:199],v[160:163],a[52:55],v[196:199]
T v[156:157],v217,0
v_cvt_pk_bf16_f32 v139, v202, v203
W 3
M v[196:199],v[172:175],a[76:79],v[196:199]
T v[158:159],v217,6144
buffer_load_dwordx4 v229, s[12:15], s79 offen offset:2048 nt lds
v_exp_f32 v201, v117
M v[196:199],v[176:179],a[84:87],v[196:199]
T v[160:161],v218,0
v_cvt_pk_bf16_f32 v142, v200, v201
v_exp_f32 v202, v118
M v[196:199],v[180:183],a[92:95],v[196:199]
T v[162:163],v218,6144
v_sub_f32 v120, v120, v248
v_exp_f32 v203, v119
M v[192:195],v[180:183],a[88:91],v[192:195]
T v[164:165],v219,0
v_cvt_pk_bf16_f32 v143, v202, v203
M v[192:195],v[176:179],a[80:83],v[192:195]
T v[166:167],v219,6144
v_sub_f32 v121, v121, v248
v_sub_f32 v122, v122, v248
M v[192:195],v[172:175],a[72:75],v[192:195]
T v[168:169],v216,128
v_sub_f32 v123, v123, v248
v_sub_f32 v124, v124, v249
v_sub_f32 v125, v125, v249
W 5
M v[0:3],v[152:155],v[136:139],0
T v[170:171],v216,6272
v_sub_f32 v126, v126, v249
v_sub_f32 v127, v127, v249
M v[4:7],v[152:155],v[140:143],0
T v[172:173],v217,128
v_sub_f32 v128, v128, v248
v_sub_f32 v129, v129, v248
M v[96:99],v[244:247],v[136:139],0
T v[174:175],v217,6272
v_sub_f32 v130, v130, v248
v_sub_f32 v131, v131, v248
v_sub_f32 v132, v132, v249
M v[100:103],v[244:247],v[140:143],0
T v[176:177],v218,128
v_sub_f32 v133, v133, v249
v_sub_f32 v134, v134, v249
M v[8:11],v[156:159],v[136:139],0
T v[178:179],v218,6272
v_sub_f32 v135, v135, v249
v_exp_f32 v200, v120
M v[12:15],v[156:159],v[140:143],0
T v[180:181],v219,128
v_exp_f32 v201, v121
W 5
M v[16:19],v[160:163],v[136:139],0
T v[182:183],v219,6272
v_cvt_pk_bf16_f32 v144, v200, v201
v_exp_f32 v202, v122
M v[20:23],v[160:163],v[140:143],0
T v[152:153],v216,256
v_exp_f32 v203, v123
M v[24:27],v[164:167],v[136:139],0
T v[154:155],v216,6400
v_cvt_pk_bf16_f32 v145, v202, v203
v_exp_f32 v200, v124
M v[28:31],v[164:167],v[140:143],0
T v[156:157],v217,256
v_exp_f32 v201, v125
M v[32:35],v[168:171],v[136:139],0
T v[158:159],v217,6400
v_cvt_pk_bf16_f32 v148, v200, v201
M v[36:39],v[168:171],v[140:143],0
T v[160:161],v218,256
v_exp_f32 v202, v126
W 5
M v[40:43],v[172:175],v[136:139],0
T v[162:163],v218,6400
v_exp_f32 v203, v127
v_exp_f32 v200, v128
M v[44:47],v[172:175],v[140:143],0
T v[164:165],v219,256
v_cvt_pk_bf16_f32 v149, v202, v203
M v[48:51],v[176:179],v[136:139],0
T v[166:167],v219,6400
v_exp_f32 v201, v129
M v[52:55],v[176:179],v[140:143],0
T v[168:169],v216,12288
v_cvt_pk_bf16_f32 v146, v200, v201
v_exp_f32 v202, v130
s_add_u32 s77, s68, s42
M v[56:59],v[180:183],v[136:139],0
T v[170:171],v216,18432
v_exp_f32 v203, v131
M v[60:63],v[180:183],v[140:143],0
T v[172:173],v217,12288
v_exp_f32 v200, v132
v_cvt_pk_bf16_f32 v147, v202, v203
W 5
M v[64:67],v[152:155],v[136:139],0
T v[174:175],v217,18432
v_exp_f32 v201, v133
M v[68:71],v[152:155],v[140:143],0
T v[176:177],v218,12288
v_cvt_pk_bf16_f32 v150, v200, v201
v_exp_f32 v202, v134
M v[72:75],v[156:159],v[136:139],0
T v[178:179],v218,18432
v_sub_f32 v204, 0, v248
M v[76:79],v[156:159],v[140:143],0
T v[180:181],v219,12288
v_sub_f32 v205, 0, v248
v_exp_f32 v203, v135
M v[80:83],v[160:163],v[136:139],0
T v[182:183],v219,18432
v_cvt_pk_bf16_f32 v151, v202, v203
v_sub_f32 v206, 0, v248
s_add_u32 s78, s77, 0
M v[84:87],v[160:163],v[140:143],0
T v[152:153],v216,12416
v_sub_f32 v207, 0, v248
v_sub_f32 v208, 0, v249
v_sub_f32 v209, 0, v249
W 5
M v[88:91],v[164:167],v[136:139],0
T v[154:155],v216,18560
v_sub_f32 v210, 0, v249
v_sub_f32 v211, 0, v249
M v[92:95],v[164:167],v[140:143],0
T v[156:157],v217,12416
s_add_u32 m0, s40, 49152
M v[0:3],v[168:171],v[144:147],v[0:3]
T v[158:159],v217,18560
buffer_load_dwordx4 v230, s[20:23], s78 offen lds
M v[4:7],v[168:171],v[148:151],v[4:7]
T v[160:161],v218,12416
v_cvt_pk_bf16_f32 v252, v184, v185
M v[96:99],v[244:247],v[144:147],v[96:99]
T v[162:163],v218,18560
v_cvt_pk_bf16_f32 v253, v186, v187
M v[100:103],v[244:247],v[148:151],v[100:103]
T v[164:165],v219,12416
v_accvgpr_write_b32 a104, v252
M v[8:11],v[172:175],v[144:147],v[8:11]
T v[166:167],v219,18560
v_accvgpr_write_b32 a105, v253
M v[12:15],v[172:175],v[148:151],v[12:15]
T v[168:169],v216,12544
W 5
M v[16:19],v[176:179],v[144:147],v[16:19]
T v[170:171],v216,18688
v_cvt_pk_bf16_f32 v252, v188, v189
M v[20:23],v[176:179],v[148:151],v[20:23]
T v[172:173],v217,12544
v_cvt_pk_bf16_f32 v253, v190, v191
M v[24:27],v[180:183],v[144:147],v[24:27]
T v[174:175],v217,18688
buffer_load_dwordx4 v231, s[20:23], s78 offen offset:1024 lds
M v[28:31],v[180:183],v[148:151],v[28:31]
T v[176:177],v218,12544
v_accvgpr_write_b32 a108, v252
M v[32:35],v[152:155],v[144:147],v[32:35]
T v[178:179],v218,18688
v_accvgpr_write_b32 a109, v253
M v[36:39],v[152:155],v[148:151],v[36:39]
T v[180:181],v219,12544
v_lshrrev_b32 v241, 4, v240
M v[40:43],v[156:159],v[144:147],v[40:43]
T v[182:183],v219,18688
v_and_b32 v242, 15, v241
M v[44:47],v[156:159],v[148:151],v[44:47]
v_mul_u32_u24 v233, 0x500, v242
W 4
M v[48:51],v[160:163],v[144:147],v[48:51]
v_lshrrev_b32 v234, 4, v241
v_add_u32 v220, 98304, v225
M v[52:55],v[160:163],v[148:151],v[52:55]
v_xor_b32 v221, 64, v220
v_and_b32 v235, 1, v234
M v[56:59],v[164:167],v[144:147],v[56:59]
s_waitcnt vmcnt(8)
s_barrier
ds_read_b128 v[152:155], v220 offset:0
M v[60:63],v[164:167],v[148:151],v[60:63]
ds_read_b128 v[156:159], v221 offset:0
buffer_load_dwordx4 v232, s[20:23], s78 offen offset:2048 lds
v_lshl_add_u32 v233, v235, 5, v233
M v[64:67],v[168:171],v[144:147],v[64:67]
ds_read_b128 v[160:163], v220 offset:128
v_lshrrev_b32 v235, 1, v234
M v[68:71],v[168:171],v[148:151],v[68:71]
ds_read_b128 v[164:167], v221 offset:128
v_lshl_add_u32 v233, v235, 4, v233
M v[72:75],v[172:175],v[144:147],v[72:75]
ds_read_b128 v[168:171], v220 offset:256
v_lshrrev_b32 v242, 2, v242
M v[76:79],v[172:175],v[148:151],v[76:79]
ds_read_b128 v[172:175], v221 offset:256
v_cmp_eq_u32 s[74:75], v242, v234
W 5
M v[80:83],v[176:179],v[144:147],v[80:83]
v_and_b32 v241, 3, v241
v_lshlrev_b32 v241, 4, v241
M v[84:87],v[176:179],v[148:151],v[84:87]
v_mov_b32 v234, 0x3f80
v_mov_b32 v235, 0
M v[88:91],v[180:183],v[144:147],v[88:91]
v_cndmask_b32 v234, 0, v234, s[74:75]
v_lshlrev_b64 v[236:237], v241, v[234:235]
M v[92:95],v[180:183],v[148:151],v[92:95]
ds_read_b128 v[176:179], v220 offset:6144
v_mov_b32 v234, 0
M v[104:107],v[152:155],a[0:3],v[204:207]
ds_read_b128 v[180:183], v221 offset:6144
s_add_u32 s79, s2, 196608
s_add_u32 m0, s40, 73728
s_nop 0
buffer_load_dwordx4 v227, s[8:11], s79 offen nt lds
W 2
M v[104:107],v[156:159],a[8:11],v[104:107]
v_mov_b32 v238, 0
v_mov_b32 v239, 0
M v[104:107],v[160:163],a[16:19],v[104:107]
s_movk_i32 s85, 4096
s_movk_i32 s86, 8192
M v[108:111],v[160:163],a[20:23],v[208:211]
s_movk_i32 s87, 12288
v_cvt_pk_bf16_f32 v252, v192, v193
M v[108:111],v[156:159],a[12:15],v[108:111]
v_cvt_pk_bf16_f32 v253, v194, v195
v_accvgpr_write_b32 a106, v252
M v[108:111],v[152:155],a[4:7],v[108:111]
ds_read_b128 v[152:155], v220 offset:6272
v_accvgpr_write_b32 a107, v253
M v[108:111],v[164:167],a[28:31],v[108:111]
ds_read_b128 v[156:159], v221 offset:6272
v_cvt_pk_bf16_f32 v252, v196, v197
M v[108:111],v[168:171],a[36:39],v[108:111]
ds_read_b128 v[160:163], v220 offset:6400
v_cvt_pk_bf16_f32 v253, v198, v199
M v[108:111],v[172:175],a[44:47],v[108:111]
v_accvgpr_write_b32 a110, v252
v_accvgpr_write_b32 a111, v253
M v[104:107],v[172:175],a[40:43],v[104:107]
W 3
M v[104:107],v[168:171],a[32:35],v[104:107]
M v[104:107],v[164:167],a[24:27],v[104:107]
ds_read_b128 v[164:167], v221 offset:6400
W 2
M v[112:115],v[176:179],a[0:3],v[204:207]
ds_read_b128 v[168:171], v220 offset:12288
M v[112:115],v[180:183],a[8:11],v[112:115]
ds_read_b128 v[172:175], v221 offset:12288
M v[112:115],v[152:155],a[16:19],v[112:115]
M v[116:119],v[152:155],a[20:23],v[208:211]
v_max3_f32 v248, v104, v105, v106
v_max3_f32 v249, v108, v109, v110
M v[116:119],v[180:183],a[12:15],v[116:119]
v_max_f32 v248, v248, v107
v_max_f32 v249, v249, v111
M v[116:119],v[176:179],a[4:7],v[116:119]
ds_read_b128 v[176:179], v220 offset:12416
W 2
M v[116:119],v[156:159],a[28:31],v[116:119]
v_exp_f32 v200, v104
M v[116:119],v[160:163],a[36:39],v[116:119]
ds_read_b128 v[180:183], v221 offset:12416
v_add_u32 v216, 0, v226
M v[116:119],v[164:167],a[44:47],v[116:119]
ds_read_b128 v[152:155], v220 offset:12544
buffer_load_dwordx4 v228, s[8:11], s79 offen offset:1024 nt lds
M v[112:115],v[164:167],a[40:43],v[112:115]
v_exp_f32 v201, v105
M v[112:115],v[160:163],a[32:35],v[112:115]
v_exp_f32 v202, v106
M v[112:115],v[156:159],a[24:27],v[112:115]
ds_read_b128 v[156:159], v221 offset:12544
v_cvt_pk_bf16_f32 v136, v200, v201
M v[120:123],v[168:171],a[0:3],v[204:207]
ds_read_b128 v[160:163], v220 offset:18432
W 3
M v[120:123],v[172:175],a[8:11],v[120:123]
v_exp_f32 v203, v107
M v[120:123],v[176:179],a[16:19],v[120:123]
ds_read_b128 v[164:167], v221 offset:18432
v_cvt_pk_bf16_f32 v137, v202, v203
M v[124:127],v[176:179],a[20:23],v[208:211]
v_exp_f32 v200, v108
M v[124:127],v[172:175],a[12:15],v[124:127]
v_exp_f32 v201, v109
M v[124:127],v[168:171],a[4:7],v[124:127]
ds_read_b128 v[168:171], v220 offset:18560
v_cvt_pk_bf16_f32 v140, v200, v201
M v[124:127],v[180:183],a[28:31],v[124:127]
ds_read_b128 v[172:175], v221 offset:18560
W 3
M v[124:127],v[152:155],a[36:39],v[124:127]
v_exp_f32 v202, v110
M v[124:127],v[156:159],a[44:47],v[124:127]
ds_read_b128 v[176:179], v220 offset:18688
v_max3_f32 v248, v248, v112, v113
M v[120:123],v[156:159],a[40:43],v[120:123]
v_exp_f32 v203, v111
M v[120:123],v[152:155],a[32:35],v[120:123]
v_cvt_pk_bf16_f32 v141, v202, v203
v_max3_f32 v249, v249, v116, v117
M v[120:123],v[180:183],a[24:27],v[120:123]
ds_read_b128 v[180:183], v221 offset:18688
v_xor_b32 v217, 32, v216
M v[128:131],v[160:163],a[0:3],v[204:207]
v_xor_b32 v218, 64, v216
W 2
M v[128:131],v[164:167],a[8:11],v[128:131]
v_xor_b32 v219, 96, v216
v_max3_f32 v248, v248, v114, v115
M v[128:131],v[168:171],a[16:19],v[128:131]
s_waitcnt vmcnt(8)
s_barrier
T v[152:153],v216,0
M v[132:135],v[168:171],a[20:23],v[208:211]
T v[154:155],v216,2048
v_max3_f32 v249, v249, v118, v119
M v[132:135],v[164:167],a[12:15],v[132:135]
T v[156:157],v216,4096
buffer_load_dwordx4 v229, s[8:11], s79 offen offset:2048 nt lds
M v[132:135],v[160:163],a[4:7],v[132:135]
T v[158:159],v216,6144
W 4
M v[132:135],v[172:175],a[28:31],v[132:135]
T v[160:161],v216,8192
T v[162:163],v216,10240
M v[132:135],v[176:179],a[36:39],v[132:135]
T v[164:165],v216,12288
M v[132:135],v[180:183],a[44:47],v[132:135]
T v[166:167],v216,14336
W 6
M v[128:131],v[180:183],a[40:43],v[128:131]
T v[168:169],v216,16384
v_exp_f32 v200, v112
M v[128:131],v[176:179],a[32:35],v[128:131]
T v[170:171],v216,18432
W 6
M v[128:131],v[172:175],a[24:27],v[128:131]
T v[172:173],v216,20480
v_exp_f32 v201, v113
M v[184:187],v[152:155],a[48:51],0
T v[174:175],v216,22528
v_cvt_pk_bf16_f32 v138, v200, v201
M v[184:187],v[156:159],a[56:59],v[184:187]
T v[176:177],v217,0
W 5
M v[184:187],v[160:163],a[64:67],v[184:187]
T v[178:179],v217,2048
s_add_u32 s77, s69, s42
M v[188:191],v[160:163],a[68:71],0
T v[180:181],v217,4096
v_exp_f32 v202, v114
M v[188:191],v[156:159],a[60:63],v[188:191]
T v[182:183],v217,6144
s_add_u32 s78, s77, 0
M v[188:191],v[152:155],a[52:55],v[188:191]
T v[152:153],v217,8192
v_exp_f32 v203, v115
M v[188:191],v[164:167],a[76:79],v[188:191]
T v[154:155],v217,10240
W 5
M v[188:191],v[168:171],a[84:87],v[188:191]
T v[156:157],v217,12288
v_cvt_pk_bf16_f32 v139, v202, v203
M v[188:191],v[172:175],a[92:95],v[188:191]
T v[158:159],v217,14336
v_exp_f32 v200, v116
M v[184:187],v[172:175],a[88:91],v[184:187]
T v[160:161],v217,16384
s_add_u32 m0, s40, 122880
M v[184:187],v[168:171],a[80:83],v[184:187]
T v[162:163],v217,18432
buffer_load_dwordx4 v230, s[20:23], s78 offen lds
M v[184:187],v[164:167],a[72:75],v[184:187]
T v[164:165],v217,20480
v_exp_f32 v201, v117
W 5
M v[192:195],v[176:179],a[48:51],0
T v[166:167],v217,22528
v_cvt_pk_bf16_f32 v142, v200, v201
M v[192:195],v[180:183],a[56:59],v[192:195]
T v[168:169],v218,0
buffer_load_dwordx4 v231, s[20:23], s78 offen offset:1024 lds
M v[192:195],v[152:155],a[64:67],v[192:195]
T v[170:171],v218,2048
v_exp_f32 v202, v118
M v[196:199],v[152:155],a[68:71],0
T v[172:173],v218,4096
v_max3_f32 v248, v248, v120, v121
M v[196:199],v[180:183],a[60:63],v[196:199]
T v[174:175],v218,6144
v_max3_f32 v249, v249, v124, v125
M v[196:199],v[176:179],a[52:55],v[196:199]
T v[176:177],v218,8192
v_exp_f32 v203, v119
W 5
M v[196:199],v[156:159],a[76:79],v[196:199]
T v[178:179],v218,10240
v_cvt_pk_bf16_f32 v143, v202, v203
M v[196:199],v[160:163],a[84:87],v[196:199]
T v[180:181],v218,12288
v_cvt_pk_bf16_f32 v252, v184, v185
v_cvt_pk_bf16_f32 v253, v186, v187
M v[196:199],v[164:167],a[92:95],v[196:199]
T v[182:183],v218,14336
v_accvgpr_write_b32 a112, v252
v_accvgpr_write_b32 a113, v253
M v[192:195],v[164:167],a[88:91],v[192:195]
T v[152:153],v218,16384
v_cvt_pk_bf16_f32 v252, v188, v189
v_cvt_pk_bf16_f32 v253, v190, v191
M v[192:195],v[160:163],a[80:83],v[192:195]
T v[154:155],v218,18432
v_accvgpr_write_b32 a116, v252
v_accvgpr_write_b32 a117, v253
M v[192:195],v[156:159],a[72:75],v[192:195]
T v[156:157],v218,20480
v_max3_f32 v248, v248, v122, v123
W 5
M v[184:187],v[168:171],a[48:51],0
T v[158:159],v218,22528
v_max3_f32 v249, v249, v126, v127
M v[184:187],v[172:175],a[56:59],v[184:187]
T v[160:161],v219,0
v_max3_f32 v248, v248, v128, v129
M v[184:187],v[176:179],a[64:67],v[184:187]
T v[162:163],v219,2048
v_max3_f32 v249, v249, v132, v133
M v[188:191],v[176:179],a[68:71],0
T v[164:165],v219,4096
v_max3_f32 v248, v248, v130, v131
M v[188:191],v[172:175],a[60:63],v[188:191]
T v[166:167],v219,6144
v_max3_f32 v249, v249, v134, v135
M v[188:191],v[168:171],a[52:55],v[188:191]
T v[168:169],v219,8192
v_cvt_pk_bf16_f32 v252, v192, v193
W 5
M v[188:191],v[180:183],a[76:79],v[188:191]
T v[170:171],v219,10240
v_cvt_pk_bf16_f32 v253, v194, v195
M v[188:191],v[152:155],a[84:87],v[188:191]
T v[172:173],v219,12288
v_accvgpr_write_b32 a114, v252
v_accvgpr_write_b32 a115, v253
M v[188:191],v[156:159],a[92:95],v[188:191]
T v[174:175],v219,14336
v_cvt_pk_bf16_f32 v252, v196, v197
M v[184:187],v[156:159],a[88:91],v[184:187]
T v[176:177],v219,16384
v_cvt_pk_bf16_f32 v253, v198, v199
v_accvgpr_write_b32 a118, v252
M v[184:187],v[152:155],a[80:83],v[184:187]
T v[178:179],v219,18432
v_accvgpr_write_b32 a119, v253
M v[184:187],v[180:183],a[72:75],v[184:187]
T v[180:181],v219,20480
v_max_f32 v250, v248, v249
W 5
M v[192:195],v[160:163],a[48:51],0
T v[182:183],v219,22528
v_cmp_lt_f32 vcc, s60, v250
M v[192:195],v[164:167],a[56:59],v[192:195]
s_nop 0
s_cbranch_vccnz LST1_1_%=
LRT1_1_%=:
v_add_u32 v220, 24576, v224
M v[192:195],v[168:171],a[64:67],v[192:195]
v_exp_f32 v200, v120
M v[196:199],v[168:171],a[68:71],0
v_xor_b32 v221, 32, v220
v_xor_b32 v222, 64, v220
M v[196:199],v[164:167],a[60:63],v[196:199]
v_xor_b32 v223, 96, v220
s_waitcnt vmcnt(8)
s_barrier
T v[152:153],v220,0
T v[154:155],v220,6144
v_exp_f32 v201, v121
M v[196:199],v[160:163],a[52:55],v[196:199]
T v[156:157],v221,0
v_exp_f32 v202, v122
W 3
M v[196:199],v[172:175],a[76:79],v[196:199]
T v[158:159],v221,6144
v_cvt_pk_bf16_f32 v144, v200, v201
M v[196:199],v[176:179],a[84:87],v[196:199]
T v[160:161],v222,0
buffer_load_dwordx4 v232, s[20:23], s78 offen offset:2048 lds
M v[196:199],v[180:183],a[92:95],v[196:199]
T v[162:163],v222,6144
v_exp_f32 v203, v123
M v[192:195],v[180:183],a[88:91],v[192:195]
T v[164:165],v223,0
v_cvt_pk_bf16_f32 v145, v202, v203
M v[192:195],v[176:179],a[80:83],v[192:195]
T v[166:167],v223,6144
W 5
M v[192:195],v[172:175],a[72:75],v[192:195]
T v[168:169],v220,128
v_exp_f32 v200, v124
M v[0:3],v[152:155],v[136:139],v[0:3]
T v[170:171],v220,6272
s_add_u32 s79, s2, 196608
s_add_u32 m0, s40, 98304
s_nop 0
buffer_load_dwordx4 v227, s[12:15], s79 offen nt lds
v_exp_f32 v201, v125
M v[4:7],v[152:155],v[140:143],v[4:7]
T v[172:173],v221,128
v_cvt_pk_bf16_f32 v148, v200, v201
M v[96:99],v[244:247],v[136:139],v[96:99]
T v[174:175],v221,6272
W 5
M v[100:103],v[244:247],v[140:143],v[100:103]
T v[176:177],v222,128
v_exp_f32 v202, v126
M v[8:11],v[156:159],v[136:139],v[8:11]
T v[178:179],v222,6272
W 5
M v[12:15],v[156:159],v[140:143],v[12:15]
T v[180:181],v223,128
v_exp_f32 v203, v127
M v[16:19],v[160:163],v[136:139],v[16:19]
T v[182:183],v223,6272
v_cvt_pk_bf16_f32 v149, v202, v203
M v[20:23],v[160:163],v[140:143],v[20:23]
T v[152:153],v220,256
W 5
M v[24:27],v[164:167],v[136:139],v[24:27]
T v[154:155],v220,6400
v_exp_f32 v200, v128
M v[28:31],v[164:167],v[140:143],v[28:31]
T v[156:157],v221,256
v_exp_f32 v201, v129
M v[32:35],v[168:171],v[136:139],v[32:35]
T v[158:159],v221,6400
v_cvt_pk_bf16_f32 v146, v200, v201
M v[36:39],v[168:171],v[140:143],v[36:39]
T v[160:161],v222,256
W 5
M v[40:43],v[172:175],v[136:139],v[40:43]
T v[162:163],v222,6400
v_exp_f32 v202, v130
M v[44:47],v[172:175],v[140:143],v[44:47]
T v[164:165],v223,256
W 5
M v[48:51],v[176:179],v[136:139],v[48:51]
T v[166:167],v223,6400
v_exp_f32 v203, v131
M v[52:55],v[176:179],v[140:143],v[52:55]
T v[168:169],v220,12288
v_cvt_pk_bf16_f32 v147, v202, v203
M v[56:59],v[180:183],v[136:139],v[56:59]
T v[170:171],v220,18432
W 5
M v[60:63],v[180:183],v[140:143],v[60:63]
T v[172:173],v221,12288
v_exp_f32 v200, v132
M v[64:67],v[152:155],v[136:139],v[64:67]
T v[174:175],v221,18432
v_exp_f32 v201, v133
M v[68:71],v[152:155],v[140:143],v[68:71]
T v[176:177],v222,12288
v_cvt_pk_bf16_f32 v150, v200, v201
M v[72:75],v[156:159],v[136:139],v[72:75]
T v[178:179],v222,18432
buffer_load_dwordx4 v228, s[12:15], s79 offen offset:1024 nt lds
M v[76:79],v[156:159],v[140:143],v[76:79]
T v[180:181],v223,12288
v_exp_f32 v202, v134
W 5
M v[80:83],v[160:163],v[136:139],v[80:83]
T v[182:183],v223,18432
v_cvt_pk_bf16_f32 v252, v184, v185
M v[84:87],v[160:163],v[140:143],v[84:87]
T v[152:153],v220,12416
v_cvt_pk_bf16_f32 v253, v186, v187
M v[88:91],v[164:167],v[136:139],v[88:91]
T v[154:155],v220,18560
v_accvgpr_write_b32 a120, v252
v_exp_f32 v203, v135
M v[92:95],v[164:167],v[140:143],v[92:95]
T v[156:157],v221,12416
v_cvt_pk_bf16_f32 v151, v202, v203
M v[0:3],v[168:171],v[144:147],v[0:3]
T v[158:159],v221,18560
v_accvgpr_write_b32 a121, v253
M v[4:7],v[168:171],v[148:151],v[4:7]
T v[160:161],v222,12416
v_cvt_pk_bf16_f32 v252, v188, v189
M v[96:99],v[244:247],v[144:147],v[96:99]
T v[162:163],v222,18560
v_cvt_pk_bf16_f32 v253, v190, v191
M v[100:103],v[244:247],v[148:151],v[100:103]
T v[164:165],v223,12416
W 5
M v[8:11],v[172:175],v[144:147],v[8:11]
T v[166:167],v223,18560
v_accvgpr_write_b32 a124, v252
M v[12:15],v[172:175],v[148:151],v[12:15]
T v[168:169],v220,12544
v_accvgpr_write_b32 a125, v253
M v[16:19],v[176:179],v[144:147],v[16:19]
T v[170:171],v220,18688
v_cvt_pk_bf16_f32 v252, v192, v193
M v[20:23],v[176:179],v[148:151],v[20:23]
T v[172:173],v221,12544
v_cvt_pk_bf16_f32 v253, v194, v195
M v[24:27],v[180:183],v[144:147],v[24:27]
T v[174:175],v221,18688
v_accvgpr_write_b32 a122, v252
M v[28:31],v[180:183],v[148:151],v[28:31]
T v[176:177],v222,12544
v_accvgpr_write_b32 a123, v253
M v[32:35],v[152:155],v[144:147],v[32:35]
T v[178:179],v222,18688
v_cvt_pk_bf16_f32 v252, v196, v197
M v[36:39],v[152:155],v[148:151],v[36:39]
T v[180:181],v223,12544
W 5
M v[40:43],v[156:159],v[144:147],v[40:43]
T v[182:183],v223,18688
v_add_u32 v216, 49152, v226
M v[44:47],v[156:159],v[148:151],v[44:47]
v_xor_b32 v217, 32, v216
v_xor_b32 v218, 64, v216
M v[48:51],v[160:163],v[144:147],v[48:51]
buffer_load_dwordx4 v229, s[12:15], s79 offen offset:2048 nt lds
v_xor_b32 v219, 96, v216
M v[52:55],v[160:163],v[148:151],v[52:55]
s_waitcnt vmcnt(9)
s_barrier
T v[152:153],v216,0
M v[56:59],v[164:167],v[144:147],v[56:59]
T v[154:155],v216,2048
v_cvt_pk_bf16_f32 v253, v198, v199
M v[60:63],v[164:167],v[148:151],v[60:63]
T v[156:157],v216,4096
v_accvgpr_write_b32 a126, v252
M v[64:67],v[168:171],v[144:147],v[64:67]
T v[158:159],v216,6144
v_accvgpr_write_b32 a127, v253
M v[68:71],v[168:171],v[148:151],v[68:71]
T v[160:161],v216,8192
W 5
M v[72:75],v[172:175],v[144:147],v[72:75]
T v[162:163],v216,10240
M v[76:79],v[172:175],v[148:151],v[76:79]
T v[164:165],v216,12288
W 5
M v[80:83],v[176:179],v[144:147],v[80:83]
T v[166:167],v216,14336
M v[84:87],v[176:179],v[148:151],v[84:87]
T v[168:169],v216,16384
T v[170:171],v216,18432
M v[88:91],v[180:183],v[144:147],v[88:91]
T v[172:173],v216,20480
T v[174:175],v216,22528
M v[92:95],v[180:183],v[148:151],v[92:95]
T v[176:177],v217,0
W 7
M v[184:187],v[152:155],a[48:51],0
T v[178:179],v217,2048
s_add_u32 s77, s70, s42
M v[184:187],v[156:159],a[56:59],v[184:187]
T v[180:181],v217,4096
s_add_u32 s78, s77, 0
M v[184:187],v[160:163],a[64:67],v[184:187]
T v[182:183],v217,6144
s_add_u32 m0, s40, 0
M v[188:191],v[160:163],a[68:71],0
buffer_load_dwordx4 v230, s[20:23], s78 offen lds
W 4
M v[188:191],v[156:159],a[60:63],v[188:191]
M v[188:191],v[152:155],a[52:55],v[188:191]
T v[152:153],v217,8192
W 3
M v[188:191],v[164:167],a[76:79],v[188:191]
T v[154:155],v217,10240
buffer_load_dwordx4 v231, s[20:23], s78 offen offset:1024 lds
M v[188:191],v[168:171],a[84:87],v[188:191]
T v[156:157],v217,12288
W 3
M v[188:191],v[172:175],a[92:95],v[188:191]
T v[158:159],v217,14336
M v[184:187],v[172:175],a[88:91],v[184:187]
T v[160:161],v217,16384
buffer_load_dwordx4 v232, s[20:23], s78 offen offset:2048 lds
M v[184:187],v[168:171],a[80:83],v[184:187]
T v[162:163],v217,18432
M v[184:187],v[164:167],a[72:75],v[184:187]
T v[164:165],v217,20480
T v[166:167],v217,22528
M v[192:195],v[176:179],a[48:51],0
T v[168:169],v218,0
W 6
M v[192:195],v[180:183],a[56:59],v[192:195]
T v[170:171],v218,2048
M v[192:195],v[152:155],a[64:67],v[192:195]
T v[172:173],v218,4096
W 6
M v[196:199],v[152:155],a[68:71],0
T v[174:175],v218,6144
M v[196:199],v[180:183],a[60:63],v[196:199]
W 4
M v[196:199],v[176:179],a[52:55],v[196:199]
T v[176:177],v218,8192
v_cvt_pk_bf16_f32 v252, v184, v185
M v[196:199],v[156:159],a[76:79],v[196:199]
T v[178:179],v218,10240
v_cvt_pk_bf16_f32 v253, v186, v187
M v[196:199],v[160:163],a[84:87],v[196:199]
T v[180:181],v218,12288
v_accvgpr_write_b32 a128, v252
M v[196:199],v[164:167],a[92:95],v[196:199]
T v[182:183],v218,14336
v_accvgpr_write_b32 a129, v253
M v[192:195],v[164:167],a[88:91],v[192:195]
T v[152:153],v218,16384
v_cvt_pk_bf16_f32 v252, v188, v189
v_cvt_pk_bf16_f32 v253, v190, v191
M v[192:195],v[160:163],a[80:83],v[192:195]
T v[154:155],v218,18432
v_accvgpr_write_b32 a132, v252
v_accvgpr_write_b32 a133, v253
M v[192:195],v[156:159],a[72:75],v[192:195]
T v[156:157],v218,20480
W 5
M v[184:187],v[168:171],a[48:51],0
T v[158:159],v218,22528
v_add_u32 v220, 73728, v225
M v[184:187],v[172:175],a[56:59],v[184:187]
T v[160:161],v219,0
v_xor_b32 v221, 64, v220
M v[184:187],v[176:179],a[64:67],v[184:187]
T v[162:163],v219,2048
W 5
M v[188:191],v[176:179],a[68:71],0
T v[164:165],v219,4096
T v[166:167],v219,6144
M v[188:191],v[172:175],a[60:63],v[188:191]
W 5
M v[188:191],v[168:171],a[52:55],v[188:191]
T v[168:169],v219,8192
v_cvt_pk_bf16_f32 v252, v192, v193
M v[188:191],v[180:183],a[76:79],v[188:191]
T v[170:171],v219,10240
v_cvt_pk_bf16_f32 v253, v194, v195
M v[188:191],v[152:155],a[84:87],v[188:191]
T v[172:173],v219,12288
v_accvgpr_write_b32 a130, v252
W 5
M v[188:191],v[156:159],a[92:95],v[188:191]
T v[174:175],v219,14336
v_accvgpr_write_b32 a131, v253
M v[184:187],v[156:159],a[88:91],v[184:187]
T v[176:177],v219,16384
v_cvt_pk_bf16_f32 v252, v196, v197
v_cvt_pk_bf16_f32 v253, v198, v199
M v[184:187],v[152:155],a[80:83],v[184:187]
T v[178:179],v219,18432
v_accvgpr_write_b32 a134, v252
v_accvgpr_write_b32 a135, v253
M v[184:187],v[180:183],a[72:75],v[184:187]
T v[180:181],v219,20480
T v[182:183],v219,22528
M v[192:195],v[160:163],a[48:51],0
s_waitcnt vmcnt(9)
s_barrier
ds_read_b128 v[152:155], v220 offset:0
W 6
M v[192:195],v[164:167],a[56:59],v[192:195]
ds_read_b128 v[156:159], v221 offset:0
M v[192:195],v[168:171],a[64:67],v[192:195]
W 5
M v[196:199],v[168:171],a[68:71],0
M v[196:199],v[164:167],a[60:63],v[196:199]
W 2
M v[196:199],v[160:163],a[52:55],v[196:199]
ds_read_b128 v[160:163], v220 offset:128
v_cvt_pk_bf16_f32 v252, v184, v185
M v[196:199],v[172:175],a[76:79],v[196:199]
ds_read_b128 v[164:167], v221 offset:128
v_cvt_pk_bf16_f32 v253, v186, v187
M v[196:199],v[176:179],a[84:87],v[196:199]
ds_read_b128 v[168:171], v220 offset:256
v_accvgpr_write_b32 a136, v252
M v[196:199],v[180:183],a[92:95],v[196:199]
v_accvgpr_write_b32 a137, v253
v_cvt_pk_bf16_f32 v252, v188, v189
M v[192:195],v[180:183],a[88:91],v[192:195]
v_cvt_pk_bf16_f32 v253, v190, v191
v_accvgpr_write_b32 a140, v252
M v[192:195],v[176:179],a[80:83],v[192:195]
v_accvgpr_write_b32 a141, v253
W 2
M v[192:195],v[172:175],a[72:75],v[192:195]
ds_read_b128 v[172:175], v221 offset:256
M v[104:107],v[152:155],a[0:3],v[204:207]
ds_read_b128 v[176:179], v220 offset:6144
s_add_u32 s79, s3, 196608
s_add_u32 m0, s40, 24576
s_nop 0
buffer_load_dwordx4 v227, s[8:11], s79 offen nt lds
M v[104:107],v[156:159],a[8:11],v[104:107]
ds_read_b128 v[180:183], v221 offset:6144
W 3
M v[104:107],v[160:163],a[16:19],v[104:107]
M v[108:111],v[160:163],a[20:23],v[208:211]
M v[108:111],v[156:159],a[12:15],v[108:111]
M v[108:111],v[152:155],a[4:7],v[108:111]
ds_read_b128 v[152:155], v220 offset:6272
v_cvt_pk_bf16_f32 v252, v192, v193
M v[108:111],v[164:167],a[28:31],v[108:111]
ds_read_b128 v[156:159], v221 offset:6272
v_cvt_pk_bf16_f32 v253, v194, v195
M v[108:111],v[168:171],a[36:39],v[108:111]
ds_read_b128 v[160:163], v220 offset:6400
W 3
M v[108:111],v[172:175],a[44:47],v[108:111]
v_accvgpr_write_b32 a138, v252
v_accvgpr_write_b32 a139, v253
M v[104:107],v[172:175],a[40:43],v[104:107]
v_cvt_pk_bf16_f32 v252, v196, v197
v_cvt_pk_bf16_f32 v253, v198, v199
M v[104:107],v[168:171],a[32:35],v[104:107]
v_accvgpr_write_b32 a142, v252
v_accvgpr_write_b32 a143, v253
M v[104:107],v[164:167],a[24:27],v[104:107]
ds_read_b128 v[164:167], v221 offset:6400
W 2
M v[112:115],v[176:179],a[0:3],v[204:207]
ds_read_b128 v[168:171], v220 offset:12288
M v[112:115],v[180:183],a[8:11],v[112:115]
ds_read_b128 v[172:175], v221 offset:12288
M v[112:115],v[152:155],a[16:19],v[112:115]
M v[116:119],v[152:155],a[20:23],v[208:211]
v_max3_f32 v248, v104, v105, v106
v_max3_f32 v249, v108, v109, v110
M v[116:119],v[180:183],a[12:15],v[116:119]
v_max_f32 v248, v248, v107
v_max_f32 v249, v249, v111
M v[116:119],v[176:179],a[4:7],v[116:119]
ds_read_b128 v[176:179], v220 offset:12416
W 2
M v[116:119],v[156:159],a[28:31],v[116:119]
v_exp_f32 v200, v104
M v[116:119],v[160:163],a[36:39],v[116:119]
ds_read_b128 v[180:183], v221 offset:12416
v_add_u32 v216, 122880, v226
M v[116:119],v[164:167],a[44:47],v[116:119]
ds_read_b128 v[152:155], v220 offset:12544
buffer_load_dwordx4 v228, s[8:11], s79 offen offset:1024 nt lds
M v[112:115],v[164:167],a[40:43],v[112:115]
v_exp_f32 v201, v105
M v[112:115],v[160:163],a[32:35],v[112:115]
v_exp_f32 v202, v106
M v[112:115],v[156:159],a[24:27],v[112:115]
ds_read_b128 v[156:159], v221 offset:12544
v_cvt_pk_bf16_f32 v136, v200, v201
M v[120:123],v[168:171],a[0:3],v[204:207]
ds_read_b128 v[160:163], v220 offset:18432
W 3
M v[120:123],v[172:175],a[8:11],v[120:123]
v_exp_f32 v203, v107
M v[120:123],v[176:179],a[16:19],v[120:123]
ds_read_b128 v[164:167], v221 offset:18432
v_cvt_pk_bf16_f32 v137, v202, v203
M v[124:127],v[176:179],a[20:23],v[208:211]
v_exp_f32 v200, v108
M v[124:127],v[172:175],a[12:15],v[124:127]
v_exp_f32 v201, v109
M v[124:127],v[168:171],a[4:7],v[124:127]
ds_read_b128 v[168:171], v220 offset:18560
v_cvt_pk_bf16_f32 v140, v200, v201
M v[124:127],v[180:183],a[28:31],v[124:127]
ds_read_b128 v[172:175], v221 offset:18560
W 3
M v[124:127],v[152:155],a[36:39],v[124:127]
v_exp_f32 v202, v110
M v[124:127],v[156:159],a[44:47],v[124:127]
ds_read_b128 v[176:179], v220 offset:18688
v_max3_f32 v248, v248, v112, v113
M v[120:123],v[156:159],a[40:43],v[120:123]
v_exp_f32 v203, v111
M v[120:123],v[152:155],a[32:35],v[120:123]
v_cvt_pk_bf16_f32 v141, v202, v203
v_max3_f32 v249, v249, v116, v117
M v[120:123],v[180:183],a[24:27],v[120:123]
ds_read_b128 v[180:183], v221 offset:18688
v_xor_b32 v217, 32, v216
M v[128:131],v[160:163],a[0:3],v[204:207]
v_xor_b32 v218, 64, v216
W 2
M v[128:131],v[164:167],a[8:11],v[128:131]
v_xor_b32 v219, 96, v216
v_max3_f32 v248, v248, v114, v115
M v[128:131],v[168:171],a[16:19],v[128:131]
s_waitcnt vmcnt(8)
s_barrier
T v[152:153],v216,0
M v[132:135],v[168:171],a[20:23],v[208:211]
T v[154:155],v216,2048
v_max3_f32 v249, v249, v118, v119
M v[132:135],v[164:167],a[12:15],v[132:135]
T v[156:157],v216,4096
buffer_load_dwordx4 v229, s[8:11], s79 offen offset:2048 nt lds
M v[132:135],v[160:163],a[4:7],v[132:135]
T v[158:159],v216,6144
W 4
M v[132:135],v[172:175],a[28:31],v[132:135]
T v[160:161],v216,8192
T v[162:163],v216,10240
M v[132:135],v[176:179],a[36:39],v[132:135]
T v[164:165],v216,12288
M v[132:135],v[180:183],a[44:47],v[132:135]
T v[166:167],v216,14336
W 6
M v[128:131],v[180:183],a[40:43],v[128:131]
T v[168:169],v216,16384
v_exp_f32 v200, v112
M v[128:131],v[176:179],a[32:35],v[128:131]
T v[170:171],v216,18432
W 6
M v[128:131],v[172:175],a[24:27],v[128:131]
T v[172:173],v216,20480
v_exp_f32 v201, v113
M v[184:187],v[152:155],a[48:51],0
T v[174:175],v216,22528
v_cvt_pk_bf16_f32 v138, v200, v201
M v[184:187],v[156:159],a[56:59],v[184:187]
T v[176:177],v217,0
W 5
M v[184:187],v[160:163],a[64:67],v[184:187]
T v[178:179],v217,2048
s_add_u32 s77, s71, s42
M v[188:191],v[160:163],a[68:71],0
T v[180:181],v217,4096
v_exp_f32 v202, v114
M v[188:191],v[156:159],a[60:63],v[188:191]
T v[182:183],v217,6144
s_add_u32 s78, s77, 0
M v[188:191],v[152:155],a[52:55],v[188:191]
T v[152:153],v217,8192
v_exp_f32 v203, v115
M v[188:191],v[164:167],a[76:79],v[188:191]
T v[154:155],v217,10240
W 5
M v[188:191],v[168:171],a[84:87],v[188:191]
T v[156:157],v217,12288
v_cvt_pk_bf16_f32 v139, v202, v203
M v[188:191],v[172:175],a[92:95],v[188:191]
T v[158:159],v217,14336
v_exp_f32 v200, v116
M v[184:187],v[172:175],a[88:91],v[184:187]
T v[160:161],v217,16384
s_add_u32 m0, s40, 49152
M v[184:187],v[168:171],a[80:83],v[184:187]
T v[162:163],v217,18432
buffer_load_dwordx4 v230, s[20:23], s78 offen lds
M v[184:187],v[164:167],a[72:75],v[184:187]
T v[164:165],v217,20480
v_exp_f32 v201, v117
W 5
M v[192:195],v[176:179],a[48:51],0
T v[166:167],v217,22528
v_cvt_pk_bf16_f32 v142, v200, v201
M v[192:195],v[180:183],a[56:59],v[192:195]
T v[168:169],v218,0
buffer_load_dwordx4 v231, s[20:23], s78 offen offset:1024 lds
M v[192:195],v[152:155],a[64:67],v[192:195]
T v[170:171],v218,2048
v_exp_f32 v202, v118
M v[196:199],v[152:155],a[68:71],0
T v[172:173],v218,4096
v_max3_f32 v248, v248, v120, v121
M v[196:199],v[180:183],a[60:63],v[196:199]
T v[174:175],v218,6144
v_max3_f32 v249, v249, v124, v125
M v[196:199],v[176:179],a[52:55],v[196:199]
T v[176:177],v218,8192
v_exp_f32 v203, v119
W 5
M v[196:199],v[156:159],a[76:79],v[196:199]
T v[178:179],v218,10240
v_cvt_pk_bf16_f32 v143, v202, v203
M v[196:199],v[160:163],a[84:87],v[196:199]
T v[180:181],v218,12288
v_cvt_pk_bf16_f32 v252, v184, v185
v_cvt_pk_bf16_f32 v253, v186, v187
M v[196:199],v[164:167],a[92:95],v[196:199]
T v[182:183],v218,14336
v_accvgpr_write_b32 a144, v252
v_accvgpr_write_b32 a145, v253
M v[192:195],v[164:167],a[88:91],v[192:195]
T v[152:153],v218,16384
v_cvt_pk_bf16_f32 v252, v188, v189
v_cvt_pk_bf16_f32 v253, v190, v191
M v[192:195],v[160:163],a[80:83],v[192:195]
T v[154:155],v218,18432
v_accvgpr_write_b32 a148, v252
v_accvgpr_write_b32 a149, v253
M v[192:195],v[156:159],a[72:75],v[192:195]
T v[156:157],v218,20480
v_max3_f32 v248, v248, v122, v123
W 5
M v[184:187],v[168:171],a[48:51],0
T v[158:159],v218,22528
v_max3_f32 v249, v249, v126, v127
M v[184:187],v[172:175],a[56:59],v[184:187]
T v[160:161],v219,0
v_max3_f32 v248, v248, v128, v129
M v[184:187],v[176:179],a[64:67],v[184:187]
T v[162:163],v219,2048
v_max3_f32 v249, v249, v132, v133
M v[188:191],v[176:179],a[68:71],0
T v[164:165],v219,4096
v_max3_f32 v248, v248, v130, v131
M v[188:191],v[172:175],a[60:63],v[188:191]
T v[166:167],v219,6144
v_max3_f32 v249, v249, v134, v135
M v[188:191],v[168:171],a[52:55],v[188:191]
T v[168:169],v219,8192
v_cvt_pk_bf16_f32 v252, v192, v193
W 5
M v[188:191],v[180:183],a[76:79],v[188:191]
T v[170:171],v219,10240
v_cvt_pk_bf16_f32 v253, v194, v195
M v[188:191],v[152:155],a[84:87],v[188:191]
T v[172:173],v219,12288
v_accvgpr_write_b32 a146, v252
v_accvgpr_write_b32 a147, v253
M v[188:191],v[156:159],a[92:95],v[188:191]
T v[174:175],v219,14336
v_cvt_pk_bf16_f32 v252, v196, v197
M v[184:187],v[156:159],a[88:91],v[184:187]
T v[176:177],v219,16384
v_cvt_pk_bf16_f32 v253, v198, v199
v_accvgpr_write_b32 a150, v252
M v[184:187],v[152:155],a[80:83],v[184:187]
T v[178:179],v219,18432
v_accvgpr_write_b32 a151, v253
M v[184:187],v[180:183],a[72:75],v[184:187]
T v[180:181],v219,20480
v_max_f32 v250, v248, v249
W 5
M v[192:195],v[160:163],a[48:51],0
T v[182:183],v219,22528
v_cmp_lt_f32 vcc, s60, v250
M v[192:195],v[164:167],a[56:59],v[192:195]
s_nop 0
s_cbranch_vccnz LST1_2_%=
LRT1_2_%=:
v_add_u32 v220, 98304, v224
M v[192:195],v[168:171],a[64:67],v[192:195]
v_exp_f32 v200, v120
M v[196:199],v[168:171],a[68:71],0
v_xor_b32 v221, 32, v220
v_xor_b32 v222, 64, v220
M v[196:199],v[164:167],a[60:63],v[196:199]
v_xor_b32 v223, 96, v220
s_waitcnt vmcnt(8)
s_barrier
T v[152:153],v220,0
T v[154:155],v220,6144
v_exp_f32 v201, v121
M v[196:199],v[160:163],a[52:55],v[196:199]
T v[156:157],v221,0
v_exp_f32 v202, v122
W 3
M v[196:199],v[172:175],a[76:79],v[196:199]
T v[158:159],v221,6144
v_cvt_pk_bf16_f32 v144, v200, v201
M v[196:199],v[176:179],a[84:87],v[196:199]
T v[160:161],v222,0
buffer_load_dwordx4 v232, s[20:23], s78 offen offset:2048 lds
M v[196:199],v[180:183],a[92:95],v[196:199]
T v[162:163],v222,6144
v_exp_f32 v203, v123
M v[192:195],v[180:183],a[88:91],v[192:195]
T v[164:165],v223,0
v_cvt_pk_bf16_f32 v145, v202, v203
M v[192:195],v[176:179],a[80:83],v[192:195]
T v[166:167],v223,6144
W 5
M v[192:195],v[172:175],a[72:75],v[192:195]
T v[168:169],v220,128
v_exp_f32 v200, v124
M v[0:3],v[152:155],v[136:139],v[0:3]
T v[170:171],v220,6272
s_add_u32 s79, s3, 196608
s_add_u32 m0, s40, 73728
s_nop 0
buffer_load_dwordx4 v227, s[12:15], s79 offen nt lds
v_exp_f32 v201, v125
M v[4:7],v[152:155],v[140:143],v[4:7]
T v[172:173],v221,128
v_cvt_pk_bf16_f32 v148, v200, v201
M v[96:99],v[244:247],v[136:139],v[96:99]
T v[174:175],v221,6272
W 5
M v[100:103],v[244:247],v[140:143],v[100:103]
T v[176:177],v222,128
v_exp_f32 v202, v126
M v[8:11],v[156:159],v[136:139],v[8:11]
T v[178:179],v222,6272
W 5
M v[12:15],v[156:159],v[140:143],v[12:15]
T v[180:181],v223,128
v_exp_f32 v203, v127
M v[16:19],v[160:163],v[136:139],v[16:19]
T v[182:183],v223,6272
v_cvt_pk_bf16_f32 v149, v202, v203
M v[20:23],v[160:163],v[140:143],v[20:23]
T v[152:153],v220,256
W 5
M v[24:27],v[164:167],v[136:139],v[24:27]
T v[154:155],v220,6400
v_exp_f32 v200, v128
M v[28:31],v[164:167],v[140:143],v[28:31]
T v[156:157],v221,256
v_exp_f32 v201, v129
M v[32:35],v[168:171],v[136:139],v[32:35]
T v[158:159],v221,6400
v_cvt_pk_bf16_f32 v146, v200, v201
M v[36:39],v[168:171],v[140:143],v[36:39]
T v[160:161],v222,256
W 5
M v[40:43],v[172:175],v[136:139],v[40:43]
T v[162:163],v222,6400
v_exp_f32 v202, v130
M v[44:47],v[172:175],v[140:143],v[44:47]
T v[164:165],v223,256
W 5
M v[48:51],v[176:179],v[136:139],v[48:51]
T v[166:167],v223,6400
v_exp_f32 v203, v131
M v[52:55],v[176:179],v[140:143],v[52:55]
T v[168:169],v220,12288
v_cvt_pk_bf16_f32 v147, v202, v203
M v[56:59],v[180:183],v[136:139],v[56:59]
T v[170:171],v220,18432
W 5
M v[60:63],v[180:183],v[140:143],v[60:63]
T v[172:173],v221,12288
v_exp_f32 v200, v132
M v[64:67],v[152:155],v[136:139],v[64:67]
T v[174:175],v221,18432
v_exp_f32 v201, v133
M v[68:71],v[152:155],v[140:143],v[68:71]
T v[176:177],v222,12288
v_cvt_pk_bf16_f32 v150, v200, v201
M v[72:75],v[156:159],v[136:139],v[72:75]
T v[178:179],v222,18432
buffer_load_dwordx4 v228, s[12:15], s79 offen offset:1024 nt lds
M v[76:79],v[156:159],v[140:143],v[76:79]
T v[180:181],v223,12288
v_exp_f32 v202, v134
W 5
M v[80:83],v[160:163],v[136:139],v[80:83]
T v[182:183],v223,18432
v_cvt_pk_bf16_f32 v252, v184, v185
M v[84:87],v[160:163],v[140:143],v[84:87]
T v[152:153],v220,12416
v_cvt_pk_bf16_f32 v253, v186, v187
M v[88:91],v[164:167],v[136:139],v[88:91]
T v[154:155],v220,18560
v_accvgpr_write_b32 a152, v252
v_exp_f32 v203, v135
M v[92:95],v[164:167],v[140:143],v[92:95]
T v[156:157],v221,12416
v_cvt_pk_bf16_f32 v151, v202, v203
M v[0:3],v[168:171],v[144:147],v[0:3]
T v[158:159],v221,18560
v_accvgpr_write_b32 a153, v253
M v[4:7],v[168:171],v[148:151],v[4:7]
T v[160:161],v222,12416
v_cvt_pk_bf16_f32 v252, v188, v189
M v[96:99],v[244:247],v[144:147],v[96:99]
T v[162:163],v222,18560
v_cvt_pk_bf16_f32 v253, v190, v191
M v[100:103],v[244:247],v[148:151],v[100:103]
T v[164:165],v223,12416
W 5
M v[8:11],v[172:175],v[144:147],v[8:11]
T v[166:167],v223,18560
buffer_load_dword v215, v214, s[28:31], 0 offen sc1
M v[12:15],v[172:175],v[148:151],v[12:15]
T v[168:169],v220,12544
v_accvgpr_write_b32 a156, v252
M v[16:19],v[176:179],v[144:147],v[16:19]
T v[170:171],v220,18688
v_accvgpr_write_b32 a157, v253
M v[20:23],v[176:179],v[148:151],v[20:23]
T v[172:173],v221,12544
v_cvt_pk_bf16_f32 v252, v192, v193
M v[24:27],v[180:183],v[144:147],v[24:27]
T v[174:175],v221,18688
v_cvt_pk_bf16_f32 v253, v194, v195
M v[28:31],v[180:183],v[148:151],v[28:31]
T v[176:177],v222,12544
v_accvgpr_write_b32 a154, v252
M v[32:35],v[152:155],v[144:147],v[32:35]
T v[178:179],v222,18688
v_accvgpr_write_b32 a155, v253
M v[36:39],v[152:155],v[148:151],v[36:39]
T v[180:181],v223,12544
W 5
M v[40:43],v[156:159],v[144:147],v[40:43]
T v[182:183],v223,18688
v_add_u32 v216, 0, v226
M v[44:47],v[156:159],v[148:151],v[44:47]
v_xor_b32 v217, 32, v216
v_xor_b32 v218, 64, v216
M v[48:51],v[160:163],v[144:147],v[48:51]
buffer_load_dwordx4 v229, s[12:15], s79 offen offset:2048 nt lds
v_xor_b32 v219, 96, v216
M v[52:55],v[160:163],v[148:151],v[52:55]
s_waitcnt vmcnt(10)
s_barrier
T v[152:153],v216,0
M v[56:59],v[164:167],v[144:147],v[56:59]
T v[154:155],v216,2048
v_cvt_pk_bf16_f32 v252, v196, v197
M v[60:63],v[164:167],v[148:151],v[60:63]
T v[156:157],v216,4096
v_cvt_pk_bf16_f32 v253, v198, v199
M v[64:67],v[168:171],v[144:147],v[64:67]
T v[158:159],v216,6144
v_accvgpr_write_b32 a158, v252
M v[68:71],v[168:171],v[148:151],v[68:71]
T v[160:161],v216,8192
W 5
M v[72:75],v[172:175],v[144:147],v[72:75]
T v[162:163],v216,10240
v_accvgpr_write_b32 a159, v253
M v[76:79],v[172:175],v[148:151],v[76:79]
T v[164:165],v216,12288
W 5
M v[80:83],v[176:179],v[144:147],v[80:83]
T v[166:167],v216,14336
M v[84:87],v[176:179],v[148:151],v[84:87]
T v[168:169],v216,16384
T v[170:171],v216,18432
M v[88:91],v[180:183],v[144:147],v[88:91]
T v[172:173],v216,20480
T v[174:175],v216,22528
M v[92:95],v[180:183],v[148:151],v[92:95]
T v[176:177],v217,0
W 7
M v[184:187],v[152:155],a[48:51],0
T v[178:179],v217,2048
M v[184:187],v[156:159],a[56:59],v[184:187]
T v[180:181],v217,4096
W 7
M v[184:187],v[160:163],a[64:67],v[184:187]
T v[182:183],v217,6144
W 6
M v[188:191],v[160:163],a[68:71],0
W 4
M v[188:191],v[156:159],a[60:63],v[188:191]
M v[188:191],v[152:155],a[52:55],v[188:191]
T v[152:153],v217,8192
T v[154:155],v217,10240
M v[188:191],v[164:167],a[76:79],v[188:191]
T v[156:157],v217,12288
s_add_u32 s79, s4, 196608
M v[188:191],v[168:171],a[84:87],v[188:191]
T v[158:159],v217,14336
s_add_u32 m0, s40, 122880
M v[188:191],v[172:175],a[92:95],v[188:191]
T v[160:161],v217,16384
buffer_load_dwordx4 v227, s[8:11], s79 offen nt lds
M v[184:187],v[172:175],a[88:91],v[184:187]
T v[162:163],v217,18432
W 6
M v[184:187],v[168:171],a[80:83],v[184:187]
W 4
M v[184:187],v[164:167],a[72:75],v[184:187]
T v[164:165],v217,20480
T v[166:167],v217,22528
M v[192:195],v[176:179],a[48:51],0
T v[168:169],v218,0
W 5
M v[192:195],v[180:183],a[56:59],v[192:195]
T v[170:171],v218,2048
M v[192:195],v[152:155],a[64:67],v[192:195]
T v[172:173],v218,4096
W 5
M v[196:199],v[152:155],a[68:71],0
T v[174:175],v218,6144
M v[196:199],v[180:183],a[60:63],v[196:199]
W 4
M v[196:199],v[176:179],a[52:55],v[196:199]
T v[176:177],v218,8192
v_cvt_pk_bf16_f32 v252, v184, v185
M v[196:199],v[156:159],a[76:79],v[196:199]
T v[178:179],v218,10240
v_cvt_pk_bf16_f32 v253, v186, v187
M v[196:199],v[160:163],a[84:87],v[196:199]
T v[180:181],v218,12288
v_accvgpr_write_b32 a160, v252
M v[196:199],v[164:167],a[92:95],v[196:199]
T v[182:183],v218,14336
v_accvgpr_write_b32 a161, v253
M v[192:195],v[164:167],a[88:91],v[192:195]
T v[152:153],v218,16384
v_cvt_pk_bf16_f32 v252, v188, v189
v_cvt_pk_bf16_f32 v253, v190, v191
M v[192:195],v[160:163],a[80:83],v[192:195]
T v[154:155],v218,18432
v_accvgpr_write_b32 a164, v252
v_accvgpr_write_b32 a165, v253
M v[192:195],v[156:159],a[72:75],v[192:195]
T v[156:157],v218,20480
W 5
M v[184:187],v[168:171],a[48:51],0
T v[158:159],v218,22528
v_add_u32 v220, 24576, v225
M v[184:187],v[172:175],a[56:59],v[184:187]
T v[160:161],v219,0
v_xor_b32 v221, 64, v220
M v[184:187],v[176:179],a[64:67],v[184:187]
T v[162:163],v219,2048
W 5
M v[188:191],v[176:179],a[68:71],0
T v[164:165],v219,4096
T v[166:167],v219,6144
M v[188:191],v[172:175],a[60:63],v[188:191]
buffer_load_dwordx4 v228, s[8:11], s79 offen offset:1024 nt lds
W 5
M v[188:191],v[168:171],a[52:55],v[188:191]
T v[168:169],v219,8192
v_cvt_pk_bf16_f32 v252, v192, v193
M v[188:191],v[180:183],a[76:79],v[188:191]
T v[170:171],v219,10240
v_cvt_pk_bf16_f32 v253, v194, v195
M v[188:191],v[152:155],a[84:87],v[188:191]
T v[172:173],v219,12288
v_accvgpr_write_b32 a162, v252
W 5
M v[188:191],v[156:159],a[92:95],v[188:191]
T v[174:175],v219,14336
v_accvgpr_write_b32 a163, v253
M v[184:187],v[156:159],a[88:91],v[184:187]
T v[176:177],v219,16384
v_cvt_pk_bf16_f32 v252, v196, v197
v_cvt_pk_bf16_f32 v253, v198, v199
M v[184:187],v[152:155],a[80:83],v[184:187]
T v[178:179],v219,18432
v_accvgpr_write_b32 a166, v252
v_accvgpr_write_b32 a167, v253
M v[184:187],v[180:183],a[72:75],v[184:187]
T v[180:181],v219,20480
T v[182:183],v219,22528
M v[192:195],v[160:163],a[48:51],0
s_waitcnt vmcnt(9)
s_barrier
ds_read_b128 v[152:155], v220 offset:0
W 6
M v[192:195],v[164:167],a[56:59],v[192:195]
ds_read_b128 v[156:159], v221 offset:0
M v[192:195],v[168:171],a[64:67],v[192:195]
W 5
M v[196:199],v[168:171],a[68:71],0
M v[196:199],v[164:167],a[60:63],v[196:199]
W 2
M v[196:199],v[160:163],a[52:55],v[196:199]
ds_read_b128 v[160:163], v220 offset:128
v_cvt_pk_bf16_f32 v252, v184, v185
M v[196:199],v[172:175],a[76:79],v[196:199]
ds_read_b128 v[164:167], v221 offset:128
v_cvt_pk_bf16_f32 v253, v186, v187
M v[196:199],v[176:179],a[84:87],v[196:199]
ds_read_b128 v[168:171], v220 offset:256
v_accvgpr_write_b32 a168, v252
M v[196:199],v[180:183],a[92:95],v[196:199]
v_accvgpr_write_b32 a169, v253
v_cvt_pk_bf16_f32 v252, v188, v189
M v[192:195],v[180:183],a[88:91],v[192:195]
v_cvt_pk_bf16_f32 v253, v190, v191
v_accvgpr_write_b32 a172, v252
M v[192:195],v[176:179],a[80:83],v[192:195]
v_accvgpr_write_b32 a173, v253
W 2
M v[192:195],v[172:175],a[72:75],v[192:195]
ds_read_b128 v[172:175], v221 offset:256
M v[104:107],v[152:155],a[0:3],v[204:207]
ds_read_b128 v[176:179], v220 offset:6144
buffer_load_dwordx4 v229, s[8:11], s79 offen offset:2048 nt lds
M v[104:107],v[156:159],a[8:11],v[104:107]
ds_read_b128 v[180:183], v221 offset:6144
s_add_u32 s77, s96, s42
M v[104:107],v[160:163],a[16:19],v[104:107]
s_add_u32 s78, s77, 0
s_add_u32 m0, s40, 98304
M v[108:111],v[160:163],a[20:23],v[208:211]
buffer_load_dwordx4 v230, s[20:23], s78 offen lds
W 3
M v[108:111],v[156:159],a[12:15],v[108:111]
M v[108:111],v[152:155],a[4:7],v[108:111]
ds_read_b128 v[152:155], v220 offset:6272
v_cvt_pk_bf16_f32 v252, v192, v193
M v[108:111],v[164:167],a[28:31],v[108:111]
ds_read_b128 v[156:159], v221 offset:6272
buffer_load_dwordx4 v231, s[20:23], s78 offen offset:1024 lds
M v[108:111],v[168:171],a[36:39],v[108:111]
ds_read_b128 v[160:163], v220 offset:6400
W 3
M v[108:111],v[172:175],a[44:47],v[108:111]
v_cvt_pk_bf16_f32 v253, v194, v195
v_accvgpr_write_b32 a170, v252
M v[104:107],v[172:175],a[40:43],v[104:107]
buffer_load_dwordx4 v232, s[20:23], s78 offen offset:2048 lds
v_accvgpr_write_b32 a171, v253
M v[104:107],v[168:171],a[32:35],v[104:107]
v_cvt_pk_bf16_f32 v252, v196, v197
v_cvt_pk_bf16_f32 v253, v198, v199
M v[104:107],v[164:167],a[24:27],v[104:107]
ds_read_b128 v[164:167], v221 offset:6400
v_accvgpr_write_b32 a174, v252
M v[112:115],v[176:179],a[0:3],v[204:207]
ds_read_b128 v[168:171], v220 offset:12288
v_accvgpr_write_b32 a175, v253
M v[112:115],v[180:183],a[8:11],v[112:115]
ds_read_b128 v[172:175], v221 offset:12288
W 3
M v[112:115],v[152:155],a[16:19],v[112:115]
M v[116:119],v[152:155],a[20:23],v[208:211]
v_max3_f32 v248, v104, v105, v106
v_max3_f32 v249, v108, v109, v110
M v[116:119],v[180:183],a[12:15],v[116:119]
v_max_f32 v248, v248, v107
v_max_f32 v249, v249, v111
M v[116:119],v[176:179],a[4:7],v[116:119]
ds_read_b128 v[176:179], v220 offset:12416
W 2
M v[116:119],v[156:159],a[28:31],v[116:119]
v_exp_f32 v200, v104
M v[116:119],v[160:163],a[36:39],v[116:119]
ds_read_b128 v[180:183], v221 offset:12416
v_add_u32 v216, 49152, v226
M v[116:119],v[164:167],a[44:47],v[116:119]
ds_read_b128 v[152:155], v220 offset:12544
v_xor_b32 v217, 32, v216
M v[112:115],v[164:167],a[40:43],v[112:115]
v_exp_f32 v201, v105
M v[112:115],v[160:163],a[32:35],v[112:115]
v_exp_f32 v202, v106
M v[112:115],v[156:159],a[24:27],v[112:115]
ds_read_b128 v[156:159], v221 offset:12544
v_cvt_pk_bf16_f32 v136, v200, v201
M v[120:123],v[168:171],a[0:3],v[204:207]
ds_read_b128 v[160:163], v220 offset:18432
W 3
M v[120:123],v[172:175],a[8:11],v[120:123]
v_exp_f32 v203, v107
M v[120:123],v[176:179],a[16:19],v[120:123]
ds_read_b128 v[164:167], v221 offset:18432
v_cvt_pk_bf16_f32 v137, v202, v203
M v[124:127],v[176:179],a[20:23],v[208:211]
v_exp_f32 v200, v108
M v[124:127],v[172:175],a[12:15],v[124:127]
v_exp_f32 v201, v109
M v[124:127],v[168:171],a[4:7],v[124:127]
ds_read_b128 v[168:171], v220 offset:18560
v_cvt_pk_bf16_f32 v140, v200, v201
M v[124:127],v[180:183],a[28:31],v[124:127]
ds_read_b128 v[172:175], v221 offset:18560
W 3
M v[124:127],v[152:155],a[36:39],v[124:127]
v_exp_f32 v202, v110
M v[124:127],v[156:159],a[44:47],v[124:127]
ds_read_b128 v[176:179], v220 offset:18688
v_max3_f32 v248, v248, v112, v113
M v[120:123],v[156:159],a[40:43],v[120:123]
v_exp_f32 v203, v111
M v[120:123],v[152:155],a[32:35],v[120:123]
v_cvt_pk_bf16_f32 v141, v202, v203
v_max3_f32 v249, v249, v116, v117
M v[120:123],v[180:183],a[24:27],v[120:123]
ds_read_b128 v[180:183], v221 offset:18688
v_xor_b32 v218, 64, v216
M v[128:131],v[160:163],a[0:3],v[204:207]
v_xor_b32 v219, 96, v216
W 2
M v[128:131],v[164:167],a[8:11],v[128:131]
s_waitcnt vmcnt(10)
s_barrier
T v[152:153],v216,0
M v[128:131],v[168:171],a[16:19],v[128:131]
T v[154:155],v216,2048
v_max3_f32 v248, v248, v114, v115
M v[132:135],v[168:171],a[20:23],v[208:211]
T v[156:157],v216,4096
v_max3_f32 v249, v249, v118, v119
M v[132:135],v[164:167],a[12:15],v[132:135]
T v[158:159],v216,6144
W 4
M v[132:135],v[160:163],a[4:7],v[132:135]
T v[160:161],v216,8192
M v[132:135],v[172:175],a[28:31],v[132:135]
T v[162:163],v216,10240
M v[132:135],v[176:179],a[36:39],v[132:135]
T v[164:165],v216,12288
W 5
M v[132:135],v[180:183],a[44:47],v[132:135]
T v[166:167],v216,14336
M v[128:131],v[180:183],a[40:43],v[128:131]
T v[168:169],v216,16384
v_exp_f32 v200, v112
M v[128:131],v[176:179],a[32:35],v[128:131]
T v[170:171],v216,18432
W 5
M v[128:131],v[172:175],a[24:27],v[128:131]
T v[172:173],v216,20480
v_exp_f32 v201, v113
M v[184:187],v[152:155],a[48:51],0
T v[174:175],v216,22528
s_add_u32 s79, s4, 196608
s_add_u32 m0, s40, 0
s_nop 0
buffer_load_dwordx4 v227, s[12:15], s79 offen nt lds
M v[184:187],v[156:159],a[56:59],v[184:187]
T v[176:177],v217,0
W 5
M v[184:187],v[160:163],a[64:67],v[184:187]
T v[178:179],v217,2048
v_cvt_pk_bf16_f32 v138, v200, v201
M v[188:191],v[160:163],a[68:71],0
T v[180:181],v217,4096
v_exp_f32 v202, v114
M v[188:191],v[156:159],a[60:63],v[188:191]
T v[182:183],v217,6144
W 5
M v[188:191],v[152:155],a[52:55],v[188:191]
T v[152:153],v217,8192
v_exp_f32 v203, v115
M v[188:191],v[164:167],a[76:79],v[188:191]
T v[154:155],v217,10240
v_cvt_pk_bf16_f32 v139, v202, v203
M v[188:191],v[168:171],a[84:87],v[188:191]
T v[156:157],v217,12288
W 5
M v[188:191],v[172:175],a[92:95],v[188:191]
T v[158:159],v217,14336
v_exp_f32 v200, v116
M v[184:187],v[172:175],a[88:91],v[184:187]
T v[160:161],v217,16384
W 5
M v[184:187],v[168:171],a[80:83],v[184:187]
T v[162:163],v217,18432
v_exp_f32 v201, v117
M v[184:187],v[164:167],a[72:75],v[184:187]
T v[164:165],v217,20480
v_cvt_pk_bf16_f32 v142, v200, v201
M v[192:195],v[176:179],a[48:51],0
T v[166:167],v217,22528
W 5
M v[192:195],v[180:183],a[56:59],v[192:195]
T v[168:169],v218,0
M v[192:195],v[152:155],a[64:67],v[192:195]
T v[170:171],v218,2048
v_exp_f32 v202, v118
M v[196:199],v[152:155],a[68:71],0
T v[172:173],v218,4096
v_max3_f32 v248, v248, v120, v121
M v[196:199],v[180:183],a[60:63],v[196:199]
T v[174:175],v218,6144
v_max3_f32 v249, v249, v124, v125
M v[196:199],v[176:179],a[52:55],v[196:199]
T v[176:177],v218,8192
v_exp_f32 v203, v119
W 5
M v[196:199],v[156:159],a[76:79],v[196:199]
T v[178:179],v218,10240
v_cvt_pk_bf16_f32 v143, v202, v203
M v[196:199],v[160:163],a[84:87],v[196:199]
T v[180:181],v218,12288
v_cvt_pk_bf16_f32 v252, v184, v185
v_cvt_pk_bf16_f32 v253, v186, v187
M v[196:199],v[164:167],a[92:95],v[196:199]
T v[182:183],v218,14336
buffer_load_dwordx4 v228, s[12:15], s79 offen offset:1024 nt lds
v_accvgpr_write_b32 a176, v252
v_accvgpr_write_b32 a177, v253
M v[192:195],v[164:167],a[88:91],v[192:195]
T v[152:153],v218,16384
v_cvt_pk_bf16_f32 v252, v188, v189
v_cvt_pk_bf16_f32 v253, v190, v191
M v[192:195],v[160:163],a[80:83],v[192:195]
T v[154:155],v218,18432
v_accvgpr_write_b32 a180, v252
v_accvgpr_write_b32 a181, v253
M v[192:195],v[156:159],a[72:75],v[192:195]
T v[156:157],v218,20480
v_max3_f32 v248, v248, v122, v123
W 5
M v[184:187],v[168:171],a[48:51],0
T v[158:159],v218,22528
v_max3_f32 v249, v249, v126, v127
M v[184:187],v[172:175],a[56:59],v[184:187]
T v[160:161],v219,0
v_max3_f32 v248, v248, v128, v129
M v[184:187],v[176:179],a[64:67],v[184:187]
T v[162:163],v219,2048
v_max3_f32 v249, v249, v132, v133
M v[188:191],v[176:179],a[68:71],0
T v[164:165],v219,4096
v_max3_f32 v248, v248, v130, v131
M v[188:191],v[172:175],a[60:63],v[188:191]
T v[166:167],v219,6144
v_max3_f32 v249, v249, v134, v135
M v[188:191],v[168:171],a[52:55],v[188:191]
T v[168:169],v219,8192
v_cvt_pk_bf16_f32 v252, v192, v193
W 5
M v[188:191],v[180:183],a[76:79],v[188:191]
T v[170:171],v219,10240
v_cvt_pk_bf16_f32 v253, v194, v195
M v[188:191],v[152:155],a[84:87],v[188:191]
T v[172:173],v219,12288
v_accvgpr_write_b32 a178, v252
v_accvgpr_write_b32 a179, v253
M v[188:191],v[156:159],a[92:95],v[188:191]
T v[174:175],v219,14336
v_cvt_pk_bf16_f32 v252, v196, v197
M v[184:187],v[156:159],a[88:91],v[184:187]
T v[176:177],v219,16384
v_cvt_pk_bf16_f32 v253, v198, v199
v_accvgpr_write_b32 a182, v252
M v[184:187],v[152:155],a[80:83],v[184:187]
T v[178:179],v219,18432
v_accvgpr_write_b32 a183, v253
M v[184:187],v[180:183],a[72:75],v[184:187]
T v[180:181],v219,20480
v_max_f32 v250, v248, v249
W 5
M v[192:195],v[160:163],a[48:51],0
T v[182:183],v219,22528
v_cmp_lt_f32 vcc, s60, v250
M v[192:195],v[164:167],a[56:59],v[192:195]
s_nop 0
s_cbranch_vccnz LST1_3_%=
LRT1_3_%=:
v_add_u32 v220, 73728, v224
M v[192:195],v[168:171],a[64:67],v[192:195]
v_exp_f32 v200, v120
M v[196:199],v[168:171],a[68:71],0
v_xor_b32 v221, 32, v220
v_xor_b32 v222, 64, v220
M v[196:199],v[164:167],a[60:63],v[196:199]
v_xor_b32 v223, 96, v220
s_waitcnt vmcnt(8)
s_barrier
T v[152:153],v220,0
T v[154:155],v220,6144
v_exp_f32 v201, v121
M v[196:199],v[160:163],a[52:55],v[196:199]
T v[156:157],v221,0
v_exp_f32 v202, v122
W 3
M v[196:199],v[172:175],a[76:79],v[196:199]
T v[158:159],v221,6144
buffer_load_dwordx4 v229, s[12:15], s79 offen offset:2048 nt lds
M v[196:199],v[176:179],a[84:87],v[196:199]
T v[160:161],v222,0
v_cvt_pk_bf16_f32 v144, v200, v201
M v[196:199],v[180:183],a[92:95],v[196:199]
T v[162:163],v222,6144
v_exp_f32 v203, v123
M v[192:195],v[180:183],a[88:91],v[192:195]
T v[164:165],v223,0
v_cvt_pk_bf16_f32 v145, v202, v203
M v[192:195],v[176:179],a[80:83],v[192:195]
T v[166:167],v223,6144
W 5
M v[192:195],v[172:175],a[72:75],v[192:195]
T v[168:169],v220,128
v_exp_f32 v200, v124
M v[0:3],v[152:155],v[136:139],v[0:3]
T v[170:171],v220,6272
v_exp_f32 v201, v125
M v[4:7],v[152:155],v[140:143],v[4:7]
T v[172:173],v221,128
v_cvt_pk_bf16_f32 v148, v200, v201
M v[96:99],v[244:247],v[136:139],v[96:99]
T v[174:175],v221,6272
W 5
M v[100:103],v[244:247],v[140:143],v[100:103]
T v[176:177],v222,128
v_exp_f32 v202, v126
M v[8:11],v[156:159],v[136:139],v[8:11]
T v[178:179],v222,6272
W 5
M v[12:15],v[156:159],v[140:143],v[12:15]
T v[180:181],v223,128
v_exp_f32 v203, v127
M v[16:19],v[160:163],v[136:139],v[16:19]
T v[182:183],v223,6272
v_cvt_pk_bf16_f32 v149, v202, v203
M v[20:23],v[160:163],v[140:143],v[20:23]
T v[152:153],v220,256
W 5
M v[24:27],v[164:167],v[136:139],v[24:27]
T v[154:155],v220,6400
v_exp_f32 v200, v128
M v[28:31],v[164:167],v[140:143],v[28:31]
T v[156:157],v221,256
v_exp_f32 v201, v129
M v[32:35],v[168:171],v[136:139],v[32:35]
T v[158:159],v221,6400
v_cvt_pk_bf16_f32 v146, v200, v201
M v[36:39],v[168:171],v[140:143],v[36:39]
T v[160:161],v222,256
W 5
M v[40:43],v[172:175],v[136:139],v[40:43]
T v[162:163],v222,6400
s_add_u32 s79, s5, 196608
v_exp_f32 v202, v130
M v[44:47],v[172:175],v[140:143],v[44:47]
T v[164:165],v223,256
s_add_u32 m0, s40, 24576
M v[48:51],v[176:179],v[136:139],v[48:51]
T v[166:167],v223,6400
buffer_load_dwordx4 v227, s[8:11], s79 offen nt lds
v_exp_f32 v203, v131
M v[52:55],v[176:179],v[140:143],v[52:55]
T v[168:169],v220,12288
v_cvt_pk_bf16_f32 v147, v202, v203
M v[56:59],v[180:183],v[136:139],v[56:59]
T v[170:171],v220,18432
W 5
M v[60:63],v[180:183],v[140:143],v[60:63]
T v[172:173],v221,12288
v_exp_f32 v200, v132
M v[64:67],v[152:155],v[136:139],v[64:67]
T v[174:175],v221,18432
v_exp_f32 v201, v133
M v[68:71],v[152:155],v[140:143],v[68:71]
T v[176:177],v222,12288
v_cvt_pk_bf16_f32 v150, v200, v201
M v[72:75],v[156:159],v[136:139],v[72:75]
T v[178:179],v222,18432
v_add_u32 v216, 122880, v225
M v[76:79],v[156:159],v[140:143],v[76:79]
T v[180:181],v223,12288
v_exp_f32 v202, v134
W 5
M v[80:83],v[160:163],v[136:139],v[80:83]
T v[182:183],v223,18432
v_cvt_pk_bf16_f32 v252, v184, v185
M v[84:87],v[160:163],v[140:143],v[84:87]
T v[152:153],v220,12416
v_cvt_pk_bf16_f32 v253, v186, v187
M v[88:91],v[164:167],v[136:139],v[88:91]
T v[154:155],v220,18560
v_accvgpr_write_b32 a184, v252
v_exp_f32 v203, v135
M v[92:95],v[164:167],v[140:143],v[92:95]
T v[156:157],v221,12416
v_cvt_pk_bf16_f32 v151, v202, v203
M v[0:3],v[168:171],v[144:147],v[0:3]
T v[158:159],v221,18560
v_accvgpr_write_b32 a185, v253
M v[4:7],v[168:171],v[148:151],v[4:7]
T v[160:161],v222,12416
v_cvt_pk_bf16_f32 v252, v188, v189
M v[96:99],v[244:247],v[144:147],v[96:99]
T v[162:163],v222,18560
v_cvt_pk_bf16_f32 v253, v190, v191
M v[100:103],v[244:247],v[148:151],v[100:103]
T v[164:165],v223,12416
W 5
M v[8:11],v[172:175],v[144:147],v[8:11]
T v[166:167],v223,18560
v_accvgpr_write_b32 a188, v252
M v[12:15],v[172:175],v[148:151],v[12:15]
T v[168:169],v220,12544
v_accvgpr_write_b32 a189, v253
M v[16:19],v[176:179],v[144:147],v[16:19]
T v[170:171],v220,18688
v_cvt_pk_bf16_f32 v252, v192, v193
M v[20:23],v[176:179],v[148:151],v[20:23]
T v[172:173],v221,12544
v_cvt_pk_bf16_f32 v253, v194, v195
M v[24:27],v[180:183],v[144:147],v[24:27]
T v[174:175],v221,18688
buffer_load_dwordx4 v228, s[8:11], s79 offen offset:1024 nt lds
M v[28:31],v[180:183],v[148:151],v[28:31]
T v[176:177],v222,12544
v_accvgpr_write_b32 a186, v252
M v[32:35],v[152:155],v[144:147],v[32:35]
T v[178:179],v222,18688
v_accvgpr_write_b32 a187, v253
M v[36:39],v[152:155],v[148:151],v[36:39]
T v[180:181],v223,12544
W 5
M v[40:43],v[156:159],v[144:147],v[40:43]
T v[182:183],v223,18688
v_xor_b32 v217, 64, v216
M v[44:47],v[156:159],v[148:151],v[44:47]
s_waitcnt vmcnt(8)
s_barrier
ds_read_b128 v[152:155], v216 offset:0
M v[48:51],v[160:163],v[144:147],v[48:51]
ds_read_b128 v[156:159], v217 offset:0
v_cvt_pk_bf16_f32 v252, v196, v197
M v[52:55],v[160:163],v[148:151],v[52:55]
ds_read_b128 v[160:163], v216 offset:128
v_cvt_pk_bf16_f32 v253, v198, v199
M v[56:59],v[164:167],v[144:147],v[56:59]
v_accvgpr_write_b32 a190, v252
v_accvgpr_write_b32 a191, v253
M v[60:63],v[164:167],v[148:151],v[60:63]
W 3
M v[64:67],v[168:171],v[144:147],v[64:67]
M v[68:71],v[168:171],v[148:151],v[68:71]
ds_read_b128 v[164:167], v217 offset:128
W 2
M v[72:75],v[172:175],v[144:147],v[72:75]
ds_read_b128 v[168:171], v216 offset:256
M v[76:79],v[172:175],v[148:151],v[76:79]
ds_read_b128 v[172:175], v217 offset:256
M v[80:83],v[176:179],v[144:147],v[80:83]
M v[84:87],v[176:179],v[148:151],v[84:87]
M v[88:91],v[180:183],v[144:147],v[88:91]
W 2
M v[92:95],v[180:183],v[148:151],v[92:95]
ds_read_b128 v[176:179], v216 offset:6144
M v[104:107],v[152:155],a[0:3],v[204:207]
ds_read_b128 v[180:183], v217 offset:6144
W 2
M v[104:107],v[156:159],a[8:11],v[104:107]
M v[104:107],v[160:163],a[16:19],v[104:107]
buffer_load_dwordx4 v229, s[8:11], s79 offen offset:2048 nt lds
s_add_u32 s77, s97, s42
M v[108:111],v[160:163],a[20:23],v[208:211]
s_add_u32 s78, s77, 0
s_add_u32 m0, s40, 49152
M v[108:111],v[156:159],a[12:15],v[108:111]
buffer_load_dwordx4 v230, s[20:23], s78 offen lds
M v[108:111],v[152:155],a[4:7],v[108:111]
ds_read_b128 v[152:155], v216 offset:6272
W 1
M v[108:111],v[164:167],a[28:31],v[108:111]
ds_read_b128 v[156:159], v217 offset:6272
M v[108:111],v[168:171],a[36:39],v[108:111]
ds_read_b128 v[160:163], v216 offset:6400
buffer_load_dwordx4 v231, s[20:23], s78 offen offset:1024 lds
M v[108:111],v[172:175],a[44:47],v[108:111]
M v[104:107],v[172:175],a[40:43],v[104:107]
M v[104:107],v[168:171],a[32:35],v[104:107]
buffer_load_dwordx4 v232, s[20:23], s78 offen offset:2048 lds
M v[104:107],v[164:167],a[24:27],v[104:107]
ds_read_b128 v[164:167], v217 offset:6400
W 2
M v[112:115],v[176:179],a[0:3],v[204:207]
ds_read_b128 v[168:171], v216 offset:12288
M v[112:115],v[180:183],a[8:11],v[112:115]
ds_read_b128 v[172:175], v217 offset:12288
M v[112:115],v[152:155],a[16:19],v[112:115]
M v[116:119],v[152:155],a[20:23],v[208:211]
v_max3_f32 v248, v104, v105, v106
v_max3_f32 v249, v108, v109, v110
M v[116:119],v[180:183],a[12:15],v[116:119]
v_max_f32 v248, v248, v107
v_max_f32 v249, v249, v111
M v[116:119],v[176:179],a[4:7],v[116:119]
ds_read_b128 v[176:179], v216 offset:12416
W 2
M v[116:119],v[156:159],a[28:31],v[116:119]
v_exp_f32 v200, v104
M v[116:119],v[160:163],a[36:39],v[116:119]
ds_read_b128 v[180:183], v217 offset:12416
v_add_u32 v220, 98304, v226
M v[116:119],v[164:167],a[44:47],v[116:119]
ds_read_b128 v[152:155], v216 offset:12544
v_xor_b32 v221, 32, v220
M v[112:115],v[164:167],a[40:43],v[112:115]
v_exp_f32 v201, v105
M v[112:115],v[160:163],a[32:35],v[112:115]
v_exp_f32 v202, v106
M v[112:115],v[156:159],a[24:27],v[112:115]
ds_read_b128 v[156:159], v217 offset:12544
v_cvt_pk_bf16_f32 v136, v200, v201
M v[120:123],v[168:171],a[0:3],v[204:207]
ds_read_b128 v[160:163], v216 offset:18432
W 3
M v[120:123],v[172:175],a[8:11],v[120:123]
v_exp_f32 v203, v107
M v[120:123],v[176:179],a[16:19],v[120:123]
ds_read_b128 v[164:167], v217 offset:18432
v_cvt_pk_bf16_f32 v137, v202, v203
M v[124:127],v[176:179],a[20:23],v[208:211]
v_exp_f32 v200, v108
M v[124:127],v[172:175],a[12:15],v[124:127]
v_exp_f32 v201, v109
M v[124:127],v[168:171],a[4:7],v[124:127]
ds_read_b128 v[168:171], v216 offset:18560
v_cvt_pk_bf16_f32 v140, v200, v201
M v[124:127],v[180:183],a[28:31],v[124:127]
ds_read_b128 v[172:175], v217 offset:18560
W 3
M v[124:127],v[152:155],a[36:39],v[124:127]
v_exp_f32 v202, v110
M v[124:127],v[156:159],a[44:47],v[124:127]
ds_read_b128 v[176:179], v216 offset:18688
v_max3_f32 v248, v248, v112, v113
M v[120:123],v[156:159],a[40:43],v[120:123]
v_exp_f32 v203, v111
M v[120:123],v[152:155],a[32:35],v[120:123]
v_cvt_pk_bf16_f32 v141, v202, v203
v_max3_f32 v249, v249, v116, v117
M v[120:123],v[180:183],a[24:27],v[120:123]
ds_read_b128 v[180:183], v217 offset:18688
v_xor_b32 v222, 64, v220
M v[128:131],v[160:163],a[0:3],v[204:207]
v_xor_b32 v223, 96, v220
W 2
M v[128:131],v[164:167],a[8:11],v[128:131]
s_waitcnt vmcnt(9)
s_barrier
T v[152:153],v220,0
M v[128:131],v[168:171],a[16:19],v[128:131]
T v[154:155],v220,2048
v_max3_f32 v248, v248, v114, v115
M v[132:135],v[168:171],a[20:23],v[208:211]
T v[156:157],v220,4096
v_max3_f32 v249, v249, v118, v119
M v[132:135],v[164:167],a[12:15],v[132:135]
T v[158:159],v220,6144
W 4
M v[132:135],v[160:163],a[4:7],v[132:135]
T v[160:161],v220,8192
M v[132:135],v[172:175],a[28:31],v[132:135]
T v[162:163],v220,10240
M v[132:135],v[176:179],a[36:39],v[132:135]
T v[164:165],v220,12288
W 5
M v[132:135],v[180:183],a[44:47],v[132:135]
T v[166:167],v220,14336
M v[128:131],v[180:183],a[40:43],v[128:131]
T v[168:169],v220,16384
v_exp_f32 v200, v112
M v[128:131],v[176:179],a[32:35],v[128:131]
T v[170:171],v220,18432
W 5
M v[128:131],v[172:175],a[24:27],v[128:131]
T v[172:173],v220,20480
v_exp_f32 v201, v113
M v[184:187],v[152:155],a[48:51],0
T v[174:175],v220,22528
s_add_u32 s79, s5, 196608
s_add_u32 m0, s40, 73728
s_nop 0
buffer_load_dwordx4 v227, s[12:15], s79 offen nt lds
M v[184:187],v[156:159],a[56:59],v[184:187]
T v[176:177],v221,0
W 5
M v[184:187],v[160:163],a[64:67],v[184:187]
T v[178:179],v221,2048
v_cvt_pk_bf16_f32 v138, v200, v201
M v[188:191],v[160:163],a[68:71],0
T v[180:181],v221,4096
v_exp_f32 v202, v114
M v[188:191],v[156:159],a[60:63],v[188:191]
T v[182:183],v221,6144
W 5
M v[188:191],v[152:155],a[52:55],v[188:191]
T v[152:153],v221,8192
v_exp_f32 v203, v115
M v[188:191],v[164:167],a[76:79],v[188:191]
T v[154:155],v221,10240
v_cvt_pk_bf16_f32 v139, v202, v203
M v[188:191],v[168:171],a[84:87],v[188:191]
T v[156:157],v221,12288
W 5
M v[188:191],v[172:175],a[92:95],v[188:191]
T v[158:159],v221,14336
v_exp_f32 v200, v116
M v[184:187],v[172:175],a[88:91],v[184:187]
T v[160:161],v221,16384
W 5
M v[184:187],v[168:171],a[80:83],v[184:187]
T v[162:163],v221,18432
v_exp_f32 v201, v117
M v[184:187],v[164:167],a[72:75],v[184:187]
T v[164:165],v221,20480
v_cvt_pk_bf16_f32 v142, v200, v201
M v[192:195],v[176:179],a[48:51],0
T v[166:167],v221,22528
W 5
M v[192:195],v[180:183],a[56:59],v[192:195]
T v[168:169],v222,0
M v[192:195],v[152:155],a[64:67],v[192:195]
T v[170:171],v222,2048
v_exp_f32 v202, v118
M v[196:199],v[152:155],a[68:71],0
T v[172:173],v222,4096
v_max3_f32 v248, v248, v120, v121
M v[196:199],v[180:183],a[60:63],v[196:199]
T v[174:175],v222,6144
v_max3_f32 v249, v249, v124, v125
M v[196:199],v[176:179],a[52:55],v[196:199]
T v[176:177],v222,8192
v_exp_f32 v203, v119
W 5
M v[196:199],v[156:159],a[76:79],v[196:199]
T v[178:179],v222,10240
v_cvt_pk_bf16_f32 v143, v202, v203
M v[196:199],v[160:163],a[84:87],v[196:199]
T v[180:181],v222,12288
v_cvt_pk_bf16_f32 v252, v184, v185
v_cvt_pk_bf16_f32 v253, v186, v187
M v[196:199],v[164:167],a[92:95],v[196:199]
T v[182:183],v222,14336
buffer_load_dwordx4 v228, s[12:15], s79 offen offset:1024 nt lds
v_accvgpr_write_b32 a192, v252
v_accvgpr_write_b32 a193, v253
M v[192:195],v[164:167],a[88:91],v[192:195]
T v[152:153],v222,16384
v_cvt_pk_bf16_f32 v252, v188, v189
v_cvt_pk_bf16_f32 v253, v190, v191
M v[192:195],v[160:163],a[80:83],v[192:195]
T v[154:155],v222,18432
v_accvgpr_write_b32 a196, v252
v_accvgpr_write_b32 a197, v253
M v[192:195],v[156:159],a[72:75],v[192:195]
T v[156:157],v222,20480
v_max3_f32 v248, v248, v122, v123
W 5
M v[184:187],v[168:171],a[48:51],0
T v[158:159],v222,22528
v_max3_f32 v249, v249, v126, v127
M v[184:187],v[172:175],a[56:59],v[184:187]
T v[160:161],v223,0
v_max3_f32 v248, v248, v128, v129
M v[184:187],v[176:179],a[64:67],v[184:187]
T v[162:163],v223,2048
v_max3_f32 v249, v249, v132, v133
M v[188:191],v[176:179],a[68:71],0
T v[164:165],v223,4096
v_max3_f32 v248, v248, v130, v131
M v[188:191],v[172:175],a[60:63],v[188:191]
T v[166:167],v223,6144
v_max3_f32 v249, v249, v134, v135
M v[188:191],v[168:171],a[52:55],v[188:191]
T v[168:169],v223,8192
v_cvt_pk_bf16_f32 v252, v192, v193
W 5
M v[188:191],v[180:183],a[76:79],v[188:191]
T v[170:171],v223,10240
v_cvt_pk_bf16_f32 v253, v194, v195
M v[188:191],v[152:155],a[84:87],v[188:191]
T v[172:173],v223,12288
v_accvgpr_write_b32 a194, v252
v_accvgpr_write_b32 a195, v253
M v[188:191],v[156:159],a[92:95],v[188:191]
T v[174:175],v223,14336
v_cvt_pk_bf16_f32 v252, v196, v197
M v[184:187],v[156:159],a[88:91],v[184:187]
T v[176:177],v223,16384
v_cvt_pk_bf16_f32 v253, v198, v199
v_accvgpr_write_b32 a198, v252
M v[184:187],v[152:155],a[80:83],v[184:187]
T v[178:179],v223,18432
v_accvgpr_write_b32 a199, v253
M v[184:187],v[180:183],a[72:75],v[184:187]
T v[180:181],v223,20480
v_max_f32 v250, v248, v249
W 5
M v[192:195],v[160:163],a[48:51],0
T v[182:183],v223,22528
v_cmp_lt_f32 vcc, s60, v250
M v[192:195],v[164:167],a[56:59],v[192:195]
s_nop 0
s_cbranch_vccnz LST1_4_%=
LRT1_4_%=:
v_add_u32 v216, 0, v224
M v[192:195],v[168:171],a[64:67],v[192:195]
v_exp_f32 v200, v120
M v[196:199],v[168:171],a[68:71],0
v_xor_b32 v217, 32, v216
v_xor_b32 v218, 64, v216
M v[196:199],v[164:167],a[60:63],v[196:199]
v_xor_b32 v219, 96, v216
s_waitcnt vmcnt(8)
s_barrier
T v[152:153],v216,0
T v[154:155],v216,6144
v_exp_f32 v201, v121
M v[196:199],v[160:163],a[52:55],v[196:199]
T v[156:157],v217,0
v_exp_f32 v202, v122
W 3
M v[196:199],v[172:175],a[76:79],v[196:199]
T v[158:159],v217,6144
buffer_load_dwordx4 v229, s[12:15], s79 offen offset:2048 nt lds
M v[196:199],v[176:179],a[84:87],v[196:199]
T v[160:161],v218,0
v_cvt_pk_bf16_f32 v144, v200, v201
M v[196:199],v[180:183],a[92:95],v[196:199]
T v[162:163],v218,6144
v_exp_f32 v203, v123
M v[192:195],v[180:183],a[88:91],v[192:195]
T v[164:165],v219,0
v_cvt_pk_bf16_f32 v145, v202, v203
M v[192:195],v[176:179],a[80:83],v[192:195]
T v[166:167],v219,6144
W 5
M v[192:195],v[172:175],a[72:75],v[192:195]
T v[168:169],v216,128
v_exp_f32 v200, v124
M v[0:3],v[152:155],v[136:139],v[0:3]
T v[170:171],v216,6272
v_exp_f32 v201, v125
M v[4:7],v[152:155],v[140:143],v[4:7]
T v[172:173],v217,128
v_cvt_pk_bf16_f32 v148, v200, v201
M v[96:99],v[244:247],v[136:139],v[96:99]
T v[174:175],v217,6272
W 5
M v[100:103],v[244:247],v[140:143],v[100:103]
T v[176:177],v218,128
v_exp_f32 v202, v126
M v[8:11],v[156:159],v[136:139],v[8:11]
T v[178:179],v218,6272
W 5
M v[12:15],v[156:159],v[140:143],v[12:15]
T v[180:181],v219,128
v_exp_f32 v203, v127
M v[16:19],v[160:163],v[136:139],v[16:19]
T v[182:183],v219,6272
v_cvt_pk_bf16_f32 v149, v202, v203
M v[20:23],v[160:163],v[140:143],v[20:23]
T v[152:153],v216,256
W 5
M v[24:27],v[164:167],v[136:139],v[24:27]
T v[154:155],v216,6400
v_exp_f32 v200, v128
M v[28:31],v[164:167],v[140:143],v[28:31]
T v[156:157],v217,256
v_exp_f32 v201, v129
M v[32:35],v[168:171],v[136:139],v[32:35]
T v[158:159],v217,6400
v_cvt_pk_bf16_f32 v146, v200, v201
M v[36:39],v[168:171],v[140:143],v[36:39]
T v[160:161],v218,256
W 5
M v[40:43],v[172:175],v[136:139],v[40:43]
T v[162:163],v218,6400
s_add_u32 s79, s6, 196608
v_exp_f32 v202, v130
M v[44:47],v[172:175],v[140:143],v[44:47]
T v[164:165],v219,256
s_add_u32 m0, s40, 122880
M v[48:51],v[176:179],v[136:139],v[48:51]
T v[166:167],v219,6400
buffer_load_dwordx4 v227, s[8:11], s79 offen nt lds
v_exp_f32 v203, v131
M v[52:55],v[176:179],v[140:143],v[52:55]
T v[168:169],v216,12288
v_cvt_pk_bf16_f32 v147, v202, v203
M v[56:59],v[180:183],v[136:139],v[56:59]
T v[170:171],v216,18432
W 5
M v[60:63],v[180:183],v[140:143],v[60:63]
T v[172:173],v217,12288
v_exp_f32 v200, v132
M v[64:67],v[152:155],v[136:139],v[64:67]
T v[174:175],v217,18432
v_exp_f32 v201, v133
M v[68:71],v[152:155],v[140:143],v[68:71]
T v[176:177],v218,12288
v_cvt_pk_bf16_f32 v150, v200, v201
M v[72:75],v[156:159],v[136:139],v[72:75]
T v[178:179],v218,18432
v_add_u32 v220, 24576, v225
M v[76:79],v[156:159],v[140:143],v[76:79]
T v[180:181],v219,12288
v_exp_f32 v202, v134
W 5
M v[80:83],v[160:163],v[136:139],v[80:83]
T v[182:183],v219,18432
v_cvt_pk_bf16_f32 v252, v184, v185
M v[84:87],v[160:163],v[140:143],v[84:87]
T v[152:153],v216,12416
v_cvt_pk_bf16_f32 v253, v186, v187
M v[88:91],v[164:167],v[136:139],v[88:91]
T v[154:155],v216,18560
v_accvgpr_write_b32 a200, v252
v_exp_f32 v203, v135
M v[92:95],v[164:167],v[140:143],v[92:95]
T v[156:157],v217,12416
v_cvt_pk_bf16_f32 v151, v202, v203
M v[0:3],v[168:171],v[144:147],v[0:3]
T v[158:159],v217,18560
v_accvgpr_write_b32 a201, v253
M v[4:7],v[168:171],v[148:151],v[4:7]
T v[160:161],v218,12416
v_cvt_pk_bf16_f32 v252, v188, v189
M v[96:99],v[244:247],v[144:147],v[96:99]
T v[162:163],v218,18560
v_cvt_pk_bf16_f32 v253, v190, v191
M v[100:103],v[244:247],v[148:151],v[100:103]
T v[164:165],v219,12416
W 5
M v[8:11],v[172:175],v[144:147],v[8:11]
T v[166:167],v219,18560
v_accvgpr_write_b32 a204, v252
M v[12:15],v[172:175],v[148:151],v[12:15]
T v[168:169],v216,12544
v_accvgpr_write_b32 a205, v253
M v[16:19],v[176:179],v[144:147],v[16:19]
T v[170:171],v216,18688
v_cvt_pk_bf16_f32 v252, v192, v193
M v[20:23],v[176:179],v[148:151],v[20:23]
T v[172:173],v217,12544
v_cvt_pk_bf16_f32 v253, v194, v195
M v[24:27],v[180:183],v[144:147],v[24:27]
T v[174:175],v217,18688
buffer_load_dwordx4 v228, s[8:11], s79 offen offset:1024 nt lds
M v[28:31],v[180:183],v[148:151],v[28:31]
T v[176:177],v218,12544
v_accvgpr_write_b32 a202, v252
M v[32:35],v[152:155],v[144:147],v[32:35]
T v[178:179],v218,18688
v_accvgpr_write_b32 a203, v253
M v[36:39],v[152:155],v[148:151],v[36:39]
T v[180:181],v219,12544
W 5
M v[40:43],v[156:159],v[144:147],v[40:43]
T v[182:183],v219,18688
v_xor_b32 v221, 64, v220
M v[44:47],v[156:159],v[148:151],v[44:47]
s_waitcnt vmcnt(8)
s_barrier
ds_read_b128 v[152:155], v220 offset:0
M v[48:51],v[160:163],v[144:147],v[48:51]
ds_read_b128 v[156:159], v221 offset:0
v_cvt_pk_bf16_f32 v252, v196, v197
M v[52:55],v[160:163],v[148:151],v[52:55]
ds_read_b128 v[160:163], v220 offset:128
v_cvt_pk_bf16_f32 v253, v198, v199
M v[56:59],v[164:167],v[144:147],v[56:59]
v_accvgpr_write_b32 a206, v252
v_accvgpr_write_b32 a207, v253
M v[60:63],v[164:167],v[148:151],v[60:63]
W 3
M v[64:67],v[168:171],v[144:147],v[64:67]
M v[68:71],v[168:171],v[148:151],v[68:71]
ds_read_b128 v[164:167], v221 offset:128
W 2
M v[72:75],v[172:175],v[144:147],v[72:75]
ds_read_b128 v[168:171], v220 offset:256
M v[76:79],v[172:175],v[148:151],v[76:79]
ds_read_b128 v[172:175], v221 offset:256
M v[80:83],v[176:179],v[144:147],v[80:83]
M v[84:87],v[176:179],v[148:151],v[84:87]
M v[88:91],v[180:183],v[144:147],v[88:91]
W 2
M v[92:95],v[180:183],v[148:151],v[92:95]
ds_read_b128 v[176:179], v220 offset:6144
M v[104:107],v[152:155],a[0:3],v[204:207]
ds_read_b128 v[180:183], v221 offset:6144
W 2
M v[104:107],v[156:159],a[8:11],v[104:107]
M v[104:107],v[160:163],a[16:19],v[104:107]
buffer_load_dwordx4 v229, s[8:11], s79 offen offset:2048 nt lds
s_add_u32 s77, s98, s42
M v[108:111],v[160:163],a[20:23],v[208:211]
s_add_u32 s78, s77, 0
s_add_u32 m0, s40, 98304
M v[108:111],v[156:159],a[12:15],v[108:111]
buffer_load_dwordx4 v230, s[20:23], s78 offen lds
M v[108:111],v[152:155],a[4:7],v[108:111]
ds_read_b128 v[152:155], v220 offset:6272
W 1
M v[108:111],v[164:167],a[28:31],v[108:111]
ds_read_b128 v[156:159], v221 offset:6272
M v[108:111],v[168:171],a[36:39],v[108:111]
ds_read_b128 v[160:163], v220 offset:6400
buffer_load_dwordx4 v231, s[20:23], s78 offen offset:1024 lds
M v[108:111],v[172:175],a[44:47],v[108:111]
M v[104:107],v[172:175],a[40:43],v[104:107]
M v[104:107],v[168:171],a[32:35],v[104:107]
buffer_load_dwordx4 v232, s[20:23], s78 offen offset:2048 lds
M v[104:107],v[164:167],a[24:27],v[104:107]
ds_read_b128 v[164:167], v221 offset:6400
W 2
M v[112:115],v[176:179],a[0:3],v[204:207]
ds_read_b128 v[168:171], v220 offset:12288
M v[112:115],v[180:183],a[8:11],v[112:115]
ds_read_b128 v[172:175], v221 offset:12288
M v[112:115],v[152:155],a[16:19],v[112:115]
M v[116:119],v[152:155],a[20:23],v[208:211]
v_max3_f32 v248, v104, v105, v106
v_max3_f32 v249, v108, v109, v110
M v[116:119],v[180:183],a[12:15],v[116:119]
v_max_f32 v248, v248, v107
v_max_f32 v249, v249, v111
M v[116:119],v[176:179],a[4:7],v[116:119]
ds_read_b128 v[176:179], v220 offset:12416
W 2
M v[116:119],v[156:159],a[28:31],v[116:119]
v_exp_f32 v200, v104
M v[116:119],v[160:163],a[36:39],v[116:119]
ds_read_b128 v[180:183], v221 offset:12416
v_add_u32 v216, 49152, v226
M v[116:119],v[164:167],a[44:47],v[116:119]
ds_read_b128 v[152:155], v220 offset:12544
v_xor_b32 v217, 32, v216
M v[112:115],v[164:167],a[40:43],v[112:115]
v_exp_f32 v201, v105
M v[112:115],v[160:163],a[32:35],v[112:115]
v_exp_f32 v202, v106
M v[112:115],v[156:159],a[24:27],v[112:115]
ds_read_b128 v[156:159], v221 offset:12544
v_cvt_pk_bf16_f32 v136, v200, v201
M v[120:123],v[168:171],a[0:3],v[204:207]
ds_read_b128 v[160:163], v220 offset:18432
W 3
M v[120:123],v[172:175],a[8:11],v[120:123]
v_exp_f32 v203, v107
M v[120:123],v[176:179],a[16:19],v[120:123]
ds_read_b128 v[164:167], v221 offset:18432
v_cvt_pk_bf16_f32 v137, v202, v203
M v[124:127],v[176:179],a[20:23],v[208:211]
v_exp_f32 v200, v108
M v[124:127],v[172:175],a[12:15],v[124:127]
v_exp_f32 v201, v109
M v[124:127],v[168:171],a[4:7],v[124:127]
ds_read_b128 v[168:171], v220 offset:18560
v_cvt_pk_bf16_f32 v140, v200, v201
M v[124:127],v[180:183],a[28:31],v[124:127]
ds_read_b128 v[172:175], v221 offset:18560
W 3
M v[124:127],v[152:155],a[36:39],v[124:127]
v_exp_f32 v202, v110
M v[124:127],v[156:159],a[44:47],v[124:127]
ds_read_b128 v[176:179], v220 offset:18688
v_max3_f32 v248, v248, v112, v113
M v[120:123],v[156:159],a[40:43],v[120:123]
v_exp_f32 v203, v111
M v[120:123],v[152:155],a[32:35],v[120:123]
v_cvt_pk_bf16_f32 v141, v202, v203
v_max3_f32 v249, v249, v116, v117
M v[120:123],v[180:183],a[24:27],v[120:123]
ds_read_b128 v[180:183], v221 offset:18688
v_xor_b32 v218, 64, v216
M v[128:131],v[160:163],a[0:3],v[204:207]
v_xor_b32 v219, 96, v216
W 2
M v[128:131],v[164:167],a[8:11],v[128:131]
s_waitcnt vmcnt(9)
s_barrier
T v[152:153],v216,0
M v[128:131],v[168:171],a[16:19],v[128:131]
T v[154:155],v216,2048
v_max3_f32 v248, v248, v114, v115
M v[132:135],v[168:171],a[20:23],v[208:211]
T v[156:157],v216,4096
v_max3_f32 v249, v249, v118, v119
M v[132:135],v[164:167],a[12:15],v[132:135]
T v[158:159],v216,6144
W 4
M v[132:135],v[160:163],a[4:7],v[132:135]
T v[160:161],v216,8192
M v[132:135],v[172:175],a[28:31],v[132:135]
T v[162:163],v216,10240
M v[132:135],v[176:179],a[36:39],v[132:135]
T v[164:165],v216,12288
W 5
M v[132:135],v[180:183],a[44:47],v[132:135]
T v[166:167],v216,14336
M v[128:131],v[180:183],a[40:43],v[128:131]
T v[168:169],v216,16384
v_exp_f32 v200, v112
M v[128:131],v[176:179],a[32:35],v[128:131]
T v[170:171],v216,18432
W 5
M v[128:131],v[172:175],a[24:27],v[128:131]
T v[172:173],v216,20480
v_exp_f32 v201, v113
M v[184:187],v[152:155],a[48:51],0
T v[174:175],v216,22528
s_add_u32 s79, s6, 196608
s_add_u32 m0, s40, 0
s_nop 0
buffer_load_dwordx4 v227, s[12:15], s79 offen nt lds
M v[184:187],v[156:159],a[56:59],v[184:187]
T v[176:177],v217,0
W 5
M v[184:187],v[160:163],a[64:67],v[184:187]
T v[178:179],v217,2048
v_cvt_pk_bf16_f32 v138, v200, v201
M v[188:191],v[160:163],a[68:71],0
T v[180:181],v217,4096
v_exp_f32 v202, v114
M v[188:191],v[156:159],a[60:63],v[188:191]
T v[182:183],v217,6144
W 5
M v[188:191],v[152:155],a[52:55],v[188:191]
T v[152:153],v217,8192
v_exp_f32 v203, v115
M v[188:191],v[164:167],a[76:79],v[188:191]
T v[154:155],v217,10240
v_cvt_pk_bf16_f32 v139, v202, v203
M v[188:191],v[168:171],a[84:87],v[188:191]
T v[156:157],v217,12288
W 5
M v[188:191],v[172:175],a[92:95],v[188:191]
T v[158:159],v217,14336
v_exp_f32 v200, v116
M v[184:187],v[172:175],a[88:91],v[184:187]
T v[160:161],v217,16384
W 5
M v[184:187],v[168:171],a[80:83],v[184:187]
T v[162:163],v217,18432
v_exp_f32 v201, v117
M v[184:187],v[164:167],a[72:75],v[184:187]
T v[164:165],v217,20480
v_cvt_pk_bf16_f32 v142, v200, v201
M v[192:195],v[176:179],a[48:51],0
T v[166:167],v217,22528
W 5
M v[192:195],v[180:183],a[56:59],v[192:195]
T v[168:169],v218,0
M v[192:195],v[152:155],a[64:67],v[192:195]
T v[170:171],v218,2048
v_exp_f32 v202, v118
M v[196:199],v[152:155],a[68:71],0
T v[172:173],v218,4096
v_max3_f32 v248, v248, v120, v121
M v[196:199],v[180:183],a[60:63],v[196:199]
T v[174:175],v218,6144
v_max3_f32 v249, v249, v124, v125
M v[196:199],v[176:179],a[52:55],v[196:199]
T v[176:177],v218,8192
v_exp_f32 v203, v119
W 5
M v[196:199],v[156:159],a[76:79],v[196:199]
T v[178:179],v218,10240
v_cvt_pk_bf16_f32 v143, v202, v203
M v[196:199],v[160:163],a[84:87],v[196:199]
T v[180:181],v218,12288
v_cvt_pk_bf16_f32 v252, v184, v185
v_cvt_pk_bf16_f32 v253, v186, v187
M v[196:199],v[164:167],a[92:95],v[196:199]
T v[182:183],v218,14336
buffer_load_dwordx4 v228, s[12:15], s79 offen offset:1024 nt lds
v_accvgpr_write_b32 a208, v252
v_accvgpr_write_b32 a209, v253
M v[192:195],v[164:167],a[88:91],v[192:195]
T v[152:153],v218,16384
v_cvt_pk_bf16_f32 v252, v188, v189
v_cvt_pk_bf16_f32 v253, v190, v191
M v[192:195],v[160:163],a[80:83],v[192:195]
T v[154:155],v218,18432
v_accvgpr_write_b32 a212, v252
v_accvgpr_write_b32 a213, v253
M v[192:195],v[156:159],a[72:75],v[192:195]
T v[156:157],v218,20480
v_max3_f32 v248, v248, v122, v123
W 5
M v[184:187],v[168:171],a[48:51],0
T v[158:159],v218,22528
v_max3_f32 v249, v249, v126, v127
M v[184:187],v[172:175],a[56:59],v[184:187]
T v[160:161],v219,0
v_max3_f32 v248, v248, v128, v129
M v[184:187],v[176:179],a[64:67],v[184:187]
T v[162:163],v219,2048
v_max3_f32 v249, v249, v132, v133
M v[188:191],v[176:179],a[68:71],0
T v[164:165],v219,4096
v_max3_f32 v248, v248, v130, v131
M v[188:191],v[172:175],a[60:63],v[188:191]
T v[166:167],v219,6144
v_max3_f32 v249, v249, v134, v135
M v[188:191],v[168:171],a[52:55],v[188:191]
T v[168:169],v219,8192
v_cvt_pk_bf16_f32 v252, v192, v193
W 5
M v[188:191],v[180:183],a[76:79],v[188:191]
T v[170:171],v219,10240
v_cvt_pk_bf16_f32 v253, v194, v195
M v[188:191],v[152:155],a[84:87],v[188:191]
T v[172:173],v219,12288
v_accvgpr_write_b32 a210, v252
v_accvgpr_write_b32 a211, v253
M v[188:191],v[156:159],a[92:95],v[188:191]
T v[174:175],v219,14336
v_cvt_pk_bf16_f32 v252, v196, v197
M v[184:187],v[156:159],a[88:91],v[184:187]
T v[176:177],v219,16384
v_cvt_pk_bf16_f32 v253, v198, v199
v_accvgpr_write_b32 a214, v252
M v[184:187],v[152:155],a[80:83],v[184:187]
T v[178:179],v219,18432
v_accvgpr_write_b32 a215, v253
M v[184:187],v[180:183],a[72:75],v[184:187]
T v[180:181],v219,20480
v_max_f32 v250, v248, v249
W 5
M v[192:195],v[160:163],a[48:51],0
T v[182:183],v219,22528
v_cmp_lt_f32 vcc, s60, v250
M v[192:195],v[164:167],a[56:59],v[192:195]
s_nop 0
s_cbranch_vccnz LST1_5_%=
LRT1_5_%=:
v_add_u32 v220, 73728, v224
M v[192:195],v[168:171],a[64:67],v[192:195]
v_exp_f32 v200, v120
M v[196:199],v[168:171],a[68:71],0
v_xor_b32 v221, 32, v220
v_xor_b32 v222, 64, v220
M v[196:199],v[164:167],a[60:63],v[196:199]
v_xor_b32 v223, 96, v220
s_waitcnt vmcnt(8)
s_barrier
T v[152:153],v220,0
T v[154:155],v220,6144
v_exp_f32 v201, v121
M v[196:199],v[160:163],a[52:55],v[196:199]
T v[156:157],v221,0
v_exp_f32 v202, v122
W 3
M v[196:199],v[172:175],a[76:79],v[196:199]
T v[158:159],v221,6144
buffer_load_dwordx4 v229, s[12:15], s79 offen offset:2048 nt lds
M v[196:199],v[176:179],a[84:87],v[196:199]
T v[160:161],v222,0
v_cvt_pk_bf16_f32 v144, v200, v201
M v[196:199],v[180:183],a[92:95],v[196:199]
T v[162:163],v222,6144
v_exp_f32 v203, v123
M v[192:195],v[180:183],a[88:91],v[192:195]
T v[164:165],v223,0
v_cvt_pk_bf16_f32 v145, v202, v203
M v[192:195],v[176:179],a[80:83],v[192:195]
T v[166:167],v223,6144
W 5
M v[192:195],v[172:175],a[72:75],v[192:195]
T v[168:169],v220,128
v_exp_f32 v200, v124
M v[0:3],v[152:155],v[136:139],v[0:3]
T v[170:171],v220,6272
v_exp_f32 v201, v125
M v[4:7],v[152:155],v[140:143],v[4:7]
T v[172:173],v221,128
v_cvt_pk_bf16_f32 v148, v200, v201
M v[96:99],v[244:247],v[136:139],v[96:99]
T v[174:175],v221,6272
W 5
M v[100:103],v[244:247],v[140:143],v[100:103]
T v[176:177],v222,128
v_exp_f32 v202, v126
M v[8:11],v[156:159],v[136:139],v[8:11]
T v[178:179],v222,6272
W 5
M v[12:15],v[156:159],v[140:143],v[12:15]
T v[180:181],v223,128
v_exp_f32 v203, v127
M v[16:19],v[160:163],v[136:139],v[16:19]
T v[182:183],v223,6272
v_cvt_pk_bf16_f32 v149, v202, v203
M v[20:23],v[160:163],v[140:143],v[20:23]
T v[152:153],v220,256
W 5
M v[24:27],v[164:167],v[136:139],v[24:27]
T v[154:155],v220,6400
v_exp_f32 v200, v128
M v[28:31],v[164:167],v[140:143],v[28:31]
T v[156:157],v221,256
v_exp_f32 v201, v129
M v[32:35],v[168:171],v[136:139],v[32:35]
T v[158:159],v221,6400
v_cvt_pk_bf16_f32 v146, v200, v201
M v[36:39],v[168:171],v[140:143],v[36:39]
T v[160:161],v222,256
W 5
M v[40:43],v[172:175],v[136:139],v[40:43]
T v[162:163],v222,6400
s_add_u32 s79, s7, 196608
v_exp_f32 v202, v130
M v[44:47],v[172:175],v[140:143],v[44:47]
T v[164:165],v223,256
s_add_u32 m0, s40, 24576
M v[48:51],v[176:179],v[136:139],v[48:51]
T v[166:167],v223,6400
buffer_load_dwordx4 v227, s[8:11], s79 offen nt lds
v_exp_f32 v203, v131
M v[52:55],v[176:179],v[140:143],v[52:55]
T v[168:169],v220,12288
v_cvt_pk_bf16_f32 v147, v202, v203
M v[56:59],v[180:183],v[136:139],v[56:59]
T v[170:171],v220,18432
W 5
M v[60:63],v[180:183],v[140:143],v[60:63]
T v[172:173],v221,12288
v_exp_f32 v200, v132
M v[64:67],v[152:155],v[136:139],v[64:67]
T v[174:175],v221,18432
v_exp_f32 v201, v133
M v[68:71],v[152:155],v[140:143],v[68:71]
T v[176:177],v222,12288
v_cvt_pk_bf16_f32 v150, v200, v201
M v[72:75],v[156:159],v[136:139],v[72:75]
T v[178:179],v222,18432
v_add_u32 v216, 122880, v225
M v[76:79],v[156:159],v[140:143],v[76:79]
T v[180:181],v223,12288
v_exp_f32 v202, v134
W 5
M v[80:83],v[160:163],v[136:139],v[80:83]
T v[182:183],v223,18432
v_cvt_pk_bf16_f32 v252, v184, v185
M v[84:87],v[160:163],v[140:143],v[84:87]
T v[152:153],v220,12416
v_cvt_pk_bf16_f32 v253, v186, v187
M v[88:91],v[164:167],v[136:139],v[88:91]
T v[154:155],v220,18560
v_accvgpr_write_b32 a216, v252
v_exp_f32 v203, v135
M v[92:95],v[164:167],v[140:143],v[92:95]
T v[156:157],v221,12416
v_cvt_pk_bf16_f32 v151, v202, v203
M v[0:3],v[168:171],v[144:147],v[0:3]
T v[158:159],v221,18560
v_accvgpr_write_b32 a217, v253
M v[4:7],v[168:171],v[148:151],v[4:7]
T v[160:161],v222,12416
v_cvt_pk_bf16_f32 v252, v188, v189
M v[96:99],v[244:247],v[144:147],v[96:99]
T v[162:163],v222,18560
v_cvt_pk_bf16_f32 v253, v190, v191
M v[100:103],v[244:247],v[148:151],v[100:103]
T v[164:165],v223,12416
W 5
M v[8:11],v[172:175],v[144:147],v[8:11]
T v[166:167],v223,18560
v_accvgpr_write_b32 a220, v252
M v[12:15],v[172:175],v[148:151],v[12:15]
T v[168:169],v220,12544
v_accvgpr_write_b32 a221, v253
M v[16:19],v[176:179],v[144:147],v[16:19]
T v[170:171],v220,18688
v_cvt_pk_bf16_f32 v252, v192, v193
M v[20:23],v[176:179],v[148:151],v[20:23]
T v[172:173],v221,12544
v_cvt_pk_bf16_f32 v253, v194, v195
M v[24:27],v[180:183],v[144:147],v[24:27]
T v[174:175],v221,18688
buffer_load_dwordx4 v228, s[8:11], s79 offen offset:1024 nt lds
M v[28:31],v[180:183],v[148:151],v[28:31]
T v[176:177],v222,12544
v_accvgpr_write_b32 a218, v252
M v[32:35],v[152:155],v[144:147],v[32:35]
T v[178:179],v222,18688
v_accvgpr_write_b32 a219, v253
M v[36:39],v[152:155],v[148:151],v[36:39]
T v[180:181],v223,12544
W 5
M v[40:43],v[156:159],v[144:147],v[40:43]
T v[182:183],v223,18688
v_xor_b32 v217, 64, v216
M v[44:47],v[156:159],v[148:151],v[44:47]
s_waitcnt vmcnt(8)
s_barrier
ds_read_b128 v[152:155], v216 offset:0
M v[48:51],v[160:163],v[144:147],v[48:51]
ds_read_b128 v[156:159], v217 offset:0
v_cvt_pk_bf16_f32 v252, v196, v197
M v[52:55],v[160:163],v[148:151],v[52:55]
ds_read_b128 v[160:163], v216 offset:128
v_cvt_pk_bf16_f32 v253, v198, v199
M v[56:59],v[164:167],v[144:147],v[56:59]
v_accvgpr_write_b32 a222, v252
v_accvgpr_write_b32 a223, v253
M v[60:63],v[164:167],v[148:151],v[60:63]
W 3
M v[64:67],v[168:171],v[144:147],v[64:67]
M v[68:71],v[168:171],v[148:151],v[68:71]
ds_read_b128 v[164:167], v217 offset:128
W 2
M v[72:75],v[172:175],v[144:147],v[72:75]
ds_read_b128 v[168:171], v216 offset:256
M v[76:79],v[172:175],v[148:151],v[76:79]
ds_read_b128 v[172:175], v217 offset:256
M v[80:83],v[176:179],v[144:147],v[80:83]
M v[84:87],v[176:179],v[148:151],v[84:87]
M v[88:91],v[180:183],v[144:147],v[88:91]
W 2
M v[92:95],v[180:183],v[148:151],v[92:95]
ds_read_b128 v[176:179], v216 offset:6144
M v[104:107],v[152:155],a[0:3],v[204:207]
ds_read_b128 v[180:183], v217 offset:6144
W 2
M v[104:107],v[156:159],a[8:11],v[104:107]
M v[104:107],v[160:163],a[16:19],v[104:107]
buffer_load_dwordx4 v229, s[8:11], s79 offen offset:2048 nt lds
s_add_u32 s77, s99, s42
M v[108:111],v[160:163],a[20:23],v[208:211]
s_add_u32 s78, s77, 0
s_add_u32 m0, s40, 49152
M v[108:111],v[156:159],a[12:15],v[108:111]
buffer_load_dwordx4 v230, s[20:23], s78 offen lds
M v[108:111],v[152:155],a[4:7],v[108:111]
ds_read_b128 v[152:155], v216 offset:6272
W 1
M v[108:111],v[164:167],a[28:31],v[108:111]
ds_read_b128 v[156:159], v217 offset:6272
M v[108:111],v[168:171],a[36:39],v[108:111]
ds_read_b128 v[160:163], v216 offset:6400
buffer_load_dwordx4 v231, s[20:23], s78 offen offset:1024 lds
M v[108:111],v[172:175],a[44:47],v[108:111]
M v[104:107],v[172:175],a[40:43],v[104:107]
M v[104:107],v[168:171],a[32:35],v[104:107]
buffer_load_dwordx4 v232, s[20:23], s78 offen offset:2048 lds
M v[104:107],v[164:167],a[24:27],v[104:107]
ds_read_b128 v[164:167], v217 offset:6400
W 2
M v[112:115],v[176:179],a[0:3],v[204:207]
ds_read_b128 v[168:171], v216 offset:12288
M v[112:115],v[180:183],a[8:11],v[112:115]
ds_read_b128 v[172:175], v217 offset:12288
M v[112:115],v[152:155],a[16:19],v[112:115]
M v[116:119],v[152:155],a[20:23],v[208:211]
v_max3_f32 v248, v104, v105, v106
v_max3_f32 v249, v108, v109, v110
M v[116:119],v[180:183],a[12:15],v[116:119]
v_max_f32 v248, v248, v107
v_max_f32 v249, v249, v111
M v[116:119],v[176:179],a[4:7],v[116:119]
ds_read_b128 v[176:179], v216 offset:12416
W 2
M v[116:119],v[156:159],a[28:31],v[116:119]
v_exp_f32 v200, v104
M v[116:119],v[160:163],a[36:39],v[116:119]
ds_read_b128 v[180:183], v217 offset:12416
v_add_u32 v220, 98304, v226
M v[116:119],v[164:167],a[44:47],v[116:119]
ds_read_b128 v[152:155], v216 offset:12544
v_xor_b32 v221, 32, v220
M v[112:115],v[164:167],a[40:43],v[112:115]
v_exp_f32 v201, v105
M v[112:115],v[160:163],a[32:35],v[112:115]
v_exp_f32 v202, v106
M v[112:115],v[156:159],a[24:27],v[112:115]
ds_read_b128 v[156:159], v217 offset:12544
v_cvt_pk_bf16_f32 v136, v200, v201
M v[120:123],v[168:171],a[0:3],v[204:207]
ds_read_b128 v[160:163], v216 offset:18432
W 3
M v[120:123],v[172:175],a[8:11],v[120:123]
v_exp_f32 v203, v107
M v[120:123],v[176:179],a[16:19],v[120:123]
ds_read_b128 v[164:167], v217 offset:18432
v_cvt_pk_bf16_f32 v137, v202, v203
M v[124:127],v[176:179],a[20:23],v[208:211]
v_exp_f32 v200, v108
M v[124:127],v[172:175],a[12:15],v[124:127]
v_exp_f32 v201, v109
M v[124:127],v[168:171],a[4:7],v[124:127]
ds_read_b128 v[168:171], v216 offset:18560
v_cvt_pk_bf16_f32 v140, v200, v201
M v[124:127],v[180:183],a[28:31],v[124:127]
ds_read_b128 v[172:175], v217 offset:18560
W 3
M v[124:127],v[152:155],a[36:39],v[124:127]
v_exp_f32 v202, v110
M v[124:127],v[156:159],a[44:47],v[124:127]
ds_read_b128 v[176:179], v216 offset:18688
v_max3_f32 v248, v248, v112, v113
M v[120:123],v[156:159],a[40:43],v[120:123]
v_exp_f32 v203, v111
M v[120:123],v[152:155],a[32:35],v[120:123]
v_cvt_pk_bf16_f32 v141, v202, v203
v_max3_f32 v249, v249, v116, v117
M v[120:123],v[180:183],a[24:27],v[120:123]
ds_read_b128 v[180:183], v217 offset:18688
v_xor_b32 v222, 64, v220
M v[128:131],v[160:163],a[0:3],v[204:207]
v_xor_b32 v223, 96, v220
W 2
M v[128:131],v[164:167],a[8:11],v[128:131]
s_waitcnt vmcnt(9)
s_barrier
T v[152:153],v220,0
M v[128:131],v[168:171],a[16:19],v[128:131]
T v[154:155],v220,2048
v_max3_f32 v248, v248, v114, v115
M v[132:135],v[168:171],a[20:23],v[208:211]
T v[156:157],v220,4096
v_max3_f32 v249, v249, v118, v119
M v[132:135],v[164:167],a[12:15],v[132:135]
T v[158:159],v220,6144
W 4
M v[132:135],v[160:163],a[4:7],v[132:135]
T v[160:161],v220,8192
M v[132:135],v[172:175],a[28:31],v[132:135]
T v[162:163],v220,10240
M v[132:135],v[176:179],a[36:39],v[132:135]
T v[164:165],v220,12288
W 5
M v[132:135],v[180:183],a[44:47],v[132:135]
T v[166:167],v220,14336
M v[128:131],v[180:183],a[40:43],v[128:131]
T v[168:169],v220,16384
v_exp_f32 v200, v112
M v[128:131],v[176:179],a[32:35],v[128:131]
T v[170:171],v220,18432
W 5
M v[128:131],v[172:175],a[24:27],v[128:131]
T v[172:173],v220,20480
v_exp_f32 v201, v113
M v[184:187],v[152:155],a[48:51],0
T v[174:175],v220,22528
s_add_u32 s79, s7, 196608
s_add_u32 m0, s40, 73728
s_nop 0
buffer_load_dwordx4 v227, s[12:15], s79 offen nt lds
M v[184:187],v[156:159],a[56:59],v[184:187]
T v[176:177],v221,0
W 5
M v[184:187],v[160:163],a[64:67],v[184:187]
T v[178:179],v221,2048
v_cvt_pk_bf16_f32 v138, v200, v201
M v[188:191],v[160:163],a[68:71],0
T v[180:181],v221,4096
v_exp_f32 v202, v114
M v[188:191],v[156:159],a[60:63],v[188:191]
T v[182:183],v221,6144
W 5
M v[188:191],v[152:155],a[52:55],v[188:191]
T v[152:153],v221,8192
v_exp_f32 v203, v115
M v[188:191],v[164:167],a[76:79],v[188:191]
T v[154:155],v221,10240
v_cvt_pk_bf16_f32 v139, v202, v203
M v[188:191],v[168:171],a[84:87],v[188:191]
T v[156:157],v221,12288
W 5
M v[188:191],v[172:175],a[92:95],v[188:191]
T v[158:159],v221,14336
v_exp_f32 v200, v116
M v[184:187],v[172:175],a[88:91],v[184:187]
T v[160:161],v221,16384
W 5
M v[184:187],v[168:171],a[80:83],v[184:187]
T v[162:163],v221,18432
v_exp_f32 v201, v117
M v[184:187],v[164:167],a[72:75],v[184:187]
T v[164:165],v221,20480
v_cvt_pk_bf16_f32 v142, v200, v201
M v[192:195],v[176:179],a[48:51],0
T v[166:167],v221,22528
W 5
M v[192:195],v[180:183],a[56:59],v[192:195]
T v[168:169],v222,0
M v[192:195],v[152:155],a[64:67],v[192:195]
T v[170:171],v222,2048
v_exp_f32 v202, v118
M v[196:199],v[152:155],a[68:71],0
T v[172:173],v222,4096
v_max3_f32 v248, v248, v120, v121
M v[196:199],v[180:183],a[60:63],v[196:199]
T v[174:175],v222,6144
v_max3_f32 v249, v249, v124, v125
M v[196:199],v[176:179],a[52:55],v[196:199]
T v[176:177],v222,8192
v_exp_f32 v203, v119
W 5
M v[196:199],v[156:159],a[76:79],v[196:199]
T v[178:179],v222,10240
v_cvt_pk_bf16_f32 v143, v202, v203
M v[196:199],v[160:163],a[84:87],v[196:199]
T v[180:181],v222,12288
v_cvt_pk_bf16_f32 v252, v184, v185
v_cvt_pk_bf16_f32 v253, v186, v187
M v[196:199],v[164:167],a[92:95],v[196:199]
T v[182:183],v222,14336
buffer_load_dwordx4 v228, s[12:15], s79 offen offset:1024 nt lds
v_accvgpr_write_b32 a224, v252
v_accvgpr_write_b32 a225, v253
M v[192:195],v[164:167],a[88:91],v[192:195]
T v[152:153],v222,16384
v_cvt_pk_bf16_f32 v252, v188, v189
v_cvt_pk_bf16_f32 v253, v190, v191
M v[192:195],v[160:163],a[80:83],v[192:195]
T v[154:155],v222,18432
v_accvgpr_write_b32 a228, v252
v_accvgpr_write_b32 a229, v253
M v[192:195],v[156:159],a[72:75],v[192:195]
T v[156:157],v222,20480
v_max3_f32 v248, v248, v122, v123
W 5
M v[184:187],v[168:171],a[48:51],0
T v[158:159],v222,22528
v_max3_f32 v249, v249, v126, v127
M v[184:187],v[172:175],a[56:59],v[184:187]
T v[160:161],v223,0
v_max3_f32 v248, v248, v128, v129
M v[184:187],v[176:179],a[64:67],v[184:187]
T v[162:163],v223,2048
v_max3_f32 v249, v249, v132, v133
M v[188:191],v[176:179],a[68:71],0
T v[164:165],v223,4096
v_max3_f32 v248, v248, v130, v131
M v[188:191],v[172:175],a[60:63],v[188:191]
T v[166:167],v223,6144
v_max3_f32 v249, v249, v134, v135
M v[188:191],v[168:171],a[52:55],v[188:191]
T v[168:169],v223,8192
v_cvt_pk_bf16_f32 v252, v192, v193
W 5
M v[188:191],v[180:183],a[76:79],v[188:191]
T v[170:171],v223,10240
v_cvt_pk_bf16_f32 v253, v194, v195
M v[188:191],v[152:155],a[84:87],v[188:191]
T v[172:173],v223,12288
v_accvgpr_write_b32 a226, v252
v_accvgpr_write_b32 a227, v253
M v[188:191],v[156:159],a[92:95],v[188:191]
T v[174:175],v223,14336
v_cvt_pk_bf16_f32 v252, v196, v197
M v[184:187],v[156:159],a[88:91],v[184:187]
T v[176:177],v223,16384
v_cvt_pk_bf16_f32 v253, v198, v199
v_accvgpr_write_b32 a230, v252
M v[184:187],v[152:155],a[80:83],v[184:187]
T v[178:179],v223,18432
v_accvgpr_write_b32 a231, v253
M v[184:187],v[180:183],a[72:75],v[184:187]
T v[180:181],v223,20480
v_max_f32 v250, v248, v249
W 5
M v[192:195],v[160:163],a[48:51],0
T v[182:183],v223,22528
v_cmp_lt_f32 vcc, s60, v250
M v[192:195],v[164:167],a[56:59],v[192:195]
s_nop 0
s_cbranch_vccnz LST1_6_%=
LRT1_6_%=:
v_add_u32 v216, 0, v224
M v[192:195],v[168:171],a[64:67],v[192:195]
v_exp_f32 v200, v120
M v[196:199],v[168:171],a[68:71],0
v_xor_b32 v217, 32, v216
v_xor_b32 v218, 64, v216
M v[196:199],v[164:167],a[60:63],v[196:199]
v_xor_b32 v219, 96, v216
s_waitcnt vmcnt(8)
s_barrier
T v[152:153],v216,0
T v[154:155],v216,6144
v_exp_f32 v201, v121
M v[196:199],v[160:163],a[52:55],v[196:199]
T v[156:157],v217,0
v_exp_f32 v202, v122
W 3
M v[196:199],v[172:175],a[76:79],v[196:199]
T v[158:159],v217,6144
buffer_load_dwordx4 v229, s[12:15], s79 offen offset:2048 nt lds
M v[196:199],v[176:179],a[84:87],v[196:199]
T v[160:161],v218,0
v_cvt_pk_bf16_f32 v144, v200, v201
M v[196:199],v[180:183],a[92:95],v[196:199]
T v[162:163],v218,6144
v_exp_f32 v203, v123
M v[192:195],v[180:183],a[88:91],v[192:195]
T v[164:165],v219,0
v_cvt_pk_bf16_f32 v145, v202, v203
M v[192:195],v[176:179],a[80:83],v[192:195]
T v[166:167],v219,6144
W 5
M v[192:195],v[172:175],a[72:75],v[192:195]
T v[168:169],v216,128
v_exp_f32 v200, v124
M v[0:3],v[152:155],v[136:139],v[0:3]
T v[170:171],v216,6272
v_exp_f32 v201, v125
M v[4:7],v[152:155],v[140:143],v[4:7]
T v[172:173],v217,128
v_cvt_pk_bf16_f32 v148, v200, v201
M v[96:99],v[244:247],v[136:139],v[96:99]
T v[174:175],v217,6272
s_add_u32 s77, s66, s42
M v[100:103],v[244:247],v[140:143],v[100:103]
T v[176:177],v218,128
v_exp_f32 v202, v126
W 5
M v[8:11],v[156:159],v[136:139],v[8:11]
T v[178:179],v218,6272
s_add_u32 s78, s77, 245760
M v[12:15],v[156:159],v[140:143],v[12:15]
T v[180:181],v219,128
v_exp_f32 v203, v127
M v[16:19],v[160:163],v[136:139],v[16:19]
T v[182:183],v219,6272
v_cvt_pk_bf16_f32 v149, v202, v203
M v[20:23],v[160:163],v[140:143],v[20:23]
T v[152:153],v216,256
s_add_u32 m0, s40, 122880
M v[24:27],v[164:167],v[136:139],v[24:27]
T v[154:155],v216,6400
v_exp_f32 v200, v128
M v[28:31],v[164:167],v[140:143],v[28:31]
T v[156:157],v217,256
v_exp_f32 v201, v129
W 5
M v[32:35],v[168:171],v[136:139],v[32:35]
T v[158:159],v217,6400
v_cvt_pk_bf16_f32 v146, v200, v201
M v[36:39],v[168:171],v[140:143],v[36:39]
T v[160:161],v218,256
buffer_load_dwordx4 v230, s[20:23], s78 offen lds
M v[40:43],v[172:175],v[136:139],v[40:43]
T v[162:163],v218,6400
v_exp_f32 v202, v130
M v[44:47],v[172:175],v[140:143],v[44:47]
T v[164:165],v219,256
W 5
M v[48:51],v[176:179],v[136:139],v[48:51]
T v[166:167],v219,6400
v_exp_f32 v203, v131
M v[52:55],v[176:179],v[140:143],v[52:55]
T v[168:169],v216,12288
v_cvt_pk_bf16_f32 v147, v202, v203
M v[56:59],v[180:183],v[136:139],v[56:59]
T v[170:171],v216,18432
buffer_load_dwordx4 v231, s[20:23], s78 offen offset:1024 lds
M v[60:63],v[180:183],v[140:143],v[60:63]
T v[172:173],v217,12288
v_exp_f32 v200, v132
M v[64:67],v[152:155],v[136:139],v[64:67]
T v[174:175],v217,18432
v_exp_f32 v201, v133
M v[68:71],v[152:155],v[140:143],v[68:71]
T v[176:177],v218,12288
W 5
M v[72:75],v[156:159],v[136:139],v[72:75]
T v[178:179],v218,18432
v_cvt_pk_bf16_f32 v150, v200, v201
M v[76:79],v[156:159],v[140:143],v[76:79]
T v[180:181],v219,12288
v_exp_f32 v202, v134
M v[80:83],v[160:163],v[136:139],v[80:83]
T v[182:183],v219,18432
v_cvt_pk_bf16_f32 v252, v184, v185
M v[84:87],v[160:163],v[140:143],v[84:87]
T v[152:153],v216,12416
v_cvt_pk_bf16_f32 v253, v186, v187
M v[88:91],v[164:167],v[136:139],v[88:91]
T v[154:155],v216,18560
v_accvgpr_write_b32 a232, v252
v_exp_f32 v203, v135
M v[92:95],v[164:167],v[140:143],v[92:95]
T v[156:157],v217,12416
v_cvt_pk_bf16_f32 v151, v202, v203
W 5
M v[0:3],v[168:171],v[144:147],v[0:3]
T v[158:159],v217,18560
buffer_load_dwordx4 v232, s[20:23], s78 offen offset:2048 lds
M v[4:7],v[168:171],v[148:151],v[4:7]
T v[160:161],v218,12416
v_accvgpr_write_b32 a233, v253
M v[96:99],v[244:247],v[144:147],v[96:99]
T v[162:163],v218,18560
v_cvt_pk_bf16_f32 v252, v188, v189
M v[100:103],v[244:247],v[148:151],v[100:103]
T v[164:165],v219,12416
v_cvt_pk_bf16_f32 v253, v190, v191
M v[8:11],v[172:175],v[144:147],v[8:11]
T v[166:167],v219,18560
v_accvgpr_write_b32 a236, v252
M v[12:15],v[172:175],v[148:151],v[12:15]
T v[168:169],v216,12544
v_accvgpr_write_b32 a237, v253
M v[16:19],v[176:179],v[144:147],v[16:19]
T v[170:171],v216,18688
v_cvt_pk_bf16_f32 v252, v192, v193
M v[20:23],v[176:179],v[148:151],v[20:23]
T v[172:173],v217,12544
W 5
M v[24:27],v[180:183],v[144:147],v[24:27]
T v[174:175],v217,18688
v_cvt_pk_bf16_f32 v253, v194, v195
M v[28:31],v[180:183],v[148:151],v[28:31]
T v[176:177],v218,12544
v_accvgpr_write_b32 a234, v252
M v[32:35],v[152:155],v[144:147],v[32:35]
T v[178:179],v218,18688
v_accvgpr_write_b32 a235, v253
M v[36:39],v[152:155],v[148:151],v[36:39]
T v[180:181],v219,12544
v_cvt_pk_bf16_f32 v252, v196, v197
M v[40:43],v[156:159],v[144:147],v[40:43]
T v[182:183],v219,18688
v_add_u32 v220, 24576, v225
M v[44:47],v[156:159],v[148:151],v[44:47]
v_xor_b32 v221, 64, v220
v_cvt_pk_bf16_f32 v253, v198, v199
M v[48:51],v[160:163],v[144:147],v[48:51]
s_waitcnt vmcnt(9)
s_barrier
ds_read_b128 v[152:155], v220 offset:0
M v[52:55],v[160:163],v[148:151],v[52:55]
ds_read_b128 v[156:159], v221 offset:0
W 4
M v[56:59],v[164:167],v[144:147],v[56:59]
ds_read_b128 v[160:163], v220 offset:128
v_accvgpr_write_b32 a238, v252
M v[60:63],v[164:167],v[148:151],v[60:63]
v_accvgpr_write_b32 a239, v253
W 3
M v[64:67],v[168:171],v[144:147],v[64:67]
M v[68:71],v[168:171],v[148:151],v[68:71]
ds_read_b128 v[164:167], v221 offset:128
M v[72:75],v[172:175],v[144:147],v[72:75]
ds_read_b128 v[168:171], v220 offset:256
W 3
M v[76:79],v[172:175],v[148:151],v[76:79]
ds_read_b128 v[172:175], v221 offset:256
M v[80:83],v[176:179],v[144:147],v[80:83]
M v[84:87],v[176:179],v[148:151],v[84:87]
M v[88:91],v[180:183],v[144:147],v[88:91]
W 2
M v[92:95],v[180:183],v[148:151],v[92:95]
ds_read_b128 v[176:179], v220 offset:6144
M v[104:107],v[152:155],a[0:3],v[204:207]
ds_read_b128 v[180:183], v221 offset:6144
s_add_u32 s77, s67, s42
M v[104:107],v[156:159],a[8:11],v[104:107]
s_add_u32 s78, s77, 245760
s_add_u32 m0, s40, 98304
M v[104:107],v[160:163],a[16:19],v[104:107]
buffer_load_dwordx4 v230, s[20:23], s78 offen lds
W 2
M v[108:111],v[160:163],a[20:23],v[208:211]
M v[108:111],v[156:159],a[12:15],v[108:111]
M v[108:111],v[152:155],a[4:7],v[108:111]
ds_read_b128 v[152:155], v220 offset:6272
buffer_load_dwordx4 v231, s[20:23], s78 offen offset:1024 lds
M v[108:111],v[164:167],a[28:31],v[108:111]
ds_read_b128 v[156:159], v221 offset:6272
W 2
M v[108:111],v[168:171],a[36:39],v[108:111]
ds_read_b128 v[160:163], v220 offset:6400
M v[108:111],v[172:175],a[44:47],v[108:111]
buffer_load_dwordx4 v232, s[20:23], s78 offen offset:2048 lds
M v[104:107],v[172:175],a[40:43],v[104:107]
M v[104:107],v[168:171],a[32:35],v[104:107]
M v[104:107],v[164:167],a[24:27],v[104:107]
ds_read_b128 v[164:167], v221 offset:6400
W 2
M v[112:115],v[176:179],a[0:3],v[204:207]
ds_read_b128 v[168:171], v220 offset:12288
M v[112:115],v[180:183],a[8:11],v[112:115]
ds_read_b128 v[172:175], v221 offset:12288
M v[112:115],v[152:155],a[16:19],v[112:115]
M v[116:119],v[152:155],a[20:23],v[208:211]
v_max3_f32 v248, v104, v105, v106
v_max3_f32 v249, v108, v109, v110
M v[116:119],v[180:183],a[12:15],v[116:119]
v_max_f32 v248, v248, v107
v_max_f32 v249, v249, v111
M v[116:119],v[176:179],a[4:7],v[116:119]
ds_read_b128 v[176:179], v220 offset:12416
W 2
M v[116:119],v[156:159],a[28:31],v[116:119]
v_exp_f32 v200, v104
M v[116:119],v[160:163],a[36:39],v[116:119]
ds_read_b128 v[180:183], v221 offset:12416
v_add_u32 v216, 49152, v226
M v[116:119],v[164:167],a[44:47],v[116:119]
ds_read_b128 v[152:155], v220 offset:12544
v_xor_b32 v217, 32, v216
M v[112:115],v[164:167],a[40:43],v[112:115]
v_exp_f32 v201, v105
M v[112:115],v[160:163],a[32:35],v[112:115]
v_exp_f32 v202, v106
M v[112:115],v[156:159],a[24:27],v[112:115]
ds_read_b128 v[156:159], v221 offset:12544
v_cvt_pk_bf16_f32 v136, v200, v201
M v[120:123],v[168:171],a[0:3],v[204:207]
ds_read_b128 v[160:163], v220 offset:18432
W 3
M v[120:123],v[172:175],a[8:11],v[120:123]
v_exp_f32 v203, v107
M v[120:123],v[176:179],a[16:19],v[120:123]
ds_read_b128 v[164:167], v221 offset:18432
v_cvt_pk_bf16_f32 v137, v202, v203
M v[124:127],v[176:179],a[20:23],v[208:211]
v_exp_f32 v200, v108
M v[124:127],v[172:175],a[12:15],v[124:127]
v_exp_f32 v201, v109
M v[124:127],v[168:171],a[4:7],v[124:127]
ds_read_b128 v[168:171], v220 offset:18560
v_cvt_pk_bf16_f32 v140, v200, v201
M v[124:127],v[180:183],a[28:31],v[124:127]
ds_read_b128 v[172:175], v221 offset:18560
W 3
M v[124:127],v[152:155],a[36:39],v[124:127]
v_exp_f32 v202, v110
M v[124:127],v[156:159],a[44:47],v[124:127]
ds_read_b128 v[176:179], v220 offset:18688
v_max3_f32 v248, v248, v112, v113
M v[120:123],v[156:159],a[40:43],v[120:123]
v_exp_f32 v203, v111
M v[120:123],v[152:155],a[32:35],v[120:123]
v_cvt_pk_bf16_f32 v141, v202, v203
v_max3_f32 v249, v249, v116, v117
M v[120:123],v[180:183],a[24:27],v[120:123]
ds_read_b128 v[180:183], v221 offset:18688
v_xor_b32 v218, 64, v216
M v[128:131],v[160:163],a[0:3],v[204:207]
v_xor_b32 v219, 96, v216
W 2
M v[128:131],v[164:167],a[8:11],v[128:131]
s_waitcnt vmcnt(9)
s_barrier
T v[152:153],v216,0
M v[128:131],v[168:171],a[16:19],v[128:131]
T v[154:155],v216,2048
v_max3_f32 v248, v248, v114, v115
M v[132:135],v[168:171],a[20:23],v[208:211]
T v[156:157],v216,4096
v_max3_f32 v249, v249, v118, v119
M v[132:135],v[164:167],a[12:15],v[132:135]
T v[158:159],v216,6144
W 4
M v[132:135],v[160:163],a[4:7],v[132:135]
T v[160:161],v216,8192
M v[132:135],v[172:175],a[28:31],v[132:135]
T v[162:163],v216,10240
M v[132:135],v[176:179],a[36:39],v[132:135]
T v[164:165],v216,12288
W 5
M v[132:135],v[180:183],a[44:47],v[132:135]
T v[166:167],v216,14336
M v[128:131],v[180:183],a[40:43],v[128:131]
T v[168:169],v216,16384
v_exp_f32 v200, v112
M v[128:131],v[176:179],a[32:35],v[128:131]
T v[170:171],v216,18432
W 5
M v[128:131],v[172:175],a[24:27],v[128:131]
T v[172:173],v216,20480
v_exp_f32 v201, v113
M v[184:187],v[152:155],a[48:51],0
T v[174:175],v216,22528
v_cvt_pk_bf16_f32 v138, v200, v201
M v[184:187],v[156:159],a[56:59],v[184:187]
T v[176:177],v217,0
W 5
M v[184:187],v[160:163],a[64:67],v[184:187]
T v[178:179],v217,2048
s_add_u32 s77, s68, s42
M v[188:191],v[160:163],a[68:71],0
T v[180:181],v217,4096
v_exp_f32 v202, v114
M v[188:191],v[156:159],a[60:63],v[188:191]
T v[182:183],v217,6144
s_add_u32 s78, s77, 245760
M v[188:191],v[152:155],a[52:55],v[188:191]
T v[152:153],v217,8192
v_exp_f32 v203, v115
M v[188:191],v[164:167],a[76:79],v[188:191]
T v[154:155],v217,10240
W 5
M v[188:191],v[168:171],a[84:87],v[188:191]
T v[156:157],v217,12288
v_cvt_pk_bf16_f32 v139, v202, v203
M v[188:191],v[172:175],a[92:95],v[188:191]
T v[158:159],v217,14336
v_exp_f32 v200, v116
M v[184:187],v[172:175],a[88:91],v[184:187]
T v[160:161],v217,16384
s_add_u32 m0, s40, 0
M v[184:187],v[168:171],a[80:83],v[184:187]
T v[162:163],v217,18432
buffer_load_dwordx4 v230, s[20:23], s78 offen lds
M v[184:187],v[164:167],a[72:75],v[184:187]
T v[164:165],v217,20480
v_exp_f32 v201, v117
W 5
M v[192:195],v[176:179],a[48:51],0
T v[166:167],v217,22528
v_cvt_pk_bf16_f32 v142, v200, v201
M v[192:195],v[180:183],a[56:59],v[192:195]
T v[168:169],v218,0
buffer_load_dwordx4 v231, s[20:23], s78 offen offset:1024 lds
M v[192:195],v[152:155],a[64:67],v[192:195]
T v[170:171],v218,2048
v_exp_f32 v202, v118
M v[196:199],v[152:155],a[68:71],0
T v[172:173],v218,4096
v_max3_f32 v248, v248, v120, v121
M v[196:199],v[180:183],a[60:63],v[196:199]
T v[174:175],v218,6144
v_max3_f32 v249, v249, v124, v125
M v[196:199],v[176:179],a[52:55],v[196:199]
T v[176:177],v218,8192
v_exp_f32 v203, v119
W 5
M v[196:199],v[156:159],a[76:79],v[196:199]
T v[178:179],v218,10240
v_cvt_pk_bf16_f32 v143, v202, v203
M v[196:199],v[160:163],a[84:87],v[196:199]
T v[180:181],v218,12288
v_cvt_pk_bf16_f32 v252, v184, v185
v_cvt_pk_bf16_f32 v253, v186, v187
M v[196:199],v[164:167],a[92:95],v[196:199]
T v[182:183],v218,14336
v_accvgpr_write_b32 a240, v252
v_accvgpr_write_b32 a241, v253
M v[192:195],v[164:167],a[88:91],v[192:195]
T v[152:153],v218,16384
v_cvt_pk_bf16_f32 v252, v188, v189
v_cvt_pk_bf16_f32 v253, v190, v191
M v[192:195],v[160:163],a[80:83],v[192:195]
T v[154:155],v218,18432
v_accvgpr_write_b32 a244, v252
v_accvgpr_write_b32 a245, v253
M v[192:195],v[156:159],a[72:75],v[192:195]
T v[156:157],v218,20480
v_max3_f32 v248, v248, v122, v123
W 5
M v[184:187],v[168:171],a[48:51],0
T v[158:159],v218,22528
v_max3_f32 v249, v249, v126, v127
M v[184:187],v[172:175],a[56:59],v[184:187]
T v[160:161],v219,0
v_max3_f32 v248, v248, v128, v129
M v[184:187],v[176:179],a[64:67],v[184:187]
T v[162:163],v219,2048
v_max3_f32 v249, v249, v132, v133
M v[188:191],v[176:179],a[68:71],0
T v[164:165],v219,4096
v_max3_f32 v248, v248, v130, v131
M v[188:191],v[172:175],a[60:63],v[188:191]
T v[166:167],v219,6144
v_max3_f32 v249, v249, v134, v135
M v[188:191],v[168:171],a[52:55],v[188:191]
T v[168:169],v219,8192
v_cvt_pk_bf16_f32 v252, v192, v193
W 5
M v[188:191],v[180:183],a[76:79],v[188:191]
T v[170:171],v219,10240
v_cvt_pk_bf16_f32 v253, v194, v195
M v[188:191],v[152:155],a[84:87],v[188:191]
T v[172:173],v219,12288
v_accvgpr_write_b32 a242, v252
v_accvgpr_write_b32 a243, v253
M v[188:191],v[156:159],a[92:95],v[188:191]
T v[174:175],v219,14336
v_cvt_pk_bf16_f32 v252, v196, v197
M v[184:187],v[156:159],a[88:91],v[184:187]
T v[176:177],v219,16384
v_cvt_pk_bf16_f32 v253, v198, v199
v_accvgpr_write_b32 a246, v252
M v[184:187],v[152:155],a[80:83],v[184:187]
T v[178:179],v219,18432
v_accvgpr_write_b32 a247, v253
M v[184:187],v[180:183],a[72:75],v[184:187]
T v[180:181],v219,20480
v_max_f32 v250, v248, v249
W 5
M v[192:195],v[160:163],a[48:51],0
T v[182:183],v219,22528
v_cmp_lt_f32 vcc, s60, v250
M v[192:195],v[164:167],a[56:59],v[192:195]
s_nop 0
s_cbranch_vccnz LST1_7_%=
LRT1_7_%=:
v_add_u32 v220, 73728, v224
M v[192:195],v[168:171],a[64:67],v[192:195]
v_exp_f32 v200, v120
M v[196:199],v[168:171],a[68:71],0
v_xor_b32 v221, 32, v220
v_xor_b32 v222, 64, v220
M v[196:199],v[164:167],a[60:63],v[196:199]
v_xor_b32 v223, 96, v220
s_waitcnt vmcnt(8)
s_barrier
T v[152:153],v220,0
T v[154:155],v220,6144
v_exp_f32 v201, v121
M v[196:199],v[160:163],a[52:55],v[196:199]
T v[156:157],v221,0
v_exp_f32 v202, v122
W 3
M v[196:199],v[172:175],a[76:79],v[196:199]
T v[158:159],v221,6144
v_cvt_pk_bf16_f32 v144, v200, v201
M v[196:199],v[176:179],a[84:87],v[196:199]
T v[160:161],v222,0
buffer_load_dwordx4 v232, s[20:23], s78 offen offset:2048 lds
M v[196:199],v[180:183],a[92:95],v[196:199]
T v[162:163],v222,6144
v_exp_f32 v203, v123
M v[192:195],v[180:183],a[88:91],v[192:195]
T v[164:165],v223,0
v_cvt_pk_bf16_f32 v145, v202, v203
M v[192:195],v[176:179],a[80:83],v[192:195]
T v[166:167],v223,6144
W 5
M v[192:195],v[172:175],a[72:75],v[192:195]
T v[168:169],v220,128
v_exp_f32 v200, v124
M v[0:3],v[152:155],v[136:139],v[0:3]
T v[170:171],v220,6272
v_exp_f32 v201, v125
M v[4:7],v[152:155],v[140:143],v[4:7]
T v[172:173],v221,128
v_cvt_pk_bf16_f32 v148, v200, v201
M v[96:99],v[244:247],v[136:139],v[96:99]
T v[174:175],v221,6272
s_add_u32 s77, s69, s42
M v[100:103],v[244:247],v[140:143],v[100:103]
T v[176:177],v222,128
v_exp_f32 v202, v126
W 5
M v[8:11],v[156:159],v[136:139],v[8:11]
T v[178:179],v222,6272
s_add_u32 s78, s77, 245760
M v[12:15],v[156:159],v[140:143],v[12:15]
T v[180:181],v223,128
v_exp_f32 v203, v127
M v[16:19],v[160:163],v[136:139],v[16:19]
T v[182:183],v223,6272
v_cvt_pk_bf16_f32 v149, v202, v203
M v[20:23],v[160:163],v[140:143],v[20:23]
T v[152:153],v220,256
s_add_u32 m0, s40, 24576
M v[24:27],v[164:167],v[136:139],v[24:27]
T v[154:155],v220,6400
v_exp_f32 v200, v128
M v[28:31],v[164:167],v[140:143],v[28:31]
T v[156:157],v221,256
v_exp_f32 v201, v129
W 5
M v[32:35],v[168:171],v[136:139],v[32:35]
T v[158:159],v221,6400
v_cvt_pk_bf16_f32 v146, v200, v201
M v[36:39],v[168:171],v[140:143],v[36:39]
T v[160:161],v222,256
buffer_load_dwordx4 v230, s[20:23], s78 offen lds
M v[40:43],v[172:175],v[136:139],v[40:43]
T v[162:163],v222,6400
v_exp_f32 v202, v130
M v[44:47],v[172:175],v[140:143],v[44:47]
T v[164:165],v223,256
W 5
M v[48:51],v[176:179],v[136:139],v[48:51]
T v[166:167],v223,6400
v_exp_f32 v203, v131
M v[52:55],v[176:179],v[140:143],v[52:55]
T v[168:169],v220,12288
v_cvt_pk_bf16_f32 v147, v202, v203
M v[56:59],v[180:183],v[136:139],v[56:59]
T v[170:171],v220,18432
buffer_load_dwordx4 v231, s[20:23], s78 offen offset:1024 lds
M v[60:63],v[180:183],v[140:143],v[60:63]
T v[172:173],v221,12288
v_exp_f32 v200, v132
M v[64:67],v[152:155],v[136:139],v[64:67]
T v[174:175],v221,18432
v_exp_f32 v201, v133
M v[68:71],v[152:155],v[140:143],v[68:71]
T v[176:177],v222,12288
W 5
M v[72:75],v[156:159],v[136:139],v[72:75]
T v[178:179],v222,18432
v_cvt_pk_bf16_f32 v150, v200, v201
M v[76:79],v[156:159],v[140:143],v[76:79]
T v[180:181],v223,12288
v_exp_f32 v202, v134
M v[80:83],v[160:163],v[136:139],v[80:83]
T v[182:183],v223,18432
v_cvt_pk_bf16_f32 v252, v184, v185
M v[84:87],v[160:163],v[140:143],v[84:87]
T v[152:153],v220,12416
v_cvt_pk_bf16_f32 v253, v186, v187
M v[88:91],v[164:167],v[136:139],v[88:91]
T v[154:155],v220,18560
v_accvgpr_write_b32 a248, v252
v_exp_f32 v203, v135
M v[92:95],v[164:167],v[140:143],v[92:95]
T v[156:157],v221,12416
v_cvt_pk_bf16_f32 v151, v202, v203
W 5
M v[0:3],v[168:171],v[144:147],v[0:3]
T v[158:159],v221,18560
buffer_load_dwordx4 v232, s[20:23], s78 offen offset:2048 lds
M v[4:7],v[168:171],v[148:151],v[4:7]
T v[160:161],v222,12416
v_accvgpr_write_b32 a249, v253
M v[96:99],v[244:247],v[144:147],v[96:99]
T v[162:163],v222,18560
v_cvt_pk_bf16_f32 v252, v188, v189
M v[100:103],v[244:247],v[148:151],v[100:103]
T v[164:165],v223,12416
v_cvt_pk_bf16_f32 v253, v190, v191
M v[8:11],v[172:175],v[144:147],v[8:11]
T v[166:167],v223,18560
v_accvgpr_write_b32 a252, v252
M v[12:15],v[172:175],v[148:151],v[12:15]
T v[168:169],v220,12544
v_accvgpr_write_b32 a253, v253
M v[16:19],v[176:179],v[144:147],v[16:19]
T v[170:171],v220,18688
v_cvt_pk_bf16_f32 v252, v192, v193
M v[20:23],v[176:179],v[148:151],v[20:23]
T v[172:173],v221,12544
W 5
M v[24:27],v[180:183],v[144:147],v[24:27]
T v[174:175],v221,18688
v_cvt_pk_bf16_f32 v253, v194, v195
M v[28:31],v[180:183],v[148:151],v[28:31]
T v[176:177],v222,12544
v_accvgpr_write_b32 a250, v252
M v[32:35],v[152:155],v[144:147],v[32:35]
T v[178:179],v222,18688
v_accvgpr_write_b32 a251, v253
M v[36:39],v[152:155],v[148:151],v[36:39]
T v[180:181],v223,12544
v_cvt_pk_bf16_f32 v252, v196, v197
M v[40:43],v[156:159],v[144:147],v[40:43]
T v[182:183],v223,18688
v_add_u32 v216, 122880, v226
M v[44:47],v[156:159],v[148:151],v[44:47]
v_xor_b32 v217, 32, v216
v_xor_b32 v218, 64, v216
M v[48:51],v[160:163],v[144:147],v[48:51]
v_xor_b32 v219, 96, v216
v_cvt_pk_bf16_f32 v253, v198, v199
M v[52:55],v[160:163],v[148:151],v[52:55]
s_waitcnt vmcnt(9)
s_barrier
T v[152:153],v216,0
W 3
M v[56:59],v[164:167],v[144:147],v[56:59]
T v[154:155],v216,2048
v_accvgpr_write_b32 a254, v252
M v[60:63],v[164:167],v[148:151],v[60:63]
T v[156:157],v216,4096
v_accvgpr_write_b32 a255, v253
M v[64:67],v[168:171],v[144:147],v[64:67]
T v[158:159],v216,6144
W 4
M v[68:71],v[168:171],v[148:151],v[68:71]
T v[160:161],v216,8192
T v[162:163],v216,10240
M v[72:75],v[172:175],v[144:147],v[72:75]
M v[76:79],v[172:175],v[148:151],v[76:79]
T v[164:165],v216,12288
W 5
M v[80:83],v[176:179],v[144:147],v[80:83]
T v[166:167],v216,14336
M v[84:87],v[176:179],v[148:151],v[84:87]
T v[168:169],v216,16384
W 5
M v[88:91],v[180:183],v[144:147],v[88:91]
T v[170:171],v216,18432
W 4
M v[92:95],v[180:183],v[148:151],v[92:95]
T v[172:173],v216,20480
v_rcp_f32 v250, v96
v_rcp_f32 v252, v100
v_pk_mul_f32 v[0:1], v[0:1], v[250:251] op_sel_hi:[1,0]
v_pk_mul_f32 v[2:3], v[2:3], v[250:251] op_sel_hi:[1,0]
v_pk_mul_f32 v[8:9], v[8:9], v[250:251] op_sel_hi:[1,0]
v_pk_mul_f32 v[10:11], v[10:11], v[250:251] op_sel_hi:[1,0]
v_cvt_pk_bf16_f32 v104, v0, v1
v_cvt_pk_bf16_f32 v105, v2, v3
v_cvt_pk_bf16_f32 v106, v8, v9
v_cvt_pk_bf16_f32 v107, v10, v11
v_pk_mul_f32 v[4:5], v[4:5], v[252:253] op_sel_hi:[1,0]
v_pk_mul_f32 v[6:7], v[6:7], v[252:253] op_sel_hi:[1,0]
v_pk_mul_f32 v[12:13], v[12:13], v[252:253] op_sel_hi:[1,0]
v_pk_mul_f32 v[14:15], v[14:15], v[252:253] op_sel_hi:[1,0]
v_cvt_pk_bf16_f32 v108, v4, v5
v_cvt_pk_bf16_f32 v109, v6, v7
s_add_u32 s77, s70, s42
s_add_u32 s78, s77, 245760
s_add_u32 m0, s40, 49152
s_nop 0
buffer_load_dwordx4 v230, s[20:23], s78 offen lds
v_cvt_pk_bf16_f32 v110, v12, v13
v_cvt_pk_bf16_f32 v111, v14, v15
v_pk_mul_f32 v[16:17], v[16:17], v[250:251] op_sel_hi:[1,0]
v_pk_mul_f32 v[18:19], v[18:19], v[250:251] op_sel_hi:[1,0]
v_pk_mul_f32 v[24:25], v[24:25], v[250:251] op_sel_hi:[1,0]
v_pk_mul_f32 v[26:27], v[26:27], v[250:251] op_sel_hi:[1,0]
v_cvt_pk_bf16_f32 v112, v16, v17
v_cvt_pk_bf16_f32 v113, v18, v19
v_cvt_pk_bf16_f32 v114, v24, v25
v_cvt_pk_bf16_f32 v115, v26, v27
v_pk_mul_f32 v[20:21], v[20:21], v[252:253] op_sel_hi:[1,0]
v_pk_mul_f32 v[22:23], v[22:23], v[252:253] op_sel_hi:[1,0]
v_pk_mul_f32 v[28:29], v[28:29], v[252:253] op_sel_hi:[1,0]
v_pk_mul_f32 v[30:31], v[30:31], v[252:253] op_sel_hi:[1,0]
v_cvt_pk_bf16_f32 v116, v20, v21
v_cvt_pk_bf16_f32 v117, v22, v23
buffer_load_dwordx4 v231, s[20:23], s78 offen offset:1024 lds
v_cvt_pk_bf16_f32 v118, v28, v29
v_cvt_pk_bf16_f32 v119, v30, v31
v_pk_mul_f32 v[32:33], v[32:33], v[250:251] op_sel_hi:[1,0]
v_pk_mul_f32 v[34:35], v[34:35], v[250:251] op_sel_hi:[1,0]
v_pk_mul_f32 v[40:41], v[40:41], v[250:251] op_sel_hi:[1,0]
v_pk_mul_f32 v[42:43], v[42:43], v[250:251] op_sel_hi:[1,0]
v_cvt_pk_bf16_f32 v120, v32, v33
v_cvt_pk_bf16_f32 v121, v34, v35
v_cvt_pk_bf16_f32 v122, v40, v41
v_cvt_pk_bf16_f32 v123, v42, v43
v_pk_mul_f32 v[36:37], v[36:37], v[252:253] op_sel_hi:[1,0]
v_pk_mul_f32 v[38:39], v[38:39], v[252:253] op_sel_hi:[1,0]
v_pk_mul_f32 v[44:45], v[44:45], v[252:253] op_sel_hi:[1,0]
v_pk_mul_f32 v[46:47], v[46:47], v[252:253] op_sel_hi:[1,0]
v_cvt_pk_bf16_f32 v124, v36, v37
v_cvt_pk_bf16_f32 v125, v38, v39
buffer_load_dwordx4 v232, s[20:23], s78 offen offset:2048 lds
v_cvt_pk_bf16_f32 v126, v44, v45
v_cvt_pk_bf16_f32 v127, v46, v47
v_pk_mul_f32 v[48:49], v[48:49], v[250:251] op_sel_hi:[1,0]
v_pk_mul_f32 v[50:51], v[50:51], v[250:251] op_sel_hi:[1,0]
v_pk_mul_f32 v[56:57], v[56:57], v[250:251] op_sel_hi:[1,0]
v_pk_mul_f32 v[58:59], v[58:59], v[250:251] op_sel_hi:[1,0]
v_cvt_pk_bf16_f32 v128, v48, v49
v_cvt_pk_bf16_f32 v129, v50, v51
v_cvt_pk_bf16_f32 v130, v56, v57
v_cvt_pk_bf16_f32 v131, v58, v59
v_pk_mul_f32 v[52:53], v[52:53], v[252:253] op_sel_hi:[1,0]
v_pk_mul_f32 v[54:55], v[54:55], v[252:253] op_sel_hi:[1,0]
v_pk_mul_f32 v[60:61], v[60:61], v[252:253] op_sel_hi:[1,0]
v_pk_mul_f32 v[62:63], v[62:63], v[252:253] op_sel_hi:[1,0]
v_cvt_pk_bf16_f32 v132, v52, v53
v_cvt_pk_bf16_f32 v133, v54, v55
v_cvt_pk_bf16_f32 v134, v60, v61
v_cvt_pk_bf16_f32 v135, v62, v63
v_pk_mul_f32 v[64:65], v[64:65], v[250:251] op_sel_hi:[1,0]
v_pk_mul_f32 v[66:67], v[66:67], v[250:251] op_sel_hi:[1,0]
v_pk_mul_f32 v[72:73], v[72:73], v[250:251] op_sel_hi:[1,0]
v_pk_mul_f32 v[74:75], v[74:75], v[250:251] op_sel_hi:[1,0]
v_cvt_pk_bf16_f32 v136, v64, v65
v_cvt_pk_bf16_f32 v137, v66, v67
v_cvt_pk_bf16_f32 v138, v72, v73
v_cvt_pk_bf16_f32 v139, v74, v75
v_pk_mul_f32 v[68:69], v[68:69], v[252:253] op_sel_hi:[1,0]
v_pk_mul_f32 v[70:71], v[70:71], v[252:253] op_sel_hi:[1,0]
v_pk_mul_f32 v[76:77], v[76:77], v[252:253] op_sel_hi:[1,0]
v_pk_mul_f32 v[78:79], v[78:79], v[252:253] op_sel_hi:[1,0]
v_cvt_pk_bf16_f32 v140, v68, v69
v_cvt_pk_bf16_f32 v141, v70, v71
v_cvt_pk_bf16_f32 v142, v76, v77
v_cvt_pk_bf16_f32 v143, v78, v79
v_pk_mul_f32 v[80:81], v[80:81], v[250:251] op_sel_hi:[1,0]
v_pk_mul_f32 v[82:83], v[82:83], v[250:251] op_sel_hi:[1,0]
v_pk_mul_f32 v[88:89], v[88:89], v[250:251] op_sel_hi:[1,0]
v_pk_mul_f32 v[90:91], v[90:91], v[250:251] op_sel_hi:[1,0]
v_cvt_pk_bf16_f32 v144, v80, v81
v_cvt_pk_bf16_f32 v145, v82, v83
v_cvt_pk_bf16_f32 v146, v88, v89
v_cvt_pk_bf16_f32 v147, v90, v91
v_pk_mul_f32 v[84:85], v[84:85], v[252:253] op_sel_hi:[1,0]
v_pk_mul_f32 v[86:87], v[86:87], v[252:253] op_sel_hi:[1,0]
v_pk_mul_f32 v[92:93], v[92:93], v[252:253] op_sel_hi:[1,0]
v_pk_mul_f32 v[94:95], v[94:95], v[252:253] op_sel_hi:[1,0]
v_cvt_pk_bf16_f32 v148, v84, v85
v_cvt_pk_bf16_f32 v149, v86, v87
v_cvt_pk_bf16_f32 v150, v92, v93
v_cvt_pk_bf16_f32 v151, v94, v95
M v[184:187],v[236:239],a[96:99],0
T v[174:175],v216,22528
M v[184:187],v[152:155],v[104:107],v[184:187]
W 4
M v[184:187],v[156:159],v[112:115],v[184:187]
T v[176:177],v217,0
M v[184:187],v[160:163],v[120:123],v[184:187]
T v[178:179],v217,2048
W 4
M v[188:191],v[236:239],a[100:103],0
T v[180:181],v217,4096
M v[188:191],v[160:163],v[124:127],v[188:191]
T v[182:183],v217,6144
W 4
M v[188:191],v[156:159],v[116:119],v[188:191]
M v[188:191],v[152:155],v[108:111],v[188:191]
T v[152:153],v217,8192
T v[154:155],v217,10240
M v[188:191],v[164:167],v[132:135],v[188:191]
W 4
M v[188:191],v[168:171],v[140:143],v[188:191]
T v[156:157],v217,12288
M v[188:191],v[172:175],v[148:151],v[188:191]
T v[158:159],v217,14336
W 4
M v[184:187],v[172:175],v[144:147],v[184:187]
T v[160:161],v217,16384
M v[184:187],v[168:171],v[136:139],v[184:187]
T v[162:163],v217,18432
W 4
M v[184:187],v[164:167],v[128:131],v[184:187]
T v[164:165],v217,20480
M v[192:195],v[234:237],a[96:99],0
T v[166:167],v217,22528
M v[192:195],v[176:179],v[104:107],v[192:195]
W 4
M v[192:195],v[180:183],v[112:115],v[192:195]
T v[168:169],v218,0
M v[192:195],v[152:155],v[120:123],v[192:195]
T v[170:171],v218,2048
W 4
M v[196:199],v[234:237],a[100:103],0
T v[172:173],v218,4096
M v[196:199],v[152:155],v[124:127],v[196:199]
T v[174:175],v218,6144
v_cvt_pk_bf16_f32 v0, v184, v185
M v[196:199],v[180:183],v[116:119],v[196:199]
v_cvt_pk_bf16_f32 v1, v186, v187
v_cvt_pk_bf16_f32 v4, v188, v189
M v[196:199],v[176:179],v[108:111],v[196:199]
T v[176:177],v218,8192
v_cvt_pk_bf16_f32 v5, v190, v191
M v[196:199],v[156:159],v[132:135],v[196:199]
T v[178:179],v218,10240
v_add_u32 v220, 98304, v226
M v[196:199],v[160:163],v[140:143],v[196:199]
T v[180:181],v218,12288
W 4
M v[196:199],v[164:167],v[148:151],v[196:199]
T v[182:183],v218,14336
v_xor_b32 v221, 32, v220
M v[192:195],v[164:167],v[144:147],v[192:195]
T v[152:153],v218,16384
v_xor_b32 v222, 64, v220
M v[192:195],v[160:163],v[136:139],v[192:195]
T v[154:155],v218,18432
v_xor_b32 v223, 96, v220
M v[192:195],v[156:159],v[128:131],v[192:195]
T v[156:157],v218,20480
W 5
M v[184:187],v[236:239],a[104:107],0
T v[158:159],v218,22528
M v[184:187],v[168:171],v[104:107],v[184:187]
W 4
M v[184:187],v[172:175],v[112:115],v[184:187]
T v[160:161],v219,0
M v[184:187],v[176:179],v[120:123],v[184:187]
T v[162:163],v219,2048
W 4
M v[188:191],v[236:239],a[108:111],0
T v[164:165],v219,4096
M v[188:191],v[176:179],v[124:127],v[188:191]
T v[166:167],v219,6144
v_cvt_pk_bf16_f32 v2, v192, v193
M v[188:191],v[172:175],v[116:119],v[188:191]
v_cvt_pk_bf16_f32 v3, v194, v195
v_cvt_pk_bf16_f32 v6, v196, v197
M v[188:191],v[168:171],v[108:111],v[188:191]
T v[168:169],v219,8192
v_cvt_pk_bf16_f32 v7, v198, v199
M v[188:191],v[180:183],v[132:135],v[188:191]
T v[170:171],v219,10240
W 4
M v[188:191],v[152:155],v[140:143],v[188:191]
T v[172:173],v219,12288
s_mov_b32 s76, 0
buffer_store_dwordx4 v[0:3], v240, s[32:35], s76 offen sc1
M v[188:191],v[156:159],v[148:151],v[188:191]
T v[174:175],v219,14336
W 4
M v[184:187],v[156:159],v[144:147],v[184:187]
T v[176:177],v219,16384
M v[184:187],v[152:155],v[136:139],v[184:187]
T v[178:179],v219,18432
M v[184:187],v[180:183],v[128:131],v[184:187]
T v[180:181],v219,20480
buffer_store_dwordx4 v[4:7], v240, s[36:39], s76 offen sc1
M v[192:195],v[234:237],a[104:107],0
T v[182:183],v219,22528
W 5
M v[192:195],v[160:163],v[104:107],v[192:195]
M v[192:195],v[164:167],v[112:115],v[192:195]
s_waitcnt vmcnt(11)
s_barrier
T v[152:153],v220,0
M v[192:195],v[168:171],v[120:123],v[192:195]
T v[154:155],v220,2048
W 4
M v[196:199],v[234:237],a[108:111],0
T v[156:157],v220,4096
M v[196:199],v[168:171],v[124:127],v[196:199]
T v[158:159],v220,6144
v_cvt_pk_bf16_f32 v8, v184, v185
M v[196:199],v[164:167],v[116:119],v[196:199]
v_cvt_pk_bf16_f32 v9, v186, v187
v_cvt_pk_bf16_f32 v12, v188, v189
M v[196:199],v[160:163],v[108:111],v[196:199]
T v[160:161],v220,8192
v_cvt_pk_bf16_f32 v13, v190, v191
M v[196:199],v[172:175],v[132:135],v[196:199]
T v[162:163],v220,10240
W 4
M v[196:199],v[176:179],v[140:143],v[196:199]
T v[164:165],v220,12288
M v[196:199],v[180:183],v[148:151],v[196:199]
T v[166:167],v220,14336
W 4
M v[192:195],v[180:183],v[144:147],v[192:195]
T v[168:169],v220,16384
M v[192:195],v[176:179],v[136:139],v[192:195]
T v[170:171],v220,18432
M v[192:195],v[172:175],v[128:131],v[192:195]
T v[172:173],v220,20480
W 5
M v[184:187],v[236:239],a[112:115],0
T v[174:175],v220,22528
s_add_u32 s77, s71, s42
M v[184:187],v[152:155],v[104:107],v[184:187]
s_add_u32 s78, s77, 245760
s_add_u32 m0, s40, 73728
M v[184:187],v[156:159],v[112:115],v[184:187]
T v[176:177],v221,0
buffer_load_dwordx4 v230, s[20:23], s78 offen lds
M v[184:187],v[160:163],v[120:123],v[184:187]
T v[178:179],v221,2048
v_add_u32 v200, s54, v240
M v[188:191],v[236:239],a[116:119],0
T v[180:181],v221,4096
v_add_u32 v201, s55, v240
M v[188:191],v[160:163],v[124:127],v[188:191]
T v[182:183],v221,6144
v_cvt_pk_bf16_f32 v10, v192, v193
M v[188:191],v[156:159],v[116:119],v[188:191]
v_cvt_pk_bf16_f32 v11, v194, v195
v_cvt_pk_bf16_f32 v14, v196, v197
M v[188:191],v[152:155],v[108:111],v[188:191]
T v[152:153],v221,8192
W 4
M v[188:191],v[164:167],v[132:135],v[188:191]
T v[154:155],v221,10240
buffer_load_dwordx4 v231, s[20:23], s78 offen offset:1024 lds
M v[188:191],v[168:171],v[140:143],v[188:191]
T v[156:157],v221,12288
v_cvt_pk_bf16_f32 v15, v198, v199
M v[188:191],v[172:175],v[148:151],v[188:191]
T v[158:159],v221,14336
v_add_u32 v202, s56, v240
M v[184:187],v[172:175],v[144:147],v[184:187]
T v[160:161],v221,16384
s_mov_b32 s76, 16384
buffer_store_dwordx4 v[8:11], v240, s[32:35], s76 offen sc1
M v[184:187],v[168:171],v[136:139],v[184:187]
T v[162:163],v221,18432
buffer_load_dwordx4 v232, s[20:23], s78 offen offset:2048 lds
M v[184:187],v[164:167],v[128:131],v[184:187]
T v[164:165],v221,20480
W 5
M v[192:195],v[234:237],a[112:115],0
T v[166:167],v221,22528
M v[192:195],v[176:179],v[104:107],v[192:195]
buffer_store_dwordx4 v[12:15], v240, s[36:39], s76 offen sc1
W 4
M v[192:195],v[180:183],v[112:115],v[192:195]
T v[168:169],v222,0
M v[192:195],v[152:155],v[120:123],v[192:195]
T v[170:171],v222,2048
W 4
M v[196:199],v[234:237],a[116:119],0
T v[172:173],v222,4096
M v[196:199],v[152:155],v[124:127],v[196:199]
T v[174:175],v222,6144
v_cvt_pk_bf16_f32 v16, v184, v185
M v[196:199],v[180:183],v[116:119],v[196:199]
v_cvt_pk_bf16_f32 v17, v186, v187
v_cvt_pk_bf16_f32 v20, v188, v189
M v[196:199],v[176:179],v[108:111],v[196:199]
T v[176:177],v222,8192
v_cvt_pk_bf16_f32 v21, v190, v191
M v[196:199],v[156:159],v[132:135],v[196:199]
T v[178:179],v222,10240
v_add_u32 v216, 0, v226
M v[196:199],v[160:163],v[140:143],v[196:199]
T v[180:181],v222,12288
W 4
M v[196:199],v[164:167],v[148:151],v[196:199]
T v[182:183],v222,14336
v_xor_b32 v217, 32, v216
M v[192:195],v[164:167],v[144:147],v[192:195]
T v[152:153],v222,16384
v_xor_b32 v218, 64, v216
M v[192:195],v[160:163],v[136:139],v[192:195]
T v[154:155],v222,18432
v_xor_b32 v219, 96, v216
M v[192:195],v[156:159],v[128:131],v[192:195]
T v[156:157],v222,20480
W 5
M v[184:187],v[236:239],a[120:123],0
T v[158:159],v222,22528
M v[184:187],v[168:171],v[104:107],v[184:187]
W 4
M v[184:187],v[172:175],v[112:115],v[184:187]
T v[160:161],v223,0
M v[184:187],v[176:179],v[120:123],v[184:187]
T v[162:163],v223,2048
W 4
M v[188:191],v[236:239],a[124:127],0
T v[164:165],v223,4096
M v[188:191],v[176:179],v[124:127],v[188:191]
T v[166:167],v223,6144
v_cvt_pk_bf16_f32 v18, v192, v193
M v[188:191],v[172:175],v[116:119],v[188:191]
v_cvt_pk_bf16_f32 v19, v194, v195
v_cvt_pk_bf16_f32 v22, v196, v197
M v[188:191],v[168:171],v[108:111],v[188:191]
T v[168:169],v223,8192
v_cvt_pk_bf16_f32 v23, v198, v199
M v[188:191],v[180:183],v[132:135],v[188:191]
T v[170:171],v223,10240
W 4
M v[188:191],v[152:155],v[140:143],v[188:191]
T v[172:173],v223,12288
s_mov_b32 s76, 229376
buffer_store_dwordx4 v[16:19], v240, s[32:35], s76 offen sc1
M v[188:191],v[156:159],v[148:151],v[188:191]
T v[174:175],v223,14336
W 4
M v[184:187],v[156:159],v[144:147],v[184:187]
T v[176:177],v223,16384
M v[184:187],v[152:155],v[136:139],v[184:187]
T v[178:179],v223,18432
M v[184:187],v[180:183],v[128:131],v[184:187]
T v[180:181],v223,20480
buffer_store_dwordx4 v[20:23], v240, s[36:39], s76 offen sc1
M v[192:195],v[234:237],a[120:123],0
T v[182:183],v223,22528
W 5
M v[192:195],v[160:163],v[104:107],v[192:195]
M v[192:195],v[164:167],v[112:115],v[192:195]
s_waitcnt vmcnt(15)
s_barrier
T v[152:153],v216,0
M v[192:195],v[168:171],v[120:123],v[192:195]
T v[154:155],v216,2048
W 4
M v[196:199],v[234:237],a[124:127],0
T v[156:157],v216,4096
M v[196:199],v[168:171],v[124:127],v[196:199]
T v[158:159],v216,6144
v_cvt_pk_bf16_f32 v24, v184, v185
M v[196:199],v[164:167],v[116:119],v[196:199]
v_cvt_pk_bf16_f32 v25, v186, v187
v_cvt_pk_bf16_f32 v28, v188, v189
M v[196:199],v[160:163],v[108:111],v[196:199]
T v[160:161],v216,8192
v_cvt_pk_bf16_f32 v29, v190, v191
M v[196:199],v[172:175],v[132:135],v[196:199]
T v[162:163],v216,10240
W 4
M v[196:199],v[176:179],v[140:143],v[196:199]
T v[164:165],v216,12288
M v[196:199],v[180:183],v[148:151],v[196:199]
T v[166:167],v216,14336
W 4
M v[192:195],v[180:183],v[144:147],v[192:195]
T v[168:169],v216,16384
M v[192:195],v[176:179],v[136:139],v[192:195]
T v[170:171],v216,18432
M v[192:195],v[172:175],v[128:131],v[192:195]
T v[172:173],v216,20480
W 5
M v[184:187],v[236:239],a[128:131],0
T v[174:175],v216,22528
s_add_u32 s77, s96, s42
M v[184:187],v[152:155],v[104:107],v[184:187]
s_add_u32 s78, s77, 245760
s_add_u32 m0, s40, 122880
M v[184:187],v[156:159],v[112:115],v[184:187]
T v[176:177],v217,0
buffer_load_dwordx4 v230, s[20:23], s78 offen lds
M v[184:187],v[160:163],v[120:123],v[184:187]
T v[178:179],v217,2048
W 4
M v[188:191],v[236:239],a[132:135],0
T v[180:181],v217,4096
M v[188:191],v[160:163],v[124:127],v[188:191]
T v[182:183],v217,6144
v_cvt_pk_bf16_f32 v26, v192, v193
M v[188:191],v[156:159],v[116:119],v[188:191]
v_cvt_pk_bf16_f32 v27, v194, v195
v_cvt_pk_bf16_f32 v30, v196, v197
M v[188:191],v[152:155],v[108:111],v[188:191]
T v[152:153],v217,8192
buffer_load_dwordx4 v231, s[20:23], s78 offen offset:1024 lds
M v[188:191],v[164:167],v[132:135],v[188:191]
T v[154:155],v217,10240
v_cvt_pk_bf16_f32 v31, v198, v199
M v[188:191],v[168:171],v[140:143],v[188:191]
T v[156:157],v217,12288
W 4
M v[188:191],v[172:175],v[148:151],v[188:191]
T v[158:159],v217,14336
buffer_load_dwordx4 v232, s[20:23], s78 offen offset:2048 lds
M v[184:187],v[172:175],v[144:147],v[184:187]
T v[160:161],v217,16384
M v[184:187],v[168:171],v[136:139],v[184:187]
T v[162:163],v217,18432
s_mov_b32 s76, 245760
buffer_store_dwordx4 v[24:27], v240, s[32:35], s76 offen sc1
M v[184:187],v[164:167],v[128:131],v[184:187]
T v[164:165],v217,20480
W 5
M v[192:195],v[234:237],a[128:131],0
T v[166:167],v217,22528
M v[192:195],v[176:179],v[104:107],v[192:195]
W 4
M v[192:195],v[180:183],v[112:115],v[192:195]
T v[168:169],v218,0
buffer_store_dwordx4 v[28:31], v240, s[36:39], s76 offen sc1
M v[192:195],v[152:155],v[120:123],v[192:195]
T v[170:171],v218,2048
W 4
M v[196:199],v[234:237],a[132:135],0
T v[172:173],v218,4096
M v[196:199],v[152:155],v[124:127],v[196:199]
T v[174:175],v218,6144
v_cvt_pk_bf16_f32 v32, v184, v185
M v[196:199],v[180:183],v[116:119],v[196:199]
v_cvt_pk_bf16_f32 v33, v186, v187
v_cvt_pk_bf16_f32 v36, v188, v189
M v[196:199],v[176:179],v[108:111],v[196:199]
T v[176:177],v218,8192
v_cvt_pk_bf16_f32 v37, v190, v191
M v[196:199],v[156:159],v[132:135],v[196:199]
T v[178:179],v218,10240
v_add_u32 v220, 24576, v226
M v[196:199],v[160:163],v[140:143],v[196:199]
T v[180:181],v218,12288
W 4
M v[196:199],v[164:167],v[148:151],v[196:199]
T v[182:183],v218,14336
v_xor_b32 v221, 32, v220
M v[192:195],v[164:167],v[144:147],v[192:195]
T v[152:153],v218,16384
v_xor_b32 v222, 64, v220
M v[192:195],v[160:163],v[136:139],v[192:195]
T v[154:155],v218,18432
v_xor_b32 v223, 96, v220
M v[192:195],v[156:159],v[128:131],v[192:195]
T v[156:157],v218,20480
W 5
M v[184:187],v[236:239],a[136:139],0
T v[158:159],v218,22528
M v[184:187],v[168:171],v[104:107],v[184:187]
W 4
M v[184:187],v[172:175],v[112:115],v[184:187]
T v[160:161],v219,0
M v[184:187],v[176:179],v[120:123],v[184:187]
T v[162:163],v219,2048
W 4
M v[188:191],v[236:239],a[140:143],0
T v[164:165],v219,4096
M v[188:191],v[176:179],v[124:127],v[188:191]
T v[166:167],v219,6144
v_cvt_pk_bf16_f32 v34, v192, v193
M v[188:191],v[172:175],v[116:119],v[188:191]
v_cvt_pk_bf16_f32 v35, v194, v195
v_cvt_pk_bf16_f32 v38, v196, v197
M v[188:191],v[168:171],v[108:111],v[188:191]
T v[168:169],v219,8192
v_cvt_pk_bf16_f32 v39, v198, v199
M v[188:191],v[180:183],v[132:135],v[188:191]
T v[170:171],v219,10240
W 4
M v[188:191],v[152:155],v[140:143],v[188:191]
T v[172:173],v219,12288
s_mov_b32 s76, 458752
buffer_store_dwordx4 v[32:35], v240, s[32:35], s76 offen sc1
M v[188:191],v[156:159],v[148:151],v[188:191]
T v[174:175],v219,14336
W 4
M v[184:187],v[156:159],v[144:147],v[184:187]
T v[176:177],v219,16384
M v[184:187],v[152:155],v[136:139],v[184:187]
T v[178:179],v219,18432
M v[184:187],v[180:183],v[128:131],v[184:187]
T v[180:181],v219,20480
buffer_store_dwordx4 v[36:39], v240, s[36:39], s76 offen sc1
M v[192:195],v[234:237],a[136:139],0
T v[182:183],v219,22528
W 5
M v[192:195],v[160:163],v[104:107],v[192:195]
M v[192:195],v[164:167],v[112:115],v[192:195]
s_waitcnt vmcnt(19)
s_barrier
T v[152:153],v220,0
M v[192:195],v[168:171],v[120:123],v[192:195]
T v[154:155],v220,2048
W 4
M v[196:199],v[234:237],a[140:143],0
T v[156:157],v220,4096
M v[196:199],v[168:171],v[124:127],v[196:199]
T v[158:159],v220,6144
v_cvt_pk_bf16_f32 v40, v184, v185
M v[196:199],v[164:167],v[116:119],v[196:199]
v_cvt_pk_bf16_f32 v41, v186, v187
v_cvt_pk_bf16_f32 v44, v188, v189
M v[196:199],v[160:163],v[108:111],v[196:199]
T v[160:161],v220,8192
v_cvt_pk_bf16_f32 v45, v190, v191
M v[196:199],v[172:175],v[132:135],v[196:199]
T v[162:163],v220,10240
W 4
M v[196:199],v[176:179],v[140:143],v[196:199]
T v[164:165],v220,12288
M v[196:199],v[180:183],v[148:151],v[196:199]
T v[166:167],v220,14336
W 4
M v[192:195],v[180:183],v[144:147],v[192:195]
T v[168:169],v220,16384
M v[192:195],v[176:179],v[136:139],v[192:195]
T v[170:171],v220,18432
M v[192:195],v[172:175],v[128:131],v[192:195]
T v[172:173],v220,20480
W 5
M v[184:187],v[236:239],a[144:147],0
T v[174:175],v220,22528
s_add_u32 s77, s97, s42
M v[184:187],v[152:155],v[104:107],v[184:187]
s_add_u32 s78, s77, 245760
s_add_u32 m0, s40, 98304
M v[184:187],v[156:159],v[112:115],v[184:187]
T v[176:177],v221,0
buffer_load_dwordx4 v230, s[20:23], s78 offen lds
M v[184:187],v[160:163],v[120:123],v[184:187]
T v[178:179],v221,2048
W 4
M v[188:191],v[236:239],a[148:151],0
T v[180:181],v221,4096
M v[188:191],v[160:163],v[124:127],v[188:191]
T v[182:183],v221,6144
v_cvt_pk_bf16_f32 v42, v192, v193
M v[188:191],v[156:159],v[116:119],v[188:191]
v_cvt_pk_bf16_f32 v43, v194, v195
v_cvt_pk_bf16_f32 v46, v196, v197
M v[188:191],v[152:155],v[108:111],v[188:191]
T v[152:153],v221,8192
buffer_load_dwordx4 v231, s[20:23], s78 offen offset:1024 lds
M v[188:191],v[164:167],v[132:135],v[188:191]
T v[154:155],v221,10240
v_cvt_pk_bf16_f32 v47, v198, v199
M v[188:191],v[168:171],v[140:143],v[188:191]
T v[156:157],v221,12288
W 4
M v[188:191],v[172:175],v[148:151],v[188:191]
T v[158:159],v221,14336
buffer_load_dwordx4 v232, s[20:23], s78 offen offset:2048 lds
M v[184:187],v[172:175],v[144:147],v[184:187]
T v[160:161],v221,16384
M v[184:187],v[168:171],v[136:139],v[184:187]
T v[162:163],v221,18432
s_mov_b32 s76, 475136
buffer_store_dwordx4 v[40:43], v240, s[32:35], s76 offen sc1
M v[184:187],v[164:167],v[128:131],v[184:187]
T v[164:165],v221,20480
W 5
M v[192:195],v[234:237],a[144:147],0
T v[166:167],v221,22528
M v[192:195],v[176:179],v[104:107],v[192:195]
W 4
M v[192:195],v[180:183],v[112:115],v[192:195]
T v[168:169],v222,0
buffer_store_dwordx4 v[44:47], v240, s[36:39], s76 offen sc1
M v[192:195],v[152:155],v[120:123],v[192:195]
T v[170:171],v222,2048
W 4
M v[196:199],v[234:237],a[148:151],0
T v[172:173],v222,4096
M v[196:199],v[152:155],v[124:127],v[196:199]
T v[174:175],v222,6144
v_cvt_pk_bf16_f32 v48, v184, v185
M v[196:199],v[180:183],v[116:119],v[196:199]
v_cvt_pk_bf16_f32 v49, v186, v187
v_cvt_pk_bf16_f32 v52, v188, v189
M v[196:199],v[176:179],v[108:111],v[196:199]
T v[176:177],v222,8192
v_cvt_pk_bf16_f32 v53, v190, v191
M v[196:199],v[156:159],v[132:135],v[196:199]
T v[178:179],v222,10240
v_add_u32 v216, 49152, v226
M v[196:199],v[160:163],v[140:143],v[196:199]
T v[180:181],v222,12288
W 4
M v[196:199],v[164:167],v[148:151],v[196:199]
T v[182:183],v222,14336
v_xor_b32 v217, 32, v216
M v[192:195],v[164:167],v[144:147],v[192:195]
T v[152:153],v222,16384
v_xor_b32 v218, 64, v216
M v[192:195],v[160:163],v[136:139],v[192:195]
T v[154:155],v222,18432
v_xor_b32 v219, 96, v216
M v[192:195],v[156:159],v[128:131],v[192:195]
T v[156:157],v222,20480
W 5
M v[184:187],v[236:239],a[152:155],0
T v[158:159],v222,22528
M v[184:187],v[168:171],v[104:107],v[184:187]
W 4
M v[184:187],v[172:175],v[112:115],v[184:187]
T v[160:161],v223,0
M v[184:187],v[176:179],v[120:123],v[184:187]
T v[162:163],v223,2048
W 4
M v[188:191],v[236:239],a[156:159],0
T v[164:165],v223,4096
M v[188:191],v[176:179],v[124:127],v[188:191]
T v[166:167],v223,6144
v_cvt_pk_bf16_f32 v50, v192, v193
M v[188:191],v[172:175],v[116:119],v[188:191]
v_cvt_pk_bf16_f32 v51, v194, v195
v_cvt_pk_bf16_f32 v54, v196, v197
M v[188:191],v[168:171],v[108:111],v[188:191]
T v[168:169],v223,8192
v_cvt_pk_bf16_f32 v55, v198, v199
M v[188:191],v[180:183],v[132:135],v[188:191]
T v[170:171],v223,10240
W 4
M v[188:191],v[152:155],v[140:143],v[188:191]
T v[172:173],v223,12288
s_mov_b32 s76, 688128
buffer_store_dwordx4 v[48:51], v240, s[32:35], s76 offen sc1
M v[188:191],v[156:159],v[148:151],v[188:191]
T v[174:175],v223,14336
W 4
M v[184:187],v[156:159],v[144:147],v[184:187]
T v[176:177],v223,16384
M v[184:187],v[152:155],v[136:139],v[184:187]
T v[178:179],v223,18432
M v[184:187],v[180:183],v[128:131],v[184:187]
T v[180:181],v223,20480
buffer_store_dwordx4 v[52:55], v240, s[36:39], s76 offen sc1
M v[192:195],v[234:237],a[152:155],0
T v[182:183],v223,22528
W 5
M v[192:195],v[160:163],v[104:107],v[192:195]
M v[192:195],v[164:167],v[112:115],v[192:195]
s_waitcnt vmcnt(23)
s_barrier
T v[152:153],v216,0
M v[192:195],v[168:171],v[120:123],v[192:195]
T v[154:155],v216,2048
W 4
M v[196:199],v[234:237],a[156:159],0
T v[156:157],v216,4096
M v[196:199],v[168:171],v[124:127],v[196:199]
T v[158:159],v216,6144
v_cvt_pk_bf16_f32 v56, v184, v185
M v[196:199],v[164:167],v[116:119],v[196:199]
v_cvt_pk_bf16_f32 v57, v186, v187
v_cvt_pk_bf16_f32 v60, v188, v189
M v[196:199],v[160:163],v[108:111],v[196:199]
T v[160:161],v216,8192
v_cvt_pk_bf16_f32 v61, v190, v191
M v[196:199],v[172:175],v[132:135],v[196:199]
T v[162:163],v216,10240
W 4
M v[196:199],v[176:179],v[140:143],v[196:199]
T v[164:165],v216,12288
M v[196:199],v[180:183],v[148:151],v[196:199]
T v[166:167],v216,14336
W 4
M v[192:195],v[180:183],v[144:147],v[192:195]
T v[168:169],v216,16384
M v[192:195],v[176:179],v[136:139],v[192:195]
T v[170:171],v216,18432
M v[192:195],v[172:175],v[128:131],v[192:195]
T v[172:173],v216,20480
W 5
M v[184:187],v[236:239],a[160:163],0
T v[174:175],v216,22528
s_add_u32 s77, s98, s42
M v[184:187],v[152:155],v[104:107],v[184:187]
s_add_u32 s78, s77, 245760
s_add_u32 m0, s40, 0
M v[184:187],v[156:159],v[112:115],v[184:187]
T v[176:177],v217,0
buffer_load_dwordx4 v230, s[20:23], s78 offen lds
M v[184:187],v[160:163],v[120:123],v[184:187]
T v[178:179],v217,2048
W 4
M v[188:191],v[236:239],a[164:167],0
T v[180:181],v217,4096
M v[188:191],v[160:163],v[124:127],v[188:191]
T v[182:183],v217,6144
v_cvt_pk_bf16_f32 v58, v192, v193
M v[188:191],v[156:159],v[116:119],v[188:191]
v_cvt_pk_bf16_f32 v59, v194, v195
v_cvt_pk_bf16_f32 v62, v196, v197
M v[188:191],v[152:155],v[108:111],v[188:191]
T v[152:153],v217,8192
buffer_load_dwordx4 v231, s[20:23], s78 offen offset:1024 lds
M v[188:191],v[164:167],v[132:135],v[188:191]
T v[154:155],v217,10240
v_cvt_pk_bf16_f32 v63, v198, v199
M v[188:191],v[168:171],v[140:143],v[188:191]
T v[156:157],v217,12288
W 4
M v[188:191],v[172:175],v[148:151],v[188:191]
T v[158:159],v217,14336
buffer_load_dwordx4 v232, s[20:23], s78 offen offset:2048 lds
M v[184:187],v[172:175],v[144:147],v[184:187]
T v[160:161],v217,16384
M v[184:187],v[168:171],v[136:139],v[184:187]
T v[162:163],v217,18432
s_mov_b32 s76, 704512
buffer_store_dwordx4 v[56:59], v240, s[32:35], s76 offen sc1
M v[184:187],v[164:167],v[128:131],v[184:187]
T v[164:165],v217,20480
W 5
M v[192:195],v[234:237],a[160:163],0
T v[166:167],v217,22528
M v[192:195],v[176:179],v[104:107],v[192:195]
W 4
M v[192:195],v[180:183],v[112:115],v[192:195]
T v[168:169],v218,0
buffer_store_dwordx4 v[60:63], v240, s[36:39], s76 offen sc1
M v[192:195],v[152:155],v[120:123],v[192:195]
T v[170:171],v218,2048
W 4
M v[196:199],v[234:237],a[164:167],0
T v[172:173],v218,4096
M v[196:199],v[152:155],v[124:127],v[196:199]
T v[174:175],v218,6144
v_cvt_pk_bf16_f32 v0, v184, v185
M v[196:199],v[180:183],v[116:119],v[196:199]
v_cvt_pk_bf16_f32 v1, v186, v187
v_cvt_pk_bf16_f32 v4, v188, v189
M v[196:199],v[176:179],v[108:111],v[196:199]
T v[176:177],v218,8192
v_cvt_pk_bf16_f32 v5, v190, v191
M v[196:199],v[156:159],v[132:135],v[196:199]
T v[178:179],v218,10240
v_add_u32 v220, 73728, v226
M v[196:199],v[160:163],v[140:143],v[196:199]
T v[180:181],v218,12288
W 4
M v[196:199],v[164:167],v[148:151],v[196:199]
T v[182:183],v218,14336
v_xor_b32 v221, 32, v220
M v[192:195],v[164:167],v[144:147],v[192:195]
T v[152:153],v218,16384
v_xor_b32 v222, 64, v220
M v[192:195],v[160:163],v[136:139],v[192:195]
T v[154:155],v218,18432
v_xor_b32 v223, 96, v220
M v[192:195],v[156:159],v[128:131],v[192:195]
T v[156:157],v218,20480
W 5
M v[184:187],v[236:239],a[168:171],0
T v[158:159],v218,22528
M v[184:187],v[168:171],v[104:107],v[184:187]
W 4
M v[184:187],v[172:175],v[112:115],v[184:187]
T v[160:161],v219,0
M v[184:187],v[176:179],v[120:123],v[184:187]
T v[162:163],v219,2048
W 4
M v[188:191],v[236:239],a[172:175],0
T v[164:165],v219,4096
M v[188:191],v[176:179],v[124:127],v[188:191]
T v[166:167],v219,6144
v_cvt_pk_bf16_f32 v2, v192, v193
M v[188:191],v[172:175],v[116:119],v[188:191]
v_cvt_pk_bf16_f32 v3, v194, v195
v_cvt_pk_bf16_f32 v6, v196, v197
M v[188:191],v[168:171],v[108:111],v[188:191]
T v[168:169],v219,8192
v_cvt_pk_bf16_f32 v7, v198, v199
M v[188:191],v[180:183],v[132:135],v[188:191]
T v[170:171],v219,10240
W 4
M v[188:191],v[152:155],v[140:143],v[188:191]
T v[172:173],v219,12288
s_mov_b32 s76, 917504
buffer_store_dwordx4 v[0:3], v240, s[32:35], s76 offen sc1
M v[188:191],v[156:159],v[148:151],v[188:191]
T v[174:175],v219,14336
W 4
M v[184:187],v[156:159],v[144:147],v[184:187]
T v[176:177],v219,16384
M v[184:187],v[152:155],v[136:139],v[184:187]
T v[178:179],v219,18432
M v[184:187],v[180:183],v[128:131],v[184:187]
T v[180:181],v219,20480
buffer_store_dwordx4 v[4:7], v240, s[36:39], s76 offen sc1
M v[192:195],v[234:237],a[168:171],0
T v[182:183],v219,22528
W 5
M v[192:195],v[160:163],v[104:107],v[192:195]
M v[192:195],v[164:167],v[112:115],v[192:195]
s_waitcnt vmcnt(24)
s_barrier
T v[152:153],v220,0
M v[192:195],v[168:171],v[120:123],v[192:195]
T v[154:155],v220,2048
W 4
M v[196:199],v[234:237],a[172:175],0
T v[156:157],v220,4096
M v[196:199],v[168:171],v[124:127],v[196:199]
T v[158:159],v220,6144
v_cvt_pk_bf16_f32 v8, v184, v185
M v[196:199],v[164:167],v[116:119],v[196:199]
v_cvt_pk_bf16_f32 v9, v186, v187
v_cvt_pk_bf16_f32 v12, v188, v189
M v[196:199],v[160:163],v[108:111],v[196:199]
T v[160:161],v220,8192
v_cvt_pk_bf16_f32 v13, v190, v191
M v[196:199],v[172:175],v[132:135],v[196:199]
T v[162:163],v220,10240
W 4
M v[196:199],v[176:179],v[140:143],v[196:199]
T v[164:165],v220,12288
M v[196:199],v[180:183],v[148:151],v[196:199]
T v[166:167],v220,14336
W 4
M v[192:195],v[180:183],v[144:147],v[192:195]
T v[168:169],v220,16384
M v[192:195],v[176:179],v[136:139],v[192:195]
T v[170:171],v220,18432
M v[192:195],v[172:175],v[128:131],v[192:195]
T v[172:173],v220,20480
W 5
M v[184:187],v[236:239],a[176:179],0
T v[174:175],v220,22528
s_add_u32 s77, s99, s42
M v[184:187],v[152:155],v[104:107],v[184:187]
s_add_u32 s78, s77, 245760
s_add_u32 m0, s40, 24576
M v[184:187],v[156:159],v[112:115],v[184:187]
T v[176:177],v221,0
buffer_load_dwordx4 v230, s[20:23], s78 offen lds
M v[184:187],v[160:163],v[120:123],v[184:187]
T v[178:179],v221,2048
W 4
M v[188:191],v[236:239],a[180:183],0
T v[180:181],v221,4096
M v[188:191],v[160:163],v[124:127],v[188:191]
T v[182:183],v221,6144
v_cvt_pk_bf16_f32 v10, v192, v193
M v[188:191],v[156:159],v[116:119],v[188:191]
v_cvt_pk_bf16_f32 v11, v194, v195
v_cvt_pk_bf16_f32 v14, v196, v197
M v[188:191],v[152:155],v[108:111],v[188:191]
T v[152:153],v221,8192
buffer_load_dwordx4 v231, s[20:23], s78 offen offset:1024 lds
M v[188:191],v[164:167],v[132:135],v[188:191]
T v[154:155],v221,10240
v_cvt_pk_bf16_f32 v15, v198, v199
M v[188:191],v[168:171],v[140:143],v[188:191]
T v[156:157],v221,12288
W 4
M v[188:191],v[172:175],v[148:151],v[188:191]
T v[158:159],v221,14336
buffer_load_dwordx4 v232, s[20:23], s78 offen offset:2048 lds
M v[184:187],v[172:175],v[144:147],v[184:187]
T v[160:161],v221,16384
M v[184:187],v[168:171],v[136:139],v[184:187]
T v[162:163],v221,18432
s_mov_b32 s76, 933888
buffer_store_dwordx4 v[8:11], v240, s[32:35], s76 offen sc1
M v[184:187],v[164:167],v[128:131],v[184:187]
T v[164:165],v221,20480
W 5
M v[192:195],v[234:237],a[176:179],0
T v[166:167],v221,22528
M v[192:195],v[176:179],v[104:107],v[192:195]
W 4
M v[192:195],v[180:183],v[112:115],v[192:195]
T v[168:169],v222,0
buffer_store_dwordx4 v[12:15], v240, s[36:39], s76 offen sc1
M v[192:195],v[152:155],v[120:123],v[192:195]
T v[170:171],v222,2048
W 4
M v[196:199],v[234:237],a[180:183],0
T v[172:173],v222,4096
M v[196:199],v[152:155],v[124:127],v[196:199]
T v[174:175],v222,6144
v_cvt_pk_bf16_f32 v16, v184, v185
M v[196:199],v[180:183],v[116:119],v[196:199]
v_cvt_pk_bf16_f32 v17, v186, v187
v_cvt_pk_bf16_f32 v20, v188, v189
M v[196:199],v[176:179],v[108:111],v[196:199]
T v[176:177],v222,8192
v_cvt_pk_bf16_f32 v21, v190, v191
M v[196:199],v[156:159],v[132:135],v[196:199]
T v[178:179],v222,10240
v_add_u32 v216, 122880, v226
M v[196:199],v[160:163],v[140:143],v[196:199]
T v[180:181],v222,12288
W 4
M v[196:199],v[164:167],v[148:151],v[196:199]
T v[182:183],v222,14336
v_xor_b32 v217, 32, v216
M v[192:195],v[164:167],v[144:147],v[192:195]
T v[152:153],v222,16384
v_xor_b32 v218, 64, v216
M v[192:195],v[160:163],v[136:139],v[192:195]
T v[154:155],v222,18432
v_xor_b32 v219, 96, v216
M v[192:195],v[156:159],v[128:131],v[192:195]
T v[156:157],v222,20480
W 5
M v[184:187],v[236:239],a[184:187],0
T v[158:159],v222,22528
M v[184:187],v[168:171],v[104:107],v[184:187]
W 4
M v[184:187],v[172:175],v[112:115],v[184:187]
T v[160:161],v223,0
M v[184:187],v[176:179],v[120:123],v[184:187]
T v[162:163],v223,2048
W 4
M v[188:191],v[236:239],a[188:191],0
T v[164:165],v223,4096
M v[188:191],v[176:179],v[124:127],v[188:191]
T v[166:167],v223,6144
v_cvt_pk_bf16_f32 v18, v192, v193
M v[188:191],v[172:175],v[116:119],v[188:191]
v_cvt_pk_bf16_f32 v19, v194, v195
v_cvt_pk_bf16_f32 v22, v196, v197
M v[188:191],v[168:171],v[108:111],v[188:191]
T v[168:169],v223,8192
v_cvt_pk_bf16_f32 v23, v198, v199
M v[188:191],v[180:183],v[132:135],v[188:191]
T v[170:171],v223,10240
W 4
M v[188:191],v[152:155],v[140:143],v[188:191]
T v[172:173],v223,12288
s_mov_b32 s76, 1146880
buffer_store_dwordx4 v[16:19], v240, s[32:35], s76 offen sc1
M v[188:191],v[156:159],v[148:151],v[188:191]
T v[174:175],v223,14336
W 4
M v[184:187],v[156:159],v[144:147],v[184:187]
T v[176:177],v223,16384
M v[184:187],v[152:155],v[136:139],v[184:187]
T v[178:179],v223,18432
M v[184:187],v[180:183],v[128:131],v[184:187]
T v[180:181],v223,20480
buffer_store_dwordx4 v[20:23], v240, s[36:39], s76 offen sc1
M v[192:195],v[234:237],a[184:187],0
T v[182:183],v223,22528
W 5
M v[192:195],v[160:163],v[104:107],v[192:195]
M v[192:195],v[164:167],v[112:115],v[192:195]
s_waitcnt vmcnt(25)
s_barrier
T v[152:153],v216,0
M v[192:195],v[168:171],v[120:123],v[192:195]
T v[154:155],v216,2048
W 4
M v[196:199],v[234:237],a[188:191],0
T v[156:157],v216,4096
M v[196:199],v[168:171],v[124:127],v[196:199]
T v[158:159],v216,6144
v_cvt_pk_bf16_f32 v24, v184, v185
M v[196:199],v[164:167],v[116:119],v[196:199]
v_cvt_pk_bf16_f32 v25, v186, v187
v_cvt_pk_bf16_f32 v28, v188, v189
M v[196:199],v[160:163],v[108:111],v[196:199]
T v[160:161],v216,8192
v_cvt_pk_bf16_f32 v29, v190, v191
M v[196:199],v[172:175],v[132:135],v[196:199]
T v[162:163],v216,10240
W 4
M v[196:199],v[176:179],v[140:143],v[196:199]
T v[164:165],v216,12288
M v[196:199],v[180:183],v[148:151],v[196:199]
T v[166:167],v216,14336
W 4
M v[192:195],v[180:183],v[144:147],v[192:195]
T v[168:169],v216,16384
M v[192:195],v[176:179],v[136:139],v[192:195]
T v[170:171],v216,18432
M v[192:195],v[172:175],v[128:131],v[192:195]
T v[172:173],v216,20480
W 5
M v[184:187],v[236:239],a[192:195],0
T v[174:175],v216,22528
M v[184:187],v[152:155],v[104:107],v[184:187]
W 4
M v[184:187],v[156:159],v[112:115],v[184:187]
T v[176:177],v217,0
M v[184:187],v[160:163],v[120:123],v[184:187]
T v[178:179],v217,2048
W 4
M v[188:191],v[236:239],a[196:199],0
T v[180:181],v217,4096
M v[188:191],v[160:163],v[124:127],v[188:191]
T v[182:183],v217,6144
v_cvt_pk_bf16_f32 v26, v192, v193
M v[188:191],v[156:159],v[116:119],v[188:191]
v_cvt_pk_bf16_f32 v27, v194, v195
v_cvt_pk_bf16_f32 v30, v196, v197
M v[188:191],v[152:155],v[108:111],v[188:191]
T v[152:153],v217,8192
v_cvt_pk_bf16_f32 v31, v198, v199
M v[188:191],v[164:167],v[132:135],v[188:191]
T v[154:155],v217,10240
W 4
M v[188:191],v[168:171],v[140:143],v[188:191]
T v[156:157],v217,12288
s_mov_b32 s76, 1163264
buffer_store_dwordx4 v[24:27], v240, s[32:35], s76 offen sc1
M v[188:191],v[172:175],v[148:151],v[188:191]
T v[158:159],v217,14336
W 4
M v[184:187],v[172:175],v[144:147],v[184:187]
T v[160:161],v217,16384
M v[184:187],v[168:171],v[136:139],v[184:187]
T v[162:163],v217,18432
M v[184:187],v[164:167],v[128:131],v[184:187]
T v[164:165],v217,20480
buffer_store_dwordx4 v[28:31], v240, s[36:39], s76 offen sc1
M v[192:195],v[234:237],a[192:195],0
T v[166:167],v217,22528
W 5
M v[192:195],v[176:179],v[104:107],v[192:195]
M v[192:195],v[180:183],v[112:115],v[192:195]
T v[168:169],v218,0
W 4
M v[192:195],v[152:155],v[120:123],v[192:195]
T v[170:171],v218,2048
M v[196:199],v[234:237],a[196:199],0
T v[172:173],v218,4096
W 4
M v[196:199],v[152:155],v[124:127],v[196:199]
T v[174:175],v218,6144
v_cvt_pk_bf16_f32 v32, v184, v185
M v[196:199],v[180:183],v[116:119],v[196:199]
v_cvt_pk_bf16_f32 v33, v186, v187
v_cvt_pk_bf16_f32 v36, v188, v189
M v[196:199],v[176:179],v[108:111],v[196:199]
T v[176:177],v218,8192
v_cvt_pk_bf16_f32 v37, v190, v191
M v[196:199],v[156:159],v[132:135],v[196:199]
T v[178:179],v218,10240
v_add_u32 v220, 98304, v226
M v[196:199],v[160:163],v[140:143],v[196:199]
T v[180:181],v218,12288
W 4
M v[196:199],v[164:167],v[148:151],v[196:199]
T v[182:183],v218,14336
v_xor_b32 v221, 32, v220
M v[192:195],v[164:167],v[144:147],v[192:195]
T v[152:153],v218,16384
v_xor_b32 v222, 64, v220
M v[192:195],v[160:163],v[136:139],v[192:195]
T v[154:155],v218,18432
v_xor_b32 v223, 96, v220
M v[192:195],v[156:159],v[128:131],v[192:195]
T v[156:157],v218,20480
W 5
M v[184:187],v[236:239],a[200:203],0
T v[158:159],v218,22528
M v[184:187],v[168:171],v[104:107],v[184:187]
W 4
M v[184:187],v[172:175],v[112:115],v[184:187]
T v[160:161],v219,0
M v[184:187],v[176:179],v[120:123],v[184:187]
T v[162:163],v219,2048
W 4
M v[188:191],v[236:239],a[204:207],0
T v[164:165],v219,4096
M v[188:191],v[176:179],v[124:127],v[188:191]
T v[166:167],v219,6144
v_cvt_pk_bf16_f32 v34, v192, v193
M v[188:191],v[172:175],v[116:119],v[188:191]
v_cvt_pk_bf16_f32 v35, v194, v195
v_cvt_pk_bf16_f32 v38, v196, v197
M v[188:191],v[168:171],v[108:111],v[188:191]
T v[168:169],v219,8192
v_cvt_pk_bf16_f32 v39, v198, v199
M v[188:191],v[180:183],v[132:135],v[188:191]
T v[170:171],v219,10240
W 4
M v[188:191],v[152:155],v[140:143],v[188:191]
T v[172:173],v219,12288
s_mov_b32 s76, 1376256
buffer_store_dwordx4 v[32:35], v240, s[32:35], s76 offen sc1
M v[188:191],v[156:159],v[148:151],v[188:191]
T v[174:175],v219,14336
W 4
M v[184:187],v[156:159],v[144:147],v[184:187]
T v[176:177],v219,16384
M v[184:187],v[152:155],v[136:139],v[184:187]
T v[178:179],v219,18432
M v[184:187],v[180:183],v[128:131],v[184:187]
T v[180:181],v219,20480
buffer_store_dwordx4 v[36:39], v240, s[36:39], s76 offen sc1
M v[192:195],v[234:237],a[200:203],0
T v[182:183],v219,22528
W 5
M v[192:195],v[160:163],v[104:107],v[192:195]
M v[192:195],v[164:167],v[112:115],v[192:195]
s_waitcnt vmcnt(22)
s_barrier
T v[152:153],v220,0
M v[192:195],v[168:171],v[120:123],v[192:195]
T v[154:155],v220,2048
W 4
M v[196:199],v[234:237],a[204:207],0
T v[156:157],v220,4096
M v[196:199],v[168:171],v[124:127],v[196:199]
T v[158:159],v220,6144
v_cvt_pk_bf16_f32 v40, v184, v185
M v[196:199],v[164:167],v[116:119],v[196:199]
v_cvt_pk_bf16_f32 v41, v186, v187
v_cvt_pk_bf16_f32 v44, v188, v189
M v[196:199],v[160:163],v[108:111],v[196:199]
T v[160:161],v220,8192
v_cvt_pk_bf16_f32 v45, v190, v191
M v[196:199],v[172:175],v[132:135],v[196:199]
T v[162:163],v220,10240
W 4
M v[196:199],v[176:179],v[140:143],v[196:199]
T v[164:165],v220,12288
M v[196:199],v[180:183],v[148:151],v[196:199]
T v[166:167],v220,14336
W 4
M v[192:195],v[180:183],v[144:147],v[192:195]
T v[168:169],v220,16384
M v[192:195],v[176:179],v[136:139],v[192:195]
T v[170:171],v220,18432
M v[192:195],v[172:175],v[128:131],v[192:195]
T v[172:173],v220,20480
W 5
M v[184:187],v[236:239],a[208:211],0
T v[174:175],v220,22528
M v[184:187],v[152:155],v[104:107],v[184:187]
W 4
M v[184:187],v[156:159],v[112:115],v[184:187]
T v[176:177],v221,0
M v[184:187],v[160:163],v[120:123],v[184:187]
T v[178:179],v221,2048
W 4
M v[188:191],v[236:239],a[212:215],0
T v[180:181],v221,4096
M v[188:191],v[160:163],v[124:127],v[188:191]
T v[182:183],v221,6144
v_cvt_pk_bf16_f32 v42, v192, v193
M v[188:191],v[156:159],v[116:119],v[188:191]
v_cvt_pk_bf16_f32 v43, v194, v195
v_cvt_pk_bf16_f32 v46, v196, v197
M v[188:191],v[152:155],v[108:111],v[188:191]
T v[152:153],v221,8192
v_cvt_pk_bf16_f32 v47, v198, v199
M v[188:191],v[164:167],v[132:135],v[188:191]
T v[154:155],v221,10240
W 4
M v[188:191],v[168:171],v[140:143],v[188:191]
T v[156:157],v221,12288
s_mov_b32 s76, 1392640
buffer_store_dwordx4 v[40:43], v240, s[32:35], s76 offen sc1
M v[188:191],v[172:175],v[148:151],v[188:191]
T v[158:159],v221,14336
W 4
M v[184:187],v[172:175],v[144:147],v[184:187]
T v[160:161],v221,16384
M v[184:187],v[168:171],v[136:139],v[184:187]
T v[162:163],v221,18432
M v[184:187],v[164:167],v[128:131],v[184:187]
T v[164:165],v221,20480
buffer_store_dwordx4 v[44:47], v240, s[36:39], s76 offen sc1
M v[192:195],v[234:237],a[208:211],0
T v[166:167],v221,22528
W 5
M v[192:195],v[176:179],v[104:107],v[192:195]
M v[192:195],v[180:183],v[112:115],v[192:195]
T v[168:169],v222,0
W 4
M v[192:195],v[152:155],v[120:123],v[192:195]
T v[170:171],v222,2048
M v[196:199],v[234:237],a[212:215],0
T v[172:173],v222,4096
W 4
M v[196:199],v[152:155],v[124:127],v[196:199]
T v[174:175],v222,6144
v_cvt_pk_bf16_f32 v48, v184, v185
M v[196:199],v[180:183],v[116:119],v[196:199]
v_cvt_pk_bf16_f32 v49, v186, v187
v_cvt_pk_bf16_f32 v52, v188, v189
M v[196:199],v[176:179],v[108:111],v[196:199]
T v[176:177],v222,8192
v_cvt_pk_bf16_f32 v53, v190, v191
M v[196:199],v[156:159],v[132:135],v[196:199]
T v[178:179],v222,10240
v_add_u32 v216, 0, v226
M v[196:199],v[160:163],v[140:143],v[196:199]
T v[180:181],v222,12288
W 4
M v[196:199],v[164:167],v[148:151],v[196:199]
T v[182:183],v222,14336
v_xor_b32 v217, 32, v216
M v[192:195],v[164:167],v[144:147],v[192:195]
T v[152:153],v222,16384
v_xor_b32 v218, 64, v216
M v[192:195],v[160:163],v[136:139],v[192:195]
T v[154:155],v222,18432
v_xor_b32 v219, 96, v216
M v[192:195],v[156:159],v[128:131],v[192:195]
T v[156:157],v222,20480
W 5
M v[184:187],v[236:239],a[216:219],0
T v[158:159],v222,22528
M v[184:187],v[168:171],v[104:107],v[184:187]
W 4
M v[184:187],v[172:175],v[112:115],v[184:187]
T v[160:161],v223,0
M v[184:187],v[176:179],v[120:123],v[184:187]
T v[162:163],v223,2048
W 4
M v[188:191],v[236:239],a[220:223],0
T v[164:165],v223,4096
M v[188:191],v[176:179],v[124:127],v[188:191]
T v[166:167],v223,6144
v_cvt_pk_bf16_f32 v50, v192, v193
M v[188:191],v[172:175],v[116:119],v[188:191]
v_cvt_pk_bf16_f32 v51, v194, v195
v_cvt_pk_bf16_f32 v54, v196, v197
M v[188:191],v[168:171],v[108:111],v[188:191]
T v[168:169],v223,8192
v_cvt_pk_bf16_f32 v55, v198, v199
M v[188:191],v[180:183],v[132:135],v[188:191]
T v[170:171],v223,10240
W 4
M v[188:191],v[152:155],v[140:143],v[188:191]
T v[172:173],v223,12288
s_mov_b32 s76, 1605632
buffer_store_dwordx4 v[48:51], v240, s[32:35], s76 offen sc1
M v[188:191],v[156:159],v[148:151],v[188:191]
T v[174:175],v223,14336
W 4
M v[184:187],v[156:159],v[144:147],v[184:187]
T v[176:177],v223,16384
M v[184:187],v[152:155],v[136:139],v[184:187]
T v[178:179],v223,18432
M v[184:187],v[180:183],v[128:131],v[184:187]
T v[180:181],v223,20480
buffer_store_dwordx4 v[52:55], v240, s[36:39], s76 offen sc1
M v[192:195],v[234:237],a[216:219],0
T v[182:183],v223,22528
W 5
M v[192:195],v[160:163],v[104:107],v[192:195]
M v[192:195],v[164:167],v[112:115],v[192:195]
s_waitcnt vmcnt(19)
s_barrier
T v[152:153],v216,0
M v[192:195],v[168:171],v[120:123],v[192:195]
T v[154:155],v216,2048
W 4
M v[196:199],v[234:237],a[220:223],0
T v[156:157],v216,4096
M v[196:199],v[168:171],v[124:127],v[196:199]
T v[158:159],v216,6144
v_cvt_pk_bf16_f32 v56, v184, v185
M v[196:199],v[164:167],v[116:119],v[196:199]
v_cvt_pk_bf16_f32 v57, v186, v187
v_cvt_pk_bf16_f32 v60, v188, v189
M v[196:199],v[160:163],v[108:111],v[196:199]
T v[160:161],v216,8192
v_cvt_pk_bf16_f32 v61, v190, v191
M v[196:199],v[172:175],v[132:135],v[196:199]
T v[162:163],v216,10240
W 4
M v[196:199],v[176:179],v[140:143],v[196:199]
T v[164:165],v216,12288
M v[196:199],v[180:183],v[148:151],v[196:199]
T v[166:167],v216,14336
W 4
M v[192:195],v[180:183],v[144:147],v[192:195]
T v[168:169],v216,16384
M v[192:195],v[176:179],v[136:139],v[192:195]
T v[170:171],v216,18432
M v[192:195],v[172:175],v[128:131],v[192:195]
T v[172:173],v216,20480
W 5
M v[184:187],v[236:239],a[224:227],0
T v[174:175],v216,22528
M v[184:187],v[152:155],v[104:107],v[184:187]
W 4
M v[184:187],v[156:159],v[112:115],v[184:187]
T v[176:177],v217,0
M v[184:187],v[160:163],v[120:123],v[184:187]
T v[178:179],v217,2048
W 4
M v[188:191],v[236:239],a[228:231],0
T v[180:181],v217,4096
M v[188:191],v[160:163],v[124:127],v[188:191]
T v[182:183],v217,6144
v_cvt_pk_bf16_f32 v58, v192, v193
M v[188:191],v[156:159],v[116:119],v[188:191]
v_cvt_pk_bf16_f32 v59, v194, v195
v_cvt_pk_bf16_f32 v62, v196, v197
M v[188:191],v[152:155],v[108:111],v[188:191]
T v[152:153],v217,8192
v_cvt_pk_bf16_f32 v63, v198, v199
M v[188:191],v[164:167],v[132:135],v[188:191]
T v[154:155],v217,10240
W 4
M v[188:191],v[168:171],v[140:143],v[188:191]
T v[156:157],v217,12288
s_mov_b32 s76, 1622016
buffer_store_dwordx4 v[56:59], v240, s[32:35], s76 offen sc1
M v[188:191],v[172:175],v[148:151],v[188:191]
T v[158:159],v217,14336
W 4
M v[184:187],v[172:175],v[144:147],v[184:187]
T v[160:161],v217,16384
M v[184:187],v[168:171],v[136:139],v[184:187]
T v[162:163],v217,18432
M v[184:187],v[164:167],v[128:131],v[184:187]
T v[164:165],v217,20480
buffer_store_dwordx4 v[60:63], v240, s[36:39], s76 offen sc1
M v[192:195],v[234:237],a[224:227],0
T v[166:167],v217,22528
W 5
M v[192:195],v[176:179],v[104:107],v[192:195]
M v[192:195],v[180:183],v[112:115],v[192:195]
T v[168:169],v218,0
W 4
M v[192:195],v[152:155],v[120:123],v[192:195]
T v[170:171],v218,2048
M v[196:199],v[234:237],a[228:231],0
T v[172:173],v218,4096
W 4
M v[196:199],v[152:155],v[124:127],v[196:199]
T v[174:175],v218,6144
v_cvt_pk_bf16_f32 v0, v184, v185
M v[196:199],v[180:183],v[116:119],v[196:199]
v_cvt_pk_bf16_f32 v1, v186, v187
v_cvt_pk_bf16_f32 v4, v188, v189
M v[196:199],v[176:179],v[108:111],v[196:199]
T v[176:177],v218,8192
v_cvt_pk_bf16_f32 v5, v190, v191
M v[196:199],v[156:159],v[132:135],v[196:199]
T v[178:179],v218,10240
v_add_u32 v220, 24576, v226
M v[196:199],v[160:163],v[140:143],v[196:199]
T v[180:181],v218,12288
W 4
M v[196:199],v[164:167],v[148:151],v[196:199]
T v[182:183],v218,14336
v_xor_b32 v221, 32, v220
M v[192:195],v[164:167],v[144:147],v[192:195]
T v[152:153],v218,16384
v_xor_b32 v222, 64, v220
M v[192:195],v[160:163],v[136:139],v[192:195]
T v[154:155],v218,18432
v_xor_b32 v223, 96, v220
M v[192:195],v[156:159],v[128:131],v[192:195]
T v[156:157],v218,20480
W 5
M v[184:187],v[236:239],a[232:235],0
T v[158:159],v218,22528
M v[184:187],v[168:171],v[104:107],v[184:187]
W 4
M v[184:187],v[172:175],v[112:115],v[184:187]
T v[160:161],v219,0
M v[184:187],v[176:179],v[120:123],v[184:187]
T v[162:163],v219,2048
W 4
M v[188:191],v[236:239],a[236:239],0
T v[164:165],v219,4096
M v[188:191],v[176:179],v[124:127],v[188:191]
T v[166:167],v219,6144
v_cvt_pk_bf16_f32 v2, v192, v193
M v[188:191],v[172:175],v[116:119],v[188:191]
v_cvt_pk_bf16_f32 v3, v194, v195
v_cvt_pk_bf16_f32 v6, v196, v197
M v[188:191],v[168:171],v[108:111],v[188:191]
T v[168:169],v219,8192
v_cvt_pk_bf16_f32 v7, v198, v199
M v[188:191],v[180:183],v[132:135],v[188:191]
T v[170:171],v219,10240
W 4
M v[188:191],v[152:155],v[140:143],v[188:191]
T v[172:173],v219,12288
s_mov_b32 s76, 1835008
buffer_store_dwordx4 v[0:3], v240, s[32:35], s76 offen sc1
M v[188:191],v[156:159],v[148:151],v[188:191]
T v[174:175],v219,14336
W 4
M v[184:187],v[156:159],v[144:147],v[184:187]
T v[176:177],v219,16384
M v[184:187],v[152:155],v[136:139],v[184:187]
T v[178:179],v219,18432
M v[184:187],v[180:183],v[128:131],v[184:187]
T v[180:181],v219,20480
buffer_store_dwordx4 v[4:7], v240, s[36:39], s76 offen sc1
M v[192:195],v[234:237],a[232:235],0
T v[182:183],v219,22528
W 5
M v[192:195],v[160:163],v[104:107],v[192:195]
M v[192:195],v[164:167],v[112:115],v[192:195]
s_waitcnt vmcnt(16)
s_barrier
T v[152:153],v220,0
M v[192:195],v[168:171],v[120:123],v[192:195]
T v[154:155],v220,2048
W 4
M v[196:199],v[234:237],a[236:239],0
T v[156:157],v220,4096
M v[196:199],v[168:171],v[124:127],v[196:199]
T v[158:159],v220,6144
v_cvt_pk_bf16_f32 v8, v184, v185
M v[196:199],v[164:167],v[116:119],v[196:199]
v_cvt_pk_bf16_f32 v9, v186, v187
v_cvt_pk_bf16_f32 v12, v188, v189
M v[196:199],v[160:163],v[108:111],v[196:199]
T v[160:161],v220,8192
v_cvt_pk_bf16_f32 v13, v190, v191
M v[196:199],v[172:175],v[132:135],v[196:199]
T v[162:163],v220,10240
W 4
M v[196:199],v[176:179],v[140:143],v[196:199]
T v[164:165],v220,12288
M v[196:199],v[180:183],v[148:151],v[196:199]
T v[166:167],v220,14336
W 4
M v[192:195],v[180:183],v[144:147],v[192:195]
T v[168:169],v220,16384
M v[192:195],v[176:179],v[136:139],v[192:195]
T v[170:171],v220,18432
M v[192:195],v[172:175],v[128:131],v[192:195]
T v[172:173],v220,20480
W 5
M v[184:187],v[236:239],a[240:243],0
T v[174:175],v220,22528
M v[184:187],v[152:155],v[104:107],v[184:187]
W 4
M v[184:187],v[156:159],v[112:115],v[184:187]
T v[176:177],v221,0
M v[184:187],v[160:163],v[120:123],v[184:187]
T v[178:179],v221,2048
W 4
M v[188:191],v[236:239],a[244:247],0
T v[180:181],v221,4096
M v[188:191],v[160:163],v[124:127],v[188:191]
T v[182:183],v221,6144
v_cvt_pk_bf16_f32 v10, v192, v193
M v[188:191],v[156:159],v[116:119],v[188:191]
v_cvt_pk_bf16_f32 v11, v194, v195
v_cvt_pk_bf16_f32 v14, v196, v197
M v[188:191],v[152:155],v[108:111],v[188:191]
T v[152:153],v221,8192
v_cvt_pk_bf16_f32 v15, v198, v199
M v[188:191],v[164:167],v[132:135],v[188:191]
T v[154:155],v221,10240
W 4
M v[188:191],v[168:171],v[140:143],v[188:191]
T v[156:157],v221,12288
s_mov_b32 s76, 1851392
buffer_store_dwordx4 v[8:11], v240, s[32:35], s76 offen sc1
M v[188:191],v[172:175],v[148:151],v[188:191]
T v[158:159],v221,14336
s_mov_b32 exec_lo, 0xffff
s_mov_b32 exec_hi, 0
buffer_store_dword v215, v243, s[28:31], 0 offen sc1
s_mov_b64 exec, -1
M v[184:187],v[172:175],v[144:147],v[184:187]
T v[160:161],v221,16384
W 5
M v[184:187],v[168:171],v[136:139],v[184:187]
T v[162:163],v221,18432
M v[184:187],v[164:167],v[128:131],v[184:187]
T v[164:165],v221,20480
buffer_store_dwordx4 v[12:15], v240, s[36:39], s76 offen sc1
M v[192:195],v[234:237],a[240:243],0
T v[166:167],v221,22528
W 5
M v[192:195],v[176:179],v[104:107],v[192:195]
M v[192:195],v[180:183],v[112:115],v[192:195]
T v[168:169],v222,0
W 4
M v[192:195],v[152:155],v[120:123],v[192:195]
T v[170:171],v222,2048
M v[196:199],v[234:237],a[244:247],0
T v[172:173],v222,4096
W 4
M v[196:199],v[152:155],v[124:127],v[196:199]
T v[174:175],v222,6144
v_cvt_pk_bf16_f32 v16, v184, v185
M v[196:199],v[180:183],v[116:119],v[196:199]
v_cvt_pk_bf16_f32 v17, v186, v187
v_cvt_pk_bf16_f32 v20, v188, v189
M v[196:199],v[176:179],v[108:111],v[196:199]
T v[176:177],v222,8192
v_cvt_pk_bf16_f32 v21, v190, v191
M v[196:199],v[156:159],v[132:135],v[196:199]
T v[178:179],v222,10240
W 4
M v[196:199],v[160:163],v[140:143],v[196:199]
T v[180:181],v222,12288
M v[196:199],v[164:167],v[148:151],v[196:199]
T v[182:183],v222,14336
W 4
M v[192:195],v[164:167],v[144:147],v[192:195]
T v[152:153],v222,16384
M v[192:195],v[160:163],v[136:139],v[192:195]
T v[154:155],v222,18432
M v[192:195],v[156:159],v[128:131],v[192:195]
T v[156:157],v222,20480
W 5
M v[184:187],v[236:239],a[248:251],0
T v[158:159],v222,22528
M v[184:187],v[168:171],v[104:107],v[184:187]
W 4
M v[184:187],v[172:175],v[112:115],v[184:187]
T v[160:161],v223,0
M v[184:187],v[176:179],v[120:123],v[184:187]
T v[162:163],v223,2048
W 4
M v[188:191],v[236:239],a[252:255],0
T v[164:165],v223,4096
M v[188:191],v[176:179],v[124:127],v[188:191]
T v[166:167],v223,6144
v_cvt_pk_bf16_f32 v18, v192, v193
M v[188:191],v[172:175],v[116:119],v[188:191]
v_cvt_pk_bf16_f32 v19, v194, v195
v_cvt_pk_bf16_f32 v22, v196, v197
M v[188:191],v[168:171],v[108:111],v[188:191]
T v[168:169],v223,8192
v_cvt_pk_bf16_f32 v23, v198, v199
M v[188:191],v[180:183],v[132:135],v[188:191]
T v[170:171],v223,10240
W 4
M v[188:191],v[152:155],v[140:143],v[188:191]
T v[172:173],v223,12288
s_mov_b32 s76, 2064384
buffer_store_dwordx4 v[16:19], v240, s[32:35], s76 offen sc1
M v[188:191],v[156:159],v[148:151],v[188:191]
T v[174:175],v223,14336
W 4
M v[184:187],v[156:159],v[144:147],v[184:187]
T v[176:177],v223,16384
M v[184:187],v[152:155],v[136:139],v[184:187]
T v[178:179],v223,18432
M v[184:187],v[180:183],v[128:131],v[184:187]
T v[180:181],v223,20480
buffer_store_dwordx4 v[20:23], v240, s[36:39], s76 offen sc1
M v[192:195],v[234:237],a[248:251],0
T v[182:183],v223,22528
W 5
M v[192:195],v[160:163],v[104:107],v[192:195]
M v[192:195],v[164:167],v[112:115],v[192:195]
W 3
M v[192:195],v[168:171],v[120:123],v[192:195]
M v[196:199],v[234:237],a[252:255],0
W 1
M v[196:199],v[168:171],v[124:127],v[196:199]
v_cvt_pk_bf16_f32 v24, v184, v185
v_cvt_pk_bf16_f32 v25, v186, v187
M v[196:199],v[164:167],v[116:119],v[196:199]
v_cvt_pk_bf16_f32 v28, v188, v189
v_cvt_pk_bf16_f32 v29, v190, v191
M v[196:199],v[160:163],v[108:111],v[196:199]
M v[196:199],v[172:175],v[132:135],v[196:199]
M v[196:199],v[176:179],v[140:143],v[196:199]
W 0
M v[196:199],v[180:183],v[148:151],v[196:199]
M v[192:195],v[180:183],v[144:147],v[192:195]
M v[192:195],v[176:179],v[136:139],v[192:195]
M v[192:195],v[172:175],v[128:131],v[192:195]
s_nop 7
v_cvt_pk_bf16_f32 v26, v192, v193
v_cvt_pk_bf16_f32 v27, v194, v195
v_cvt_pk_bf16_f32 v30, v196, v197
v_cvt_pk_bf16_f32 v31, v198, v199
s_mov_b32 s76, 2080768
buffer_store_dwordx4 v[24:27], v240, s[32:35], s76 offen sc1
buffer_store_dwordx4 v[28:31], v240, s[36:39], s76 offen sc1
LJOIN_%=:
buffer_load_dword v248, v213, s[28:31], 0 offen sc1
s_waitcnt vmcnt(1)
s_mov_b32 exec_lo, 0xffff0000
s_mov_b32 exec_hi, 0xff
buffer_store_dword v215, v243, s[28:31], 0 offen sc1
s_mov_b64 exec, -1
s_waitcnt vmcnt(1)
v_cmp_ne_u32 vcc, v248, v212
s_nop 1
s_and_b32 s74, vcc_lo, 0xffff
s_and_b32 s75, vcc_hi, 0
s_or_b32 s74, s74, s75
s_cbranch_scc1 LWA_%=
LWAR_%=:
buffer_load_dwordx4 a[96:99], v200, s[28:31], 0 offen sc1
buffer_load_dwordx4 a[100:103], v200, s[28:31], 0 offen offset:1024 sc1
buffer_load_dwordx4 a[104:107], v200, s[28:31], 0 offen offset:2048 sc1
buffer_load_dwordx4 a[108:111], v200, s[28:31], 0 offen offset:3072 sc1
buffer_load_dwordx4 a[112:115], v200, s[28:31], s85 offen sc1
buffer_load_dwordx4 a[116:119], v200, s[28:31], s85 offen offset:1024 sc1
buffer_load_dwordx4 a[120:123], v200, s[28:31], s85 offen offset:2048 sc1
buffer_load_dwordx4 a[124:127], v200, s[28:31], s85 offen offset:3072 sc1
buffer_load_dwordx4 a[128:131], v200, s[28:31], s86 offen sc1
buffer_load_dwordx4 a[132:135], v200, s[28:31], s86 offen offset:1024 sc1
buffer_load_dword v248, v213, s[28:31], 0 offen sc1
buffer_load_dwordx4 a[136:139], v200, s[28:31], s86 offen offset:2048 sc1
buffer_load_dwordx4 a[140:143], v200, s[28:31], s86 offen offset:3072 sc1
buffer_load_dwordx4 a[144:147], v200, s[28:31], s87 offen sc1
buffer_load_dwordx4 a[148:151], v200, s[28:31], s87 offen offset:1024 sc1
buffer_load_dwordx4 a[152:155], v200, s[28:31], s87 offen offset:2048 sc1
buffer_load_dwordx4 a[156:159], v200, s[28:31], s87 offen offset:3072 sc1
s_waitcnt vmcnt(6)
v_cmp_ne_u32 vcc, v248, v212
s_nop 1
s_and_b32 s74, vcc_lo, 0xffff0000
s_and_b32 s75, vcc_hi, 0
s_or_b32 s74, s74, s75
s_cbranch_scc1 LWB_%=
LWBR_%=:
buffer_load_dwordx4 a[0:3], v201, s[28:31], 0 offen sc1
buffer_load_dwordx4 a[4:7], v201, s[28:31], 0 offen offset:1024 sc1
buffer_load_dwordx4 a[8:11], v201, s[28:31], 0 offen offset:2048 sc1
buffer_load_dwordx4 a[12:15], v201, s[28:31], 0 offen offset:3072 sc1
buffer_load_dwordx4 a[16:19], v201, s[28:31], s85 offen sc1
buffer_load_dwordx4 a[20:23], v201, s[28:31], s85 offen offset:1024 sc1
buffer_load_dwordx4 a[24:27], v201, s[28:31], s85 offen offset:2048 sc1
buffer_load_dwordx4 a[28:31], v201, s[28:31], s85 offen offset:3072 sc1
buffer_load_dwordx4 a[32:35], v201, s[28:31], s86 offen sc1
M v[184:187],v[236:239],a[96:99],0
M v[188:191],v[234:237],a[96:99],0
M v[184:187],v[236:239],a[100:103],v[184:187]
M v[188:191],v[234:237],a[100:103],v[188:191]
buffer_load_dwordx4 a[36:39], v201, s[28:31], s86 offen offset:1024 sc1
M v[184:187],v[236:239],a[104:107],v[184:187]
M v[188:191],v[234:237],a[104:107],v[188:191]
M v[184:187],v[236:239],a[108:111],v[184:187]
M v[188:191],v[234:237],a[108:111],v[188:191]
buffer_load_dword v249, v214, s[28:31], 0 offen sc1
buffer_load_dword v248, v213, s[28:31], 0 offen sc1
buffer_load_dwordx4 a[40:43], v201, s[28:31], s86 offen offset:2048 sc1
M v[184:187],v[236:239],a[112:115],v[184:187]
M v[188:191],v[234:237],a[112:115],v[188:191]
M v[184:187],v[236:239],a[116:119],v[184:187]
M v[188:191],v[234:237],a[116:119],v[188:191]
buffer_load_dwordx4 a[44:47], v201, s[28:31], s86 offen offset:3072 sc1
M v[184:187],v[236:239],a[120:123],v[184:187]
M v[188:191],v[234:237],a[120:123],v[188:191]
M v[184:187],v[236:239],a[124:127],v[184:187]
M v[188:191],v[234:237],a[124:127],v[188:191]
buffer_load_dwordx4 a[48:51], v201, s[28:31], s87 offen sc1
M v[184:187],v[236:239],a[128:131],v[184:187]
M v[188:191],v[234:237],a[128:131],v[188:191]
M v[184:187],v[236:239],a[132:135],v[184:187]
M v[188:191],v[234:237],a[132:135],v[188:191]
buffer_load_dwordx4 a[52:55], v201, s[28:31], s87 offen offset:1024 sc1
s_waitcnt vmcnt(21)
M v[184:187],v[236:239],a[136:139],v[184:187]
M v[188:191],v[234:237],a[136:139],v[188:191]
s_waitcnt vmcnt(20)
M v[184:187],v[236:239],a[140:143],v[184:187]
M v[188:191],v[234:237],a[140:143],v[188:191]
buffer_load_dwordx4 a[56:59], v201, s[28:31], s87 offen offset:2048 sc1
s_waitcnt vmcnt(20)
M v[184:187],v[236:239],a[144:147],v[184:187]
M v[188:191],v[234:237],a[144:147],v[188:191]
s_waitcnt vmcnt(19)
M v[184:187],v[236:239],a[148:151],v[184:187]
M v[188:191],v[234:237],a[148:151],v[188:191]
buffer_load_dwordx4 a[60:63], v201, s[28:31], s87 offen offset:3072 sc1
s_waitcnt vmcnt(19)
M v[184:187],v[236:239],a[152:155],v[184:187]
M v[188:191],v[234:237],a[152:155],v[188:191]
s_waitcnt vmcnt(18)
M v[184:187],v[236:239],a[156:159],v[184:187]
M v[188:191],v[234:237],a[156:159],v[188:191]
s_waitcnt vmcnt(6)
v_cmp_ne_u32 vcc, v248, v212
s_nop 1
s_and_b32 s74, vcc_lo, 0
s_and_b32 s75, vcc_hi, 0xffff
s_or_b32 s74, s74, s75
s_cbranch_scc1 LWC_%=
LWCR_%=:
buffer_load_dwordx4 a[160:163], v202, s[28:31], 0 offen sc1
buffer_load_dwordx4 a[164:167], v202, s[28:31], 0 offen offset:1024 sc1
v_add_u32 v250, s57, v233
v_cvt_pk_bf16_f32 v0, v184, v185
v_cvt_pk_bf16_f32 v2, v188, v189
v_cvt_pk_bf16_f32 v1, v186, v187
v_cvt_pk_bf16_f32 v3, v190, v191
v_permlane16_swap_b32 v0, v2
s_nop 0
v_permlane16_swap_b32 v1, v3
buffer_store_dwordx4 v[0:3], v250, s[92:95], 0 offen
buffer_load_dwordx4 a[168:171], v202, s[28:31], 0 offen offset:2048 sc1
buffer_load_dwordx4 a[172:175], v202, s[28:31], 0 offen offset:3072 sc1
buffer_load_dwordx4 a[176:179], v202, s[28:31], s85 offen sc1
buffer_load_dwordx4 a[180:183], v202, s[28:31], s85 offen offset:1024 sc1
buffer_load_dwordx4 a[184:187], v202, s[28:31], s85 offen offset:2048 sc1
buffer_load_dwordx4 a[188:191], v202, s[28:31], s85 offen offset:3072 sc1
buffer_load_dwordx4 a[192:195], v202, s[28:31], s86 offen sc1
M v[192:195],v[236:239],a[0:3],0
M v[196:199],v[234:237],a[0:3],0
M v[192:195],v[236:239],a[4:7],v[192:195]
M v[196:199],v[234:237],a[4:7],v[196:199]
buffer_load_dwordx4 a[196:199], v202, s[28:31], s86 offen offset:1024 sc1
M v[192:195],v[236:239],a[8:11],v[192:195]
M v[196:199],v[234:237],a[8:11],v[196:199]
M v[192:195],v[236:239],a[12:15],v[192:195]
M v[196:199],v[234:237],a[12:15],v[196:199]
buffer_load_dwordx4 a[200:203], v202, s[28:31], s86 offen offset:2048 sc1
M v[192:195],v[236:239],a[16:19],v[192:195]
M v[196:199],v[234:237],a[16:19],v[196:199]
M v[192:195],v[236:239],a[20:23],v[192:195]
M v[196:199],v[234:237],a[20:23],v[196:199]
buffer_load_dwordx4 a[204:207], v202, s[28:31], s86 offen offset:3072 sc1
M v[192:195],v[236:239],a[24:27],v[192:195]
M v[196:199],v[234:237],a[24:27],v[196:199]
M v[192:195],v[236:239],a[28:31],v[192:195]
M v[196:199],v[234:237],a[28:31],v[196:199]
buffer_load_dwordx4 a[208:211], v202, s[28:31], s87 offen sc1
M v[192:195],v[236:239],a[32:35],v[192:195]
M v[196:199],v[234:237],a[32:35],v[196:199]
M v[192:195],v[236:239],a[36:39],v[192:195]
M v[196:199],v[234:237],a[36:39],v[196:199]
buffer_load_dwordx4 a[212:215], v202, s[28:31], s87 offen offset:1024 sc1
s_waitcnt vmcnt(20)
M v[192:195],v[236:239],a[40:43],v[192:195]
M v[196:199],v[234:237],a[40:43],v[196:199]
s_waitcnt vmcnt(19)
M v[192:195],v[236:239],a[44:47],v[192:195]
M v[196:199],v[234:237],a[44:47],v[196:199]
buffer_load_dwordx4 a[216:219], v202, s[28:31], s87 offen offset:2048 sc1
s_waitcnt vmcnt(19)
M v[192:195],v[236:239],a[48:51],v[192:195]
M v[196:199],v[234:237],a[48:51],v[196:199]
s_waitcnt vmcnt(18)
M v[192:195],v[236:239],a[52:55],v[192:195]
M v[196:199],v[234:237],a[52:55],v[196:199]
buffer_load_dwordx4 a[220:223], v202, s[28:31], s87 offen offset:3072 sc1
s_waitcnt vmcnt(18)
M v[192:195],v[236:239],a[56:59],v[192:195]
M v[196:199],v[234:237],a[56:59],v[196:199]
s_waitcnt vmcnt(17)
M v[192:195],v[236:239],a[60:63],v[192:195]
M v[196:199],v[234:237],a[60:63],v[196:199]
v_cmp_ne_u32 vcc, v249, v215
s_nop 1
s_cbranch_vccnz LRF_%=
LRFR_%=:
s_waitcnt vmcnt(16)
M v[184:187],v[236:239],a[160:163],0
M v[188:191],v[234:237],a[160:163],0
s_waitcnt vmcnt(15)
M v[184:187],v[236:239],a[164:167],v[184:187]
M v[188:191],v[234:237],a[164:167],v[188:191]
v_add_u32 v251, s58, v233
v_cvt_pk_bf16_f32 v4, v192, v193
v_cvt_pk_bf16_f32 v6, v196, v197
v_cvt_pk_bf16_f32 v5, v194, v195
v_cvt_pk_bf16_f32 v7, v198, v199
v_permlane16_swap_b32 v4, v6
s_nop 0
v_permlane16_swap_b32 v5, v7
buffer_store_dwordx4 v[4:7], v251, s[92:95], 0 offen
s_waitcnt vmcnt(14)
M v[184:187],v[236:239],a[168:171],v[184:187]
M v[188:191],v[234:237],a[168:171],v[188:191]
s_waitcnt vmcnt(13)
M v[184:187],v[236:239],a[172:175],v[184:187]
M v[188:191],v[234:237],a[172:175],v[188:191]
s_waitcnt vmcnt(12)
M v[184:187],v[236:239],a[176:179],v[184:187]
M v[188:191],v[234:237],a[176:179],v[188:191]
s_waitcnt vmcnt(11)
M v[184:187],v[236:239],a[180:183],v[184:187]
M v[188:191],v[234:237],a[180:183],v[188:191]
s_waitcnt vmcnt(10)
M v[184:187],v[236:239],a[184:187],v[184:187]
M v[188:191],v[234:237],a[184:187],v[188:191]
s_waitcnt vmcnt(9)
M v[184:187],v[236:239],a[188:191],v[184:187]
M v[188:191],v[234:237],a[188:191],v[188:191]
s_waitcnt vmcnt(8)
M v[184:187],v[236:239],a[192:195],v[184:187]
M v[188:191],v[234:237],a[192:195],v[188:191]
s_waitcnt vmcnt(7)
M v[184:187],v[236:239],a[196:199],v[184:187]
M v[188:191],v[234:237],a[196:199],v[188:191]
s_waitcnt vmcnt(6)
M v[184:187],v[236:239],a[200:203],v[184:187]
M v[188:191],v[234:237],a[200:203],v[188:191]
s_waitcnt vmcnt(5)
M v[184:187],v[236:239],a[204:207],v[184:187]
M v[188:191],v[234:237],a[204:207],v[188:191]
s_waitcnt vmcnt(4)
M v[184:187],v[236:239],a[208:211],v[184:187]
M v[188:191],v[234:237],a[208:211],v[188:191]
s_waitcnt vmcnt(3)
M v[184:187],v[236:239],a[212:215],v[184:187]
M v[188:191],v[234:237],a[212:215],v[188:191]
s_waitcnt vmcnt(2)
M v[184:187],v[236:239],a[216:219],v[184:187]
M v[188:191],v[234:237],a[216:219],v[188:191]
s_waitcnt vmcnt(1)
M v[184:187],v[236:239],a[220:223],v[184:187]
M v[188:191],v[234:237],a[220:223],v[188:191]
v_add_u32 v252, s59, v233
s_nop 5
v_cvt_pk_bf16_f32 v8, v184, v185
v_cvt_pk_bf16_f32 v10, v188, v189
v_cvt_pk_bf16_f32 v9, v186, v187
v_cvt_pk_bf16_f32 v11, v190, v191
v_permlane16_swap_b32 v8, v10
s_nop 0
v_permlane16_swap_b32 v9, v11
buffer_store_dwordx4 v[8:11], v252, s[92:95], 0 offen
s_branch LEND_%=
LST0_1_%=:
s_getpc_b64 s[80:81]
s_branch LRARE_%=
s_branch LRT0_1_%=
LST0_2_%=:
s_getpc_b64 s[80:81]
s_branch LRARE_%=
s_branch LRT0_2_%=
LST0_3_%=:
s_getpc_b64 s[80:81]
s_branch LRARE_%=
s_branch LRT0_3_%=
LST0_4_%=:
s_getpc_b64 s[80:81]
s_branch LRARE_%=
s_branch LRT0_4_%=
LST0_5_%=:
s_getpc_b64 s[80:81]
s_branch LRARE_%=
s_branch LRT0_5_%=
LST0_6_%=:
s_getpc_b64 s[80:81]
s_branch LRARE_%=
s_branch LRT0_6_%=
LST0_7_%=:
s_getpc_b64 s[80:81]
s_branch LRARE_%=
s_branch LRT0_7_%=
LST1_1_%=:
s_getpc_b64 s[80:81]
s_branch LRARE_%=
s_branch LRT1_1_%=
LST1_2_%=:
s_getpc_b64 s[80:81]
s_branch LRARE_%=
s_branch LRT1_2_%=
LST1_3_%=:
s_getpc_b64 s[80:81]
s_branch LRARE_%=
s_branch LRT1_3_%=
LST1_4_%=:
s_getpc_b64 s[80:81]
s_branch LRARE_%=
s_branch LRT1_4_%=
LST1_5_%=:
s_getpc_b64 s[80:81]
s_branch LRARE_%=
s_branch LRT1_5_%=
LST1_6_%=:
s_getpc_b64 s[80:81]
s_branch LRARE_%=
s_branch LRT1_6_%=
LST1_7_%=:
s_getpc_b64 s[80:81]
s_branch LRARE_%=
s_branch LRT1_7_%=
LRARE_%=:
s_nop 7
s_nop 7
v_mov_b32 v250, v248
s_nop 1
v_permlane32_swap_b32 v248, v250
v_max_f32 v248, v248, v250
v_mov_b32 v250, v248
s_nop 1
v_permlane16_swap_b32 v248, v250
v_max_f32 v248, v248, v250
v_max_f32 v248, 0, v248
v_exp_f32 v250, -v248
v_sub_f32 v204, v204, v248
v_sub_f32 v205, v205, v248
v_sub_f32 v206, v206, v248
v_sub_f32 v207, v207, v248
v_sub_f32 v104, v104, v248
v_sub_f32 v105, v105, v248
v_sub_f32 v106, v106, v248
v_sub_f32 v107, v107, v248
v_sub_f32 v112, v112, v248
v_sub_f32 v113, v113, v248
v_sub_f32 v114, v114, v248
v_sub_f32 v115, v115, v248
v_sub_f32 v120, v120, v248
v_sub_f32 v121, v121, v248
v_sub_f32 v122, v122, v248
v_sub_f32 v123, v123, v248
v_sub_f32 v128, v128, v248
v_sub_f32 v129, v129, v248
v_sub_f32 v130, v130, v248
v_sub_f32 v131, v131, v248
v_mul_f32 v0, v0, v250
v_mul_f32 v1, v1, v250
v_mul_f32 v2, v2, v250
v_mul_f32 v3, v3, v250
v_mul_f32 v8, v8, v250
v_mul_f32 v9, v9, v250
v_mul_f32 v10, v10, v250
v_mul_f32 v11, v11, v250
v_mul_f32 v16, v16, v250
v_mul_f32 v17, v17, v250
v_mul_f32 v18, v18, v250
v_mul_f32 v19, v19, v250
v_mul_f32 v24, v24, v250
v_mul_f32 v25, v25, v250
v_mul_f32 v26, v26, v250
v_mul_f32 v27, v27, v250
v_mul_f32 v32, v32, v250
v_mul_f32 v33, v33, v250
v_mul_f32 v34, v34, v250
v_mul_f32 v35, v35, v250
v_mul_f32 v40, v40, v250
v_mul_f32 v41, v41, v250
v_mul_f32 v42, v42, v250
v_mul_f32 v43, v43, v250
v_mul_f32 v48, v48, v250
v_mul_f32 v49, v49, v250
v_mul_f32 v50, v50, v250
v_mul_f32 v51, v51, v250
v_mul_f32 v56, v56, v250
v_mul_f32 v57, v57, v250
v_mul_f32 v58, v58, v250
v_mul_f32 v59, v59, v250
v_mul_f32 v64, v64, v250
v_mul_f32 v65, v65, v250
v_mul_f32 v66, v66, v250
v_mul_f32 v67, v67, v250
v_mul_f32 v72, v72, v250
v_mul_f32 v73, v73, v250
v_mul_f32 v74, v74, v250
v_mul_f32 v75, v75, v250
v_mul_f32 v80, v80, v250
v_mul_f32 v81, v81, v250
v_mul_f32 v82, v82, v250
v_mul_f32 v83, v83, v250
v_mul_f32 v88, v88, v250
v_mul_f32 v89, v89, v250
v_mul_f32 v90, v90, v250
v_mul_f32 v91, v91, v250
v_mul_f32 v96, v96, v250
v_mul_f32 v97, v97, v250
v_mul_f32 v98, v98, v250
v_mul_f32 v99, v99, v250
v_mov_b32 v251, v249
s_nop 1
v_permlane32_swap_b32 v249, v251
v_max_f32 v249, v249, v251
v_mov_b32 v251, v249
s_nop 1
v_permlane16_swap_b32 v249, v251
v_max_f32 v249, v249, v251
v_max_f32 v249, 0, v249
v_exp_f32 v251, -v249
v_sub_f32 v208, v208, v249
v_sub_f32 v209, v209, v249
v_sub_f32 v210, v210, v249
v_sub_f32 v211, v211, v249
v_sub_f32 v108, v108, v249
v_sub_f32 v109, v109, v249
v_sub_f32 v110, v110, v249
v_sub_f32 v111, v111, v249
v_sub_f32 v116, v116, v249
v_sub_f32 v117, v117, v249
v_sub_f32 v118, v118, v249
v_sub_f32 v119, v119, v249
v_sub_f32 v124, v124, v249
v_sub_f32 v125, v125, v249
v_sub_f32 v126, v126, v249
v_sub_f32 v127, v127, v249
v_sub_f32 v132, v132, v249
v_sub_f32 v133, v133, v249
v_sub_f32 v134, v134, v249
v_sub_f32 v135, v135, v249
v_mul_f32 v4, v4, v251
v_mul_f32 v5, v5, v251
v_mul_f32 v6, v6, v251
v_mul_f32 v7, v7, v251
v_mul_f32 v12, v12, v251
v_mul_f32 v13, v13, v251
v_mul_f32 v14, v14, v251
v_mul_f32 v15, v15, v251
v_mul_f32 v20, v20, v251
v_mul_f32 v21, v21, v251
v_mul_f32 v22, v22, v251
v_mul_f32 v23, v23, v251
v_mul_f32 v28, v28, v251
v_mul_f32 v29, v29, v251
v_mul_f32 v30, v30, v251
v_mul_f32 v31, v31, v251
v_mul_f32 v36, v36, v251
v_mul_f32 v37, v37, v251
v_mul_f32 v38, v38, v251
v_mul_f32 v39, v39, v251
v_mul_f32 v44, v44, v251
v_mul_f32 v45, v45, v251
v_mul_f32 v46, v46, v251
v_mul_f32 v47, v47, v251
v_mul_f32 v52, v52, v251
v_mul_f32 v53, v53, v251
v_mul_f32 v54, v54, v251
v_mul_f32 v55, v55, v251
v_mul_f32 v60, v60, v251
v_mul_f32 v61, v61, v251
v_mul_f32 v62, v62, v251
v_mul_f32 v63, v63, v251
v_mul_f32 v68, v68, v251
v_mul_f32 v69, v69, v251
v_mul_f32 v70, v70, v251
v_mul_f32 v71, v71, v251
v_mul_f32 v76, v76, v251
v_mul_f32 v77, v77, v251
v_mul_f32 v78, v78, v251
v_mul_f32 v79, v79, v251
v_mul_f32 v84, v84, v251
v_mul_f32 v85, v85, v251
v_mul_f32 v86, v86, v251
v_mul_f32 v87, v87, v251
v_mul_f32 v92, v92, v251
v_mul_f32 v93, v93, v251
v_mul_f32 v94, v94, v251
v_mul_f32 v95, v95, v251
v_mul_f32 v100, v100, v251
v_mul_f32 v101, v101, v251
v_mul_f32 v102, v102, v251
v_mul_f32 v103, v103, v251
v_exp_f32 v200, v104
v_exp_f32 v201, v105
v_exp_f32 v202, v106
v_cvt_pk_bf16_f32 v136, v200, v201
v_exp_f32 v203, v107
v_exp_f32 v200, v108
v_cvt_pk_bf16_f32 v137, v202, v203
v_exp_f32 v201, v109
v_exp_f32 v202, v110
v_cvt_pk_bf16_f32 v140, v200, v201
v_exp_f32 v203, v111
v_exp_f32 v200, v112
v_cvt_pk_bf16_f32 v141, v202, v203
v_exp_f32 v201, v113
v_exp_f32 v202, v114
v_cvt_pk_bf16_f32 v138, v200, v201
v_exp_f32 v203, v115
v_exp_f32 v200, v116
v_cvt_pk_bf16_f32 v139, v202, v203
v_exp_f32 v201, v117
v_exp_f32 v202, v118
v_cvt_pk_bf16_f32 v142, v200, v201
v_exp_f32 v203, v119
s_nop 0
v_cvt_pk_bf16_f32 v143, v202, v203
s_nop 3
s_add_u32 s80, s80, 4
s_addc_u32 s81, s81, 0
s_setpc_b64 s[80:81]
LWA_%=:
s_mov_b32 s84, 0
LWAL_%=:
buffer_load_dword v249, v214, s[28:31], 0 offen sc1
buffer_load_dword v248, v213, s[28:31], 0 offen sc1
s_waitcnt vmcnt(0)
v_cmp_ne_u32 vcc, v249, v215
s_nop 3
s_cbranch_vccz LWAK_%=
v_mov_b32 v215, v249
buffer_store_dword v215, v243, s[28:31], 0 offen sc1
s_waitcnt vmcnt(0)
LWAK_%=:
v_cmp_ne_u32 vcc, v248, v212
s_nop 3
s_and_b32 s74, vcc_lo, 0xffff
s_and_b32 s75, vcc_hi, 0
s_or_b32 s74, s74, s75
s_cbranch_scc0 LWAR_%=
s_add_u32 s84, s84, 1
s_cmp_gt_u32 s84, 300000
s_cbranch_scc1 LWAR_%=
s_sleep 1
s_branch LWAL_%=
LWB_%=:
s_mov_b32 s84, 0
LWBL_%=:
buffer_load_dword v249, v214, s[28:31], 0 offen sc1
buffer_load_dword v248, v213, s[28:31], 0 offen sc1
s_waitcnt vmcnt(0)
v_cmp_ne_u32 vcc, v249, v215
s_nop 3
s_cbranch_vccz LWBK_%=
v_mov_b32 v215, v249
buffer_store_dword v215, v243, s[28:31], 0 offen sc1
s_waitcnt vmcnt(0)
LWBK_%=:
v_cmp_ne_u32 vcc, v248, v212
s_nop 3
s_and_b32 s74, vcc_lo, 0xffff0000
s_and_b32 s75, vcc_hi, 0
s_or_b32 s74, s74, s75
s_cbranch_scc0 LWBR_%=
s_add_u32 s84, s84, 1
s_cmp_gt_u32 s84, 300000
s_cbranch_scc1 LWBR_%=
s_sleep 1
s_branch LWBL_%=
LWC_%=:
s_mov_b32 s84, 0
LWCL_%=:
buffer_load_dword v249, v214, s[28:31], 0 offen sc1
buffer_load_dword v248, v213, s[28:31], 0 offen sc1
s_waitcnt vmcnt(0)
v_cmp_ne_u32 vcc, v249, v215
s_nop 3
s_cbranch_vccz LWCK_%=
v_mov_b32 v215, v249
buffer_store_dword v215, v243, s[28:31], 0 offen sc1
s_waitcnt vmcnt(0)
LWCK_%=:
v_cmp_ne_u32 vcc, v248, v212
s_nop 3
s_and_b32 s74, vcc_lo, 0
s_and_b32 s75, vcc_hi, 0xffff
s_or_b32 s74, s74, s75
s_cbranch_scc0 LWCR_%=
s_add_u32 s84, s84, 1
s_cmp_gt_u32 s84, 300000
s_cbranch_scc1 LWCR_%=
s_sleep 1
s_branch LWCL_%=
LRF_%=:
v_mov_b32 v215, v249
buffer_store_dword v215, v243, s[28:31], 0 offen sc1
s_waitcnt vmcnt(0)
s_branch LRFR_%=
LW3_%=:
v_mov_b32 v244, 0x3f803f80
v_mov_b32 v245, 0x3f803f80
v_mov_b32 v246, 0x3f803f80
v_mov_b32 v247, 0x3f803f80
s_waitcnt vmcnt(17)
ds_read_b128 a[144:147], v241
ds_read_b128 a[152:155], v241 offset:2048
ds_read_b128 a[148:151], v242
ds_read_b128 a[156:159], v242 offset:2048
v_add_u32 v216, 0, v224
v_xor_b32 v217, 32, v216
v_xor_b32 v218, 64, v216
v_xor_b32 v219, 96, v216
s_waitcnt vmcnt(11)
s_barrier
T v[152:153],v216,0
T v[154:155],v216,6144
W 0
M v[0:3],v[152:155],a[144:147],0
T v[156:157],v216,12288
T v[158:159],v216,18432
W 0
M v[0:3],v[156:159],a[148:151],v[0:3]
T v[160:161],v217,0
T v[162:163],v217,6144
T v[164:165],v217,12288
T v[166:167],v217,18432
s_add_u32 m0, s43, 147456
s_nop 0
buffer_load_dwordx4 v236, s[24:27], s46 offen lds
T v[168:169],v218,0
T v[170:171],v218,6144
W 4
M v[8:11],v[160:163],a[144:147],0
T v[172:173],v218,12288
T v[174:175],v218,18432
buffer_load_dwordx4 v237, s[24:27], s46 offen offset:1024 lds
W 4
M v[8:11],v[164:167],a[148:151],v[8:11]
T v[176:177],v219,0
T v[178:179],v219,6144
s_mul_i32 s77, s46, 6144
s_add_u32 s78, s77, 196608
T v[180:181],v219,12288
T v[182:183],v219,18432
s_mov_b32 m0, 52224
s_nop 0
buffer_load_dwordx4 v233, s[16:19], s78 offen lds
T v[152:153],v216,128
W 7
M v[16:19],v[168:171],a[144:147],0
T v[154:155],v216,6272
buffer_load_dwordx4 v234, s[16:19], s78 offen offset:1024 lds
W 6
M v[16:19],v[172:175],a[148:151],v[16:19]
T v[156:157],v216,12416
T v[158:159],v216,18560
T v[160:161],v217,128
buffer_load_dwordx4 v235, s[16:19], s78 offen offset:2048 lds
s_add_u32 s78, s77, 245760
s_mov_b32 m0, 58368
T v[162:163],v217,6272
W 6
M v[24:27],v[176:179],a[144:147],0
T v[164:165],v217,12416
buffer_load_dwordx4 v233, s[16:19], s78 offen lds
M v[24:27],v[180:183],a[148:151],v[24:27]
T v[166:167],v217,18560
W 6
T v[168:169],v218,128
buffer_load_dwordx4 v234, s[16:19], s78 offen offset:1024 lds
W 5
T v[170:171],v218,6272
M v[32:35],v[152:155],a[144:147],0
T v[172:173],v218,12416
buffer_load_dwordx4 v235, s[16:19], s78 offen offset:2048 lds
s_add_u32 s78, s77, 589824
s_mov_b32 m0, 64512
M v[32:35],v[156:159],a[148:151],v[32:35]
T v[174:175],v218,18560
W 5
T v[176:177],v219,128
buffer_load_dwordx4 v233, s[16:19], s78 offen lds
T v[178:179],v219,6272
v_add_u32 v220, 24576, v224
v_xor_b32 v221, 32, v220
v_xor_b32 v222, 64, v220
M v[40:43],v[160:163],a[144:147],0
T v[180:181],v219,12416
buffer_load_dwordx4 v234, s[16:19], s78 offen offset:1024 lds
W 5
M v[40:43],v[164:167],a[148:151],v[40:43]
T v[182:183],v219,18560
s_waitcnt vmcnt(16)
T v[152:153],v216,256
buffer_load_dwordx4 v235, s[16:19], s78 offen offset:2048 lds
s_add_u32 s78, s77, 442368
s_mov_b32 m0, 67584
T v[154:155],v216,6400
v_add_u32 v250, -24576, v241
v_add_u32 v251, -24576, v242
ds_read_b128 a[160:163], v250
M v[48:51],v[168:171],a[144:147],0
T v[156:157],v216,12544
buffer_load_dwordx4 v233, s[16:19], s78 offen lds
W 6
M v[48:51],v[172:175],a[148:151],v[48:51]
T v[158:159],v216,18688
ds_read_b128 a[164:167], v251
T v[160:161],v217,256
buffer_load_dwordx4 v234, s[16:19], s78 offen offset:1024 lds
v_xor_b32 v223, 96, v220
W 7
T v[162:163],v217,6400
buffer_load_dwordx4 v235, s[16:19], s78 offen offset:2048 lds
s_add_u32 s78, s77, 638976
s_mov_b32 m0, 70656
s_nop 0
buffer_load_dwordx4 v233, s[16:19], s78 offen lds
buffer_load_dwordx4 v234, s[16:19], s78 offen offset:1024 lds
buffer_load_dwordx4 v235, s[16:19], s78 offen offset:2048 lds
M v[56:59],v[176:179],a[144:147],0
T v[164:165],v217,12544
s_mul_i32 s77, s47, 6144
s_add_u32 s78, s77, 196608
s_mov_b32 m0, 76800
s_nop 0
buffer_load_dwordx4 v233, s[16:19], s78 offen lds
M v[56:59],v[180:183],a[148:151],v[56:59]
T v[166:167],v217,18688
W 6
T v[168:169],v218,256
buffer_load_dwordx4 v234, s[16:19], s78 offen offset:1024 lds
W 5
T v[170:171],v218,6400
buffer_load_dwordx4 v235, s[16:19], s78 offen offset:2048 lds
s_add_u32 s78, s77, 245760
s_mov_b32 m0, 82944
s_nop 0
buffer_load_dwordx4 v233, s[16:19], s78 offen lds
buffer_load_dwordx4 v234, s[16:19], s78 offen offset:1024 lds
buffer_load_dwordx4 v235, s[16:19], s78 offen offset:2048 lds
s_add_u32 s78, s77, 589824
s_mov_b32 m0, 89088
s_nop 0
buffer_load_dwordx4 v233, s[16:19], s78 offen lds
buffer_load_dwordx4 v234, s[16:19], s78 offen offset:1024 lds
buffer_load_dwordx4 v235, s[16:19], s78 offen offset:2048 lds
s_add_u32 s78, s77, 442368
s_mov_b32 m0, 92160
s_nop 0
buffer_load_dwordx4 v233, s[16:19], s78 offen lds
buffer_load_dwordx4 v234, s[16:19], s78 offen offset:1024 lds
buffer_load_dwordx4 v235, s[16:19], s78 offen offset:2048 lds
s_add_u32 s78, s77, 638976
s_mov_b32 m0, 95232
s_nop 0
buffer_load_dwordx4 v233, s[16:19], s78 offen lds
buffer_load_dwordx4 v234, s[16:19], s78 offen offset:1024 lds
buffer_load_dwordx4 v235, s[16:19], s78 offen offset:2048 lds
M v[64:67],v[152:155],a[144:147],0
T v[172:173],v218,12544
s_add_u32 m0, s43, 122880
s_nop 0
buffer_load_dwordx4 v236, s[24:27], s47 offen lds
M v[64:67],v[156:159],a[148:151],v[64:67]
T v[174:175],v218,18688
W 5
T v[176:177],v219,256
buffer_load_dwordx4 v237, s[24:27], s47 offen offset:1024 lds
T v[178:179],v219,6400
T v[180:181],v219,12544
W 6
M v[72:75],v[160:163],a[144:147],0
T v[182:183],v219,18688
M v[72:75],v[164:167],a[148:151],v[72:75]
s_waitcnt vmcnt(34)
s_barrier
T v[152:153],v220,0
T v[154:155],v220,6144
W 6
T v[156:157],v220,12288
T v[158:159],v220,18432
M v[80:83],v[168:171],a[144:147],0
T v[160:161],v221,0
W 6
M v[80:83],v[172:175],a[148:151],v[80:83]
T v[162:163],v221,6144
T v[164:165],v221,12288
W 6
T v[166:167],v221,18432
M v[88:91],v[176:179],a[144:147],0
T v[168:169],v222,0
W 5
M v[88:91],v[180:183],a[148:151],v[88:91]
T v[170:171],v222,6144
T v[172:173],v222,12288
W 5
T v[174:175],v222,18432
M v[0:3],v[152:155],a[160:163],v[0:3]
T v[176:177],v223,0
W 5
M v[0:3],v[156:159],a[164:167],v[0:3]
T v[178:179],v223,6144
T v[180:181],v223,12288
W 5
T v[182:183],v223,18432
M v[8:11],v[160:163],a[160:163],v[8:11]
T v[152:153],v220,128
W 5
M v[8:11],v[164:167],a[164:167],v[8:11]
T v[154:155],v220,6272
T v[156:157],v220,12416
W 5
T v[158:159],v220,18560
M v[16:19],v[168:171],a[160:163],v[16:19]
T v[160:161],v221,128
W 5
M v[16:19],v[172:175],a[164:167],v[16:19]
T v[162:163],v221,6272
T v[164:165],v221,12416
W 5
T v[166:167],v221,18560
M v[24:27],v[176:179],a[160:163],v[24:27]
T v[168:169],v222,128
W 5
M v[24:27],v[180:183],a[164:167],v[24:27]
T v[170:171],v222,6272
T v[172:173],v222,12416
W 5
T v[174:175],v222,18560
M v[32:35],v[152:155],a[160:163],v[32:35]
T v[176:177],v223,128
W 5
M v[32:35],v[156:159],a[164:167],v[32:35]
T v[178:179],v223,6272
v_add_u32 v216, 49152, v224
T v[180:181],v223,12416
v_xor_b32 v217, 32, v216
v_xor_b32 v218, 64, v216
v_xor_b32 v219, 96, v216
T v[182:183],v223,18560
W 5
M v[40:43],v[160:163],a[160:163],v[40:43]
T v[152:153],v220,256
M v[40:43],v[164:167],a[164:167],v[40:43]
T v[154:155],v220,6400
s_waitcnt vmcnt(32)
T v[156:157],v220,12544
ds_read_b128 a[176:179], v241
ds_read_b128 a[180:183], v242
W 7
T v[158:159],v220,18688
W 1
s_add_u32 m0, s43, 147456
s_nop 0
buffer_load_dwordx4 v236, s[24:27], s48 offen lds
M v[48:51],v[168:171],a[160:163],v[48:51]
T v[160:161],v221,256
M v[48:51],v[172:175],a[164:167],v[48:51]
T v[162:163],v221,6400
buffer_load_dwordx4 v237, s[24:27], s48 offen offset:1024 lds
T v[164:165],v221,12544
T v[166:167],v221,18688
M v[56:59],v[176:179],a[160:163],v[56:59]
T v[168:169],v222,256
M v[56:59],v[180:183],a[164:167],v[56:59]
T v[170:171],v222,6400
W 5
T v[172:173],v222,12544
T v[174:175],v222,18688
W 5
M v[64:67],v[152:155],a[160:163],v[64:67]
T v[176:177],v223,256
M v[64:67],v[156:159],a[164:167],v[64:67]
T v[178:179],v223,6400
W 5
T v[180:181],v223,12544
T v[182:183],v223,18688
W 5
M v[72:75],v[160:163],a[160:163],v[72:75]
s_waitcnt vmcnt(19)
s_barrier
T v[152:153],v216,0
M v[72:75],v[164:167],a[164:167],v[72:75]
T v[154:155],v216,6144
W 5
T v[156:157],v216,12288
T v[158:159],v216,18432
W 5
M v[80:83],v[168:171],a[160:163],v[80:83]
T v[160:161],v217,0
M v[80:83],v[172:175],a[164:167],v[80:83]
T v[162:163],v217,6144
W 5
T v[164:165],v217,12288
T v[166:167],v217,18432
W 5
M v[88:91],v[176:179],a[160:163],v[88:91]
T v[168:169],v218,0
M v[88:91],v[180:183],a[164:167],v[88:91]
T v[170:171],v218,6144
W 5
T v[172:173],v218,12288
T v[174:175],v218,18432
W 5
M v[0:3],v[152:155],a[176:179],v[0:3]
T v[176:177],v219,0
s_mul_i32 s77, s48, 6144
M v[0:3],v[156:159],a[180:183],v[0:3]
T v[178:179],v219,6144
s_add_u32 s78, s77, 196608
T v[180:181],v219,12288
s_mov_b32 m0, 101376
s_nop 0
buffer_load_dwordx4 v233, s[16:19], s78 offen lds
v_mov_b32 v213, v104
T v[182:183],v219,18432
v_mov_b32 v214, v109
W 5
M v[8:11],v[160:163],a[176:179],v[8:11]
T v[152:153],v216,128
buffer_load_dwordx4 v234, s[16:19], s78 offen offset:1024 lds
M v[8:11],v[164:167],a[180:183],v[8:11]
T v[154:155],v216,6272
buffer_load_dword v110, v213, s[28:31], 0 offen sc1
T v[156:157],v216,12416
buffer_load_dwordx4 v235, s[16:19], s78 offen offset:2048 lds
s_add_u32 s78, s77, 245760
s_mov_b32 m0, 107520
T v[158:159],v216,18560
W 5
M v[16:19],v[168:171],a[176:179],v[16:19]
T v[160:161],v217,128
buffer_load_dwordx4 v233, s[16:19], s78 offen lds
M v[16:19],v[172:175],a[180:183],v[16:19]
T v[162:163],v217,6272
W 5
T v[164:165],v217,12416
buffer_load_dwordx4 v234, s[16:19], s78 offen offset:1024 lds
T v[166:167],v217,18560
W 5
M v[24:27],v[176:179],a[176:179],v[24:27]
T v[168:169],v218,128
buffer_load_dwordx4 v235, s[16:19], s78 offen offset:2048 lds
M v[24:27],v[180:183],a[180:183],v[24:27]
T v[170:171],v218,6272
s_add_u32 s78, s77, 589824
T v[172:173],v218,12416
s_mov_b32 m0, 113664
s_nop 0
buffer_load_dwordx4 v233, s[16:19], s78 offen lds
W 5
T v[174:175],v218,18560
M v[32:35],v[152:155],a[176:179],v[32:35]
T v[176:177],v219,128
buffer_load_dwordx4 v234, s[16:19], s78 offen offset:1024 lds
M v[32:35],v[156:159],a[180:183],v[32:35]
T v[178:179],v219,6272
v_add_u32 v220, 73728, v224
T v[180:181],v219,12416
buffer_load_dwordx4 v235, s[16:19], s78 offen offset:2048 lds
v_xor_b32 v221, 32, v220
v_xor_b32 v222, 64, v220
T v[182:183],v219,18560
v_xor_b32 v223, 96, v220
s_add_u32 s78, s77, 442368
s_mov_b32 m0, 116736
M v[40:43],v[160:163],a[176:179],v[40:43]
T v[152:153],v216,256
W 5
M v[40:43],v[164:167],a[180:183],v[40:43]
T v[154:155],v216,6400
buffer_load_dwordx4 v233, s[16:19], s78 offen lds
T v[156:157],v216,12544
s_waitcnt vmcnt(13)
W 5
T v[158:159],v216,18688
buffer_load_dwordx4 v234, s[16:19], s78 offen offset:1024 lds
v_add_u32 v250, -24576, v241
v_add_u32 v251, -24576, v242
ds_read_b128 a[192:195], v250
M v[48:51],v[168:171],a[176:179],v[48:51]
T v[160:161],v217,256
ds_read_b128 a[196:199], v251
M v[48:51],v[172:175],a[180:183],v[48:51]
T v[162:163],v217,6400
buffer_load_dwordx4 v235, s[16:19], s78 offen offset:2048 lds
s_add_u32 s78, s77, 638976
s_mov_b32 m0, 119808
T v[164:165],v217,12544
W 7
T v[166:167],v217,18688
buffer_load_dwordx4 v233, s[16:19], s78 offen lds
buffer_load_dwordx4 v234, s[16:19], s78 offen offset:1024 lds
buffer_load_dwordx4 v235, s[16:19], s78 offen offset:2048 lds
M v[56:59],v[176:179],a[176:179],v[56:59]
T v[168:169],v218,256
W 4
s_add_u32 m0, s43, 122880
M v[56:59],v[180:183],a[180:183],v[56:59]
T v[170:171],v218,6400
buffer_load_dwordx4 v236, s[24:27], s49 offen lds
T v[172:173],v218,12544
T v[174:175],v218,18688
buffer_load_dwordx4 v237, s[24:27], s49 offen offset:1024 lds
W 5
M v[64:67],v[152:155],a[176:179],v[64:67]
T v[176:177],v219,256
M v[64:67],v[156:159],a[180:183],v[64:67]
T v[178:179],v219,6400
W 5
T v[180:181],v219,12544
T v[182:183],v219,18688
W 5
M v[72:75],v[160:163],a[176:179],v[72:75]
s_barrier
T v[152:153],v220,0
M v[72:75],v[164:167],a[180:183],v[72:75]
T v[154:155],v220,6144
W 5
T v[156:157],v220,12288
T v[158:159],v220,18432
W 5
M v[80:83],v[168:171],a[176:179],v[80:83]
T v[160:161],v221,0
M v[80:83],v[172:175],a[180:183],v[80:83]
T v[162:163],v221,6144
W 5
T v[164:165],v221,12288
T v[166:167],v221,18432
W 5
M v[88:91],v[176:179],a[176:179],v[88:91]
T v[168:169],v222,0
M v[88:91],v[180:183],a[180:183],v[88:91]
T v[170:171],v222,6144
W 5
T v[172:173],v222,12288
T v[174:175],v222,18432
W 5
M v[0:3],v[152:155],a[192:195],v[0:3]
T v[176:177],v223,0
s_mul_i32 s77, s49, 6144
M v[0:3],v[156:159],a[196:199],v[0:3]
T v[178:179],v223,6144
s_add_u32 s78, s77, 196608
T v[180:181],v223,12288
s_mov_b32 m0, 3072
s_nop 0
buffer_load_dwordx4 v233, s[16:19], s78 offen lds
W 5
T v[182:183],v223,18432
M v[8:11],v[160:163],a[192:195],v[8:11]
T v[152:153],v220,128
buffer_load_dwordx4 v234, s[16:19], s78 offen offset:1024 lds
M v[8:11],v[164:167],a[196:199],v[8:11]
T v[154:155],v220,6272
W 5
T v[156:157],v220,12416
buffer_load_dwordx4 v235, s[16:19], s78 offen offset:2048 lds
s_add_u32 s78, s77, 245760
s_mov_b32 m0, 9216
T v[158:159],v220,18560
W 5
M v[16:19],v[168:171],a[192:195],v[16:19]
T v[160:161],v221,128
buffer_load_dwordx4 v233, s[16:19], s78 offen lds
M v[16:19],v[172:175],a[196:199],v[16:19]
T v[162:163],v221,6272
W 5
T v[164:165],v221,12416
buffer_load_dwordx4 v234, s[16:19], s78 offen offset:1024 lds
T v[166:167],v221,18560
W 5
M v[24:27],v[176:179],a[192:195],v[24:27]
T v[168:169],v222,128
buffer_load_dwordx4 v235, s[16:19], s78 offen offset:2048 lds
M v[24:27],v[180:183],a[196:199],v[24:27]
T v[170:171],v222,6272
s_add_u32 s78, s77, 589824
T v[172:173],v222,12416
s_mov_b32 m0, 15360
s_nop 0
buffer_load_dwordx4 v233, s[16:19], s78 offen lds
W 5
T v[174:175],v222,18560
M v[32:35],v[152:155],a[192:195],v[32:35]
T v[176:177],v223,128
buffer_load_dwordx4 v234, s[16:19], s78 offen offset:1024 lds
M v[32:35],v[156:159],a[196:199],v[32:35]
T v[178:179],v223,6272
v_add_u32 v216, 98304, v224
T v[180:181],v223,12416
buffer_load_dwordx4 v235, s[16:19], s78 offen offset:2048 lds
v_xor_b32 v217, 32, v216
v_xor_b32 v218, 64, v216
T v[182:183],v223,18560
v_xor_b32 v219, 96, v216
s_add_u32 s78, s77, 442368
s_mov_b32 m0, 18432
M v[40:43],v[160:163],a[192:195],v[40:43]
T v[152:153],v220,256
W 5
M v[40:43],v[164:167],a[196:199],v[40:43]
T v[154:155],v220,6400
buffer_load_dwordx4 v233, s[16:19], s78 offen lds
T v[156:157],v220,12544
s_waitcnt vmcnt(28)
ds_read_b128 a[208:211], v241
ds_read_b128 a[212:215], v242
T v[158:159],v220,18688
buffer_load_dwordx4 v234, s[16:19], s78 offen offset:1024 lds
W 7
M v[48:51],v[168:171],a[192:195],v[48:51]
T v[160:161],v221,256
M v[48:51],v[172:175],a[196:199],v[48:51]
T v[162:163],v221,6400
buffer_load_dwordx4 v235, s[16:19], s78 offen offset:2048 lds
s_add_u32 s78, s77, 638976
s_mov_b32 m0, 21504
T v[164:165],v221,12544
W 7
T v[166:167],v221,18688
buffer_load_dwordx4 v233, s[16:19], s78 offen lds
W 5
buffer_load_dwordx4 v234, s[16:19], s78 offen offset:1024 lds
buffer_load_dwordx4 v235, s[16:19], s78 offen offset:2048 lds
M v[56:59],v[176:179],a[192:195],v[56:59]
T v[168:169],v222,256
s_add_u32 m0, s43, 147456
M v[56:59],v[180:183],a[196:199],v[56:59]
T v[170:171],v222,6400
buffer_load_dwordx4 v236, s[24:27], s50 offen lds
T v[172:173],v222,12544
W 5
T v[174:175],v222,18688
buffer_load_dwordx4 v237, s[24:27], s50 offen offset:1024 lds
M v[64:67],v[152:155],a[192:195],v[64:67]
T v[176:177],v223,256
W 5
M v[64:67],v[156:159],a[196:199],v[64:67]
T v[178:179],v223,6400
T v[180:181],v223,12544
W 5
T v[182:183],v223,18688
M v[72:75],v[160:163],a[192:195],v[72:75]
s_waitcnt vmcnt(19)
s_barrier
T v[152:153],v216,0
M v[72:75],v[164:167],a[196:199],v[72:75]
T v[154:155],v216,6144
W 5
T v[156:157],v216,12288
T v[158:159],v216,18432
W 5
M v[80:83],v[168:171],a[192:195],v[80:83]
T v[160:161],v217,0
M v[80:83],v[172:175],a[196:199],v[80:83]
T v[162:163],v217,6144
W 5
T v[164:165],v217,12288
T v[166:167],v217,18432
W 5
M v[88:91],v[176:179],a[192:195],v[88:91]
T v[168:169],v218,0
M v[88:91],v[180:183],a[196:199],v[88:91]
T v[170:171],v218,6144
W 5
T v[172:173],v218,12288
T v[174:175],v218,18432
W 5
M v[0:3],v[152:155],a[208:211],v[0:3]
T v[176:177],v219,0
s_mul_i32 s77, s50, 6144
M v[0:3],v[156:159],a[212:215],v[0:3]
T v[178:179],v219,6144
s_add_u32 s78, s77, 196608
T v[180:181],v219,12288
s_mov_b32 m0, 27648
s_nop 0
buffer_load_dwordx4 v233, s[16:19], s78 offen lds
W 5
T v[182:183],v219,18432
M v[8:11],v[160:163],a[208:211],v[8:11]
T v[152:153],v216,128
buffer_load_dwordx4 v234, s[16:19], s78 offen offset:1024 lds
M v[8:11],v[164:167],a[212:215],v[8:11]
T v[154:155],v216,6272
W 5
T v[156:157],v216,12416
buffer_load_dwordx4 v235, s[16:19], s78 offen offset:2048 lds
s_add_u32 s78, s77, 245760
s_mov_b32 m0, 33792
T v[158:159],v216,18560
W 5
M v[16:19],v[168:171],a[208:211],v[16:19]
T v[160:161],v217,128
buffer_load_dwordx4 v233, s[16:19], s78 offen lds
M v[16:19],v[172:175],a[212:215],v[16:19]
T v[162:163],v217,6272
W 5
T v[164:165],v217,12416
buffer_load_dwordx4 v234, s[16:19], s78 offen offset:1024 lds
T v[166:167],v217,18560
W 5
M v[24:27],v[176:179],a[208:211],v[24:27]
T v[168:169],v218,128
buffer_load_dwordx4 v235, s[16:19], s78 offen offset:2048 lds
M v[24:27],v[180:183],a[212:215],v[24:27]
T v[170:171],v218,6272
s_add_u32 s78, s77, 589824
T v[172:173],v218,12416
s_mov_b32 m0, 39936
s_nop 0
buffer_load_dwordx4 v233, s[16:19], s78 offen lds
W 5
T v[174:175],v218,18560
M v[32:35],v[152:155],a[208:211],v[32:35]
T v[176:177],v219,128
buffer_load_dwordx4 v234, s[16:19], s78 offen offset:1024 lds
M v[32:35],v[156:159],a[212:215],v[32:35]
T v[178:179],v219,6272
v_add_u32 v220, 0, v224
T v[180:181],v219,12416
buffer_load_dwordx4 v235, s[16:19], s78 offen offset:2048 lds
v_xor_b32 v221, 32, v220
v_xor_b32 v222, 64, v220
T v[182:183],v219,18560
v_xor_b32 v223, 96, v220
s_add_u32 s78, s77, 442368
s_mov_b32 m0, 43008
M v[40:43],v[160:163],a[208:211],v[40:43]
T v[152:153],v216,256
W 5
M v[40:43],v[164:167],a[212:215],v[40:43]
T v[154:155],v216,6400
buffer_load_dwordx4 v233, s[16:19], s78 offen lds
T v[156:157],v216,12544
s_waitcnt vmcnt(27)
W 5
T v[158:159],v216,18688
buffer_load_dwordx4 v234, s[16:19], s78 offen offset:1024 lds
v_add_u32 v250, -24576, v241
v_add_u32 v251, -24576, v242
ds_read_b128 a[224:227], v250
M v[48:51],v[168:171],a[208:211],v[48:51]
T v[160:161],v217,256
ds_read_b128 a[228:231], v251
M v[48:51],v[172:175],a[212:215],v[48:51]
T v[162:163],v217,6400
buffer_load_dwordx4 v235, s[16:19], s78 offen offset:2048 lds
s_add_u32 s78, s77, 638976
s_mov_b32 m0, 46080
T v[164:165],v217,12544
W 7
T v[166:167],v217,18688
buffer_load_dwordx4 v233, s[16:19], s78 offen lds
buffer_load_dwordx4 v234, s[16:19], s78 offen offset:1024 lds
buffer_load_dwordx4 v235, s[16:19], s78 offen offset:2048 lds
M v[56:59],v[176:179],a[208:211],v[56:59]
T v[168:169],v218,256
W 4
s_add_u32 m0, s43, 122880
M v[56:59],v[180:183],a[212:215],v[56:59]
T v[170:171],v218,6400
buffer_load_dwordx4 v236, s[24:27], s51 offen lds
T v[172:173],v218,12544
T v[174:175],v218,18688
buffer_load_dwordx4 v237, s[24:27], s51 offen offset:1024 lds
W 5
M v[64:67],v[152:155],a[208:211],v[64:67]
T v[176:177],v219,256
M v[64:67],v[156:159],a[212:215],v[64:67]
T v[178:179],v219,6400
W 5
T v[180:181],v219,12544
T v[182:183],v219,18688
W 5
M v[72:75],v[160:163],a[208:211],v[72:75]
s_waitcnt vmcnt(19)
s_barrier
T v[152:153],v220,0
M v[72:75],v[164:167],a[212:215],v[72:75]
T v[154:155],v220,6144
W 5
T v[156:157],v220,12288
T v[158:159],v220,18432
W 5
M v[80:83],v[168:171],a[208:211],v[80:83]
T v[160:161],v221,0
M v[80:83],v[172:175],a[212:215],v[80:83]
T v[162:163],v221,6144
W 5
T v[164:165],v221,12288
T v[166:167],v221,18432
W 5
M v[88:91],v[176:179],a[208:211],v[88:91]
T v[168:169],v222,0
M v[88:91],v[180:183],a[212:215],v[88:91]
T v[170:171],v222,6144
W 5
T v[172:173],v222,12288
T v[174:175],v222,18432
W 5
M v[0:3],v[152:155],a[224:227],v[0:3]
T v[176:177],v223,0
s_mul_i32 s77, s51, 6144
M v[0:3],v[156:159],a[228:231],v[0:3]
T v[178:179],v223,6144
s_add_u32 s78, s77, 196608
T v[180:181],v223,12288
s_mov_b32 m0, 52224
s_nop 0
buffer_load_dwordx4 v233, s[16:19], s78 offen lds
W 5
T v[182:183],v223,18432
M v[8:11],v[160:163],a[224:227],v[8:11]
T v[152:153],v220,128
buffer_load_dwordx4 v234, s[16:19], s78 offen offset:1024 lds
M v[8:11],v[164:167],a[228:231],v[8:11]
T v[154:155],v220,6272
W 5
T v[156:157],v220,12416
buffer_load_dwordx4 v235, s[16:19], s78 offen offset:2048 lds
s_add_u32 s78, s77, 245760
s_mov_b32 m0, 58368
T v[158:159],v220,18560
W 5
M v[16:19],v[168:171],a[224:227],v[16:19]
T v[160:161],v221,128
buffer_load_dwordx4 v233, s[16:19], s78 offen lds
M v[16:19],v[172:175],a[228:231],v[16:19]
T v[162:163],v221,6272
W 5
T v[164:165],v221,12416
buffer_load_dwordx4 v234, s[16:19], s78 offen offset:1024 lds
T v[166:167],v221,18560
W 5
M v[24:27],v[176:179],a[224:227],v[24:27]
T v[168:169],v222,128
buffer_load_dwordx4 v235, s[16:19], s78 offen offset:2048 lds
M v[24:27],v[180:183],a[228:231],v[24:27]
T v[170:171],v222,6272
s_add_u32 s78, s77, 589824
T v[172:173],v222,12416
s_mov_b32 m0, 64512
s_nop 0
buffer_load_dwordx4 v233, s[16:19], s78 offen lds
W 5
T v[174:175],v222,18560
M v[32:35],v[152:155],a[224:227],v[32:35]
T v[176:177],v223,128
buffer_load_dwordx4 v234, s[16:19], s78 offen offset:1024 lds
M v[32:35],v[156:159],a[228:231],v[32:35]
T v[178:179],v223,6272
v_add_u32 v216, 24576, v224
T v[180:181],v223,12416
buffer_load_dwordx4 v235, s[16:19], s78 offen offset:2048 lds
v_xor_b32 v217, 32, v216
v_xor_b32 v218, 64, v216
T v[182:183],v223,18560
v_xor_b32 v219, 96, v216
s_add_u32 s78, s77, 442368
s_mov_b32 m0, 67584
M v[40:43],v[160:163],a[224:227],v[40:43]
T v[152:153],v220,256
W 5
M v[40:43],v[164:167],a[228:231],v[40:43]
T v[154:155],v220,6400
buffer_load_dwordx4 v233, s[16:19], s78 offen lds
T v[156:157],v220,12544
s_waitcnt vmcnt(27)
ds_read_b128 a[240:243], v241
ds_read_b128 a[244:247], v242
T v[158:159],v220,18688
buffer_load_dwordx4 v234, s[16:19], s78 offen offset:1024 lds
W 7
M v[48:51],v[168:171],a[224:227],v[48:51]
T v[160:161],v221,256
M v[48:51],v[172:175],a[228:231],v[48:51]
T v[162:163],v221,6400
buffer_load_dwordx4 v235, s[16:19], s78 offen offset:2048 lds
s_add_u32 s78, s77, 638976
s_mov_b32 m0, 70656
T v[164:165],v221,12544
W 7
T v[166:167],v221,18688
buffer_load_dwordx4 v233, s[16:19], s78 offen lds
W 5
buffer_load_dwordx4 v234, s[16:19], s78 offen offset:1024 lds
buffer_load_dwordx4 v235, s[16:19], s78 offen offset:2048 lds
M v[56:59],v[176:179],a[224:227],v[56:59]
T v[168:169],v222,256
s_add_u32 m0, s43, 147456
M v[56:59],v[180:183],a[228:231],v[56:59]
T v[170:171],v222,6400
buffer_load_dwordx4 v236, s[24:27], s52 offen lds
T v[172:173],v222,12544
W 5
T v[174:175],v222,18688
buffer_load_dwordx4 v237, s[24:27], s52 offen offset:1024 lds
M v[64:67],v[152:155],a[224:227],v[64:67]
T v[176:177],v223,256
W 5
M v[64:67],v[156:159],a[228:231],v[64:67]
T v[178:179],v223,6400
T v[180:181],v223,12544
W 5
T v[182:183],v223,18688
M v[72:75],v[160:163],a[224:227],v[72:75]
s_waitcnt vmcnt(19)
s_barrier
T v[152:153],v216,0
M v[72:75],v[164:167],a[228:231],v[72:75]
T v[154:155],v216,6144
W 5
T v[156:157],v216,12288
T v[158:159],v216,18432
W 5
M v[80:83],v[168:171],a[224:227],v[80:83]
T v[160:161],v217,0
M v[80:83],v[172:175],a[228:231],v[80:83]
T v[162:163],v217,6144
W 5
T v[164:165],v217,12288
T v[166:167],v217,18432
W 5
M v[88:91],v[176:179],a[224:227],v[88:91]
T v[168:169],v218,0
M v[88:91],v[180:183],a[228:231],v[88:91]
T v[170:171],v218,6144
W 5
T v[172:173],v218,12288
T v[174:175],v218,18432
W 5
M v[0:3],v[152:155],a[240:243],v[0:3]
T v[176:177],v219,0
s_mul_i32 s77, s52, 6144
M v[0:3],v[156:159],a[244:247],v[0:3]
T v[178:179],v219,6144
s_add_u32 s78, s77, 196608
T v[180:181],v219,12288
s_mov_b32 m0, 76800
s_nop 0
buffer_load_dwordx4 v233, s[16:19], s78 offen lds
W 5
T v[182:183],v219,18432
M v[8:11],v[160:163],a[240:243],v[8:11]
T v[152:153],v216,128
buffer_load_dwordx4 v234, s[16:19], s78 offen offset:1024 lds
M v[8:11],v[164:167],a[244:247],v[8:11]
T v[154:155],v216,6272
W 5
T v[156:157],v216,12416
buffer_load_dwordx4 v235, s[16:19], s78 offen offset:2048 lds
s_add_u32 s78, s77, 245760
s_mov_b32 m0, 82944
T v[158:159],v216,18560
W 5
M v[16:19],v[168:171],a[240:243],v[16:19]
T v[160:161],v217,128
buffer_load_dwordx4 v233, s[16:19], s78 offen lds
M v[16:19],v[172:175],a[244:247],v[16:19]
T v[162:163],v217,6272
W 5
T v[164:165],v217,12416
buffer_load_dwordx4 v234, s[16:19], s78 offen offset:1024 lds
T v[166:167],v217,18560
W 5
M v[24:27],v[176:179],a[240:243],v[24:27]
T v[168:169],v218,128
buffer_load_dwordx4 v235, s[16:19], s78 offen offset:2048 lds
M v[24:27],v[180:183],a[244:247],v[24:27]
T v[170:171],v218,6272
s_add_u32 s78, s77, 589824
T v[172:173],v218,12416
s_mov_b32 m0, 89088
s_nop 0
buffer_load_dwordx4 v233, s[16:19], s78 offen lds
W 5
T v[174:175],v218,18560
M v[32:35],v[152:155],a[240:243],v[32:35]
T v[176:177],v219,128
buffer_load_dwordx4 v234, s[16:19], s78 offen offset:1024 lds
M v[32:35],v[156:159],a[244:247],v[32:35]
T v[178:179],v219,6272
v_add_u32 v220, 49152, v224
T v[180:181],v219,12416
buffer_load_dwordx4 v235, s[16:19], s78 offen offset:2048 lds
v_xor_b32 v221, 32, v220
v_xor_b32 v222, 64, v220
T v[182:183],v219,18560
v_xor_b32 v223, 96, v220
s_add_u32 s78, s77, 442368
s_mov_b32 m0, 92160
M v[40:43],v[160:163],a[240:243],v[40:43]
T v[152:153],v216,256
W 5
M v[40:43],v[164:167],a[244:247],v[40:43]
T v[154:155],v216,6400
buffer_load_dwordx4 v233, s[16:19], s78 offen lds
T v[156:157],v216,12544
s_waitcnt vmcnt(27)
W 5
T v[158:159],v216,18688
buffer_load_dwordx4 v234, s[16:19], s78 offen offset:1024 lds
v_add_u32 v250, -24576, v241
v_add_u32 v251, -24576, v242
ds_read_b128 v[184:187], v250
M v[48:51],v[168:171],a[240:243],v[48:51]
T v[160:161],v217,256
ds_read_b128 v[188:191], v251
M v[48:51],v[172:175],a[244:247],v[48:51]
T v[162:163],v217,6400
buffer_load_dwordx4 v235, s[16:19], s78 offen offset:2048 lds
s_add_u32 s78, s77, 638976
s_mov_b32 m0, 95232
T v[164:165],v217,12544
W 7
T v[166:167],v217,18688
buffer_load_dwordx4 v233, s[16:19], s78 offen lds
buffer_load_dwordx4 v234, s[16:19], s78 offen offset:1024 lds
buffer_load_dwordx4 v235, s[16:19], s78 offen offset:2048 lds
M v[56:59],v[176:179],a[240:243],v[56:59]
T v[168:169],v218,256
W 4
s_add_u32 m0, s43, 122880
M v[56:59],v[180:183],a[244:247],v[56:59]
T v[170:171],v218,6400
buffer_load_dwordx4 v236, s[24:27], s53 offen lds
T v[172:173],v218,12544
T v[174:175],v218,18688
buffer_load_dwordx4 v237, s[24:27], s53 offen offset:1024 lds
W 5
M v[64:67],v[152:155],a[240:243],v[64:67]
T v[176:177],v219,256
M v[64:67],v[156:159],a[244:247],v[64:67]
T v[178:179],v219,6400
W 5
T v[180:181],v219,12544
T v[182:183],v219,18688
W 5
M v[72:75],v[160:163],a[240:243],v[72:75]
s_waitcnt vmcnt(19)
s_barrier
T v[152:153],v220,0
M v[72:75],v[164:167],a[244:247],v[72:75]
T v[154:155],v220,6144
W 5
T v[156:157],v220,12288
T v[158:159],v220,18432
W 5
M v[80:83],v[168:171],a[240:243],v[80:83]
T v[160:161],v221,0
M v[80:83],v[172:175],a[244:247],v[80:83]
T v[162:163],v221,6144
W 5
T v[164:165],v221,12288
T v[166:167],v221,18432
W 5
M v[88:91],v[176:179],a[240:243],v[88:91]
T v[168:169],v222,0
M v[88:91],v[180:183],a[244:247],v[88:91]
T v[170:171],v222,6144
W 5
T v[172:173],v222,12288
T v[174:175],v222,18432
W 5
M v[0:3],v[152:155],v[184:187],v[0:3]
T v[176:177],v223,0
s_mul_i32 s77, s53, 6144
M v[0:3],v[156:159],v[188:191],v[0:3]
T v[178:179],v223,6144
s_add_u32 s78, s77, 196608
T v[180:181],v223,12288
s_mov_b32 m0, 101376
s_nop 0
buffer_load_dwordx4 v233, s[16:19], s78 offen lds
W 5
T v[182:183],v223,18432
M v[8:11],v[160:163],v[184:187],v[8:11]
T v[152:153],v220,128
buffer_load_dwordx4 v234, s[16:19], s78 offen offset:1024 lds
M v[8:11],v[164:167],v[188:191],v[8:11]
T v[154:155],v220,6272
W 5
T v[156:157],v220,12416
buffer_load_dwordx4 v235, s[16:19], s78 offen offset:2048 lds
s_add_u32 s78, s77, 245760
s_mov_b32 m0, 107520
T v[158:159],v220,18560
W 5
M v[16:19],v[168:171],v[184:187],v[16:19]
T v[160:161],v221,128
buffer_load_dwordx4 v233, s[16:19], s78 offen lds
M v[16:19],v[172:175],v[188:191],v[16:19]
T v[162:163],v221,6272
W 5
T v[164:165],v221,12416
buffer_load_dwordx4 v234, s[16:19], s78 offen offset:1024 lds
T v[166:167],v221,18560
W 5
M v[24:27],v[176:179],v[184:187],v[24:27]
T v[168:169],v222,128
buffer_load_dwordx4 v235, s[16:19], s78 offen offset:2048 lds
s_add_u32 s78, s77, 589824
s_mov_b32 m0, 113664
M v[24:27],v[180:183],v[188:191],v[24:27]
T v[170:171],v222,6272
W 5
T v[172:173],v222,12416
buffer_load_dwordx4 v233, s[16:19], s78 offen lds
T v[174:175],v222,18560
W 5
M v[32:35],v[152:155],v[184:187],v[32:35]
T v[176:177],v223,128
buffer_load_dwordx4 v234, s[16:19], s78 offen offset:1024 lds
M v[32:35],v[156:159],v[188:191],v[32:35]
T v[178:179],v223,6272
v_add_u32 v216, 73728, v224
T v[180:181],v223,12416
buffer_load_dwordx4 v235, s[16:19], s78 offen offset:2048 lds
s_add_u32 s78, s77, 442368
s_mov_b32 m0, 116736
T v[182:183],v223,18560
v_xor_b32 v217, 32, v216
v_xor_b32 v218, 64, v216
v_xor_b32 v219, 96, v216
M v[40:43],v[160:163],v[184:187],v[40:43]
T v[152:153],v220,256
buffer_load_dwordx4 v233, s[16:19], s78 offen lds
W 5
M v[40:43],v[164:167],v[188:191],v[40:43]
T v[154:155],v220,6400
s_waitcnt vmcnt(27)
T v[156:157],v220,12544
buffer_load_dwordx4 v234, s[16:19], s78 offen offset:1024 lds
ds_read_b128 a[144:147], v241
ds_read_b128 a[148:151], v242
T v[158:159],v220,18688
W 7
M v[48:51],v[168:171],v[184:187],v[48:51]
T v[160:161],v221,256
buffer_load_dwordx4 v235, s[16:19], s78 offen offset:2048 lds
s_add_u32 s78, s77, 638976
s_mov_b32 m0, 119808
M v[48:51],v[172:175],v[188:191],v[48:51]
T v[162:163],v221,6400
W 7
T v[164:165],v221,12544
buffer_load_dwordx4 v233, s[16:19], s78 offen lds
T v[166:167],v221,18688
W 5
buffer_load_dwordx4 v234, s[16:19], s78 offen offset:1024 lds
buffer_load_dwordx4 v235, s[16:19], s78 offen offset:2048 lds
s_add_u32 s79, s0, -15360
s_mov_b32 m0, 3072
s_nop 0
buffer_load_dwordx4 v227, s[8:11], s79 offen nt lds
buffer_load_dwordx4 v228, s[8:11], s79 offen offset:1024 nt lds
buffer_load_dwordx4 v229, s[8:11], s79 offen offset:2048 nt lds
s_add_u32 s79, s0, -9216
s_mov_b32 m0, 9216
s_nop 0
buffer_load_dwordx4 v227, s[8:11], s79 offen nt lds
buffer_load_dwordx4 v228, s[8:11], s79 offen offset:1024 nt lds
buffer_load_dwordx4 v229, s[8:11], s79 offen offset:2048 nt lds
s_add_u32 s79, s0, -3072
s_mov_b32 m0, 15360
s_nop 0
buffer_load_dwordx4 v227, s[8:11], s79 offen nt lds
buffer_load_dwordx4 v228, s[8:11], s79 offen offset:1024 nt lds
buffer_load_dwordx4 v229, s[8:11], s79 offen offset:2048 nt lds
s_add_u32 s79, s0, 0
s_mov_b32 m0, 18432
s_nop 0
buffer_load_dwordx4 v227, s[8:11], s79 offen nt lds
buffer_load_dwordx4 v228, s[8:11], s79 offen offset:1024 nt lds
buffer_load_dwordx4 v229, s[8:11], s79 offen offset:2048 nt lds
s_add_u32 s79, s0, 3072
s_mov_b32 m0, 21504
s_nop 0
buffer_load_dwordx4 v227, s[8:11], s79 offen nt lds
buffer_load_dwordx4 v228, s[8:11], s79 offen offset:1024 nt lds
buffer_load_dwordx4 v229, s[8:11], s79 offen offset:2048 nt lds
M v[56:59],v[176:179],v[184:187],v[56:59]
T v[168:169],v222,256
M v[56:59],v[180:183],v[188:191],v[56:59]
T v[170:171],v222,6400
W 5
T v[172:173],v222,12544
T v[174:175],v222,18688
W 5
M v[64:67],v[152:155],v[184:187],v[64:67]
T v[176:177],v223,256
M v[64:67],v[156:159],v[188:191],v[64:67]
T v[178:179],v223,6400
W 5
T v[180:181],v223,12544
T v[182:183],v223,18688
W 5
M v[72:75],v[160:163],v[184:187],v[72:75]
s_waitcnt vmcnt(32)
s_barrier
T v[152:153],v216,0
M v[72:75],v[164:167],v[188:191],v[72:75]
T v[154:155],v216,6144
s_mul_i32 s77, s52, 6144
T v[156:157],v216,12288
s_add_u32 s78, s77, 196992
s_mov_b32 m0, 27648
s_nop 0
buffer_load_dwordx4 v233, s[16:19], s78 offen lds
T v[158:159],v216,18432
W 5
M v[80:83],v[168:171],v[184:187],v[80:83]
T v[160:161],v217,0
buffer_load_dwordx4 v234, s[16:19], s78 offen offset:1024 lds
M v[80:83],v[172:175],v[188:191],v[80:83]
T v[162:163],v217,6144
W 5
T v[164:165],v217,12288
buffer_load_dwordx4 v235, s[16:19], s78 offen offset:2048 lds
s_add_u32 s78, s77, 246144
s_mov_b32 m0, 33792
T v[166:167],v217,18432
W 5
M v[88:91],v[176:179],v[184:187],v[88:91]
T v[168:169],v218,0
buffer_load_dwordx4 v233, s[16:19], s78 offen lds
M v[88:91],v[180:183],v[188:191],v[88:91]
T v[170:171],v218,6144
W 5
T v[172:173],v218,12288
buffer_load_dwordx4 v234, s[16:19], s78 offen offset:1024 lds
T v[174:175],v218,18432
W 5
M v[0:3],v[152:155],a[144:147],v[0:3]
T v[176:177],v219,0
buffer_load_dwordx4 v235, s[16:19], s78 offen offset:2048 lds
M v[0:3],v[156:159],a[148:151],v[0:3]
T v[178:179],v219,6144
s_add_u32 s78, s77, 590208
T v[180:181],v219,12288
s_mov_b32 m0, 39936
s_nop 0
buffer_load_dwordx4 v233, s[16:19], s78 offen lds
v_lshrrev_b32 v110, v108, v110
T v[182:183],v219,18432
v_and_b32 v110, 1, v110
v_cmp_eq_u32 s[74:75], 0, v110
s_nop 1
v_lshrrev_b64 v[110:111], v107, s[74:75]
M v[8:11],v[160:163],a[144:147],v[8:11]
T v[152:153],v216,128
W 5
M v[8:11],v[164:167],a[148:151],v[8:11]
T v[154:155],v216,6272
buffer_load_dwordx4 v234, s[16:19], s78 offen offset:1024 lds
T v[156:157],v216,12416
v_and_b32 v212, v106, v110
W 5
T v[158:159],v216,18560
buffer_load_dwordx4 v235, s[16:19], s78 offen offset:2048 lds
s_add_u32 s78, s77, 442752
s_mov_b32 m0, 43008
M v[16:19],v[168:171],a[144:147],v[16:19]
T v[160:161],v217,128
buffer_store_dword v212, v105, s[28:31], 0 offen sc1
M v[16:19],v[172:175],a[148:151],v[16:19]
T v[162:163],v217,6272
buffer_load_dwordx4 v233, s[16:19], s78 offen lds
T v[164:165],v217,12416
W 5
T v[166:167],v217,18560
buffer_load_dwordx4 v234, s[16:19], s78 offen offset:1024 lds
M v[24:27],v[176:179],a[144:147],v[24:27]
T v[168:169],v218,128
W 5
M v[24:27],v[180:183],a[148:151],v[24:27]
T v[170:171],v218,6272
buffer_load_dwordx4 v235, s[16:19], s78 offen offset:2048 lds
s_add_u32 s78, s77, 639360
s_mov_b32 m0, 46080
T v[172:173],v218,12416
W 5
T v[174:175],v218,18560
buffer_load_dwordx4 v233, s[16:19], s78 offen lds
buffer_load_dwordx4 v234, s[16:19], s78 offen offset:1024 lds
buffer_load_dwordx4 v235, s[16:19], s78 offen offset:2048 lds
M v[32:35],v[152:155],a[144:147],v[32:35]
T v[176:177],v219,128
W 5
M v[32:35],v[156:159],a[148:151],v[32:35]
T v[178:179],v219,6272
v_add_u32 v220, 98304, v224
T v[180:181],v219,12416
v_xor_b32 v221, 32, v220
v_xor_b32 v222, 64, v220
v_xor_b32 v223, 96, v220
T v[182:183],v219,18560
W 5
M v[40:43],v[160:163],a[144:147],v[40:43]
T v[152:153],v216,256
M v[40:43],v[164:167],a[148:151],v[40:43]
T v[154:155],v216,6400
s_waitcnt vmcnt(46)
T v[156:157],v216,12544
v_add_u32 v250, -24576, v241
v_add_u32 v251, -24576, v242
ds_read_b128 a[160:163], v250
T v[158:159],v216,18688
ds_read_b128 a[164:167], v251
W 7
M v[48:51],v[168:171],a[144:147],v[48:51]
T v[160:161],v217,256
M v[48:51],v[172:175],a[148:151],v[48:51]
T v[162:163],v217,6400
W 7
T v[164:165],v217,12544
T v[166:167],v217,18688
W 6
M v[56:59],v[176:179],a[144:147],v[56:59]
T v[168:169],v218,256
W 5
M v[56:59],v[180:183],a[148:151],v[56:59]
T v[170:171],v218,6400
T v[172:173],v218,12544
W 5
T v[174:175],v218,18688
M v[64:67],v[152:155],a[144:147],v[64:67]
T v[176:177],v219,256
W 5
M v[64:67],v[156:159],a[148:151],v[64:67]
T v[178:179],v219,6400
T v[180:181],v219,12544
W 5
T v[182:183],v219,18688
M v[72:75],v[160:163],a[144:147],v[72:75]
s_waitcnt vmcnt(31)
s_barrier
T v[152:153],v220,0
M v[72:75],v[164:167],a[148:151],v[72:75]
T v[154:155],v220,6144
s_add_u32 s79, s0, -15360
T v[156:157],v220,12288
s_mov_b32 m0, 52224
s_nop 0
buffer_load_dwordx4 v227, s[12:15], s79 offen nt lds
W 5
T v[158:159],v220,18432
M v[80:83],v[168:171],a[144:147],v[80:83]
T v[160:161],v221,0
buffer_load_dwordx4 v228, s[12:15], s79 offen offset:1024 nt lds
M v[80:83],v[172:175],a[148:151],v[80:83]
T v[162:163],v221,6144
W 5
T v[164:165],v221,12288
buffer_load_dwordx4 v229, s[12:15], s79 offen offset:2048 nt lds
s_add_u32 s79, s0, -9216
s_mov_b32 m0, 58368
T v[166:167],v221,18432
W 5
M v[88:91],v[176:179],a[144:147],v[88:91]
T v[168:169],v222,0
buffer_load_dwordx4 v227, s[12:15], s79 offen nt lds
M v[88:91],v[180:183],a[148:151],v[88:91]
T v[170:171],v222,6144
W 5
T v[172:173],v222,12288
buffer_load_dwordx4 v228, s[12:15], s79 offen offset:1024 nt lds
T v[174:175],v222,18432
W 5
M v[0:3],v[152:155],a[160:163],v[0:3]
T v[176:177],v223,0
buffer_load_dwordx4 v229, s[12:15], s79 offen offset:2048 nt lds
M v[0:3],v[156:159],a[164:167],v[0:3]
T v[178:179],v223,6144
s_add_u32 s79, s0, -3072
T v[180:181],v223,12288
s_mov_b32 m0, 64512
s_nop 0
buffer_load_dwordx4 v227, s[12:15], s79 offen nt lds
W 5
T v[182:183],v223,18432
M v[8:11],v[160:163],a[160:163],v[8:11]
T v[152:153],v220,128
buffer_load_dwordx4 v228, s[12:15], s79 offen offset:1024 nt lds
M v[8:11],v[164:167],a[164:167],v[8:11]
T v[154:155],v220,6272
W 5
T v[156:157],v220,12416
buffer_load_dwordx4 v229, s[12:15], s79 offen offset:2048 nt lds
s_add_u32 s79, s0, 0
s_mov_b32 m0, 67584
T v[158:159],v220,18560
W 5
M v[16:19],v[168:171],a[160:163],v[16:19]
T v[160:161],v221,128
buffer_load_dwordx4 v227, s[12:15], s79 offen nt lds
M v[16:19],v[172:175],a[164:167],v[16:19]
T v[162:163],v221,6272
W 5
T v[164:165],v221,12416
buffer_load_dwordx4 v228, s[12:15], s79 offen offset:1024 nt lds
T v[166:167],v221,18560
v_pk_mul_f32 v[0:1], v[0:1], s[64:65]
v_pk_mul_f32 v[8:9], v[8:9], s[64:65]
v_pk_mul_f32 v[2:3], v[2:3], s[64:65]
M v[24:27],v[176:179],a[160:163],v[24:27]
T v[168:169],v222,128
buffer_load_dwordx4 v229, s[12:15], s79 offen offset:2048 nt lds
M v[24:27],v[180:183],a[164:167],v[24:27]
T v[170:171],v222,6272
s_add_u32 s79, s0, 3072
s_mov_b32 m0, 70656
T v[172:173],v222,12416
buffer_load_dwordx4 v227, s[12:15], s79 offen nt lds
v_pk_mul_f32 v[10:11], v[10:11], s[64:65]
v_cvt_pk_bf16_f32 v204, v0, v1
T v[174:175],v222,18560
v_cvt_pk_bf16_f32 v206, v8, v9
v_cvt_pk_bf16_f32 v205, v2, v3
buffer_load_dwordx4 v228, s[12:15], s79 offen offset:1024 nt lds
buffer_load_dwordx4 v229, s[12:15], s79 offen offset:2048 nt lds
W 5
M v[32:35],v[152:155],a[160:163],v[32:35]
T v[176:177],v223,128
v_cvt_pk_bf16_f32 v207, v10, v11
M v[32:35],v[156:159],a[164:167],v[32:35]
T v[178:179],v223,6272
v_permlane32_swap_b32 v204, v206
T v[180:181],v223,12416
v_permlane32_swap_b32 v205, v207
v_permlane16_swap_b32 v204, v206
s_nop 0
v_permlane16_swap_b32 v205, v207
T v[182:183],v223,18560
v_accvgpr_write_b32 a0, v204
v_accvgpr_write_b32 a1, v205
v_accvgpr_write_b32 a2, v206
M v[40:43],v[160:163],a[160:163],v[40:43]
T v[152:153],v220,256
W 5
M v[40:43],v[164:167],a[164:167],v[40:43]
T v[154:155],v220,6400
v_accvgpr_write_b32 a3, v207
T v[156:157],v220,12544
v_pk_mul_f32 v[16:17], v[16:17], s[64:65]
v_pk_mul_f32 v[24:25], v[24:25], s[64:65]
v_pk_mul_f32 v[18:19], v[18:19], s[64:65]
T v[158:159],v220,18688
v_pk_mul_f32 v[26:27], v[26:27], s[64:65]
v_cvt_pk_bf16_f32 v204, v16, v17
v_cvt_pk_bf16_f32 v206, v24, v25
M v[48:51],v[168:171],a[160:163],v[48:51]
T v[160:161],v221,256
v_cvt_pk_bf16_f32 v205, v18, v19
M v[48:51],v[172:175],a[164:167],v[48:51]
T v[162:163],v221,6400
v_cvt_pk_bf16_f32 v207, v26, v27
T v[164:165],v221,12544
v_permlane32_swap_b32 v204, v206
v_permlane32_swap_b32 v205, v207
s_nop 0
v_permlane16_swap_b32 v204, v206
T v[166:167],v221,18688
v_permlane16_swap_b32 v205, v207
v_accvgpr_write_b32 a8, v204
W 5
M v[56:59],v[176:179],a[160:163],v[56:59]
T v[168:169],v222,256
v_accvgpr_write_b32 a9, v205
M v[56:59],v[180:183],a[164:167],v[56:59]
T v[170:171],v222,6400
v_accvgpr_write_b32 a10, v206
T v[172:173],v222,12544
v_accvgpr_write_b32 a11, v207
v_pk_mul_f32 v[32:33], v[32:33], s[64:65]
v_pk_mul_f32 v[40:41], v[40:41], s[64:65]
T v[174:175],v222,18688
v_pk_mul_f32 v[34:35], v[34:35], s[64:65]
v_pk_mul_f32 v[42:43], v[42:43], s[64:65]
v_cvt_pk_bf16_f32 v204, v32, v33
M v[64:67],v[152:155],a[160:163],v[64:67]
T v[176:177],v223,256
W 5
M v[64:67],v[156:159],a[164:167],v[64:67]
T v[178:179],v223,6400
v_cvt_pk_bf16_f32 v206, v40, v41
T v[180:181],v223,12544
v_cvt_pk_bf16_f32 v205, v34, v35
v_cvt_pk_bf16_f32 v207, v42, v43
v_permlane32_swap_b32 v204, v206
T v[182:183],v223,18688
v_add_u32 v216, 0, v225
v_xor_b32 v217, 64, v216
v_permlane32_swap_b32 v205, v207
M v[72:75],v[160:163],a[160:163],v[72:75]
s_waitcnt vmcnt(31)
s_barrier
ds_read_b128 v[152:155], v216 offset:0
M v[72:75],v[164:167],a[164:167],v[72:75]
ds_read_b128 v[156:159], v216 offset:6144
s_add_u32 s79, s1, -15360
s_mov_b32 m0, 76800
s_nop 0
buffer_load_dwordx4 v227, s[8:11], s79 offen nt lds
v_permlane16_swap_b32 v204, v206
v_permlane16_swap_b32 v205, v207
v_accvgpr_write_b32 a16, v204
v_accvgpr_write_b32 a17, v205
ds_read_b128 v[160:163], v216 offset:12288
v_accvgpr_write_b32 a18, v206
v_accvgpr_write_b32 a19, v207
W 4
M v[80:83],v[168:171],a[160:163],v[80:83]
v_pk_mul_f32 v[48:49], v[48:49], s[64:65]
v_pk_mul_f32 v[56:57], v[56:57], s[64:65]
M v[80:83],v[172:175],a[164:167],v[80:83]
ds_read_b128 v[164:167], v216 offset:18432
v_pk_mul_f32 v[50:51], v[50:51], s[64:65]
buffer_load_dwordx4 v228, s[8:11], s79 offen offset:1024 nt lds
v_pk_mul_f32 v[58:59], v[58:59], s[64:65]
v_cvt_pk_bf16_f32 v204, v48, v49
v_cvt_pk_bf16_f32 v206, v56, v57
ds_read_b128 v[168:171], v217 offset:0
v_cvt_pk_bf16_f32 v205, v50, v51
v_cvt_pk_bf16_f32 v207, v58, v59
v_permlane32_swap_b32 v204, v206
M v[88:91],v[176:179],a[160:163],v[88:91]
v_permlane32_swap_b32 v205, v207
W 2
M v[88:91],v[180:183],a[164:167],v[88:91]
ds_read_b128 v[172:175], v217 offset:6144
v_permlane16_swap_b32 v204, v206
v_permlane16_swap_b32 v205, v207
v_accvgpr_write_b32 a24, v204
v_accvgpr_write_b32 a25, v205
v_accvgpr_write_b32 a26, v206
ds_read_b128 v[176:179], v217 offset:12288
buffer_load_dwordx4 v229, s[8:11], s79 offen offset:2048 nt lds
v_accvgpr_write_b32 a27, v207
v_pk_mul_f32 v[64:65], v[64:65], s[64:65]
M v[104:107],v[152:155],a[0:3],0
v_pk_mul_f32 v[72:73], v[72:73], s[64:65]
v_pk_mul_f32 v[66:67], v[66:67], s[64:65]
ds_read_b128 v[180:183], v217 offset:18432
v_pk_mul_f32 v[74:75], v[74:75], s[64:65]
v_cvt_pk_bf16_f32 v204, v64, v65
v_cvt_pk_bf16_f32 v206, v72, v73
M v[112:115],v[156:159],a[0:3],0
s_add_u32 s79, s1, -9216
s_mov_b32 m0, 82944
ds_read_b128 v[152:155], v216 offset:128
v_cvt_pk_bf16_f32 v205, v66, v67
v_cvt_pk_bf16_f32 v207, v74, v75
v_permlane32_swap_b32 v204, v206
M v[120:123],v[160:163],a[0:3],0
buffer_load_dwordx4 v227, s[8:11], s79 offen nt lds
s_mul_i32 s77, s53, 6144
ds_read_b128 v[156:159], v216 offset:6272
s_add_u32 s78, s77, 196992
s_mov_b32 m0, 125952
W 3
M v[128:131],v[164:167],a[0:3],0
buffer_load_dwordx4 v233, s[16:19], s78 offen lds
v_permlane32_swap_b32 v205, v207
ds_read_b128 v[160:163], v216 offset:12416
s_mov_b32 m0, 82944
s_mov_b32 m0, 125952
s_mov_b32 m0, 82944
M v[104:107],v[168:171],a[8:11],v[104:107]
v_permlane16_swap_b32 v204, v206
v_permlane16_swap_b32 v205, v207
ds_read_b128 v[164:167], v216 offset:18560
buffer_load_dwordx4 v228, s[8:11], s79 offen offset:1024 nt lds
s_mov_b32 m0, 125952
v_accvgpr_write_b32 a32, v204
M v[112:115],v[172:175],a[8:11],v[112:115]
buffer_load_dwordx4 v234, s[16:19], s78 offen offset:1024 lds
v_accvgpr_write_b32 a33, v205
ds_read_b128 v[168:171], v217 offset:128
v_accvgpr_write_b32 a34, v206
v_accvgpr_write_b32 a35, v207
v_pk_mul_f32 v[80:81], v[80:81], s[64:65]
M v[120:123],v[176:179],a[8:11],v[120:123]
s_mov_b32 m0, 82944
s_mov_b32 m0, 125952
ds_read_b128 v[172:175], v217 offset:6272
s_mov_b32 m0, 82944
s_mov_b32 m0, 125952
W 3
M v[128:131],v[180:183],a[8:11],v[128:131]
s_mov_b32 m0, 82944
s_nop 0
buffer_load_dwordx4 v229, s[8:11], s79 offen offset:2048 nt lds
ds_read_b128 v[176:179], v217 offset:12416
s_mov_b32 m0, 125952
s_nop 0
buffer_load_dwordx4 v235, s[16:19], s78 offen offset:2048 lds
s_add_u32 s78, s77, 246144
M v[104:107],v[152:155],a[16:19],v[104:107]
s_mov_b32 m0, 132096
v_pk_mul_f32 v[88:89], v[88:89], s[64:65]
ds_read_b128 v[180:183], v217 offset:18560
s_add_u32 s79, s1, -3072
s_mov_b32 m0, 89088
s_mov_b32 m0, 132096
M v[112:115],v[156:159],a[16:19],v[112:115]
s_mov_b32 m0, 89088
s_mov_b32 m0, 132096
ds_read_b128 v[152:155], v216 offset:256
s_mov_b32 m0, 89088
s_nop 0
buffer_load_dwordx4 v227, s[8:11], s79 offen nt lds
v_pk_mul_f32 v[82:83], v[82:83], s[64:65]
M v[120:123],v[160:163],a[16:19],v[120:123]
s_mov_b32 m0, 132096
s_nop 0
buffer_load_dwordx4 v233, s[16:19], s78 offen lds
ds_read_b128 v[156:159], v216 offset:6400
v_pk_mul_f32 v[90:91], v[90:91], s[64:65]
v_cvt_pk_bf16_f32 v204, v80, v81
W 3
M v[128:131],v[164:167],a[16:19],v[128:131]
s_mov_b32 m0, 89088
s_mov_b32 m0, 132096
ds_read_b128 v[160:163], v216 offset:12544
s_mov_b32 m0, 89088
s_mov_b32 m0, 132096
s_mov_b32 m0, 89088
M v[104:107],v[168:171],a[24:27],v[104:107]
buffer_load_dwordx4 v228, s[8:11], s79 offen offset:1024 nt lds
v_cvt_pk_bf16_f32 v206, v88, v89
ds_read_b128 v[164:167], v216 offset:18688
v_cvt_pk_bf16_f32 v205, v82, v83
v_cvt_pk_bf16_f32 v207, v90, v91
v_permlane32_swap_b32 v204, v206
M v[112:115],v[172:175],a[24:27],v[112:115]
s_mov_b32 m0, 132096
s_nop 0
buffer_load_dwordx4 v234, s[16:19], s78 offen offset:1024 lds
ds_read_b128 v[168:171], v217 offset:256
s_mov_b32 m0, 89088
s_mov_b32 m0, 132096
s_mov_b32 m0, 89088
M v[120:123],v[176:179],a[24:27],v[120:123]
s_mov_b32 m0, 132096
s_mov_b32 m0, 89088
ds_read_b128 v[172:175], v217 offset:6400
buffer_load_dwordx4 v229, s[8:11], s79 offen offset:2048 nt lds
v_permlane32_swap_b32 v205, v207
W 3
M v[128:131],v[180:183],a[24:27],v[128:131]
s_mov_b32 m0, 132096
s_nop 0
buffer_load_dwordx4 v235, s[16:19], s78 offen offset:2048 lds
ds_read_b128 v[176:179], v217 offset:12544
v_permlane16_swap_b32 v204, v206
v_permlane16_swap_b32 v205, v207
v_accvgpr_write_b32 a40, v204
M v[104:107],v[152:155],a[32:35],v[104:107]
s_add_u32 s79, s1, 0
s_mov_b32 m0, 92160
ds_read_b128 v[180:183], v217 offset:18688
v_add_u32 v220, 24576, v224
v_xor_b32 v221, 32, v220
v_xor_b32 v222, 64, v220
M v[112:115],v[156:159],a[32:35],v[112:115]
buffer_load_dwordx4 v227, s[8:11], s79 offen nt lds
v_xor_b32 v223, 96, v220
s_waitcnt vmcnt(31)
s_barrier
T v[152:153],v220,0
v_accvgpr_write_b32 a41, v205
M v[120:123],v[160:163],a[32:35],v[120:123]
T v[154:155],v220,6144
s_add_u32 s78, s77, 590208
T v[156:157],v220,12288
v_accvgpr_write_b32 a42, v206
v_accvgpr_write_b32 a43, v207
W 4
M v[128:131],v[164:167],a[32:35],v[128:131]
T v[158:159],v220,18432
s_mov_b32 m0, 138240
T v[160:161],v221,0
s_mov_b32 m0, 92160
s_nop 0
buffer_load_dwordx4 v228, s[8:11], s79 offen offset:1024 nt lds
s_mov_b32 m0, 138240
M v[104:107],v[168:171],a[40:43],v[104:107]
T v[162:163],v221,6144
buffer_load_dwordx4 v233, s[16:19], s78 offen lds
T v[164:165],v221,12288
W 5
M v[112:115],v[172:175],a[40:43],v[112:115]
T v[166:167],v221,18432
s_mov_b32 m0, 92160
s_mov_b32 m0, 138240
s_nop 0
buffer_load_dwordx4 v234, s[16:19], s78 offen offset:1024 lds
T v[168:169],v222,0
s_mov_b32 m0, 92160
s_mov_b32 m0, 138240
s_mov_b32 m0, 92160
s_mov_b32 m0, 138240
M v[120:123],v[176:179],a[40:43],v[120:123]
T v[170:171],v222,6144
s_mov_b32 m0, 92160
s_mov_b32 m0, 138240
s_nop 0
buffer_load_dwordx4 v235, s[16:19], s78 offen offset:2048 lds
s_add_u32 s78, s77, 442752
s_mov_b32 m0, 141312
T v[172:173],v222,12288
s_mov_b32 m0, 92160
s_nop 0
buffer_load_dwordx4 v229, s[8:11], s79 offen offset:2048 nt lds
s_mov_b32 m0, 141312
M v[128:131],v[180:183],a[40:43],v[128:131]
T v[174:175],v222,18432
buffer_load_dwordx4 v233, s[16:19], s78 offen lds
T v[176:177],v223,0
v_max3_f32 v248, v104, v105, v106
v_max_f32 v248, v248, v107
v_max3_f32 v248, v248, v112, v113
buffer_load_dwordx4 v234, s[16:19], s78 offen offset:1024 lds
buffer_load_dwordx4 v235, s[16:19], s78 offen offset:2048 lds
s_add_u32 s78, s77, 639360
s_mov_b32 m0, 144384
s_nop 0
buffer_load_dwordx4 v233, s[16:19], s78 offen lds
buffer_load_dwordx4 v234, s[16:19], s78 offen offset:1024 lds
buffer_load_dwordx4 v235, s[16:19], s78 offen offset:2048 lds
M a[48:51],v[152:155],a[144:147],0
T v[178:179],v223,6144
s_add_u32 s79, s1, 3072
s_mov_b32 m0, 95232
W 5
M a[48:51],v[156:159],a[148:151],a[48:51]
T v[180:181],v223,12288
buffer_load_dwordx4 v227, s[8:11], s79 offen nt lds
T v[182:183],v223,18432
v_max3_f32 v248, v248, v114, v115
v_max3_f32 v248, v248, v120, v121
v_max3_f32 v248, v248, v122, v123
T v[152:153],v220,128
buffer_load_dwordx4 v228, s[8:11], s79 offen offset:1024 nt lds
v_max3_f32 v248, v248, v128, v129
v_max3_f32 v248, v248, v130, v131
M a[56:59],v[160:163],a[144:147],0
T v[154:155],v220,6272
v_mov_b32 v250, v248
M a[56:59],v[164:167],a[148:151],a[56:59]
T v[156:157],v220,12416
buffer_load_dwordx4 v229, s[8:11], s79 offen offset:2048 nt lds
T v[158:159],v220,18560
s_add_u32 s79, s1, -15360
s_mov_b32 m0, 101376
v_permlane32_swap_b32 v248, v250
T v[160:161],v221,128
buffer_load_dwordx4 v227, s[12:15], s79 offen nt lds
v_max_f32 v248, v248, v250
W 5
M a[64:67],v[168:171],a[144:147],0
T v[162:163],v221,6272
v_mov_b32 v250, v248
M a[64:67],v[172:175],a[148:151],a[64:67]
T v[164:165],v221,12416
v_permlane16_swap_b32 v248, v250
T v[166:167],v221,18560
v_max_f32 v248, v248, v250
v_sub_f32 v104, v104, v248
v_sub_f32 v105, v105, v248
T v[168:169],v222,128
v_sub_f32 v106, v106, v248
v_sub_f32 v107, v107, v248
v_sub_f32 v112, v112, v248
M a[72:75],v[176:179],a[144:147],0
T v[170:171],v222,6272
buffer_load_dwordx4 v228, s[12:15], s79 offen offset:1024 nt lds
M a[72:75],v[180:183],a[148:151],a[72:75]
T v[172:173],v222,12416
v_sub_f32 v113, v113, v248
T v[174:175],v222,18560
v_sub_f32 v114, v114, v248
v_sub_f32 v115, v115, v248
W 5
T v[176:177],v223,128
v_exp_f32 v200, v104
M a[80:83],v[152:155],a[144:147],0
T v[178:179],v223,6272
buffer_load_dwordx4 v229, s[12:15], s79 offen offset:2048 nt lds
M a[80:83],v[156:159],a[148:151],a[80:83]
T v[180:181],v223,12416
v_add_u32 v216, 49152, v224
T v[182:183],v223,18560
v_exp_f32 v201, v105
s_nop 0
v_cvt_pk_bf16_f32 v136, v200, v201
T v[152:153],v220,256
s_add_u32 s79, s1, -9216
s_mov_b32 m0, 107520
v_xor_b32 v217, 32, v216
M a[88:91],v[160:163],a[144:147],0
T v[154:155],v220,6400
W 5
M a[88:91],v[164:167],a[148:151],a[88:91]
T v[156:157],v220,12544
buffer_load_dwordx4 v227, s[12:15], s79 offen nt lds
T v[158:159],v220,18688
v_exp_f32 v202, v106
v_xor_b32 v218, 64, v216
T v[160:161],v221,256
v_exp_f32 v203, v107
s_nop 0
v_cvt_pk_bf16_f32 v137, v202, v203
M a[96:99],v[168:171],a[144:147],0
T v[162:163],v221,6400
v_xor_b32 v219, 96, v216
M a[96:99],v[172:175],a[148:151],a[96:99]
T v[164:165],v221,12544
W 5
T v[166:167],v221,18688
buffer_load_dwordx4 v228, s[12:15], s79 offen offset:1024 nt lds
v_exp_f32 v200, v112
T v[168:169],v222,256
v_exp_f32 v201, v113
s_nop 0
v_cvt_pk_bf16_f32 v138, v200, v201
M a[104:107],v[176:179],a[144:147],0
T v[170:171],v222,6400
W 5
M a[104:107],v[180:183],a[148:151],a[104:107]
T v[172:173],v222,12544
T v[174:175],v222,18688
v_exp_f32 v202, v114
v_sub_f32 v120, v120, v248
T v[176:177],v223,256
buffer_load_dwordx4 v229, s[12:15], s79 offen offset:2048 nt lds
v_exp_f32 v203, v115
M a[112:115],v[152:155],a[144:147],0
T v[178:179],v223,6400
v_cvt_pk_bf16_f32 v139, v202, v203
M a[112:115],v[156:159],a[148:151],a[112:115]
T v[180:181],v223,12544
v_sub_f32 v121, v121, v248
T v[182:183],v223,18688
s_add_u32 s79, s1, -3072
s_mov_b32 m0, 113664
v_sub_f32 v122, v122, v248
s_waitcnt vmcnt(36)
s_barrier
T v[152:153],v216,0
W 5
M a[120:123],v[160:163],a[144:147],0
T v[154:155],v216,6144
buffer_load_dwordx4 v227, s[12:15], s79 offen nt lds
M a[120:123],v[164:167],a[148:151],a[120:123]
v_sub_f32 v123, v123, v248
v_sub_f32 v128, v128, v248
v_sub_f32 v129, v129, v248
v_sub_f32 v130, v130, v248
v_sub_f32 v131, v131, v248
W 3
T v[156:157],v217,0
v_exp_f32 v200, v120
M a[128:131],v[168:171],a[144:147],0
T v[158:159],v217,6144
W 3
M a[128:131],v[172:175],a[148:151],a[128:131]
T v[160:161],v218,0
buffer_load_dwordx4 v228, s[12:15], s79 offen offset:1024 nt lds
T v[162:163],v218,6144
v_exp_f32 v201, v121
s_nop 0
v_cvt_pk_bf16_f32 v144, v200, v201
T v[164:165],v219,0
v_exp_f32 v202, v122
M a[136:139],v[176:179],a[144:147],0
T v[166:167],v219,6144
W 5
M a[136:139],v[180:183],a[148:151],a[136:139]
T v[168:169],v216,128
T v[170:171],v216,6272
buffer_load_dwordx4 v229, s[12:15], s79 offen offset:2048 nt lds
v_exp_f32 v203, v123
T v[172:173],v217,128
v_cvt_pk_bf16_f32 v145, v202, v203
v_exp_f32 v200, v128
M v[0:3],v[152:155],v[136:139],0
T v[174:175],v217,6272
s_add_u32 s79, s1, 0
T v[176:177],v218,128
s_mov_b32 m0, 116736
s_nop 0
buffer_load_dwordx4 v227, s[12:15], s79 offen nt lds
W 5
M v[96:99],v[244:247],v[136:139],0
T v[178:179],v218,6272
T v[180:181],v219,128
buffer_load_dwordx4 v228, s[12:15], s79 offen offset:1024 nt lds
v_exp_f32 v201, v129
M v[8:11],v[156:159],v[136:139],0
T v[182:183],v219,6272
v_cvt_pk_bf16_f32 v146, v200, v201
T v[152:153],v216,256
buffer_load_dwordx4 v229, s[12:15], s79 offen offset:2048 nt lds
v_exp_f32 v202, v130
M v[16:19],v[160:163],v[136:139],0
T v[154:155],v216,6400
s_add_u32 s79, s1, 3072
T v[156:157],v217,256
s_mov_b32 m0, 119808
s_nop 0
buffer_load_dwordx4 v227, s[12:15], s79 offen nt lds
v_sub_f32 v204, 0, v248
M v[24:27],v[164:167],v[136:139],0
T v[158:159],v217,6400
v_sub_f32 v205, 0, v248
T v[160:161],v218,256
buffer_load_dwordx4 v228, s[12:15], s79 offen offset:1024 nt lds
v_sub_f32 v206, 0, v248
W 5
M v[32:35],v[168:171],v[136:139],0
T v[162:163],v218,6400
v_sub_f32 v207, 0, v248
T v[164:165],v219,256
buffer_load_dwordx4 v229, s[12:15], s79 offen offset:2048 nt lds
v_exp_f32 v203, v131
M v[40:43],v[172:175],v[136:139],0
T v[166:167],v219,6400
s_add_u32 s79, s2, -15360
T v[168:169],v216,12288
s_mov_b32 m0, 3072
s_nop 0
buffer_load_dwordx4 v227, s[8:11], s79 offen nt lds
v_cvt_pk_bf16_f32 v147, v202, v203
M v[48:51],v[176:179],v[136:139],0
T v[170:171],v216,18432
W 5
buffer_load_dwordx4 v228, s[8:11], s79 offen offset:1024 nt lds
M v[56:59],v[180:183],v[136:139],0
W 3
T v[172:173],v217,12288
buffer_load_dwordx4 v229, s[8:11], s79 offen offset:2048 nt lds
M v[64:67],v[152:155],v[136:139],0
T v[174:175],v217,18432
W 3
T v[176:177],v218,12288
M v[72:75],v[156:159],v[136:139],0
T v[178:179],v218,18432
s_add_u32 s79, s2, -9216
T v[180:181],v219,12288
s_mov_b32 m0, 9216
v_add_u32 v220, 73728, v225
v_xor_b32 v221, 64, v220
M v[80:83],v[160:163],v[136:139],0
T v[182:183],v219,18432
buffer_load_dwordx4 v227, s[8:11], s79 offen nt lds
T v[152:153],v216,12416
W 5
M v[88:91],v[164:167],v[136:139],0
T v[154:155],v216,18560
T v[156:157],v217,12416
W 5
M v[0:3],v[168:171],v[144:147],v[0:3]
T v[158:159],v217,18560
buffer_load_dwordx4 v228, s[8:11], s79 offen offset:1024 nt lds
T v[160:161],v218,12416
W 5
M v[96:99],v[244:247],v[144:147],v[96:99]
T v[162:163],v218,18560
T v[164:165],v219,12416
W 5
M v[8:11],v[172:175],v[144:147],v[8:11]
T v[166:167],v219,18560
T v[168:169],v216,12544
buffer_load_dwordx4 v229, s[8:11], s79 offen offset:2048 nt lds
W 5
M v[16:19],v[176:179],v[144:147],v[16:19]
T v[170:171],v216,18688
T v[172:173],v217,12544
W 5
M v[24:27],v[180:183],v[144:147],v[24:27]
T v[174:175],v217,18688
s_add_u32 s79, s2, -3072
T v[176:177],v218,12544
s_mov_b32 m0, 15360
W 5
M v[32:35],v[152:155],v[144:147],v[32:35]
T v[178:179],v218,18688
buffer_load_dwordx4 v227, s[8:11], s79 offen nt lds
T v[180:181],v219,12544
W 5
M v[40:43],v[156:159],v[144:147],v[40:43]
T v[182:183],v219,18688
s_waitcnt vmcnt(22)
s_barrier
ds_read_b128 v[152:155], v220 offset:0
W 5
M v[48:51],v[160:163],v[144:147],v[48:51]
ds_read_b128 v[156:159], v221 offset:0
ds_read_b128 v[160:163], v220 offset:128
buffer_load_dwordx4 v228, s[8:11], s79 offen offset:1024 nt lds
W 5
M v[56:59],v[164:167],v[144:147],v[56:59]
W 3
M v[64:67],v[168:171],v[144:147],v[64:67]
ds_read_b128 v[164:167], v221 offset:128
W 2
M v[72:75],v[172:175],v[144:147],v[72:75]
ds_read_b128 v[168:171], v220 offset:256
buffer_load_dwordx4 v229, s[8:11], s79 offen offset:2048 nt lds
ds_read_b128 v[172:175], v221 offset:256
M v[80:83],v[176:179],v[144:147],v[80:83]
s_add_u32 s79, s2, 0
s_mov_b32 m0, 18432
M v[88:91],v[180:183],v[144:147],v[88:91]
W 2
ds_read_b128 v[176:179], v220 offset:6144
buffer_load_dwordx4 v227, s[8:11], s79 offen nt lds
M v[104:107],v[152:155],a[0:3],v[204:207]
ds_read_b128 v[180:183], v221 offset:6144
s_mul_i32 s77, s46, 6144
M v[104:107],v[156:159],a[8:11],v[104:107]
s_add_u32 s78, s77, 196992
s_mov_b32 m0, 27648
M v[104:107],v[160:163],a[16:19],v[104:107]
s_mov_b32 m0, 18432
s_mov_b32 m0, 27648
s_mov_b32 m0, 18432
s_mov_b32 m0, 27648
s_mov_b32 m0, 18432
s_mov_b32 m0, 27648
s_mov_b32 m0, 18432
s_nop 0
buffer_load_dwordx4 v228, s[8:11], s79 offen offset:1024 nt lds
s_mov_b32 m0, 27648
W 1
ds_read_b128 v[152:155], v220 offset:6272
buffer_load_dwordx4 v233, s[16:19], s78 offen lds
ds_read_b128 v[156:159], v221 offset:6272
ds_read_b128 v[160:163], v220 offset:6400
s_mov_b32 m0, 18432
s_mov_b32 m0, 27648
s_mov_b32 m0, 18432
s_mov_b32 m0, 27648
s_mov_b32 m0, 18432
s_mov_b32 m0, 27648
s_mov_b32 m0, 18432
M v[104:107],v[172:175],a[40:43],v[104:107]
buffer_load_dwordx4 v229, s[8:11], s79 offen offset:2048 nt lds
s_mov_b32 m0, 27648
M v[104:107],v[168:171],a[32:35],v[104:107]
buffer_load_dwordx4 v234, s[16:19], s78 offen offset:1024 lds
W 2
M v[104:107],v[164:167],a[24:27],v[104:107]
ds_read_b128 v[164:167], v221 offset:6400
M v[112:115],v[176:179],a[0:3],v[204:207]
ds_read_b128 v[168:171], v220 offset:12288
s_add_u32 s79, s2, 3072
M v[112:115],v[180:183],a[8:11],v[112:115]
ds_read_b128 v[172:175], v221 offset:12288
s_mov_b32 m0, 21504
M v[112:115],v[152:155],a[16:19],v[112:115]
buffer_load_dwordx4 v227, s[8:11], s79 offen nt lds
s_mov_b32 m0, 27648
s_nop 0
buffer_load_dwordx4 v235, s[16:19], s78 offen offset:2048 lds
s_add_u32 s78, s77, 246144
s_mov_b32 m0, 33792
v_max3_f32 v248, v104, v105, v106
v_max_f32 v248, v248, v107
v_exp_f32 v200, v104
W 2
ds_read_b128 v[176:179], v220 offset:12416
s_mov_b32 m0, 21504
s_mov_b32 m0, 33792
s_mov_b32 m0, 21504
ds_read_b128 v[180:183], v221 offset:12416
s_mov_b32 m0, 33792
s_mov_b32 m0, 21504
s_mov_b32 m0, 33792
ds_read_b128 v[152:155], v220 offset:12544
s_mov_b32 m0, 21504
s_nop 0
buffer_load_dwordx4 v228, s[8:11], s79 offen offset:1024 nt lds
s_mov_b32 m0, 33792
s_nop 0
buffer_load_dwordx4 v233, s[16:19], s78 offen lds
v_exp_f32 v201, v105
s_nop 0
v_cvt_pk_bf16_f32 v136, v200, v201
M v[112:115],v[164:167],a[40:43],v[112:115]
v_exp_f32 v202, v106
M v[112:115],v[160:163],a[32:35],v[112:115]
s_mov_b32 m0, 21504
s_mov_b32 m0, 33792
M v[112:115],v[156:159],a[24:27],v[112:115]
ds_read_b128 v[156:159], v221 offset:12544
W 2
M v[120:123],v[168:171],a[0:3],v[204:207]
s_mov_b32 m0, 21504
s_mov_b32 m0, 33792
s_mov_b32 m0, 21504
M v[120:123],v[172:175],a[8:11],v[120:123]
ds_read_b128 v[160:163], v220 offset:18432
buffer_load_dwordx4 v229, s[8:11], s79 offen offset:2048 nt lds
M v[120:123],v[176:179],a[16:19],v[120:123]
ds_read_b128 v[164:167], v221 offset:18432
s_mov_b32 m0, 33792
s_nop 0
buffer_load_dwordx4 v234, s[16:19], s78 offen offset:1024 lds
v_exp_f32 v203, v107
s_nop 0
v_cvt_pk_bf16_f32 v137, v202, v203
v_max3_f32 v248, v248, v112, v113
v_max3_f32 v248, v248, v114, v115
v_exp_f32 v200, v112
ds_read_b128 v[168:171], v220 offset:18560
buffer_load_dwordx4 v235, s[16:19], s78 offen offset:2048 lds
s_add_u32 s78, s77, 590208
s_mov_b32 m0, 39936
ds_read_b128 v[172:175], v221 offset:18560
v_exp_f32 v201, v113
s_nop 0
v_cvt_pk_bf16_f32 v138, v200, v201
ds_read_b128 v[176:179], v220 offset:18688
buffer_load_dwordx4 v233, s[16:19], s78 offen lds
v_exp_f32 v202, v114
v_exp_f32 v203, v115
s_nop 0
v_cvt_pk_bf16_f32 v139, v202, v203
W 3
M v[120:123],v[156:159],a[40:43],v[120:123]
buffer_load_dwordx4 v234, s[16:19], s78 offen offset:1024 lds
v_add_u32 v216, 122880, v224
M v[120:123],v[152:155],a[32:35],v[120:123]
v_xor_b32 v217, 32, v216
v_xor_b32 v218, 64, v216
M v[120:123],v[180:183],a[24:27],v[120:123]
ds_read_b128 v[180:183], v221 offset:18688
buffer_load_dwordx4 v235, s[16:19], s78 offen offset:2048 lds
M v[128:131],v[160:163],a[0:3],v[204:207]
v_xor_b32 v219, 96, v216
s_add_u32 s78, s77, 442752
M v[128:131],v[164:167],a[8:11],v[128:131]
s_barrier
T v[152:153],v216,0
W 2
M v[128:131],v[168:171],a[16:19],v[128:131]
T v[154:155],v216,6144
s_mov_b32 m0, 43008
T v[156:157],v216,12288
buffer_load_dwordx4 v233, s[16:19], s78 offen lds
v_max3_f32 v248, v248, v120, v121
v_max3_f32 v248, v248, v122, v123
T v[158:159],v216,18432
T v[160:161],v217,0
buffer_load_dwordx4 v234, s[16:19], s78 offen offset:1024 lds
T v[162:163],v217,6144
W 5
T v[164:165],v217,12288
buffer_load_dwordx4 v235, s[16:19], s78 offen offset:2048 lds
s_add_u32 s78, s77, 639360
s_mov_b32 m0, 46080
T v[166:167],v217,18432
W 5
M v[128:131],v[180:183],a[40:43],v[128:131]
T v[168:169],v218,0
buffer_load_dwordx4 v233, s[16:19], s78 offen lds
M v[128:131],v[176:179],a[32:35],v[128:131]
T v[170:171],v218,6144
W 5
M v[128:131],v[172:175],a[24:27],v[128:131]
T v[172:173],v218,12288
buffer_load_dwordx4 v234, s[16:19], s78 offen offset:1024 lds
buffer_load_dwordx4 v235, s[16:19], s78 offen offset:2048 lds
M a[48:51],v[152:155],a[160:163],a[48:51]
T v[174:175],v218,18432
s_add_u32 s79, s2, -15360
s_mov_b32 m0, 52224
s_nop 0
buffer_load_dwordx4 v227, s[12:15], s79 offen nt lds
M a[48:51],v[156:159],a[164:167],a[48:51]
T v[176:177],v219,0
W 5
T v[178:179],v219,6144
T v[180:181],v219,12288
v_max3_f32 v248, v248, v128, v129
v_max3_f32 v248, v248, v130, v131
v_cmp_lt_f32 vcc, s60, v248
M a[56:59],v[160:163],a[160:163],a[56:59]
T v[182:183],v219,18432
s_cbranch_vccnz LST0_1w_%=
LRT0_1w_%=:
M a[56:59],v[164:167],a[164:167],a[56:59]
T v[152:153],v216,128
buffer_load_dwordx4 v228, s[12:15], s79 offen offset:1024 nt lds
T v[154:155],v216,6272
v_exp_f32 v200, v120
W 5
T v[156:157],v216,12416
v_exp_f32 v201, v121
s_nop 0
v_cvt_pk_bf16_f32 v144, v200, v201
M a[64:67],v[168:171],a[160:163],a[64:67]
T v[158:159],v216,18560
W 5
M a[64:67],v[172:175],a[164:167],a[64:67]
T v[160:161],v217,128
T v[162:163],v217,6272
buffer_load_dwordx4 v229, s[12:15], s79 offen offset:2048 nt lds
v_exp_f32 v202, v122
T v[164:165],v217,12416
v_exp_f32 v203, v123
s_nop 0
v_cvt_pk_bf16_f32 v145, v202, v203
M a[72:75],v[176:179],a[160:163],a[72:75]
T v[166:167],v217,18560
W 5
M a[72:75],v[180:183],a[164:167],a[72:75]
T v[168:169],v218,128
s_add_u32 s79, s2, -9216
T v[170:171],v218,6272
s_mov_b32 m0, 58368
v_exp_f32 v200, v128
T v[172:173],v218,12416
buffer_load_dwordx4 v227, s[12:15], s79 offen nt lds
v_exp_f32 v201, v129
M a[80:83],v[152:155],a[160:163],a[80:83]
T v[174:175],v218,18560
W 5
M a[80:83],v[156:159],a[164:167],a[80:83]
T v[176:177],v219,128
v_cvt_pk_bf16_f32 v146, v200, v201
T v[178:179],v219,6272
v_exp_f32 v202, v130
v_add_u32 v220, 98304, v224
T v[180:181],v219,12416
v_exp_f32 v203, v131
s_nop 0
v_cvt_pk_bf16_f32 v147, v202, v203
M a[88:91],v[160:163],a[160:163],a[88:91]
T v[182:183],v219,18560
W 5
M a[88:91],v[164:167],a[164:167],a[88:91]
T v[152:153],v216,256
buffer_load_dwordx4 v228, s[12:15], s79 offen offset:1024 nt lds
T v[154:155],v216,6400
v_xor_b32 v221, 32, v220
v_xor_b32 v222, 64, v220
v_xor_b32 v223, 96, v220
T v[156:157],v216,12544
T v[158:159],v216,18688
W 6
M a[96:99],v[168:171],a[160:163],a[96:99]
T v[160:161],v217,256
M a[96:99],v[172:175],a[164:167],a[96:99]
T v[162:163],v217,6400
W 6
T v[164:165],v217,12544
buffer_load_dwordx4 v229, s[12:15], s79 offen offset:2048 nt lds
T v[166:167],v217,18688
W 6
M a[104:107],v[176:179],a[160:163],a[104:107]
T v[168:169],v218,256
W 5
M a[104:107],v[180:183],a[164:167],a[104:107]
T v[170:171],v218,6400
s_add_u32 s79, s2, -3072
T v[172:173],v218,12544
s_mov_b32 m0, 64512
W 5
T v[174:175],v218,18688
buffer_load_dwordx4 v227, s[12:15], s79 offen nt lds
M a[112:115],v[152:155],a[160:163],a[112:115]
T v[176:177],v219,256
W 5
M a[112:115],v[156:159],a[164:167],a[112:115]
T v[178:179],v219,6400
T v[180:181],v219,12544
W 5
T v[182:183],v219,18688
M a[120:123],v[160:163],a[160:163],a[120:123]
s_waitcnt vmcnt(37)
s_barrier
T v[152:153],v220,0
M a[120:123],v[164:167],a[164:167],a[120:123]
T v[154:155],v220,6144
buffer_load_dwordx4 v228, s[12:15], s79 offen offset:1024 nt lds
W 4
T v[156:157],v221,0
M a[128:131],v[168:171],a[160:163],a[128:131]
T v[158:159],v221,6144
W 4
M a[128:131],v[172:175],a[164:167],a[128:131]
T v[160:161],v222,0
T v[162:163],v222,6144
buffer_load_dwordx4 v229, s[12:15], s79 offen offset:2048 nt lds
W 4
T v[164:165],v223,0
M a[136:139],v[176:179],a[160:163],a[136:139]
T v[166:167],v223,6144
M a[136:139],v[180:183],a[164:167],a[136:139]
T v[168:169],v220,128
s_add_u32 s79, s2, 0
T v[170:171],v220,6272
s_mov_b32 m0, 67584
W 5
T v[172:173],v221,128
buffer_load_dwordx4 v227, s[12:15], s79 offen nt lds
M v[0:3],v[152:155],v[136:139],v[0:3]
T v[174:175],v221,6272
W 5
T v[176:177],v222,128
buffer_load_dwordx4 v228, s[12:15], s79 offen offset:1024 nt lds
M v[96:99],v[244:247],v[136:139],v[96:99]
T v[178:179],v222,6272
W 5
T v[180:181],v223,128
buffer_load_dwordx4 v229, s[12:15], s79 offen offset:2048 nt lds
M v[8:11],v[156:159],v[136:139],v[8:11]
T v[182:183],v223,6272
s_add_u32 s79, s2, 3072
T v[152:153],v220,256
s_mov_b32 m0, 70656
s_nop 0
buffer_load_dwordx4 v227, s[12:15], s79 offen nt lds
W 5
M v[16:19],v[160:163],v[136:139],v[16:19]
T v[154:155],v220,6400
T v[156:157],v221,256
buffer_load_dwordx4 v228, s[12:15], s79 offen offset:1024 nt lds
W 5
M v[24:27],v[164:167],v[136:139],v[24:27]
T v[158:159],v221,6400
T v[160:161],v222,256
buffer_load_dwordx4 v229, s[12:15], s79 offen offset:2048 nt lds
W 5
M v[32:35],v[168:171],v[136:139],v[32:35]
T v[162:163],v222,6400
s_add_u32 s79, s3, -15360
T v[164:165],v223,256
s_mov_b32 m0, 76800
s_nop 0
buffer_load_dwordx4 v227, s[8:11], s79 offen nt lds
s_add_u32 m0, s43, 147456
M v[40:43],v[172:175],v[136:139],v[40:43]
T v[166:167],v223,6400
s_mov_b32 m0, 76800
T v[168:169],v220,12288
buffer_load_dwordx4 v228, s[8:11], s79 offen offset:1024 nt lds
s_add_u32 m0, s43, 147456
W 5
M v[48:51],v[176:179],v[136:139],v[48:51]
T v[170:171],v220,18432
buffer_load_dwordx4 v236, s[24:27], s44 offen lds
W 4
M v[56:59],v[180:183],v[136:139],v[56:59]
s_mov_b32 m0, 76800
s_nop 0
buffer_load_dwordx4 v229, s[8:11], s79 offen offset:2048 nt lds
T v[172:173],v221,12288
s_add_u32 m0, s43, 147456
s_nop 0
buffer_load_dwordx4 v237, s[24:27], s44 offen offset:1024 lds
W 3
M v[64:67],v[152:155],v[136:139],v[64:67]
T v[174:175],v221,18432
T v[176:177],v222,12288
s_add_u32 s79, s3, -9216
s_mov_b32 m0, 82944
W 3
M v[72:75],v[156:159],v[136:139],v[72:75]
T v[178:179],v222,18432
v_add_u32 v216, 0, v225
T v[180:181],v223,12288
buffer_load_dwordx4 v227, s[8:11], s79 offen nt lds
v_xor_b32 v217, 64, v216
M v[80:83],v[160:163],v[136:139],v[80:83]
T v[182:183],v223,18432
T v[152:153],v220,12416
W 5
M v[88:91],v[164:167],v[136:139],v[88:91]
T v[154:155],v220,18560
T v[156:157],v221,12416
W 5
M v[0:3],v[168:171],v[144:147],v[0:3]
T v[158:159],v221,18560
buffer_load_dwordx4 v228, s[8:11], s79 offen offset:1024 nt lds
T v[160:161],v222,12416
W 5
M v[96:99],v[244:247],v[144:147],v[96:99]
T v[162:163],v222,18560
T v[164:165],v223,12416
W 5
M v[8:11],v[172:175],v[144:147],v[8:11]
T v[166:167],v223,18560
T v[168:169],v220,12544
buffer_load_dwordx4 v229, s[8:11], s79 offen offset:2048 nt lds
W 5
M v[16:19],v[176:179],v[144:147],v[16:19]
T v[170:171],v220,18688
T v[172:173],v221,12544
W 5
M v[24:27],v[180:183],v[144:147],v[24:27]
T v[174:175],v221,18688
s_add_u32 s79, s3, -3072
T v[176:177],v222,12544
s_mov_b32 m0, 89088
W 5
M v[32:35],v[152:155],v[144:147],v[32:35]
T v[178:179],v222,18688
buffer_load_dwordx4 v227, s[8:11], s79 offen nt lds
T v[180:181],v223,12544
W 5
M v[40:43],v[156:159],v[144:147],v[40:43]
T v[182:183],v223,18688
s_waitcnt vmcnt(35)
s_barrier
ds_read_b128 v[152:155], v216 offset:0
W 5
M v[48:51],v[160:163],v[144:147],v[48:51]
ds_read_b128 v[156:159], v217 offset:0
ds_read_b128 v[160:163], v216 offset:128
buffer_load_dwordx4 v228, s[8:11], s79 offen offset:1024 nt lds
W 5
M v[56:59],v[164:167],v[144:147],v[56:59]
W 3
M v[64:67],v[168:171],v[144:147],v[64:67]
ds_read_b128 v[164:167], v217 offset:128
W 2
M v[72:75],v[172:175],v[144:147],v[72:75]
ds_read_b128 v[168:171], v216 offset:256
buffer_load_dwordx4 v229, s[8:11], s79 offen offset:2048 nt lds
ds_read_b128 v[172:175], v217 offset:256
M v[80:83],v[176:179],v[144:147],v[80:83]
s_add_u32 s79, s3, 0
s_mov_b32 m0, 92160
M v[88:91],v[180:183],v[144:147],v[88:91]
W 2
ds_read_b128 v[176:179], v216 offset:6144
buffer_load_dwordx4 v227, s[8:11], s79 offen nt lds
M v[104:107],v[152:155],a[0:3],v[204:207]
ds_read_b128 v[180:183], v217 offset:6144
s_mul_i32 s77, s47, 6144
M v[104:107],v[156:159],a[8:11],v[104:107]
s_add_u32 s78, s77, 196992
s_mov_b32 m0, 125952
M v[104:107],v[160:163],a[16:19],v[104:107]
s_mov_b32 m0, 92160
s_mov_b32 m0, 125952
s_mov_b32 m0, 92160
s_mov_b32 m0, 125952
s_mov_b32 m0, 92160
s_mov_b32 m0, 125952
s_mov_b32 m0, 92160
s_nop 0
buffer_load_dwordx4 v228, s[8:11], s79 offen offset:1024 nt lds
s_mov_b32 m0, 125952
W 1
ds_read_b128 v[152:155], v216 offset:6272
buffer_load_dwordx4 v233, s[16:19], s78 offen lds
ds_read_b128 v[156:159], v217 offset:6272
ds_read_b128 v[160:163], v216 offset:6400
s_mov_b32 m0, 92160
s_mov_b32 m0, 125952
s_mov_b32 m0, 92160
s_mov_b32 m0, 125952
s_mov_b32 m0, 92160
s_mov_b32 m0, 125952
s_mov_b32 m0, 92160
M v[104:107],v[172:175],a[40:43],v[104:107]
buffer_load_dwordx4 v229, s[8:11], s79 offen offset:2048 nt lds
s_mov_b32 m0, 125952
M v[104:107],v[168:171],a[32:35],v[104:107]
buffer_load_dwordx4 v234, s[16:19], s78 offen offset:1024 lds
W 2
M v[104:107],v[164:167],a[24:27],v[104:107]
ds_read_b128 v[164:167], v217 offset:6400
M v[112:115],v[176:179],a[0:3],v[204:207]
ds_read_b128 v[168:171], v216 offset:12288
s_add_u32 s79, s3, 3072
M v[112:115],v[180:183],a[8:11],v[112:115]
ds_read_b128 v[172:175], v217 offset:12288
s_mov_b32 m0, 95232
M v[112:115],v[152:155],a[16:19],v[112:115]
buffer_load_dwordx4 v227, s[8:11], s79 offen nt lds
s_mov_b32 m0, 125952
s_nop 0
buffer_load_dwordx4 v235, s[16:19], s78 offen offset:2048 lds
s_add_u32 s78, s77, 246144
s_mov_b32 m0, 132096
v_max3_f32 v248, v104, v105, v106
v_max_f32 v248, v248, v107
v_exp_f32 v200, v104
W 2
ds_read_b128 v[176:179], v216 offset:12416
s_mov_b32 m0, 95232
s_mov_b32 m0, 132096
s_mov_b32 m0, 95232
ds_read_b128 v[180:183], v217 offset:12416
s_mov_b32 m0, 132096
s_mov_b32 m0, 95232
s_mov_b32 m0, 132096
ds_read_b128 v[152:155], v216 offset:12544
s_mov_b32 m0, 95232
s_nop 0
buffer_load_dwordx4 v228, s[8:11], s79 offen offset:1024 nt lds
s_mov_b32 m0, 132096
s_nop 0
buffer_load_dwordx4 v233, s[16:19], s78 offen lds
v_exp_f32 v201, v105
s_nop 0
v_cvt_pk_bf16_f32 v136, v200, v201
M v[112:115],v[164:167],a[40:43],v[112:115]
v_exp_f32 v202, v106
M v[112:115],v[160:163],a[32:35],v[112:115]
s_mov_b32 m0, 95232
s_mov_b32 m0, 132096
M v[112:115],v[156:159],a[24:27],v[112:115]
ds_read_b128 v[156:159], v217 offset:12544
W 2
M v[120:123],v[168:171],a[0:3],v[204:207]
s_mov_b32 m0, 95232
s_mov_b32 m0, 132096
s_mov_b32 m0, 95232
M v[120:123],v[172:175],a[8:11],v[120:123]
ds_read_b128 v[160:163], v216 offset:18432
buffer_load_dwordx4 v229, s[8:11], s79 offen offset:2048 nt lds
M v[120:123],v[176:179],a[16:19],v[120:123]
ds_read_b128 v[164:167], v217 offset:18432
s_mov_b32 m0, 132096
s_nop 0
buffer_load_dwordx4 v234, s[16:19], s78 offen offset:1024 lds
v_exp_f32 v203, v107
s_nop 0
v_cvt_pk_bf16_f32 v137, v202, v203
v_max3_f32 v248, v248, v112, v113
v_max3_f32 v248, v248, v114, v115
v_exp_f32 v200, v112
ds_read_b128 v[168:171], v216 offset:18560
buffer_load_dwordx4 v235, s[16:19], s78 offen offset:2048 lds
s_add_u32 s78, s77, 590208
s_mov_b32 m0, 138240
ds_read_b128 v[172:175], v217 offset:18560
v_exp_f32 v201, v113
s_nop 0
v_cvt_pk_bf16_f32 v138, v200, v201
ds_read_b128 v[176:179], v216 offset:18688
buffer_load_dwordx4 v233, s[16:19], s78 offen lds
v_exp_f32 v202, v114
v_exp_f32 v203, v115
s_nop 0
v_cvt_pk_bf16_f32 v139, v202, v203
W 3
M v[120:123],v[156:159],a[40:43],v[120:123]
buffer_load_dwordx4 v234, s[16:19], s78 offen offset:1024 lds
v_add_u32 v220, 24576, v224
M v[120:123],v[152:155],a[32:35],v[120:123]
v_xor_b32 v221, 32, v220
v_xor_b32 v222, 64, v220
M v[120:123],v[180:183],a[24:27],v[120:123]
ds_read_b128 v[180:183], v217 offset:18688
buffer_load_dwordx4 v235, s[16:19], s78 offen offset:2048 lds
M v[128:131],v[160:163],a[0:3],v[204:207]
v_xor_b32 v223, 96, v220
s_add_u32 s78, s77, 442752
M v[128:131],v[164:167],a[8:11],v[128:131]
s_waitcnt vmcnt(41)
s_barrier
T v[152:153],v220,0
W 2
M v[128:131],v[168:171],a[16:19],v[128:131]
T v[154:155],v220,6144
s_mov_b32 m0, 141312
T v[156:157],v220,12288
buffer_load_dwordx4 v233, s[16:19], s78 offen lds
v_max3_f32 v248, v248, v120, v121
v_max3_f32 v248, v248, v122, v123
T v[158:159],v220,18432
T v[160:161],v221,0
buffer_load_dwordx4 v234, s[16:19], s78 offen offset:1024 lds
T v[162:163],v221,6144
W 5
T v[164:165],v221,12288
buffer_load_dwordx4 v235, s[16:19], s78 offen offset:2048 lds
s_add_u32 s78, s77, 639360
s_mov_b32 m0, 144384
T v[166:167],v221,18432
W 5
M v[128:131],v[180:183],a[40:43],v[128:131]
T v[168:169],v222,0
buffer_load_dwordx4 v233, s[16:19], s78 offen lds
M v[128:131],v[176:179],a[32:35],v[128:131]
T v[170:171],v222,6144
W 5
M v[128:131],v[172:175],a[24:27],v[128:131]
T v[172:173],v222,12288
buffer_load_dwordx4 v234, s[16:19], s78 offen offset:1024 lds
buffer_load_dwordx4 v235, s[16:19], s78 offen offset:2048 lds
M a[48:51],v[152:155],a[176:179],a[48:51]
T v[174:175],v222,18432
s_add_u32 s79, s3, -15360
s_mov_b32 m0, 101376
s_nop 0
buffer_load_dwordx4 v227, s[12:15], s79 offen nt lds
M a[48:51],v[156:159],a[180:183],a[48:51]
T v[176:177],v223,0
W 5
T v[178:179],v223,6144
T v[180:181],v223,12288
v_max3_f32 v248, v248, v128, v129
v_max3_f32 v248, v248, v130, v131
v_cmp_lt_f32 vcc, s60, v248
M a[56:59],v[160:163],a[176:179],a[56:59]
T v[182:183],v223,18432
s_cbranch_vccnz LST0_2w_%=
LRT0_2w_%=:
M a[56:59],v[164:167],a[180:183],a[56:59]
T v[152:153],v220,128
buffer_load_dwordx4 v228, s[12:15], s79 offen offset:1024 nt lds
T v[154:155],v220,6272
v_exp_f32 v200, v120
W 5
T v[156:157],v220,12416
v_exp_f32 v201, v121
s_nop 0
v_cvt_pk_bf16_f32 v144, v200, v201
M a[64:67],v[168:171],a[176:179],a[64:67]
T v[158:159],v220,18560
W 5
M a[64:67],v[172:175],a[180:183],a[64:67]
T v[160:161],v221,128
T v[162:163],v221,6272
buffer_load_dwordx4 v229, s[12:15], s79 offen offset:2048 nt lds
v_exp_f32 v202, v122
T v[164:165],v221,12416
v_exp_f32 v203, v123
s_nop 0
v_cvt_pk_bf16_f32 v145, v202, v203
M a[72:75],v[176:179],a[176:179],a[72:75]
T v[166:167],v221,18560
W 5
M a[72:75],v[180:183],a[180:183],a[72:75]
T v[168:169],v222,128
s_add_u32 s79, s3, -9216
T v[170:171],v222,6272
s_mov_b32 m0, 107520
v_exp_f32 v200, v128
T v[172:173],v222,12416
buffer_load_dwordx4 v227, s[12:15], s79 offen nt lds
v_exp_f32 v201, v129
M a[80:83],v[152:155],a[176:179],a[80:83]
T v[174:175],v222,18560
W 5
M a[80:83],v[156:159],a[180:183],a[80:83]
T v[176:177],v223,128
v_cvt_pk_bf16_f32 v146, v200, v201
T v[178:179],v223,6272
v_exp_f32 v202, v130
v_add_u32 v216, 49152, v224
T v[180:181],v223,12416
v_exp_f32 v203, v131
s_nop 0
v_cvt_pk_bf16_f32 v147, v202, v203
M a[88:91],v[160:163],a[176:179],a[88:91]
T v[182:183],v223,18560
W 5
M a[88:91],v[164:167],a[180:183],a[88:91]
T v[152:153],v220,256
buffer_load_dwordx4 v228, s[12:15], s79 offen offset:1024 nt lds
T v[154:155],v220,6400
v_xor_b32 v217, 32, v216
v_xor_b32 v218, 64, v216
v_xor_b32 v219, 96, v216
T v[156:157],v220,12544
T v[158:159],v220,18688
W 6
M a[96:99],v[168:171],a[176:179],a[96:99]
T v[160:161],v221,256
M a[96:99],v[172:175],a[180:183],a[96:99]
T v[162:163],v221,6400
W 6
T v[164:165],v221,12544
buffer_load_dwordx4 v229, s[12:15], s79 offen offset:2048 nt lds
T v[166:167],v221,18688
W 6
M a[104:107],v[176:179],a[176:179],a[104:107]
T v[168:169],v222,256
W 5
M a[104:107],v[180:183],a[180:183],a[104:107]
T v[170:171],v222,6400
s_add_u32 s79, s3, -3072
T v[172:173],v222,12544
s_mov_b32 m0, 113664
W 5
T v[174:175],v222,18688
buffer_load_dwordx4 v227, s[12:15], s79 offen nt lds
M a[112:115],v[152:155],a[176:179],a[112:115]
T v[176:177],v223,256
W 5
M a[112:115],v[156:159],a[180:183],a[112:115]
T v[178:179],v223,6400
T v[180:181],v223,12544
W 5
T v[182:183],v223,18688
M a[120:123],v[160:163],a[176:179],a[120:123]
s_waitcnt vmcnt(39)
s_barrier
T v[152:153],v216,0
M a[120:123],v[164:167],a[180:183],a[120:123]
T v[154:155],v216,6144
buffer_load_dwordx4 v228, s[12:15], s79 offen offset:1024 nt lds
W 4
T v[156:157],v217,0
M a[128:131],v[168:171],a[176:179],a[128:131]
T v[158:159],v217,6144
W 4
M a[128:131],v[172:175],a[180:183],a[128:131]
T v[160:161],v218,0
T v[162:163],v218,6144
buffer_load_dwordx4 v229, s[12:15], s79 offen offset:2048 nt lds
W 4
T v[164:165],v219,0
M a[136:139],v[176:179],a[176:179],a[136:139]
T v[166:167],v219,6144
M a[136:139],v[180:183],a[180:183],a[136:139]
T v[168:169],v216,128
s_add_u32 s79, s3, 0
T v[170:171],v216,6272
s_mov_b32 m0, 116736
W 5
T v[172:173],v217,128
buffer_load_dwordx4 v227, s[12:15], s79 offen nt lds
M v[0:3],v[152:155],v[136:139],v[0:3]
T v[174:175],v217,6272
s_mul_i32 s77, s48, 6144
T v[176:177],v218,128
s_add_u32 s78, s77, 196992
s_mov_b32 m0, 3072
s_nop 0
buffer_load_dwordx4 v233, s[16:19], s78 offen lds
M v[96:99],v[244:247],v[136:139],v[96:99]
T v[178:179],v218,6272
s_mov_b32 m0, 116736
T v[180:181],v219,128
s_mov_b32 m0, 3072
s_mov_b32 m0, 116736
s_mov_b32 m0, 3072
M v[8:11],v[156:159],v[136:139],v[8:11]
T v[182:183],v219,6272
s_mov_b32 m0, 116736
T v[152:153],v216,256
buffer_load_dwordx4 v228, s[12:15], s79 offen offset:1024 nt lds
s_mov_b32 m0, 3072
W 5
M v[16:19],v[160:163],v[136:139],v[16:19]
T v[154:155],v216,6400
buffer_load_dwordx4 v234, s[16:19], s78 offen offset:1024 lds
T v[156:157],v217,256
W 5
M v[24:27],v[164:167],v[136:139],v[24:27]
T v[158:159],v217,6400
s_mov_b32 m0, 116736
T v[160:161],v218,256
s_mov_b32 m0, 3072
s_mov_b32 m0, 116736
s_mov_b32 m0, 3072
M v[32:35],v[168:171],v[136:139],v[32:35]
T v[162:163],v218,6400
s_mov_b32 m0, 116736
T v[164:165],v219,256
buffer_load_dwordx4 v229, s[12:15], s79 offen offset:2048 nt lds
s_mov_b32 m0, 3072
W 5
M v[40:43],v[172:175],v[136:139],v[40:43]
T v[166:167],v219,6400
buffer_load_dwordx4 v235, s[16:19], s78 offen offset:2048 lds
T v[168:169],v216,12288
s_add_u32 s78, s77, 246144
s_mov_b32 m0, 9216
W 5
M v[48:51],v[176:179],v[136:139],v[48:51]
T v[170:171],v216,18432
s_add_u32 s79, s3, 3072
s_mov_b32 m0, 119808
s_mov_b32 m0, 9216
s_mov_b32 m0, 119808
s_mov_b32 m0, 9216
M v[56:59],v[180:183],v[136:139],v[56:59]
s_mov_b32 m0, 119808
s_nop 0
buffer_load_dwordx4 v227, s[12:15], s79 offen nt lds
T v[172:173],v217,12288
s_mov_b32 m0, 9216
s_nop 0
buffer_load_dwordx4 v233, s[16:19], s78 offen lds
W 3
M v[64:67],v[152:155],v[136:139],v[64:67]
T v[174:175],v217,18432
T v[176:177],v218,12288
s_mov_b32 m0, 119808
s_mov_b32 m0, 9216
s_mov_b32 m0, 119808
M v[72:75],v[156:159],v[136:139],v[72:75]
T v[178:179],v218,18432
v_add_u32 v220, 73728, v225
T v[180:181],v219,12288
buffer_load_dwordx4 v228, s[12:15], s79 offen offset:1024 nt lds
v_xor_b32 v221, 64, v220
s_mov_b32 m0, 9216
M v[80:83],v[160:163],v[136:139],v[80:83]
T v[182:183],v219,18432
buffer_load_dwordx4 v234, s[16:19], s78 offen offset:1024 lds
T v[152:153],v216,12416
W 5
M v[88:91],v[164:167],v[136:139],v[88:91]
T v[154:155],v216,18560
s_mov_b32 m0, 119808
T v[156:157],v217,12416
s_mov_b32 m0, 9216
s_mov_b32 m0, 119808
s_mov_b32 m0, 9216
M v[0:3],v[168:171],v[144:147],v[0:3]
T v[158:159],v217,18560
s_mov_b32 m0, 119808
T v[160:161],v218,12416
buffer_load_dwordx4 v229, s[12:15], s79 offen offset:2048 nt lds
s_mov_b32 m0, 9216
W 5
M v[96:99],v[244:247],v[144:147],v[96:99]
T v[162:163],v218,18560
buffer_load_dwordx4 v235, s[16:19], s78 offen offset:2048 lds
T v[164:165],v219,12416
s_add_u32 s78, s77, 590208
s_mov_b32 m0, 15360
W 5
M v[8:11],v[172:175],v[144:147],v[8:11]
T v[166:167],v219,18560
buffer_load_dwordx4 v233, s[16:19], s78 offen lds
T v[168:169],v216,12544
W 5
M v[16:19],v[176:179],v[144:147],v[16:19]
T v[170:171],v216,18688
buffer_load_dwordx4 v234, s[16:19], s78 offen offset:1024 lds
T v[172:173],v217,12544
W 5
M v[24:27],v[180:183],v[144:147],v[24:27]
T v[174:175],v217,18688
buffer_load_dwordx4 v235, s[16:19], s78 offen offset:2048 lds
T v[176:177],v218,12544
s_add_u32 s78, s77, 442752
s_mov_b32 m0, 18432
W 5
M v[32:35],v[152:155],v[144:147],v[32:35]
T v[178:179],v218,18688
buffer_load_dwordx4 v233, s[16:19], s78 offen lds
T v[180:181],v219,12544
W 5
M v[40:43],v[156:159],v[144:147],v[40:43]
T v[182:183],v219,18688
buffer_load_dwordx4 v234, s[16:19], s78 offen offset:1024 lds
s_waitcnt vmcnt(37)
s_barrier
ds_read_b128 v[152:155], v220 offset:0
W 5
M v[48:51],v[160:163],v[144:147],v[48:51]
ds_read_b128 v[156:159], v221 offset:0
buffer_load_dwordx4 v235, s[16:19], s78 offen offset:2048 lds
ds_read_b128 v[160:163], v220 offset:128
s_add_u32 s78, s77, 639360
s_mov_b32 m0, 21504
W 5
M v[56:59],v[164:167],v[144:147],v[56:59]
buffer_load_dwordx4 v233, s[16:19], s78 offen lds
W 3
M v[64:67],v[168:171],v[144:147],v[64:67]
buffer_load_dwordx4 v234, s[16:19], s78 offen offset:1024 lds
ds_read_b128 v[164:167], v221 offset:128
W 2
M v[72:75],v[172:175],v[144:147],v[72:75]
ds_read_b128 v[168:171], v220 offset:256
buffer_load_dwordx4 v235, s[16:19], s78 offen offset:2048 lds
ds_read_b128 v[172:175], v221 offset:256
M v[80:83],v[176:179],v[144:147],v[80:83]
M v[88:91],v[180:183],v[144:147],v[88:91]
W 2
ds_read_b128 v[176:179], v220 offset:6144
M v[104:107],v[152:155],a[0:3],v[204:207]
ds_read_b128 v[180:183], v221 offset:6144
s_add_u32 s79, s4, -15360
s_mov_b32 m0, 27648
s_nop 0
buffer_load_dwordx4 v227, s[8:11], s79 offen nt lds
M v[104:107],v[156:159],a[8:11],v[104:107]
W 2
M v[104:107],v[160:163],a[16:19],v[104:107]
ds_read_b128 v[152:155], v220 offset:6272
buffer_load_dwordx4 v228, s[8:11], s79 offen offset:1024 nt lds
W 1
ds_read_b128 v[156:159], v221 offset:6272
ds_read_b128 v[160:163], v220 offset:6400
M v[104:107],v[172:175],a[40:43],v[104:107]
M v[104:107],v[168:171],a[32:35],v[104:107]
buffer_load_dwordx4 v229, s[8:11], s79 offen offset:2048 nt lds
M v[104:107],v[164:167],a[24:27],v[104:107]
ds_read_b128 v[164:167], v221 offset:6400
W 2
M v[112:115],v[176:179],a[0:3],v[204:207]
ds_read_b128 v[168:171], v220 offset:12288
M v[112:115],v[180:183],a[8:11],v[112:115]
ds_read_b128 v[172:175], v221 offset:12288
s_add_u32 s79, s4, -9216
M v[112:115],v[152:155],a[16:19],v[112:115]
s_mov_b32 m0, 33792
s_nop 0
buffer_load_dwordx4 v227, s[8:11], s79 offen nt lds
v_max3_f32 v248, v104, v105, v106
v_max_f32 v248, v248, v107
v_exp_f32 v200, v104
v_exp_f32 v201, v105
ds_read_b128 v[176:179], v220 offset:12416
v_exp_f32 v202, v106
v_cvt_pk_bf16_f32 v136, v200, v201
ds_read_b128 v[180:183], v221 offset:12416
v_exp_f32 v203, v107
s_nop 0
v_cvt_pk_bf16_f32 v137, v202, v203
ds_read_b128 v[152:155], v220 offset:12544
v_add_u32 v216, 122880, v224
v_xor_b32 v217, 32, v216
v_xor_b32 v218, 64, v216
buffer_load_dwordx4 v228, s[8:11], s79 offen offset:1024 nt lds
v_xor_b32 v219, 96, v216
W 3
M v[112:115],v[164:167],a[40:43],v[112:115]
M v[112:115],v[160:163],a[32:35],v[112:115]
M v[112:115],v[156:159],a[24:27],v[112:115]
ds_read_b128 v[156:159], v221 offset:12544
W 2
M v[120:123],v[168:171],a[0:3],v[204:207]
ds_read_b128 v[160:163], v220 offset:18432
M v[120:123],v[172:175],a[8:11],v[120:123]
ds_read_b128 v[164:167], v221 offset:18432
buffer_load_dwordx4 v229, s[8:11], s79 offen offset:2048 nt lds
M v[120:123],v[176:179],a[16:19],v[120:123]
v_max3_f32 v248, v248, v112, v113
v_max3_f32 v248, v248, v114, v115
v_exp_f32 v200, v112
s_add_u32 s79, s4, -3072
s_mov_b32 m0, 39936
v_exp_f32 v201, v113
ds_read_b128 v[168:171], v220 offset:18560
v_exp_f32 v202, v114
v_cvt_pk_bf16_f32 v138, v200, v201
ds_read_b128 v[172:175], v221 offset:18560
buffer_load_dwordx4 v227, s[8:11], s79 offen nt lds
v_exp_f32 v203, v115
ds_read_b128 v[176:179], v220 offset:18688
v_cvt_pk_bf16_f32 v139, v202, v203
W 3
M v[120:123],v[156:159],a[40:43],v[120:123]
M v[120:123],v[152:155],a[32:35],v[120:123]
M v[120:123],v[180:183],a[24:27],v[120:123]
ds_read_b128 v[180:183], v221 offset:18688
buffer_load_dwordx4 v228, s[8:11], s79 offen offset:1024 nt lds
M v[128:131],v[160:163],a[0:3],v[204:207]
s_waitcnt vmcnt(38)
s_barrier
T v[152:153],v216,0
M v[128:131],v[164:167],a[8:11],v[128:131]
T v[154:155],v216,6144
W 3
M v[128:131],v[168:171],a[16:19],v[128:131]
T v[156:157],v216,12288
T v[158:159],v216,18432
v_max3_f32 v248, v248, v120, v121
v_max3_f32 v248, v248, v122, v123
buffer_load_dwordx4 v229, s[8:11], s79 offen offset:2048 nt lds
T v[160:161],v217,0
W 4
T v[162:163],v217,6144
T v[164:165],v217,12288
T v[166:167],v217,18432
s_add_u32 s79, s4, 0
s_mov_b32 m0, 43008
T v[168:169],v218,0
W 5
M v[128:131],v[180:183],a[40:43],v[128:131]
T v[170:171],v218,6144
buffer_load_dwordx4 v227, s[8:11], s79 offen nt lds
M v[128:131],v[176:179],a[32:35],v[128:131]
M v[128:131],v[172:175],a[24:27],v[128:131]
T v[172:173],v218,12288
T v[174:175],v218,18432
M a[48:51],v[152:155],a[192:195],a[48:51]
T v[176:177],v219,0
s_mul_i32 s77, s49, 6144
M a[48:51],v[156:159],a[196:199],a[48:51]
T v[178:179],v219,6144
s_add_u32 s78, s77, 196992
T v[180:181],v219,12288
buffer_load_dwordx4 v228, s[8:11], s79 offen offset:1024 nt lds
s_mov_b32 m0, 52224
W 5
T v[182:183],v219,18432
buffer_load_dwordx4 v233, s[16:19], s78 offen lds
v_max3_f32 v248, v248, v128, v129
v_max3_f32 v248, v248, v130, v131
M a[56:59],v[160:163],a[192:195],a[56:59]
T v[152:153],v216,128
v_cmp_lt_f32 vcc, s60, v248
M a[56:59],v[164:167],a[196:199],a[56:59]
T v[154:155],v216,6272
s_mov_b32 m0, 43008
T v[156:157],v216,12416
s_mov_b32 m0, 52224
s_mov_b32 m0, 43008
s_mov_b32 m0, 52224
T v[158:159],v216,18560
s_mov_b32 m0, 43008
s_nop 0
buffer_load_dwordx4 v229, s[8:11], s79 offen offset:2048 nt lds
s_mov_b32 m0, 52224
M a[64:67],v[168:171],a[192:195],a[64:67]
T v[160:161],v217,128
W 5
M a[64:67],v[172:175],a[196:199],a[64:67]
T v[162:163],v217,6272
buffer_load_dwordx4 v234, s[16:19], s78 offen offset:1024 lds
T v[164:165],v217,12416
s_add_u32 s79, s4, 3072
s_mov_b32 m0, 46080
s_mov_b32 m0, 52224
T v[166:167],v217,18560
s_mov_b32 m0, 46080
s_mov_b32 m0, 52224
s_mov_b32 m0, 46080
M a[72:75],v[176:179],a[192:195],a[72:75]
T v[168:169],v218,128
buffer_load_dwordx4 v227, s[8:11], s79 offen nt lds
M a[72:75],v[180:183],a[196:199],a[72:75]
T v[170:171],v218,6272
s_mov_b32 m0, 52224
T v[172:173],v218,12416
buffer_load_dwordx4 v235, s[16:19], s78 offen offset:2048 lds
s_add_u32 s78, s77, 246144
s_mov_b32 m0, 58368
T v[174:175],v218,18560
s_mov_b32 m0, 46080
s_mov_b32 m0, 58368
W 5
M a[80:83],v[152:155],a[192:195],a[80:83]
T v[176:177],v219,128
s_mov_b32 m0, 46080
M a[80:83],v[156:159],a[196:199],a[80:83]
T v[178:179],v219,6272
buffer_load_dwordx4 v228, s[8:11], s79 offen offset:1024 nt lds
s_cbranch_vccnz LST0_3w_%=
LRT0_3w_%=:
T v[180:181],v219,12416
s_mov_b32 m0, 58368
s_nop 0
buffer_load_dwordx4 v233, s[16:19], s78 offen lds
v_add_u32 v220, 98304, v224
T v[182:183],v219,18560
v_exp_f32 v200, v120
v_xor_b32 v221, 32, v220
M a[88:91],v[160:163],a[192:195],a[88:91]
T v[152:153],v216,256
W 5
M a[88:91],v[164:167],a[196:199],a[88:91]
T v[154:155],v216,6400
s_mov_b32 m0, 46080
T v[156:157],v216,12544
buffer_load_dwordx4 v229, s[8:11], s79 offen offset:2048 nt lds
s_mov_b32 m0, 58368
v_xor_b32 v222, 64, v220
T v[158:159],v216,18688
buffer_load_dwordx4 v234, s[16:19], s78 offen offset:1024 lds
v_exp_f32 v201, v121
M a[96:99],v[168:171],a[192:195],a[96:99]
T v[160:161],v217,256
v_cvt_pk_bf16_f32 v144, v200, v201
M a[96:99],v[172:175],a[196:199],a[96:99]
T v[162:163],v217,6400
buffer_load_dwordx4 v235, s[16:19], s78 offen offset:2048 lds
T v[164:165],v217,12544
s_add_u32 s78, s77, 590208
s_mov_b32 m0, 64512
v_xor_b32 v223, 96, v220
T v[166:167],v217,18688
buffer_load_dwordx4 v233, s[16:19], s78 offen lds
W 5
M a[104:107],v[176:179],a[192:195],a[104:107]
T v[168:169],v218,256
M a[104:107],v[180:183],a[196:199],a[104:107]
T v[170:171],v218,6400
buffer_load_dwordx4 v234, s[16:19], s78 offen offset:1024 lds
T v[172:173],v218,12544
v_exp_f32 v202, v122
W 5
T v[174:175],v218,18688
buffer_load_dwordx4 v235, s[16:19], s78 offen offset:2048 lds
s_add_u32 s78, s77, 442752
s_mov_b32 m0, 67584
M a[112:115],v[152:155],a[192:195],a[112:115]
T v[176:177],v219,256
W 5
M a[112:115],v[156:159],a[196:199],a[112:115]
T v[178:179],v219,6400
buffer_load_dwordx4 v233, s[16:19], s78 offen lds
T v[180:181],v219,12544
v_exp_f32 v203, v123
s_nop 0
v_cvt_pk_bf16_f32 v145, v202, v203
T v[182:183],v219,18688
buffer_load_dwordx4 v234, s[16:19], s78 offen offset:1024 lds
v_exp_f32 v200, v128
M a[120:123],v[160:163],a[192:195],a[120:123]
s_waitcnt vmcnt(36)
s_barrier
T v[152:153],v220,0
M a[120:123],v[164:167],a[196:199],a[120:123]
T v[154:155],v220,6144
buffer_load_dwordx4 v235, s[16:19], s78 offen offset:2048 lds
s_add_u32 s78, s77, 639360
s_mov_b32 m0, 70656
v_exp_f32 v201, v129
T v[156:157],v221,0
buffer_load_dwordx4 v233, s[16:19], s78 offen lds
v_cvt_pk_bf16_f32 v146, v200, v201
W 4
M a[128:131],v[168:171],a[192:195],a[128:131]
T v[158:159],v221,6144
M a[128:131],v[172:175],a[196:199],a[128:131]
T v[160:161],v222,0
buffer_load_dwordx4 v234, s[16:19], s78 offen offset:1024 lds
T v[162:163],v222,6144
v_exp_f32 v202, v130
W 4
T v[164:165],v223,0
buffer_load_dwordx4 v235, s[16:19], s78 offen offset:2048 lds
v_exp_f32 v203, v131
M a[136:139],v[176:179],a[192:195],a[136:139]
T v[166:167],v223,6144
v_cvt_pk_bf16_f32 v147, v202, v203
M a[136:139],v[180:183],a[196:199],a[136:139]
T v[168:169],v220,128
W 5
T v[170:171],v220,6272
T v[172:173],v221,128
W 5
M v[0:3],v[152:155],v[136:139],v[0:3]
T v[174:175],v221,6272
s_add_u32 s79, s4, -15360
s_mov_b32 m0, 76800
s_nop 0
buffer_load_dwordx4 v227, s[12:15], s79 offen nt lds
T v[176:177],v222,128
W 5
M v[96:99],v[244:247],v[136:139],v[96:99]
T v[178:179],v222,6272
T v[180:181],v223,128
W 5
M v[8:11],v[156:159],v[136:139],v[8:11]
T v[182:183],v223,6272
T v[152:153],v220,256
buffer_load_dwordx4 v228, s[12:15], s79 offen offset:1024 nt lds
W 5
M v[16:19],v[160:163],v[136:139],v[16:19]
T v[154:155],v220,6400
T v[156:157],v221,256
W 5
M v[24:27],v[164:167],v[136:139],v[24:27]
T v[158:159],v221,6400
T v[160:161],v222,256
W 5
M v[32:35],v[168:171],v[136:139],v[32:35]
T v[162:163],v222,6400
buffer_load_dwordx4 v229, s[12:15], s79 offen offset:2048 nt lds
T v[164:165],v223,256
W 5
M v[40:43],v[172:175],v[136:139],v[40:43]
T v[166:167],v223,6400
T v[168:169],v220,12288
s_add_u32 s79, s4, -9216
s_mov_b32 m0, 82944
W 5
M v[48:51],v[176:179],v[136:139],v[48:51]
T v[170:171],v220,18432
buffer_load_dwordx4 v227, s[12:15], s79 offen nt lds
W 4
M v[56:59],v[180:183],v[136:139],v[56:59]
T v[172:173],v221,12288
W 3
M v[64:67],v[152:155],v[136:139],v[64:67]
T v[174:175],v221,18432
T v[176:177],v222,12288
W 3
M v[72:75],v[156:159],v[136:139],v[72:75]
T v[178:179],v222,18432
buffer_load_dwordx4 v228, s[12:15], s79 offen offset:1024 nt lds
T v[180:181],v223,12288
v_add_u32 v216, 0, v224
v_xor_b32 v217, 32, v216
v_xor_b32 v218, 64, v216
M v[80:83],v[160:163],v[136:139],v[80:83]
T v[182:183],v223,18432
v_xor_b32 v219, 96, v216
T v[152:153],v220,12416
W 5
M v[88:91],v[164:167],v[136:139],v[88:91]
T v[154:155],v220,18560
T v[156:157],v221,12416
buffer_load_dwordx4 v229, s[12:15], s79 offen offset:2048 nt lds
W 5
M v[0:3],v[168:171],v[144:147],v[0:3]
T v[158:159],v221,18560
T v[160:161],v222,12416
W 5
M v[96:99],v[244:247],v[144:147],v[96:99]
T v[162:163],v222,18560
s_add_u32 s79, s4, -3072
T v[164:165],v223,12416
s_mov_b32 m0, 89088
W 5
M v[8:11],v[172:175],v[144:147],v[8:11]
T v[166:167],v223,18560
buffer_load_dwordx4 v227, s[12:15], s79 offen nt lds
T v[168:169],v220,12544
ds_read_b128 a[144:147], v241
ds_read_b128 a[148:151], v242
W 7
M v[16:19],v[176:179],v[144:147],v[16:19]
T v[170:171],v220,18688
T v[172:173],v221,12544
W 7
M v[24:27],v[180:183],v[144:147],v[24:27]
T v[174:175],v221,18688
T v[176:177],v222,12544
buffer_load_dwordx4 v228, s[12:15], s79 offen offset:1024 nt lds
W 7
M v[32:35],v[152:155],v[144:147],v[32:35]
T v[178:179],v222,18688
W 5
T v[180:181],v223,12544
M v[40:43],v[156:159],v[144:147],v[40:43]
T v[182:183],v223,18688
s_add_u32 m0, s43, 147456
s_waitcnt vmcnt(38)
s_barrier
T v[152:153],v216,0
s_mov_b32 m0, 89088
M v[48:51],v[160:163],v[144:147],v[48:51]
T v[154:155],v216,6144
buffer_load_dwordx4 v229, s[12:15], s79 offen offset:2048 nt lds
T v[156:157],v216,12288
s_add_u32 m0, s43, 147456
s_nop 0
buffer_load_dwordx4 v236, s[24:27], s45 offen lds
W 5
M v[56:59],v[164:167],v[144:147],v[56:59]
T v[158:159],v216,18432
T v[160:161],v217,0
s_add_u32 s79, s4, 0
s_mov_b32 m0, 92160
s_add_u32 m0, s43, 147456
M v[64:67],v[168:171],v[144:147],v[64:67]
T v[162:163],v217,6144
s_mov_b32 m0, 92160
T v[164:165],v217,12288
buffer_load_dwordx4 v227, s[12:15], s79 offen nt lds
s_add_u32 m0, s43, 147456
W 5
M v[72:75],v[172:175],v[144:147],v[72:75]
T v[166:167],v217,18432
buffer_load_dwordx4 v237, s[24:27], s45 offen offset:1024 lds
T v[168:169],v218,0
W 5
M v[80:83],v[176:179],v[144:147],v[80:83]
T v[170:171],v218,6144
s_mov_b32 m0, 92160
T v[172:173],v218,12288
W 5
M v[88:91],v[180:183],v[144:147],v[88:91]
T v[174:175],v218,18432
buffer_load_dwordx4 v228, s[12:15], s79 offen offset:1024 nt lds
T v[176:177],v219,0
W 5
M a[48:51],v[152:155],a[208:211],a[48:51]
T v[178:179],v219,6144
buffer_load_dwordx4 v229, s[12:15], s79 offen offset:2048 nt lds
M a[48:51],v[156:159],a[212:215],a[48:51]
T v[180:181],v219,12288
s_add_u32 s79, s4, 3072
T v[182:183],v219,18432
s_mov_b32 m0, 95232
s_nop 0
buffer_load_dwordx4 v227, s[12:15], s79 offen nt lds
W 5
T v[152:153],v216,128
M a[56:59],v[160:163],a[208:211],a[56:59]
T v[154:155],v216,6272
buffer_load_dwordx4 v228, s[12:15], s79 offen offset:1024 nt lds
M a[56:59],v[164:167],a[212:215],a[56:59]
T v[156:157],v216,12416
W 5
T v[158:159],v216,18560
buffer_load_dwordx4 v229, s[12:15], s79 offen offset:2048 nt lds
T v[160:161],v217,128
s_add_u32 s79, s5, -15360
s_mov_b32 m0, 125952
W 5
M a[64:67],v[168:171],a[208:211],a[64:67]
T v[162:163],v217,6272
buffer_load_dwordx4 v227, s[8:11], s79 offen nt lds
M a[64:67],v[172:175],a[212:215],a[64:67]
T v[164:165],v217,12416
W 5
T v[166:167],v217,18560
T v[168:169],v218,128
W 5
M a[72:75],v[176:179],a[208:211],a[72:75]
T v[170:171],v218,6272
buffer_load_dwordx4 v228, s[8:11], s79 offen offset:1024 nt lds
M a[72:75],v[180:183],a[212:215],a[72:75]
T v[172:173],v218,12416
W 5
T v[174:175],v218,18560
T v[176:177],v219,128
W 5
M a[80:83],v[152:155],a[208:211],a[80:83]
T v[178:179],v219,6272
buffer_load_dwordx4 v229, s[8:11], s79 offen offset:2048 nt lds
M a[80:83],v[156:159],a[212:215],a[80:83]
T v[180:181],v219,12416
v_add_u32 v220, 24576, v225
T v[182:183],v219,18560
v_xor_b32 v221, 64, v220
W 5
T v[152:153],v216,256
s_add_u32 s79, s5, -9216
s_mov_b32 m0, 132096
M a[88:91],v[160:163],a[208:211],a[88:91]
T v[154:155],v216,6400
W 5
M a[88:91],v[164:167],a[212:215],a[88:91]
T v[156:157],v216,12544
buffer_load_dwordx4 v227, s[8:11], s79 offen nt lds
T v[158:159],v216,18688
W 5
T v[160:161],v217,256
M a[96:99],v[168:171],a[208:211],a[96:99]
T v[162:163],v217,6400
W 5
M a[96:99],v[172:175],a[212:215],a[96:99]
T v[164:165],v217,12544
T v[166:167],v217,18688
buffer_load_dwordx4 v228, s[8:11], s79 offen offset:1024 nt lds
W 5
T v[168:169],v218,256
M a[104:107],v[176:179],a[208:211],a[104:107]
T v[170:171],v218,6400
W 5
M a[104:107],v[180:183],a[212:215],a[104:107]
T v[172:173],v218,12544
T v[174:175],v218,18688
W 5
T v[176:177],v219,256
buffer_load_dwordx4 v229, s[8:11], s79 offen offset:2048 nt lds
M a[112:115],v[152:155],a[208:211],a[112:115]
T v[178:179],v219,6400
W 5
M a[112:115],v[156:159],a[212:215],a[112:115]
T v[180:181],v219,12544
T v[182:183],v219,18688
s_add_u32 s79, s5, -3072
s_mov_b32 m0, 138240
W 5
s_waitcnt vmcnt(34)
s_barrier
ds_read_b128 v[152:155], v220 offset:0
M a[120:123],v[160:163],a[208:211],a[120:123]
ds_read_b128 v[156:159], v221 offset:0
buffer_load_dwordx4 v227, s[8:11], s79 offen nt lds
M a[120:123],v[164:167],a[212:215],a[120:123]
W 4
ds_read_b128 v[160:163], v220 offset:128
W 3
M a[128:131],v[168:171],a[208:211],a[128:131]
M a[128:131],v[172:175],a[212:215],a[128:131]
ds_read_b128 v[164:167], v221 offset:128
buffer_load_dwordx4 v228, s[8:11], s79 offen offset:1024 nt lds
W 2
ds_read_b128 v[168:171], v220 offset:256
M a[136:139],v[176:179],a[208:211],a[136:139]
ds_read_b128 v[172:175], v221 offset:256
M a[136:139],v[180:183],a[212:215],a[136:139]
buffer_load_dwordx4 v229, s[8:11], s79 offen offset:2048 nt lds
W 2
ds_read_b128 v[176:179], v220 offset:6144
M v[104:107],v[152:155],a[0:3],v[204:207]
ds_read_b128 v[180:183], v221 offset:6144
s_mul_i32 s77, s50, 6144
M v[104:107],v[156:159],a[8:11],v[104:107]
s_add_u32 s79, s5, 0
s_mov_b32 m0, 141312
M v[104:107],v[160:163],a[16:19],v[104:107]
s_add_u32 s78, s77, 196992
s_mov_b32 m0, 101376
s_mov_b32 m0, 141312
s_nop 0
buffer_load_dwordx4 v227, s[8:11], s79 offen nt lds
s_mov_b32 m0, 101376
W 2
buffer_load_dwordx4 v233, s[16:19], s78 offen lds
ds_read_b128 v[152:155], v220 offset:6272
W 1
ds_read_b128 v[156:159], v221 offset:6272
s_mov_b32 m0, 141312
s_mov_b32 m0, 101376
s_mov_b32 m0, 141312
ds_read_b128 v[160:163], v220 offset:6400
s_mov_b32 m0, 101376
s_mov_b32 m0, 141312
s_mov_b32 m0, 101376
s_mov_b32 m0, 141312
s_nop 0
buffer_load_dwordx4 v228, s[8:11], s79 offen offset:1024 nt lds
s_mov_b32 m0, 101376
M v[104:107],v[172:175],a[40:43],v[104:107]
buffer_load_dwordx4 v234, s[16:19], s78 offen offset:1024 lds
M v[104:107],v[168:171],a[32:35],v[104:107]
M v[104:107],v[164:167],a[24:27],v[104:107]
ds_read_b128 v[164:167], v221 offset:6400
s_mov_b32 m0, 141312
M v[112:115],v[176:179],a[0:3],v[204:207]
ds_read_b128 v[168:171], v220 offset:12288
s_mov_b32 m0, 101376
M v[112:115],v[180:183],a[8:11],v[112:115]
ds_read_b128 v[172:175], v221 offset:12288
W 3
M v[112:115],v[152:155],a[16:19],v[112:115]
s_mov_b32 m0, 141312
s_nop 0
buffer_load_dwordx4 v229, s[8:11], s79 offen offset:2048 nt lds
s_mov_b32 m0, 101376
s_nop 0
buffer_load_dwordx4 v235, s[16:19], s78 offen offset:2048 lds
s_add_u32 s78, s77, 246144
s_mov_b32 m0, 107520
v_max3_f32 v248, v104, v105, v106
v_max_f32 v248, v248, v107
v_exp_f32 v200, v104
ds_read_b128 v[176:179], v220 offset:12416
s_add_u32 s79, s5, 3072
s_mov_b32 m0, 144384
s_mov_b32 m0, 107520
ds_read_b128 v[180:183], v221 offset:12416
s_mov_b32 m0, 144384
s_nop 0
buffer_load_dwordx4 v227, s[8:11], s79 offen nt lds
s_mov_b32 m0, 107520
ds_read_b128 v[152:155], v220 offset:12544
buffer_load_dwordx4 v233, s[16:19], s78 offen lds
v_exp_f32 v201, v105
v_exp_f32 v202, v106
v_cvt_pk_bf16_f32 v136, v200, v201
W 3
M v[112:115],v[164:167],a[40:43],v[112:115]
s_mov_b32 m0, 144384
s_mov_b32 m0, 107520
M v[112:115],v[160:163],a[32:35],v[112:115]
s_mov_b32 m0, 144384
s_mov_b32 m0, 107520
M v[112:115],v[156:159],a[24:27],v[112:115]
ds_read_b128 v[156:159], v221 offset:12544
s_mov_b32 m0, 144384
M v[120:123],v[168:171],a[0:3],v[204:207]
v_exp_f32 v203, v107
M v[120:123],v[172:175],a[8:11],v[120:123]
ds_read_b128 v[160:163], v220 offset:18432
buffer_load_dwordx4 v228, s[8:11], s79 offen offset:1024 nt lds
W 2
M v[120:123],v[176:179],a[16:19],v[120:123]
ds_read_b128 v[164:167], v221 offset:18432
s_mov_b32 m0, 107520
s_nop 0
buffer_load_dwordx4 v234, s[16:19], s78 offen offset:1024 lds
v_cvt_pk_bf16_f32 v137, v202, v203
v_max3_f32 v248, v248, v112, v113
v_max3_f32 v248, v248, v114, v115
s_mov_b32 m0, 144384
s_nop 0
buffer_load_dwordx4 v229, s[8:11], s79 offen offset:2048 nt lds
s_mov_b32 m0, 107520
v_add_u32 v216, 49152, v224
ds_read_b128 v[168:171], v220 offset:18560
buffer_load_dwordx4 v235, s[16:19], s78 offen offset:2048 lds
s_add_u32 s78, s77, 590208
s_mov_b32 m0, 113664
ds_read_b128 v[172:175], v221 offset:18560
v_exp_f32 v200, v112
v_xor_b32 v217, 32, v216
ds_read_b128 v[176:179], v220 offset:18688
buffer_load_dwordx4 v233, s[16:19], s78 offen lds
v_exp_f32 v201, v113
v_exp_f32 v202, v114
v_cvt_pk_bf16_f32 v138, v200, v201
W 3
M v[120:123],v[156:159],a[40:43],v[120:123]
buffer_load_dwordx4 v234, s[16:19], s78 offen offset:1024 lds
v_xor_b32 v218, 64, v216
M v[120:123],v[152:155],a[32:35],v[120:123]
v_exp_f32 v203, v115
M v[120:123],v[180:183],a[24:27],v[120:123]
ds_read_b128 v[180:183], v221 offset:18688
buffer_load_dwordx4 v235, s[16:19], s78 offen offset:2048 lds
M v[128:131],v[160:163],a[0:3],v[204:207]
v_xor_b32 v219, 96, v216
s_add_u32 s78, s77, 442752
M v[128:131],v[164:167],a[8:11],v[128:131]
s_waitcnt vmcnt(41)
s_barrier
T v[152:153],v216,0
W 2
M v[128:131],v[168:171],a[16:19],v[128:131]
T v[154:155],v216,6144
s_mov_b32 m0, 116736
T v[156:157],v216,12288
buffer_load_dwordx4 v233, s[16:19], s78 offen lds
v_cvt_pk_bf16_f32 v139, v202, v203
v_max3_f32 v248, v248, v120, v121
T v[158:159],v216,18432
v_max3_f32 v248, v248, v122, v123
T v[160:161],v217,0
buffer_load_dwordx4 v234, s[16:19], s78 offen offset:1024 lds
T v[162:163],v217,6144
W 5
T v[164:165],v217,12288
buffer_load_dwordx4 v235, s[16:19], s78 offen offset:2048 lds
s_add_u32 s78, s77, 639360
s_mov_b32 m0, 119808
T v[166:167],v217,18432
W 5
M v[128:131],v[180:183],a[40:43],v[128:131]
T v[168:169],v218,0
buffer_load_dwordx4 v233, s[16:19], s78 offen lds
M v[128:131],v[176:179],a[32:35],v[128:131]
T v[170:171],v218,6144
W 5
M v[128:131],v[172:175],a[24:27],v[128:131]
T v[172:173],v218,12288
buffer_load_dwordx4 v234, s[16:19], s78 offen offset:1024 lds
buffer_load_dwordx4 v235, s[16:19], s78 offen offset:2048 lds
M a[48:51],v[152:155],a[224:227],a[48:51]
T v[174:175],v218,18432
s_add_u32 s79, s5, -15360
s_mov_b32 m0, 3072
s_nop 0
buffer_load_dwordx4 v227, s[12:15], s79 offen nt lds
M a[48:51],v[156:159],a[228:231],a[48:51]
T v[176:177],v219,0
W 5
T v[178:179],v219,6144
T v[180:181],v219,12288
v_max3_f32 v248, v248, v128, v129
v_max3_f32 v248, v248, v130, v131
v_cmp_lt_f32 vcc, s60, v248
M a[56:59],v[160:163],a[224:227],a[56:59]
T v[182:183],v219,18432
s_cbranch_vccnz LST0_4w_%=
LRT0_4w_%=:
M a[56:59],v[164:167],a[228:231],a[56:59]
T v[152:153],v216,128
buffer_load_dwordx4 v228, s[12:15], s79 offen offset:1024 nt lds
T v[154:155],v216,6272
v_exp_f32 v200, v120
W 5
T v[156:157],v216,12416
v_exp_f32 v201, v121
s_nop 0
v_cvt_pk_bf16_f32 v144, v200, v201
M a[64:67],v[168:171],a[224:227],a[64:67]
T v[158:159],v216,18560
W 5
M a[64:67],v[172:175],a[228:231],a[64:67]
T v[160:161],v217,128
T v[162:163],v217,6272
buffer_load_dwordx4 v229, s[12:15], s79 offen offset:2048 nt lds
v_exp_f32 v202, v122
T v[164:165],v217,12416
v_exp_f32 v203, v123
s_nop 0
v_cvt_pk_bf16_f32 v145, v202, v203
M a[72:75],v[176:179],a[224:227],a[72:75]
T v[166:167],v217,18560
W 5
M a[72:75],v[180:183],a[228:231],a[72:75]
T v[168:169],v218,128
s_add_u32 s79, s5, -9216
T v[170:171],v218,6272
s_mov_b32 m0, 9216
v_exp_f32 v200, v128
T v[172:173],v218,12416
buffer_load_dwordx4 v227, s[12:15], s79 offen nt lds
v_exp_f32 v201, v129
M a[80:83],v[152:155],a[224:227],a[80:83]
T v[174:175],v218,18560
W 5
M a[80:83],v[156:159],a[228:231],a[80:83]
T v[176:177],v219,128
v_cvt_pk_bf16_f32 v146, v200, v201
T v[178:179],v219,6272
v_exp_f32 v202, v130
v_add_u32 v220, 73728, v224
T v[180:181],v219,12416
v_exp_f32 v203, v131
s_nop 0
v_cvt_pk_bf16_f32 v147, v202, v203
M a[88:91],v[160:163],a[224:227],a[88:91]
T v[182:183],v219,18560
W 5
M a[88:91],v[164:167],a[228:231],a[88:91]
T v[152:153],v216,256
buffer_load_dwordx4 v228, s[12:15], s79 offen offset:1024 nt lds
T v[154:155],v216,6400
v_xor_b32 v221, 32, v220
v_xor_b32 v222, 64, v220
v_xor_b32 v223, 96, v220
T v[156:157],v216,12544
T v[158:159],v216,18688
W 6
M a[96:99],v[168:171],a[224:227],a[96:99]
T v[160:161],v217,256
M a[96:99],v[172:175],a[228:231],a[96:99]
T v[162:163],v217,6400
W 6
T v[164:165],v217,12544
buffer_load_dwordx4 v229, s[12:15], s79 offen offset:2048 nt lds
T v[166:167],v217,18688
W 6
M a[104:107],v[176:179],a[224:227],a[104:107]
T v[168:169],v218,256
W 5
M a[104:107],v[180:183],a[228:231],a[104:107]
T v[170:171],v218,6400
s_add_u32 s79, s5, -3072
T v[172:173],v218,12544
s_mov_b32 m0, 15360
W 5
T v[174:175],v218,18688
buffer_load_dwordx4 v227, s[12:15], s79 offen nt lds
M a[112:115],v[152:155],a[224:227],a[112:115]
T v[176:177],v219,256
W 5
M a[112:115],v[156:159],a[228:231],a[112:115]
T v[178:179],v219,6400
T v[180:181],v219,12544
W 5
T v[182:183],v219,18688
M a[120:123],v[160:163],a[224:227],a[120:123]
s_waitcnt vmcnt(37)
s_barrier
T v[152:153],v220,0
M a[120:123],v[164:167],a[228:231],a[120:123]
T v[154:155],v220,6144
buffer_load_dwordx4 v228, s[12:15], s79 offen offset:1024 nt lds
W 4
T v[156:157],v221,0
M a[128:131],v[168:171],a[224:227],a[128:131]
T v[158:159],v221,6144
W 4
M a[128:131],v[172:175],a[228:231],a[128:131]
T v[160:161],v222,0
T v[162:163],v222,6144
buffer_load_dwordx4 v229, s[12:15], s79 offen offset:2048 nt lds
W 4
T v[164:165],v223,0
M a[136:139],v[176:179],a[224:227],a[136:139]
T v[166:167],v223,6144
M a[136:139],v[180:183],a[228:231],a[136:139]
T v[168:169],v220,128
s_add_u32 s79, s5, 0
T v[170:171],v220,6272
s_mov_b32 m0, 18432
W 5
T v[172:173],v221,128
buffer_load_dwordx4 v227, s[12:15], s79 offen nt lds
M v[0:3],v[152:155],v[136:139],v[0:3]
T v[174:175],v221,6272
W 5
T v[176:177],v222,128
buffer_load_dwordx4 v228, s[12:15], s79 offen offset:1024 nt lds
M v[96:99],v[244:247],v[136:139],v[96:99]
T v[178:179],v222,6272
W 5
T v[180:181],v223,128
buffer_load_dwordx4 v229, s[12:15], s79 offen offset:2048 nt lds
M v[8:11],v[156:159],v[136:139],v[8:11]
T v[182:183],v223,6272
s_add_u32 s79, s5, 3072
T v[152:153],v220,256
s_mov_b32 m0, 21504
s_nop 0
buffer_load_dwordx4 v227, s[12:15], s79 offen nt lds
W 5
M v[16:19],v[160:163],v[136:139],v[16:19]
T v[154:155],v220,6400
T v[156:157],v221,256
buffer_load_dwordx4 v228, s[12:15], s79 offen offset:1024 nt lds
W 5
M v[24:27],v[164:167],v[136:139],v[24:27]
T v[158:159],v221,6400
T v[160:161],v222,256
buffer_load_dwordx4 v229, s[12:15], s79 offen offset:2048 nt lds
W 5
M v[32:35],v[168:171],v[136:139],v[32:35]
T v[162:163],v222,6400
s_add_u32 s79, s6, -15360
T v[164:165],v223,256
s_mov_b32 m0, 27648
s_nop 0
buffer_load_dwordx4 v227, s[8:11], s79 offen nt lds
W 5
M v[40:43],v[172:175],v[136:139],v[40:43]
T v[166:167],v223,6400
T v[168:169],v220,12288
buffer_load_dwordx4 v228, s[8:11], s79 offen offset:1024 nt lds
W 5
M v[48:51],v[176:179],v[136:139],v[48:51]
T v[170:171],v220,18432
W 4
M v[56:59],v[180:183],v[136:139],v[56:59]
buffer_load_dwordx4 v229, s[8:11], s79 offen offset:2048 nt lds
T v[172:173],v221,12288
W 3
M v[64:67],v[152:155],v[136:139],v[64:67]
T v[174:175],v221,18432
T v[176:177],v222,12288
s_add_u32 s79, s6, -9216
s_mov_b32 m0, 33792
W 3
M v[72:75],v[156:159],v[136:139],v[72:75]
T v[178:179],v222,18432
v_add_u32 v216, 122880, v225
T v[180:181],v223,12288
buffer_load_dwordx4 v227, s[8:11], s79 offen nt lds
v_xor_b32 v217, 64, v216
M v[80:83],v[160:163],v[136:139],v[80:83]
T v[182:183],v223,18432
T v[152:153],v220,12416
W 5
M v[88:91],v[164:167],v[136:139],v[88:91]
T v[154:155],v220,18560
T v[156:157],v221,12416
W 5
M v[0:3],v[168:171],v[144:147],v[0:3]
T v[158:159],v221,18560
buffer_load_dwordx4 v228, s[8:11], s79 offen offset:1024 nt lds
T v[160:161],v222,12416
W 5
M v[96:99],v[244:247],v[144:147],v[96:99]
T v[162:163],v222,18560
T v[164:165],v223,12416
W 5
M v[8:11],v[172:175],v[144:147],v[8:11]
T v[166:167],v223,18560
T v[168:169],v220,12544
buffer_load_dwordx4 v229, s[8:11], s79 offen offset:2048 nt lds
W 5
M v[16:19],v[176:179],v[144:147],v[16:19]
T v[170:171],v220,18688
T v[172:173],v221,12544
W 5
M v[24:27],v[180:183],v[144:147],v[24:27]
T v[174:175],v221,18688
s_add_u32 s79, s6, -3072
T v[176:177],v222,12544
s_mov_b32 m0, 39936
W 5
M v[32:35],v[152:155],v[144:147],v[32:35]
T v[178:179],v222,18688
buffer_load_dwordx4 v227, s[8:11], s79 offen nt lds
T v[180:181],v223,12544
W 5
M v[40:43],v[156:159],v[144:147],v[40:43]
T v[182:183],v223,18688
s_waitcnt vmcnt(32)
s_barrier
ds_read_b128 v[152:155], v216 offset:0
W 5
M v[48:51],v[160:163],v[144:147],v[48:51]
ds_read_b128 v[156:159], v217 offset:0
ds_read_b128 v[160:163], v216 offset:128
buffer_load_dwordx4 v228, s[8:11], s79 offen offset:1024 nt lds
W 5
M v[56:59],v[164:167],v[144:147],v[56:59]
W 3
M v[64:67],v[168:171],v[144:147],v[64:67]
ds_read_b128 v[164:167], v217 offset:128
W 2
M v[72:75],v[172:175],v[144:147],v[72:75]
ds_read_b128 v[168:171], v216 offset:256
buffer_load_dwordx4 v229, s[8:11], s79 offen offset:2048 nt lds
ds_read_b128 v[172:175], v217 offset:256
M v[80:83],v[176:179],v[144:147],v[80:83]
s_add_u32 s79, s6, 0
s_mov_b32 m0, 43008
M v[88:91],v[180:183],v[144:147],v[88:91]
W 2
ds_read_b128 v[176:179], v216 offset:6144
buffer_load_dwordx4 v227, s[8:11], s79 offen nt lds
M v[104:107],v[152:155],a[0:3],v[204:207]
ds_read_b128 v[180:183], v217 offset:6144
s_mul_i32 s77, s51, 6144
M v[104:107],v[156:159],a[8:11],v[104:107]
s_add_u32 s78, s77, 196992
s_mov_b32 m0, 52224
M v[104:107],v[160:163],a[16:19],v[104:107]
s_mov_b32 m0, 43008
s_mov_b32 m0, 52224
s_mov_b32 m0, 43008
s_mov_b32 m0, 52224
s_mov_b32 m0, 43008
s_mov_b32 m0, 52224
s_mov_b32 m0, 43008
s_nop 0
buffer_load_dwordx4 v228, s[8:11], s79 offen offset:1024 nt lds
s_mov_b32 m0, 52224
W 1
ds_read_b128 v[152:155], v216 offset:6272
buffer_load_dwordx4 v233, s[16:19], s78 offen lds
ds_read_b128 v[156:159], v217 offset:6272
ds_read_b128 v[160:163], v216 offset:6400
s_mov_b32 m0, 43008
s_mov_b32 m0, 52224
s_mov_b32 m0, 43008
s_mov_b32 m0, 52224
s_mov_b32 m0, 43008
s_mov_b32 m0, 52224
s_mov_b32 m0, 43008
M v[104:107],v[172:175],a[40:43],v[104:107]
buffer_load_dwordx4 v229, s[8:11], s79 offen offset:2048 nt lds
s_mov_b32 m0, 52224
M v[104:107],v[168:171],a[32:35],v[104:107]
buffer_load_dwordx4 v234, s[16:19], s78 offen offset:1024 lds
W 2
M v[104:107],v[164:167],a[24:27],v[104:107]
ds_read_b128 v[164:167], v217 offset:6400
M v[112:115],v[176:179],a[0:3],v[204:207]
ds_read_b128 v[168:171], v216 offset:12288
s_add_u32 s79, s6, 3072
M v[112:115],v[180:183],a[8:11],v[112:115]
ds_read_b128 v[172:175], v217 offset:12288
s_mov_b32 m0, 46080
M v[112:115],v[152:155],a[16:19],v[112:115]
buffer_load_dwordx4 v227, s[8:11], s79 offen nt lds
s_mov_b32 m0, 52224
s_nop 0
buffer_load_dwordx4 v235, s[16:19], s78 offen offset:2048 lds
s_add_u32 s78, s77, 246144
s_mov_b32 m0, 58368
v_max3_f32 v248, v104, v105, v106
v_max_f32 v248, v248, v107
v_exp_f32 v200, v104
W 2
ds_read_b128 v[176:179], v216 offset:12416
s_mov_b32 m0, 46080
s_mov_b32 m0, 58368
s_mov_b32 m0, 46080
ds_read_b128 v[180:183], v217 offset:12416
s_mov_b32 m0, 58368
s_mov_b32 m0, 46080
s_mov_b32 m0, 58368
ds_read_b128 v[152:155], v216 offset:12544
s_mov_b32 m0, 46080
s_nop 0
buffer_load_dwordx4 v228, s[8:11], s79 offen offset:1024 nt lds
s_mov_b32 m0, 58368
s_nop 0
buffer_load_dwordx4 v233, s[16:19], s78 offen lds
v_exp_f32 v201, v105
s_nop 0
v_cvt_pk_bf16_f32 v136, v200, v201
M v[112:115],v[164:167],a[40:43],v[112:115]
v_exp_f32 v202, v106
M v[112:115],v[160:163],a[32:35],v[112:115]
s_mov_b32 m0, 46080
s_mov_b32 m0, 58368
M v[112:115],v[156:159],a[24:27],v[112:115]
ds_read_b128 v[156:159], v217 offset:12544
W 2
M v[120:123],v[168:171],a[0:3],v[204:207]
s_mov_b32 m0, 46080
s_mov_b32 m0, 58368
s_mov_b32 m0, 46080
M v[120:123],v[172:175],a[8:11],v[120:123]
ds_read_b128 v[160:163], v216 offset:18432
buffer_load_dwordx4 v229, s[8:11], s79 offen offset:2048 nt lds
M v[120:123],v[176:179],a[16:19],v[120:123]
ds_read_b128 v[164:167], v217 offset:18432
s_mov_b32 m0, 58368
s_nop 0
buffer_load_dwordx4 v234, s[16:19], s78 offen offset:1024 lds
v_exp_f32 v203, v107
s_nop 0
v_cvt_pk_bf16_f32 v137, v202, v203
v_max3_f32 v248, v248, v112, v113
v_max3_f32 v248, v248, v114, v115
v_exp_f32 v200, v112
ds_read_b128 v[168:171], v216 offset:18560
buffer_load_dwordx4 v235, s[16:19], s78 offen offset:2048 lds
s_add_u32 s78, s77, 590208
s_mov_b32 m0, 64512
ds_read_b128 v[172:175], v217 offset:18560
v_exp_f32 v201, v113
s_nop 0
v_cvt_pk_bf16_f32 v138, v200, v201
ds_read_b128 v[176:179], v216 offset:18688
buffer_load_dwordx4 v233, s[16:19], s78 offen lds
v_exp_f32 v202, v114
v_exp_f32 v203, v115
s_nop 0
v_cvt_pk_bf16_f32 v139, v202, v203
W 3
M v[120:123],v[156:159],a[40:43],v[120:123]
buffer_load_dwordx4 v234, s[16:19], s78 offen offset:1024 lds
v_add_u32 v220, 98304, v224
M v[120:123],v[152:155],a[32:35],v[120:123]
v_xor_b32 v221, 32, v220
v_xor_b32 v222, 64, v220
M v[120:123],v[180:183],a[24:27],v[120:123]
ds_read_b128 v[180:183], v217 offset:18688
buffer_load_dwordx4 v235, s[16:19], s78 offen offset:2048 lds
M v[128:131],v[160:163],a[0:3],v[204:207]
v_xor_b32 v223, 96, v220
s_add_u32 s78, s77, 442752
M v[128:131],v[164:167],a[8:11],v[128:131]
s_waitcnt vmcnt(39)
s_barrier
T v[152:153],v220,0
W 2
M v[128:131],v[168:171],a[16:19],v[128:131]
T v[154:155],v220,6144
s_mov_b32 m0, 67584
T v[156:157],v220,12288
buffer_load_dwordx4 v233, s[16:19], s78 offen lds
v_max3_f32 v248, v248, v120, v121
v_max3_f32 v248, v248, v122, v123
T v[158:159],v220,18432
T v[160:161],v221,0
buffer_load_dwordx4 v234, s[16:19], s78 offen offset:1024 lds
T v[162:163],v221,6144
W 5
T v[164:165],v221,12288
buffer_load_dwordx4 v235, s[16:19], s78 offen offset:2048 lds
s_add_u32 s78, s77, 639360
s_mov_b32 m0, 70656
T v[166:167],v221,18432
W 5
M v[128:131],v[180:183],a[40:43],v[128:131]
T v[168:169],v222,0
buffer_load_dwordx4 v233, s[16:19], s78 offen lds
M v[128:131],v[176:179],a[32:35],v[128:131]
T v[170:171],v222,6144
W 5
M v[128:131],v[172:175],a[24:27],v[128:131]
T v[172:173],v222,12288
buffer_load_dwordx4 v234, s[16:19], s78 offen offset:1024 lds
buffer_load_dwordx4 v235, s[16:19], s78 offen offset:2048 lds
M a[48:51],v[152:155],a[240:243],a[48:51]
T v[174:175],v222,18432
s_add_u32 s79, s6, -15360
s_mov_b32 m0, 76800
s_nop 0
buffer_load_dwordx4 v227, s[12:15], s79 offen nt lds
M a[48:51],v[156:159],a[244:247],a[48:51]
T v[176:177],v223,0
W 5
T v[178:179],v223,6144
T v[180:181],v223,12288
v_max3_f32 v248, v248, v128, v129
v_max3_f32 v248, v248, v130, v131
v_cmp_lt_f32 vcc, s60, v248
M a[56:59],v[160:163],a[240:243],a[56:59]
T v[182:183],v223,18432
s_cbranch_vccnz LST0_5w_%=
LRT0_5w_%=:
M a[56:59],v[164:167],a[244:247],a[56:59]
T v[152:153],v220,128
buffer_load_dwordx4 v228, s[12:15], s79 offen offset:1024 nt lds
T v[154:155],v220,6272
v_exp_f32 v200, v120
W 5
T v[156:157],v220,12416
v_exp_f32 v201, v121
s_nop 0
v_cvt_pk_bf16_f32 v144, v200, v201
M a[64:67],v[168:171],a[240:243],a[64:67]
T v[158:159],v220,18560
W 5
M a[64:67],v[172:175],a[244:247],a[64:67]
T v[160:161],v221,128
T v[162:163],v221,6272
buffer_load_dwordx4 v229, s[12:15], s79 offen offset:2048 nt lds
v_exp_f32 v202, v122
T v[164:165],v221,12416
v_exp_f32 v203, v123
s_nop 0
v_cvt_pk_bf16_f32 v145, v202, v203
M a[72:75],v[176:179],a[240:243],a[72:75]
T v[166:167],v221,18560
W 5
M a[72:75],v[180:183],a[244:247],a[72:75]
T v[168:169],v222,128
s_add_u32 s79, s6, -9216
T v[170:171],v222,6272
s_mov_b32 m0, 82944
v_exp_f32 v200, v128
T v[172:173],v222,12416
buffer_load_dwordx4 v227, s[12:15], s79 offen nt lds
v_exp_f32 v201, v129
M a[80:83],v[152:155],a[240:243],a[80:83]
T v[174:175],v222,18560
W 5
M a[80:83],v[156:159],a[244:247],a[80:83]
T v[176:177],v223,128
v_cvt_pk_bf16_f32 v146, v200, v201
T v[178:179],v223,6272
v_exp_f32 v202, v130
v_add_u32 v216, 0, v224
T v[180:181],v223,12416
v_exp_f32 v203, v131
s_nop 0
v_cvt_pk_bf16_f32 v147, v202, v203
M a[88:91],v[160:163],a[240:243],a[88:91]
T v[182:183],v223,18560
W 5
M a[88:91],v[164:167],a[244:247],a[88:91]
T v[152:153],v220,256
buffer_load_dwordx4 v228, s[12:15], s79 offen offset:1024 nt lds
T v[154:155],v220,6400
v_xor_b32 v217, 32, v216
v_xor_b32 v218, 64, v216
v_xor_b32 v219, 96, v216
T v[156:157],v220,12544
T v[158:159],v220,18688
W 6
M a[96:99],v[168:171],a[240:243],a[96:99]
T v[160:161],v221,256
M a[96:99],v[172:175],a[244:247],a[96:99]
T v[162:163],v221,6400
W 6
T v[164:165],v221,12544
buffer_load_dwordx4 v229, s[12:15], s79 offen offset:2048 nt lds
T v[166:167],v221,18688
W 6
M a[104:107],v[176:179],a[240:243],a[104:107]
T v[168:169],v222,256
W 5
M a[104:107],v[180:183],a[244:247],a[104:107]
T v[170:171],v222,6400
s_add_u32 s79, s6, -3072
T v[172:173],v222,12544
s_mov_b32 m0, 89088
W 5
T v[174:175],v222,18688
buffer_load_dwordx4 v227, s[12:15], s79 offen nt lds
M a[112:115],v[152:155],a[240:243],a[112:115]
T v[176:177],v223,256
W 5
M a[112:115],v[156:159],a[244:247],a[112:115]
T v[178:179],v223,6400
T v[180:181],v223,12544
W 5
T v[182:183],v223,18688
M a[120:123],v[160:163],a[240:243],a[120:123]
s_waitcnt vmcnt(37)
s_barrier
T v[152:153],v216,0
M a[120:123],v[164:167],a[244:247],a[120:123]
T v[154:155],v216,6144
buffer_load_dwordx4 v228, s[12:15], s79 offen offset:1024 nt lds
W 4
T v[156:157],v217,0
M a[128:131],v[168:171],a[240:243],a[128:131]
T v[158:159],v217,6144
W 4
M a[128:131],v[172:175],a[244:247],a[128:131]
T v[160:161],v218,0
T v[162:163],v218,6144
buffer_load_dwordx4 v229, s[12:15], s79 offen offset:2048 nt lds
W 4
T v[164:165],v219,0
M a[136:139],v[176:179],a[240:243],a[136:139]
T v[166:167],v219,6144
M a[136:139],v[180:183],a[244:247],a[136:139]
T v[168:169],v216,128
s_add_u32 s79, s6, 0
T v[170:171],v216,6272
s_mov_b32 m0, 92160
W 5
T v[172:173],v217,128
buffer_load_dwordx4 v227, s[12:15], s79 offen nt lds
M v[0:3],v[152:155],v[136:139],v[0:3]
T v[174:175],v217,6272
s_mul_i32 s77, s44, 6144
T v[176:177],v218,128
s_add_u32 s78, s77, 196992
s_mov_b32 m0, 125952
s_nop 0
buffer_load_dwordx4 v233, s[16:19], s78 offen lds
M v[96:99],v[244:247],v[136:139],v[96:99]
T v[178:179],v218,6272
s_mov_b32 m0, 92160
T v[180:181],v219,128
s_mov_b32 m0, 125952
s_mov_b32 m0, 92160
s_mov_b32 m0, 125952
M v[8:11],v[156:159],v[136:139],v[8:11]
T v[182:183],v219,6272
s_mov_b32 m0, 92160
T v[152:153],v216,256
buffer_load_dwordx4 v228, s[12:15], s79 offen offset:1024 nt lds
s_mov_b32 m0, 125952
W 5
M v[16:19],v[160:163],v[136:139],v[16:19]
T v[154:155],v216,6400
buffer_load_dwordx4 v234, s[16:19], s78 offen offset:1024 lds
T v[156:157],v217,256
W 5
M v[24:27],v[164:167],v[136:139],v[24:27]
T v[158:159],v217,6400
s_mov_b32 m0, 92160
T v[160:161],v218,256
s_mov_b32 m0, 125952
s_mov_b32 m0, 92160
s_mov_b32 m0, 125952
M v[32:35],v[168:171],v[136:139],v[32:35]
T v[162:163],v218,6400
s_mov_b32 m0, 92160
T v[164:165],v219,256
buffer_load_dwordx4 v229, s[12:15], s79 offen offset:2048 nt lds
s_mov_b32 m0, 125952
W 5
M v[40:43],v[172:175],v[136:139],v[40:43]
T v[166:167],v219,6400
buffer_load_dwordx4 v235, s[16:19], s78 offen offset:2048 lds
T v[168:169],v216,12288
s_add_u32 s78, s77, 246144
s_mov_b32 m0, 132096
W 5
M v[48:51],v[176:179],v[136:139],v[48:51]
T v[170:171],v216,18432
s_add_u32 s79, s6, 3072
s_mov_b32 m0, 95232
s_mov_b32 m0, 132096
s_mov_b32 m0, 95232
s_mov_b32 m0, 132096
M v[56:59],v[180:183],v[136:139],v[56:59]
s_mov_b32 m0, 95232
s_nop 0
buffer_load_dwordx4 v227, s[12:15], s79 offen nt lds
T v[172:173],v217,12288
s_mov_b32 m0, 132096
s_nop 0
buffer_load_dwordx4 v233, s[16:19], s78 offen lds
W 3
M v[64:67],v[152:155],v[136:139],v[64:67]
T v[174:175],v217,18432
T v[176:177],v218,12288
s_mov_b32 m0, 95232
s_mov_b32 m0, 132096
s_mov_b32 m0, 95232
M v[72:75],v[156:159],v[136:139],v[72:75]
T v[178:179],v218,18432
v_add_u32 v220, 24576, v225
T v[180:181],v219,12288
buffer_load_dwordx4 v228, s[12:15], s79 offen offset:1024 nt lds
v_xor_b32 v221, 64, v220
s_mov_b32 m0, 132096
M v[80:83],v[160:163],v[136:139],v[80:83]
T v[182:183],v219,18432
buffer_load_dwordx4 v234, s[16:19], s78 offen offset:1024 lds
T v[152:153],v216,12416
W 5
M v[88:91],v[164:167],v[136:139],v[88:91]
T v[154:155],v216,18560
s_mov_b32 m0, 95232
T v[156:157],v217,12416
s_mov_b32 m0, 132096
s_mov_b32 m0, 95232
s_mov_b32 m0, 132096
M v[0:3],v[168:171],v[144:147],v[0:3]
T v[158:159],v217,18560
s_mov_b32 m0, 95232
T v[160:161],v218,12416
buffer_load_dwordx4 v229, s[12:15], s79 offen offset:2048 nt lds
s_mov_b32 m0, 132096
W 5
M v[96:99],v[244:247],v[144:147],v[96:99]
T v[162:163],v218,18560
buffer_load_dwordx4 v235, s[16:19], s78 offen offset:2048 lds
T v[164:165],v219,12416
s_add_u32 s78, s77, 590208
s_mov_b32 m0, 138240
W 5
M v[8:11],v[172:175],v[144:147],v[8:11]
T v[166:167],v219,18560
buffer_load_dwordx4 v233, s[16:19], s78 offen lds
T v[168:169],v216,12544
W 5
M v[16:19],v[176:179],v[144:147],v[16:19]
T v[170:171],v216,18688
buffer_load_dwordx4 v234, s[16:19], s78 offen offset:1024 lds
T v[172:173],v217,12544
W 5
M v[24:27],v[180:183],v[144:147],v[24:27]
T v[174:175],v217,18688
buffer_load_dwordx4 v235, s[16:19], s78 offen offset:2048 lds
T v[176:177],v218,12544
s_add_u32 s78, s77, 442752
s_mov_b32 m0, 141312
W 5
M v[32:35],v[152:155],v[144:147],v[32:35]
T v[178:179],v218,18688
buffer_load_dwordx4 v233, s[16:19], s78 offen lds
T v[180:181],v219,12544
W 5
M v[40:43],v[156:159],v[144:147],v[40:43]
T v[182:183],v219,18688
buffer_load_dwordx4 v234, s[16:19], s78 offen offset:1024 lds
s_waitcnt vmcnt(37)
s_barrier
ds_read_b128 v[152:155], v220 offset:0
W 5
M v[48:51],v[160:163],v[144:147],v[48:51]
ds_read_b128 v[156:159], v221 offset:0
buffer_load_dwordx4 v235, s[16:19], s78 offen offset:2048 lds
ds_read_b128 v[160:163], v220 offset:128
s_add_u32 s78, s77, 639360
s_mov_b32 m0, 144384
W 5
M v[56:59],v[164:167],v[144:147],v[56:59]
buffer_load_dwordx4 v233, s[16:19], s78 offen lds
W 3
M v[64:67],v[168:171],v[144:147],v[64:67]
buffer_load_dwordx4 v234, s[16:19], s78 offen offset:1024 lds
ds_read_b128 v[164:167], v221 offset:128
W 2
M v[72:75],v[172:175],v[144:147],v[72:75]
ds_read_b128 v[168:171], v220 offset:256
buffer_load_dwordx4 v235, s[16:19], s78 offen offset:2048 lds
ds_read_b128 v[172:175], v221 offset:256
M v[80:83],v[176:179],v[144:147],v[80:83]
M v[88:91],v[180:183],v[144:147],v[88:91]
W 2
ds_read_b128 v[176:179], v220 offset:6144
M v[104:107],v[152:155],a[0:3],v[204:207]
ds_read_b128 v[180:183], v221 offset:6144
s_add_u32 s79, s7, -15360
s_mov_b32 m0, 101376
s_nop 0
buffer_load_dwordx4 v227, s[8:11], s79 offen nt lds
M v[104:107],v[156:159],a[8:11],v[104:107]
W 2
M v[104:107],v[160:163],a[16:19],v[104:107]
ds_read_b128 v[152:155], v220 offset:6272
buffer_load_dwordx4 v228, s[8:11], s79 offen offset:1024 nt lds
W 1
ds_read_b128 v[156:159], v221 offset:6272
ds_read_b128 v[160:163], v220 offset:6400
M v[104:107],v[172:175],a[40:43],v[104:107]
M v[104:107],v[168:171],a[32:35],v[104:107]
buffer_load_dwordx4 v229, s[8:11], s79 offen offset:2048 nt lds
M v[104:107],v[164:167],a[24:27],v[104:107]
ds_read_b128 v[164:167], v221 offset:6400
W 2
M v[112:115],v[176:179],a[0:3],v[204:207]
ds_read_b128 v[168:171], v220 offset:12288
M v[112:115],v[180:183],a[8:11],v[112:115]
ds_read_b128 v[172:175], v221 offset:12288
s_add_u32 s79, s7, -9216
M v[112:115],v[152:155],a[16:19],v[112:115]
s_mov_b32 m0, 107520
s_nop 0
buffer_load_dwordx4 v227, s[8:11], s79 offen nt lds
v_max3_f32 v248, v104, v105, v106
v_max_f32 v248, v248, v107
v_exp_f32 v200, v104
v_exp_f32 v201, v105
ds_read_b128 v[176:179], v220 offset:12416
v_exp_f32 v202, v106
v_cvt_pk_bf16_f32 v136, v200, v201
ds_read_b128 v[180:183], v221 offset:12416
v_exp_f32 v203, v107
s_nop 0
v_cvt_pk_bf16_f32 v137, v202, v203
ds_read_b128 v[152:155], v220 offset:12544
v_add_u32 v216, 49152, v224
v_xor_b32 v217, 32, v216
v_xor_b32 v218, 64, v216
buffer_load_dwordx4 v228, s[8:11], s79 offen offset:1024 nt lds
v_xor_b32 v219, 96, v216
W 3
M v[112:115],v[164:167],a[40:43],v[112:115]
M v[112:115],v[160:163],a[32:35],v[112:115]
M v[112:115],v[156:159],a[24:27],v[112:115]
ds_read_b128 v[156:159], v221 offset:12544
W 2
M v[120:123],v[168:171],a[0:3],v[204:207]
ds_read_b128 v[160:163], v220 offset:18432
M v[120:123],v[172:175],a[8:11],v[120:123]
ds_read_b128 v[164:167], v221 offset:18432
buffer_load_dwordx4 v229, s[8:11], s79 offen offset:2048 nt lds
M v[120:123],v[176:179],a[16:19],v[120:123]
v_max3_f32 v248, v248, v112, v113
v_max3_f32 v248, v248, v114, v115
v_exp_f32 v200, v112
s_add_u32 s79, s7, -3072
s_mov_b32 m0, 113664
v_exp_f32 v201, v113
ds_read_b128 v[168:171], v220 offset:18560
v_exp_f32 v202, v114
v_cvt_pk_bf16_f32 v138, v200, v201
ds_read_b128 v[172:175], v221 offset:18560
buffer_load_dwordx4 v227, s[8:11], s79 offen nt lds
v_exp_f32 v203, v115
ds_read_b128 v[176:179], v220 offset:18688
v_cvt_pk_bf16_f32 v139, v202, v203
W 3
M v[120:123],v[156:159],a[40:43],v[120:123]
M v[120:123],v[152:155],a[32:35],v[120:123]
M v[120:123],v[180:183],a[24:27],v[120:123]
ds_read_b128 v[180:183], v221 offset:18688
buffer_load_dwordx4 v228, s[8:11], s79 offen offset:1024 nt lds
M v[128:131],v[160:163],a[0:3],v[204:207]
s_waitcnt vmcnt(38)
s_barrier
T v[152:153],v216,0
M v[128:131],v[164:167],a[8:11],v[128:131]
T v[154:155],v216,6144
W 3
M v[128:131],v[168:171],a[16:19],v[128:131]
T v[156:157],v216,12288
T v[158:159],v216,18432
v_max3_f32 v248, v248, v120, v121
v_max3_f32 v248, v248, v122, v123
buffer_load_dwordx4 v229, s[8:11], s79 offen offset:2048 nt lds
T v[160:161],v217,0
W 4
T v[162:163],v217,6144
T v[164:165],v217,12288
T v[166:167],v217,18432
s_add_u32 s79, s7, 0
s_mov_b32 m0, 116736
T v[168:169],v218,0
W 5
M v[128:131],v[180:183],a[40:43],v[128:131]
T v[170:171],v218,6144
buffer_load_dwordx4 v227, s[8:11], s79 offen nt lds
M v[128:131],v[176:179],a[32:35],v[128:131]
M v[128:131],v[172:175],a[24:27],v[128:131]
T v[172:173],v218,12288
T v[174:175],v218,18432
M a[48:51],v[152:155],v[184:187],a[48:51]
T v[176:177],v219,0
s_mul_i32 s77, s45, 6144
M a[48:51],v[156:159],v[188:191],a[48:51]
T v[178:179],v219,6144
s_add_u32 s78, s77, 196992
T v[180:181],v219,12288
buffer_load_dwordx4 v228, s[8:11], s79 offen offset:1024 nt lds
s_mov_b32 m0, 3072
W 5
T v[182:183],v219,18432
buffer_load_dwordx4 v233, s[16:19], s78 offen lds
v_max3_f32 v248, v248, v128, v129
v_max3_f32 v248, v248, v130, v131
M a[56:59],v[160:163],v[184:187],a[56:59]
T v[152:153],v216,128
v_cmp_lt_f32 vcc, s60, v248
M a[56:59],v[164:167],v[188:191],a[56:59]
T v[154:155],v216,6272
s_mov_b32 m0, 116736
T v[156:157],v216,12416
s_mov_b32 m0, 3072
s_mov_b32 m0, 116736
s_mov_b32 m0, 3072
T v[158:159],v216,18560
s_mov_b32 m0, 116736
s_nop 0
buffer_load_dwordx4 v229, s[8:11], s79 offen offset:2048 nt lds
s_mov_b32 m0, 3072
M a[64:67],v[168:171],v[184:187],a[64:67]
T v[160:161],v217,128
W 5
M a[64:67],v[172:175],v[188:191],a[64:67]
T v[162:163],v217,6272
buffer_load_dwordx4 v234, s[16:19], s78 offen offset:1024 lds
T v[164:165],v217,12416
s_add_u32 s79, s7, 3072
s_mov_b32 m0, 119808
s_mov_b32 m0, 3072
T v[166:167],v217,18560
s_mov_b32 m0, 119808
s_mov_b32 m0, 3072
s_mov_b32 m0, 119808
M a[72:75],v[176:179],v[184:187],a[72:75]
T v[168:169],v218,128
buffer_load_dwordx4 v227, s[8:11], s79 offen nt lds
M a[72:75],v[180:183],v[188:191],a[72:75]
T v[170:171],v218,6272
s_mov_b32 m0, 3072
T v[172:173],v218,12416
buffer_load_dwordx4 v235, s[16:19], s78 offen offset:2048 lds
s_add_u32 s78, s77, 246144
s_mov_b32 m0, 9216
T v[174:175],v218,18560
s_mov_b32 m0, 119808
s_mov_b32 m0, 9216
W 5
M a[80:83],v[152:155],v[184:187],a[80:83]
T v[176:177],v219,128
s_mov_b32 m0, 119808
M a[80:83],v[156:159],v[188:191],a[80:83]
T v[178:179],v219,6272
buffer_load_dwordx4 v228, s[8:11], s79 offen offset:1024 nt lds
s_cbranch_vccnz LST0_6w_%=
LRT0_6w_%=:
T v[180:181],v219,12416
s_mov_b32 m0, 9216
s_nop 0
buffer_load_dwordx4 v233, s[16:19], s78 offen lds
v_add_u32 v220, 73728, v224
T v[182:183],v219,18560
v_exp_f32 v200, v120
v_xor_b32 v221, 32, v220
M a[88:91],v[160:163],v[184:187],a[88:91]
T v[152:153],v216,256
W 5
M a[88:91],v[164:167],v[188:191],a[88:91]
T v[154:155],v216,6400
s_mov_b32 m0, 119808
T v[156:157],v216,12544
buffer_load_dwordx4 v229, s[8:11], s79 offen offset:2048 nt lds
s_mov_b32 m0, 9216
v_xor_b32 v222, 64, v220
T v[158:159],v216,18688
buffer_load_dwordx4 v234, s[16:19], s78 offen offset:1024 lds
v_exp_f32 v201, v121
M a[96:99],v[168:171],v[184:187],a[96:99]
T v[160:161],v217,256
v_cvt_pk_bf16_f32 v144, v200, v201
M a[96:99],v[172:175],v[188:191],a[96:99]
T v[162:163],v217,6400
buffer_load_dwordx4 v235, s[16:19], s78 offen offset:2048 lds
T v[164:165],v217,12544
s_add_u32 s78, s77, 590208
s_mov_b32 m0, 15360
v_xor_b32 v223, 96, v220
T v[166:167],v217,18688
buffer_load_dwordx4 v233, s[16:19], s78 offen lds
W 5
M a[104:107],v[176:179],v[184:187],a[104:107]
T v[168:169],v218,256
M a[104:107],v[180:183],v[188:191],a[104:107]
T v[170:171],v218,6400
buffer_load_dwordx4 v234, s[16:19], s78 offen offset:1024 lds
T v[172:173],v218,12544
v_exp_f32 v202, v122
W 5
T v[174:175],v218,18688
buffer_load_dwordx4 v235, s[16:19], s78 offen offset:2048 lds
s_add_u32 s78, s77, 442752
s_mov_b32 m0, 18432
M a[112:115],v[152:155],v[184:187],a[112:115]
T v[176:177],v219,256
W 5
M a[112:115],v[156:159],v[188:191],a[112:115]
T v[178:179],v219,6400
buffer_load_dwordx4 v233, s[16:19], s78 offen lds
T v[180:181],v219,12544
v_exp_f32 v203, v123
s_nop 0
v_cvt_pk_bf16_f32 v145, v202, v203
T v[182:183],v219,18688
buffer_load_dwordx4 v234, s[16:19], s78 offen offset:1024 lds
v_exp_f32 v200, v128
M a[120:123],v[160:163],v[184:187],a[120:123]
s_waitcnt vmcnt(36)
s_barrier
T v[152:153],v220,0
M a[120:123],v[164:167],v[188:191],a[120:123]
T v[154:155],v220,6144
buffer_load_dwordx4 v235, s[16:19], s78 offen offset:2048 lds
s_add_u32 s78, s77, 639360
s_mov_b32 m0, 21504
v_exp_f32 v201, v129
T v[156:157],v221,0
buffer_load_dwordx4 v233, s[16:19], s78 offen lds
v_cvt_pk_bf16_f32 v146, v200, v201
W 4
M a[128:131],v[168:171],v[184:187],a[128:131]
T v[158:159],v221,6144
M a[128:131],v[172:175],v[188:191],a[128:131]
T v[160:161],v222,0
buffer_load_dwordx4 v234, s[16:19], s78 offen offset:1024 lds
T v[162:163],v222,6144
v_exp_f32 v202, v130
W 4
T v[164:165],v223,0
buffer_load_dwordx4 v235, s[16:19], s78 offen offset:2048 lds
v_exp_f32 v203, v131
M a[136:139],v[176:179],v[184:187],a[136:139]
T v[166:167],v223,6144
v_cvt_pk_bf16_f32 v147, v202, v203
M a[136:139],v[180:183],v[188:191],a[136:139]
T v[168:169],v220,128
W 5
T v[170:171],v220,6272
T v[172:173],v221,128
W 5
M v[0:3],v[152:155],v[136:139],v[0:3]
T v[174:175],v221,6272
s_add_u32 s79, s7, -15360
s_mov_b32 m0, 27648
s_nop 0
buffer_load_dwordx4 v227, s[12:15], s79 offen nt lds
T v[176:177],v222,128
W 5
M v[96:99],v[244:247],v[136:139],v[96:99]
T v[178:179],v222,6272
T v[180:181],v223,128
W 5
M v[8:11],v[156:159],v[136:139],v[8:11]
T v[182:183],v223,6272
T v[152:153],v220,256
buffer_load_dwordx4 v228, s[12:15], s79 offen offset:1024 nt lds
W 5
M v[16:19],v[160:163],v[136:139],v[16:19]
T v[154:155],v220,6400
T v[156:157],v221,256
W 5
M v[24:27],v[164:167],v[136:139],v[24:27]
T v[158:159],v221,6400
T v[160:161],v222,256
W 5
M v[32:35],v[168:171],v[136:139],v[32:35]
T v[162:163],v222,6400
buffer_load_dwordx4 v229, s[12:15], s79 offen offset:2048 nt lds
T v[164:165],v223,256
W 5
M v[40:43],v[172:175],v[136:139],v[40:43]
T v[166:167],v223,6400
T v[168:169],v220,12288
s_add_u32 s79, s7, -9216
s_mov_b32 m0, 33792
W 5
M v[48:51],v[176:179],v[136:139],v[48:51]
T v[170:171],v220,18432
buffer_load_dwordx4 v227, s[12:15], s79 offen nt lds
W 4
M v[56:59],v[180:183],v[136:139],v[56:59]
T v[172:173],v221,12288
W 3
M v[64:67],v[152:155],v[136:139],v[64:67]
T v[174:175],v221,18432
T v[176:177],v222,12288
W 3
M v[72:75],v[156:159],v[136:139],v[72:75]
T v[178:179],v222,18432
buffer_load_dwordx4 v228, s[12:15], s79 offen offset:1024 nt lds
T v[180:181],v223,12288
v_add_u32 v216, 122880, v224
v_xor_b32 v217, 32, v216
v_xor_b32 v218, 64, v216
M v[80:83],v[160:163],v[136:139],v[80:83]
T v[182:183],v223,18432
v_xor_b32 v219, 96, v216
T v[152:153],v220,12416
W 5
M v[88:91],v[164:167],v[136:139],v[88:91]
T v[154:155],v220,18560
T v[156:157],v221,12416
buffer_load_dwordx4 v229, s[12:15], s79 offen offset:2048 nt lds
W 5
M v[0:3],v[168:171],v[144:147],v[0:3]
T v[158:159],v221,18560
T v[160:161],v222,12416
W 5
M v[96:99],v[244:247],v[144:147],v[96:99]
T v[162:163],v222,18560
s_add_u32 s79, s7, -3072
T v[164:165],v223,12416
s_mov_b32 m0, 39936
W 5
M v[8:11],v[172:175],v[144:147],v[8:11]
T v[166:167],v223,18560
buffer_load_dwordx4 v227, s[12:15], s79 offen nt lds
T v[168:169],v220,12544
ds_read_b128 a[160:163], v241
ds_read_b128 a[164:167], v242
W 7
M v[16:19],v[176:179],v[144:147],v[16:19]
T v[170:171],v220,18688
T v[172:173],v221,12544
W 7
M v[24:27],v[180:183],v[144:147],v[24:27]
T v[174:175],v221,18688
T v[176:177],v222,12544
buffer_load_dwordx4 v228, s[12:15], s79 offen offset:1024 nt lds
W 7
M v[32:35],v[152:155],v[144:147],v[32:35]
T v[178:179],v222,18688
W 5
T v[180:181],v223,12544
M v[40:43],v[156:159],v[144:147],v[40:43]
T v[182:183],v223,18688
W 5
s_waitcnt vmcnt(38)
s_barrier
T v[152:153],v216,0
M v[48:51],v[160:163],v[144:147],v[48:51]
T v[154:155],v216,6144
buffer_load_dwordx4 v229, s[12:15], s79 offen offset:2048 nt lds
T v[156:157],v216,12288
W 5
M v[56:59],v[164:167],v[144:147],v[56:59]
T v[158:159],v216,18432
T v[160:161],v217,0
s_add_u32 s79, s7, 0
s_mov_b32 m0, 43008
W 5
M v[64:67],v[168:171],v[144:147],v[64:67]
T v[162:163],v217,6144
T v[164:165],v217,12288
buffer_load_dwordx4 v227, s[12:15], s79 offen nt lds
W 5
M v[72:75],v[172:175],v[144:147],v[72:75]
T v[166:167],v217,18432
T v[168:169],v218,0
W 5
M v[80:83],v[176:179],v[144:147],v[80:83]
T v[170:171],v218,6144
T v[172:173],v218,12288
W 5
M v[88:91],v[180:183],v[144:147],v[88:91]
T v[174:175],v218,18432
buffer_load_dwordx4 v228, s[12:15], s79 offen offset:1024 nt lds
T v[176:177],v219,0
W 5
M a[48:51],v[152:155],a[144:147],a[48:51]
T v[178:179],v219,6144
buffer_load_dwordx4 v229, s[12:15], s79 offen offset:2048 nt lds
M a[48:51],v[156:159],a[148:151],a[48:51]
T v[180:181],v219,12288
s_add_u32 s79, s7, 3072
T v[182:183],v219,18432
s_mov_b32 m0, 46080
s_nop 0
buffer_load_dwordx4 v227, s[12:15], s79 offen nt lds
W 5
T v[152:153],v216,128
M a[56:59],v[160:163],a[144:147],a[56:59]
T v[154:155],v216,6272
buffer_load_dwordx4 v228, s[12:15], s79 offen offset:1024 nt lds
M a[56:59],v[164:167],a[148:151],a[56:59]
T v[156:157],v216,12416
W 5
T v[158:159],v216,18560
buffer_load_dwordx4 v229, s[12:15], s79 offen offset:2048 nt lds
T v[160:161],v217,128
s_add_u32 s79, s0, 181248
s_mov_b32 m0, 52224
W 5
M a[64:67],v[168:171],a[144:147],a[64:67]
T v[162:163],v217,6272
buffer_load_dwordx4 v227, s[8:11], s79 offen nt lds
M a[64:67],v[172:175],a[148:151],a[64:67]
T v[164:165],v217,12416
W 5
T v[166:167],v217,18560
T v[168:169],v218,128
W 5
M a[72:75],v[176:179],a[144:147],a[72:75]
T v[170:171],v218,6272
buffer_load_dwordx4 v228, s[8:11], s79 offen offset:1024 nt lds
M a[72:75],v[180:183],a[148:151],a[72:75]
T v[172:173],v218,12416
W 5
T v[174:175],v218,18560
T v[176:177],v219,128
W 5
M a[80:83],v[152:155],a[144:147],a[80:83]
T v[178:179],v219,6272
buffer_load_dwordx4 v229, s[8:11], s79 offen offset:2048 nt lds
M a[80:83],v[156:159],a[148:151],a[80:83]
T v[180:181],v219,12416
v_add_u32 v220, 98304, v225
T v[182:183],v219,18560
v_xor_b32 v221, 64, v220
W 5
T v[152:153],v216,256
s_add_u32 s79, s0, 187392
s_mov_b32 m0, 58368
M a[88:91],v[160:163],a[144:147],a[88:91]
T v[154:155],v216,6400
W 5
M a[88:91],v[164:167],a[148:151],a[88:91]
T v[156:157],v216,12544
buffer_load_dwordx4 v227, s[8:11], s79 offen nt lds
T v[158:159],v216,18688
W 5
T v[160:161],v217,256
M a[96:99],v[168:171],a[144:147],a[96:99]
T v[162:163],v217,6400
W 5
M a[96:99],v[172:175],a[148:151],a[96:99]
T v[164:165],v217,12544
T v[166:167],v217,18688
buffer_load_dwordx4 v228, s[8:11], s79 offen offset:1024 nt lds
W 5
T v[168:169],v218,256
M a[104:107],v[176:179],a[144:147],a[104:107]
T v[170:171],v218,6400
W 5
M a[104:107],v[180:183],a[148:151],a[104:107]
T v[172:173],v218,12544
T v[174:175],v218,18688
W 5
T v[176:177],v219,256
buffer_load_dwordx4 v229, s[8:11], s79 offen offset:2048 nt lds
M a[112:115],v[152:155],a[144:147],a[112:115]
T v[178:179],v219,6400
W 5
M a[112:115],v[156:159],a[148:151],a[112:115]
T v[180:181],v219,12544
T v[182:183],v219,18688
s_add_u32 s79, s0, 193536
s_mov_b32 m0, 64512
W 5
s_waitcnt vmcnt(32)
s_barrier
ds_read_b128 v[152:155], v220 offset:0
M a[120:123],v[160:163],a[144:147],a[120:123]
ds_read_b128 v[156:159], v221 offset:0
buffer_load_dwordx4 v227, s[8:11], s79 offen nt lds
M a[120:123],v[164:167],a[148:151],a[120:123]
W 4
ds_read_b128 v[160:163], v220 offset:128
W 3
M a[128:131],v[168:171],a[144:147],a[128:131]
M a[128:131],v[172:175],a[148:151],a[128:131]
ds_read_b128 v[164:167], v221 offset:128
buffer_load_dwordx4 v228, s[8:11], s79 offen offset:1024 nt lds
W 2
ds_read_b128 v[168:171], v220 offset:256
M a[136:139],v[176:179],a[144:147],a[136:139]
ds_read_b128 v[172:175], v221 offset:256
M a[136:139],v[180:183],a[148:151],a[136:139]
buffer_load_dwordx4 v229, s[8:11], s79 offen offset:2048 nt lds
W 2
ds_read_b128 v[176:179], v220 offset:6144
M v[104:107],v[152:155],a[0:3],v[204:207]
ds_read_b128 v[180:183], v221 offset:6144
s_add_u32 s78, s66, 30720
M v[104:107],v[156:159],a[8:11],v[104:107]
s_add_u32 s79, s0, 196608
s_mov_b32 m0, 67584
M v[104:107],v[160:163],a[16:19],v[104:107]
s_mov_b32 m0, 76800
s_mov_b32 m0, 67584
s_nop 0
buffer_load_dwordx4 v227, s[8:11], s79 offen nt lds
s_mov_b32 m0, 76800
W 2
buffer_load_dwordx4 v230, s[20:23], s78 offen lds
ds_read_b128 v[152:155], v220 offset:6272
W 1
ds_read_b128 v[156:159], v221 offset:6272
s_mov_b32 m0, 67584
s_mov_b32 m0, 76800
s_mov_b32 m0, 67584
ds_read_b128 v[160:163], v220 offset:6400
s_mov_b32 m0, 76800
s_mov_b32 m0, 67584
s_mov_b32 m0, 76800
s_mov_b32 m0, 67584
s_nop 0
buffer_load_dwordx4 v228, s[8:11], s79 offen offset:1024 nt lds
s_mov_b32 m0, 76800
M v[104:107],v[172:175],a[40:43],v[104:107]
buffer_load_dwordx4 v231, s[20:23], s78 offen offset:1024 lds
M v[104:107],v[168:171],a[32:35],v[104:107]
M v[104:107],v[164:167],a[24:27],v[104:107]
ds_read_b128 v[164:167], v221 offset:6400
s_mov_b32 m0, 67584
M v[112:115],v[176:179],a[0:3],v[204:207]
ds_read_b128 v[168:171], v220 offset:12288
s_mov_b32 m0, 76800
M v[112:115],v[180:183],a[8:11],v[112:115]
ds_read_b128 v[172:175], v221 offset:12288
W 3
M v[112:115],v[152:155],a[16:19],v[112:115]
s_mov_b32 m0, 67584
s_nop 0
buffer_load_dwordx4 v229, s[8:11], s79 offen offset:2048 nt lds
s_mov_b32 m0, 76800
s_nop 0
buffer_load_dwordx4 v232, s[20:23], s78 offen offset:2048 lds
s_add_u32 s78, s66, 92160
s_mov_b32 m0, 82944
v_max3_f32 v248, v104, v105, v106
v_max_f32 v248, v248, v107
v_exp_f32 v200, v104
ds_read_b128 v[176:179], v220 offset:12416
s_add_u32 s79, s0, 199680
s_mov_b32 m0, 70656
s_mov_b32 m0, 82944
ds_read_b128 v[180:183], v221 offset:12416
s_mov_b32 m0, 70656
s_nop 0
buffer_load_dwordx4 v227, s[8:11], s79 offen nt lds
s_mov_b32 m0, 82944
ds_read_b128 v[152:155], v220 offset:12544
buffer_load_dwordx4 v230, s[20:23], s78 offen lds
v_exp_f32 v201, v105
v_exp_f32 v202, v106
v_cvt_pk_bf16_f32 v136, v200, v201
W 3
M v[112:115],v[164:167],a[40:43],v[112:115]
s_mov_b32 m0, 70656
s_mov_b32 m0, 82944
M v[112:115],v[160:163],a[32:35],v[112:115]
s_mov_b32 m0, 70656
s_mov_b32 m0, 82944
M v[112:115],v[156:159],a[24:27],v[112:115]
ds_read_b128 v[156:159], v221 offset:12544
s_mov_b32 m0, 70656
M v[120:123],v[168:171],a[0:3],v[204:207]
v_exp_f32 v203, v107
M v[120:123],v[172:175],a[8:11],v[120:123]
ds_read_b128 v[160:163], v220 offset:18432
buffer_load_dwordx4 v228, s[8:11], s79 offen offset:1024 nt lds
W 2
M v[120:123],v[176:179],a[16:19],v[120:123]
ds_read_b128 v[164:167], v221 offset:18432
s_mov_b32 m0, 82944
s_nop 0
buffer_load_dwordx4 v231, s[20:23], s78 offen offset:1024 lds
v_cvt_pk_bf16_f32 v137, v202, v203
v_max3_f32 v248, v248, v112, v113
v_max3_f32 v248, v248, v114, v115
s_mov_b32 m0, 70656
s_nop 0
buffer_load_dwordx4 v229, s[8:11], s79 offen offset:2048 nt lds
s_mov_b32 m0, 82944
v_add_u32 v216, 0, v224
ds_read_b128 v[168:171], v220 offset:18560
buffer_load_dwordx4 v232, s[20:23], s78 offen offset:2048 lds
s_add_u32 s78, s66, 153600
s_mov_b32 m0, 89088
ds_read_b128 v[172:175], v221 offset:18560
v_exp_f32 v200, v112
v_xor_b32 v217, 32, v216
ds_read_b128 v[176:179], v220 offset:18688
buffer_load_dwordx4 v230, s[20:23], s78 offen lds
v_exp_f32 v201, v113
v_exp_f32 v202, v114
v_cvt_pk_bf16_f32 v138, v200, v201
W 3
M v[120:123],v[156:159],a[40:43],v[120:123]
buffer_load_dwordx4 v231, s[20:23], s78 offen offset:1024 lds
v_xor_b32 v218, 64, v216
M v[120:123],v[152:155],a[32:35],v[120:123]
v_exp_f32 v203, v115
M v[120:123],v[180:183],a[24:27],v[120:123]
ds_read_b128 v[180:183], v221 offset:18688
buffer_load_dwordx4 v232, s[20:23], s78 offen offset:2048 lds
M v[128:131],v[160:163],a[0:3],v[204:207]
v_xor_b32 v219, 96, v216
s_add_u32 s78, s66, 184320
M v[128:131],v[164:167],a[8:11],v[128:131]
s_waitcnt vmcnt(39)
s_barrier
T v[152:153],v216,0
W 2
M v[128:131],v[168:171],a[16:19],v[128:131]
T v[154:155],v216,6144
s_mov_b32 m0, 92160
T v[156:157],v216,12288
buffer_load_dwordx4 v230, s[20:23], s78 offen lds
v_cvt_pk_bf16_f32 v139, v202, v203
v_max3_f32 v248, v248, v120, v121
T v[158:159],v216,18432
v_max3_f32 v248, v248, v122, v123
T v[160:161],v217,0
buffer_load_dwordx4 v231, s[20:23], s78 offen offset:1024 lds
T v[162:163],v217,6144
W 5
T v[164:165],v217,12288
buffer_load_dwordx4 v232, s[20:23], s78 offen offset:2048 lds
s_add_u32 s78, s66, 215040
s_mov_b32 m0, 95232
T v[166:167],v217,18432
W 5
M v[128:131],v[180:183],a[40:43],v[128:131]
T v[168:169],v218,0
buffer_load_dwordx4 v230, s[20:23], s78 offen lds
M v[128:131],v[176:179],a[32:35],v[128:131]
T v[170:171],v218,6144
W 5
M v[128:131],v[172:175],a[24:27],v[128:131]
T v[172:173],v218,12288
buffer_load_dwordx4 v231, s[20:23], s78 offen offset:1024 lds
buffer_load_dwordx4 v232, s[20:23], s78 offen offset:2048 lds
M a[48:51],v[152:155],a[160:163],a[48:51]
T v[174:175],v218,18432
s_add_u32 s79, s0, 181248
s_mov_b32 m0, 125952
s_nop 0
buffer_load_dwordx4 v227, s[12:15], s79 offen nt lds
M a[48:51],v[156:159],a[164:167],a[48:51]
T v[176:177],v219,0
W 5
T v[178:179],v219,6144
T v[180:181],v219,12288
v_max3_f32 v248, v248, v128, v129
v_max3_f32 v248, v248, v130, v131
v_cmp_lt_f32 vcc, s60, v248
M a[56:59],v[160:163],a[160:163],a[56:59]
T v[182:183],v219,18432
s_cbranch_vccnz LST0_7w_%=
LRT0_7w_%=:
M a[56:59],v[164:167],a[164:167],a[56:59]
T v[152:153],v216,128
buffer_load_dwordx4 v228, s[12:15], s79 offen offset:1024 nt lds
T v[154:155],v216,6272
v_exp_f32 v200, v120
W 5
T v[156:157],v216,12416
v_exp_f32 v201, v121
s_nop 0
v_cvt_pk_bf16_f32 v144, v200, v201
M a[64:67],v[168:171],a[160:163],a[64:67]
T v[158:159],v216,18560
W 5
M a[64:67],v[172:175],a[164:167],a[64:67]
T v[160:161],v217,128
T v[162:163],v217,6272
buffer_load_dwordx4 v229, s[12:15], s79 offen offset:2048 nt lds
v_exp_f32 v202, v122
T v[164:165],v217,12416
v_exp_f32 v203, v123
s_nop 0
v_cvt_pk_bf16_f32 v145, v202, v203
M a[72:75],v[176:179],a[160:163],a[72:75]
T v[166:167],v217,18560
W 5
M a[72:75],v[180:183],a[164:167],a[72:75]
T v[168:169],v218,128
s_add_u32 s79, s0, 187392
T v[170:171],v218,6272
s_mov_b32 m0, 132096
v_exp_f32 v200, v128
T v[172:173],v218,12416
buffer_load_dwordx4 v227, s[12:15], s79 offen nt lds
v_exp_f32 v201, v129
M a[80:83],v[152:155],a[160:163],a[80:83]
T v[174:175],v218,18560
W 5
M a[80:83],v[156:159],a[164:167],a[80:83]
T v[176:177],v219,128
v_cvt_pk_bf16_f32 v146, v200, v201
T v[178:179],v219,6272
v_exp_f32 v202, v130
v_add_u32 v220, 24576, v224
T v[180:181],v219,12416
v_exp_f32 v203, v131
s_nop 0
v_cvt_pk_bf16_f32 v147, v202, v203
M a[88:91],v[160:163],a[160:163],a[88:91]
T v[182:183],v219,18560
W 5
M a[88:91],v[164:167],a[164:167],a[88:91]
T v[152:153],v216,256
buffer_load_dwordx4 v228, s[12:15], s79 offen offset:1024 nt lds
T v[154:155],v216,6400
v_xor_b32 v221, 32, v220
v_xor_b32 v222, 64, v220
v_xor_b32 v223, 96, v220
T v[156:157],v216,12544
T v[158:159],v216,18688
W 6
M a[96:99],v[168:171],a[160:163],a[96:99]
T v[160:161],v217,256
M a[96:99],v[172:175],a[164:167],a[96:99]
T v[162:163],v217,6400
W 6
T v[164:165],v217,12544
buffer_load_dwordx4 v229, s[12:15], s79 offen offset:2048 nt lds
T v[166:167],v217,18688
W 6
M a[104:107],v[176:179],a[160:163],a[104:107]
T v[168:169],v218,256
W 5
M a[104:107],v[180:183],a[164:167],a[104:107]
T v[170:171],v218,6400
s_add_u32 s79, s0, 193536
T v[172:173],v218,12544
s_mov_b32 m0, 138240
W 5
T v[174:175],v218,18688
buffer_load_dwordx4 v227, s[12:15], s79 offen nt lds
M a[112:115],v[152:155],a[160:163],a[112:115]
T v[176:177],v219,256
W 5
M a[112:115],v[156:159],a[164:167],a[112:115]
T v[178:179],v219,6400
T v[180:181],v219,12544
W 5
T v[182:183],v219,18688
M a[120:123],v[160:163],a[160:163],a[120:123]
s_waitcnt vmcnt(37)
s_barrier
T v[152:153],v220,0
M a[120:123],v[164:167],a[164:167],a[120:123]
T v[154:155],v220,6144
buffer_load_dwordx4 v228, s[12:15], s79 offen offset:1024 nt lds
W 4
T v[156:157],v221,0
M a[128:131],v[168:171],a[160:163],a[128:131]
T v[158:159],v221,6144
W 4
M a[128:131],v[172:175],a[164:167],a[128:131]
T v[160:161],v222,0
T v[162:163],v222,6144
buffer_load_dwordx4 v229, s[12:15], s79 offen offset:2048 nt lds
W 4
T v[164:165],v223,0
M a[136:139],v[176:179],a[160:163],a[136:139]
T v[166:167],v223,6144
M a[136:139],v[180:183],a[164:167],a[136:139]
T v[168:169],v220,128
s_add_u32 s79, s0, 196608
T v[170:171],v220,6272
s_mov_b32 m0, 141312
W 5
T v[172:173],v221,128
buffer_load_dwordx4 v227, s[12:15], s79 offen nt lds
M v[0:3],v[152:155],v[136:139],v[0:3]
T v[174:175],v221,6272
W 5
T v[176:177],v222,128
buffer_load_dwordx4 v228, s[12:15], s79 offen offset:1024 nt lds
M v[96:99],v[244:247],v[136:139],v[96:99]
T v[178:179],v222,6272
W 5
T v[180:181],v223,128
buffer_load_dwordx4 v229, s[12:15], s79 offen offset:2048 nt lds
M v[8:11],v[156:159],v[136:139],v[8:11]
T v[182:183],v223,6272
s_add_u32 s79, s0, 199680
T v[152:153],v220,256
s_mov_b32 m0, 144384
s_nop 0
buffer_load_dwordx4 v227, s[12:15], s79 offen nt lds
W 5
M v[16:19],v[160:163],v[136:139],v[16:19]
T v[154:155],v220,6400
T v[156:157],v221,256
buffer_load_dwordx4 v228, s[12:15], s79 offen offset:1024 nt lds
W 5
M v[24:27],v[164:167],v[136:139],v[24:27]
T v[158:159],v221,6400
T v[160:161],v222,256
buffer_load_dwordx4 v229, s[12:15], s79 offen offset:2048 nt lds
W 5
M v[32:35],v[168:171],v[136:139],v[32:35]
T v[162:163],v222,6400
s_add_u32 s79, s1, 181248
T v[164:165],v223,256
s_mov_b32 m0, 101376
s_nop 0
buffer_load_dwordx4 v227, s[8:11], s79 offen nt lds
W 5
M v[40:43],v[172:175],v[136:139],v[40:43]
T v[166:167],v223,6400
T v[168:169],v220,12288
buffer_load_dwordx4 v228, s[8:11], s79 offen offset:1024 nt lds
W 5
M v[48:51],v[176:179],v[136:139],v[48:51]
T v[170:171],v220,18432
W 4
M v[56:59],v[180:183],v[136:139],v[56:59]
buffer_load_dwordx4 v229, s[8:11], s79 offen offset:2048 nt lds
T v[172:173],v221,12288
v_accvgpr_read_b32 v184, a48
v_accvgpr_read_b32 v185, a49
v_accvgpr_read_b32 v186, a50
M v[64:67],v[152:155],v[136:139],v[64:67]
T v[174:175],v221,18432
v_accvgpr_read_b32 v187, a51
T v[176:177],v222,12288
s_add_u32 s79, s1, 187392
s_mov_b32 m0, 107520
v_accvgpr_read_b32 v188, a56
M v[72:75],v[156:159],v[136:139],v[72:75]
T v[178:179],v222,18432
v_accvgpr_read_b32 v189, a57
T v[180:181],v223,12288
buffer_load_dwordx4 v227, s[8:11], s79 offen nt lds
v_accvgpr_read_b32 v190, a58
v_accvgpr_read_b32 v191, a59
M v[80:83],v[160:163],v[136:139],v[80:83]
T v[182:183],v223,18432
v_pk_mul_f32 v[184:185], v[184:185], s[64:65]
v_pk_mul_f32 v[188:189], v[188:189], s[64:65]
T v[152:153],v220,12416
v_pk_mul_f32 v[186:187], v[186:187], s[64:65]
v_pk_mul_f32 v[190:191], v[190:191], s[64:65]
W 5
M v[88:91],v[164:167],v[136:139],v[88:91]
T v[154:155],v220,18560
v_cvt_pk_bf16_f32 v204, v184, v185
v_cvt_pk_bf16_f32 v206, v188, v189
T v[156:157],v221,12416
v_cvt_pk_bf16_f32 v205, v186, v187
v_cvt_pk_bf16_f32 v207, v190, v191
v_permlane32_swap_b32 v204, v206
M v[0:3],v[168:171],v[144:147],v[0:3]
T v[158:159],v221,18560
buffer_load_dwordx4 v228, s[8:11], s79 offen offset:1024 nt lds
v_permlane32_swap_b32 v205, v207
T v[160:161],v222,12416
v_permlane16_swap_b32 v204, v206
v_permlane16_swap_b32 v205, v207
v_accvgpr_write_b32 a0, v204
M v[96:99],v[244:247],v[144:147],v[96:99]
T v[162:163],v222,18560
v_accvgpr_write_b32 a1, v205
T v[164:165],v223,12416
v_accvgpr_write_b32 a2, v206
v_accvgpr_write_b32 a3, v207
v_accvgpr_read_b32 v184, a64
M v[8:11],v[172:175],v[144:147],v[8:11]
T v[166:167],v223,18560
v_accvgpr_read_b32 v185, a65
T v[168:169],v220,12544
buffer_load_dwordx4 v229, s[8:11], s79 offen offset:2048 nt lds
v_accvgpr_read_b32 v186, a66
v_accvgpr_read_b32 v187, a67
W 5
M v[16:19],v[176:179],v[144:147],v[16:19]
T v[170:171],v220,18688
v_accvgpr_read_b32 v188, a72
v_accvgpr_read_b32 v189, a73
T v[172:173],v221,12544
v_accvgpr_read_b32 v190, a74
v_accvgpr_read_b32 v191, a75
v_pk_mul_f32 v[184:185], v[184:185], s[64:65]
M v[24:27],v[180:183],v[144:147],v[24:27]
T v[174:175],v221,18688
s_add_u32 s79, s1, 193536
v_pk_mul_f32 v[188:189], v[188:189], s[64:65]
T v[176:177],v222,12544
s_mov_b32 m0, 113664
v_pk_mul_f32 v[186:187], v[186:187], s[64:65]
v_pk_mul_f32 v[190:191], v[190:191], s[64:65]
M v[32:35],v[152:155],v[144:147],v[32:35]
T v[178:179],v222,18688
buffer_load_dwordx4 v227, s[8:11], s79 offen nt lds
v_cvt_pk_bf16_f32 v204, v184, v185
v_cvt_pk_bf16_f32 v206, v188, v189
T v[180:181],v223,12544
v_cvt_pk_bf16_f32 v205, v186, v187
v_cvt_pk_bf16_f32 v207, v190, v191
v_permlane32_swap_b32 v204, v206
M v[40:43],v[156:159],v[144:147],v[40:43]
T v[182:183],v223,18688
v_permlane32_swap_b32 v205, v207
v_permlane16_swap_b32 v204, v206
s_nop 0
v_permlane16_swap_b32 v205, v207
v_add_u32 v216, 49152, v225
W 4
M v[48:51],v[160:163],v[144:147],v[48:51]
v_accvgpr_write_b32 a8, v204
v_accvgpr_write_b32 a9, v205
buffer_load_dwordx4 v228, s[8:11], s79 offen offset:1024 nt lds
v_accvgpr_write_b32 a10, v206
v_xor_b32 v217, 64, v216
v_accvgpr_write_b32 a11, v207
M v[56:59],v[164:167],v[144:147],v[56:59]
s_waitcnt vmcnt(33)
s_barrier
ds_read_b128 v[152:155], v216 offset:0
v_accvgpr_read_b32 v184, a80
ds_read_b128 v[156:159], v216 offset:6144
v_accvgpr_read_b32 v185, a81
v_accvgpr_read_b32 v186, a82
v_accvgpr_read_b32 v187, a83
M v[64:67],v[168:171],v[144:147],v[64:67]
ds_read_b128 v[160:163], v216 offset:12288
v_accvgpr_read_b32 v188, a88
ds_read_b128 v[164:167], v216 offset:18432
v_accvgpr_read_b32 v189, a89
v_accvgpr_read_b32 v190, a90
v_accvgpr_read_b32 v191, a91
M v[72:75],v[172:175],v[144:147],v[72:75]
buffer_load_dwordx4 v229, s[8:11], s79 offen offset:2048 nt lds
v_pk_mul_f32 v[184:185], v[184:185], s[64:65]
ds_read_b128 v[168:171], v217 offset:0
v_pk_mul_f32 v[188:189], v[188:189], s[64:65]
v_pk_mul_f32 v[186:187], v[186:187], s[64:65]
W 4
M v[80:83],v[176:179],v[144:147],v[80:83]
v_pk_mul_f32 v[190:191], v[190:191], s[64:65]
v_cvt_pk_bf16_f32 v204, v184, v185
ds_read_b128 v[172:175], v217 offset:6144
s_add_u32 s79, s1, 196608
s_mov_b32 m0, 116736
v_cvt_pk_bf16_f32 v206, v188, v189
v_cvt_pk_bf16_f32 v205, v186, v187
M v[88:91],v[180:183],v[144:147],v[88:91]
v_cvt_pk_bf16_f32 v207, v190, v191
v_permlane32_swap_b32 v204, v206
ds_read_b128 v[176:179], v217 offset:12288
buffer_load_dwordx4 v227, s[8:11], s79 offen nt lds
v_permlane32_swap_b32 v205, v207
v_permlane16_swap_b32 v204, v206
M v[104:107],v[152:155],a[0:3],0
v_permlane16_swap_b32 v205, v207
v_accvgpr_write_b32 a16, v204
ds_read_b128 v[180:183], v217 offset:18432
v_accvgpr_write_b32 a17, v205
v_accvgpr_write_b32 a18, v206
W 3
M v[112:115],v[156:159],a[0:3],0
v_accvgpr_write_b32 a19, v207
v_accvgpr_read_b32 v184, a96
ds_read_b128 v[152:155], v216 offset:128
v_accvgpr_read_b32 v185, a97
v_accvgpr_read_b32 v186, a98
v_accvgpr_read_b32 v187, a99
M v[120:123],v[160:163],a[0:3],0
buffer_load_dwordx4 v228, s[8:11], s79 offen offset:1024 nt lds
v_accvgpr_read_b32 v188, a104
v_accvgpr_read_b32 v189, a105
v_accvgpr_read_b32 v190, a106
v_accvgpr_read_b32 v191, a107
s_add_u32 s78, s67, 30720
ds_read_b128 v[156:159], v216 offset:6272
v_pk_mul_f32 v[184:185], v[184:185], s[64:65]
v_pk_mul_f32 v[188:189], v[188:189], s[64:65]
v_pk_mul_f32 v[186:187], v[186:187], s[64:65]
M v[128:131],v[164:167],a[0:3],0
v_pk_mul_f32 v[190:191], v[190:191], s[64:65]
v_cvt_pk_bf16_f32 v204, v184, v185
v_cvt_pk_bf16_f32 v206, v188, v189
s_mov_b32 m0, 3072
ds_read_b128 v[160:163], v216 offset:12416
s_mov_b32 m0, 116736
v_cvt_pk_bf16_f32 v205, v186, v187
v_cvt_pk_bf16_f32 v207, v190, v191
v_permlane32_swap_b32 v204, v206
s_nop 0
v_permlane32_swap_b32 v205, v207
M v[104:107],v[168:171],a[8:11],v[104:107]
v_permlane16_swap_b32 v204, v206
v_permlane16_swap_b32 v205, v207
v_accvgpr_write_b32 a24, v204
s_mov_b32 m0, 3072
ds_read_b128 v[164:167], v216 offset:18560
s_mov_b32 m0, 116736
s_nop 0
buffer_load_dwordx4 v229, s[8:11], s79 offen offset:2048 nt lds
v_accvgpr_write_b32 a25, v205
v_accvgpr_write_b32 a26, v206
v_accvgpr_write_b32 a27, v207
W 3
M v[112:115],v[172:175],a[8:11],v[112:115]
v_accvgpr_read_b32 v184, a112
v_accvgpr_read_b32 v185, a113
v_accvgpr_read_b32 v186, a114
s_mov_b32 m0, 3072
ds_read_b128 v[168:171], v217 offset:128
buffer_load_dwordx4 v230, s[20:23], s78 offen lds
v_accvgpr_read_b32 v187, a115
v_accvgpr_read_b32 v188, a120
v_accvgpr_read_b32 v189, a121
v_accvgpr_read_b32 v190, a122
M v[120:123],v[176:179],a[8:11],v[120:123]
s_add_u32 s79, s1, 199680
s_mov_b32 m0, 119808
v_accvgpr_read_b32 v191, a123
v_pk_mul_f32 v[184:185], v[184:185], s[64:65]
v_pk_mul_f32 v[188:189], v[188:189], s[64:65]
ds_read_b128 v[172:175], v217 offset:6272
v_pk_mul_f32 v[186:187], v[186:187], s[64:65]
v_pk_mul_f32 v[190:191], v[190:191], s[64:65]
v_cvt_pk_bf16_f32 v204, v184, v185
M v[128:131],v[180:183],a[8:11],v[128:131]
buffer_load_dwordx4 v227, s[8:11], s79 offen nt lds
v_cvt_pk_bf16_f32 v206, v188, v189
v_cvt_pk_bf16_f32 v205, v186, v187
v_cvt_pk_bf16_f32 v207, v190, v191
v_permlane32_swap_b32 v204, v206
s_mov_b32 m0, 3072
ds_read_b128 v[176:179], v217 offset:12416
buffer_load_dwordx4 v231, s[20:23], s78 offen offset:1024 lds
v_permlane32_swap_b32 v205, v207
v_permlane16_swap_b32 v204, v206
s_nop 0
v_permlane16_swap_b32 v205, v207
M v[104:107],v[152:155],a[16:19],v[104:107]
v_accvgpr_write_b32 a32, v204
v_accvgpr_write_b32 a33, v205
v_accvgpr_write_b32 a34, v206
ds_read_b128 v[180:183], v217 offset:18560
s_mov_b32 m0, 119808
v_accvgpr_write_b32 a35, v207
v_accvgpr_read_b32 v184, a128
v_accvgpr_read_b32 v185, a129
v_accvgpr_read_b32 v186, a130
W 3
M v[112:115],v[156:159],a[16:19],v[112:115]
v_accvgpr_read_b32 v187, a131
v_accvgpr_read_b32 v188, a136
v_accvgpr_read_b32 v189, a137
s_mov_b32 m0, 3072
ds_read_b128 v[152:155], v216 offset:256
s_mov_b32 m0, 119808
s_nop 0
buffer_load_dwordx4 v228, s[8:11], s79 offen offset:1024 nt lds
v_accvgpr_read_b32 v190, a138
v_accvgpr_read_b32 v191, a139
v_pk_mul_f32 v[184:185], v[184:185], s[64:65]
s_mov_b32 m0, 3072
M v[120:123],v[160:163],a[16:19],v[120:123]
buffer_load_dwordx4 v232, s[20:23], s78 offen offset:2048 lds
v_pk_mul_f32 v[188:189], v[188:189], s[64:65]
v_pk_mul_f32 v[186:187], v[186:187], s[64:65]
v_pk_mul_f32 v[190:191], v[190:191], s[64:65]
ds_read_b128 v[156:159], v216 offset:6400
v_cvt_pk_bf16_f32 v204, v184, v185
v_cvt_pk_bf16_f32 v206, v188, v189
v_cvt_pk_bf16_f32 v205, v186, v187
v_cvt_pk_bf16_f32 v207, v190, v191
M v[128:131],v[164:167],a[16:19],v[128:131]
s_mov_b32 m0, 119808
v_permlane32_swap_b32 v204, v206
v_permlane32_swap_b32 v205, v207
s_nop 0
v_permlane16_swap_b32 v204, v206
ds_read_b128 v[160:163], v216 offset:12544
v_permlane16_swap_b32 v205, v207
v_accvgpr_write_b32 a40, v204
v_accvgpr_write_b32 a41, v205
s_add_u32 s78, s67, 92160
M v[104:107],v[168:171],a[24:27],v[104:107]
buffer_load_dwordx4 v229, s[8:11], s79 offen offset:2048 nt lds
v_accvgpr_write_b32 a42, v206
v_accvgpr_write_b32 a43, v207
v_rcp_f32 v250, v96
s_mov_b32 m0, 9216
ds_read_b128 v[164:167], v216 offset:18688
buffer_load_dwordx4 v230, s[20:23], s78 offen lds
v_pk_mul_f32 v[0:1], v[0:1], v[250:251] op_sel_hi:[1,0]
v_pk_mul_f32 v[2:3], v[2:3], v[250:251] op_sel_hi:[1,0]
v_pk_mul_f32 v[8:9], v[8:9], v[250:251] op_sel_hi:[1,0]
W 3
M v[112:115],v[172:175],a[24:27],v[112:115]
v_pk_mul_f32 v[10:11], v[10:11], v[250:251] op_sel_hi:[1,0]
v_cvt_pk_bf16_f32 v136, v0, v1
v_cvt_pk_bf16_f32 v137, v2, v3
ds_read_b128 v[168:171], v217 offset:256
buffer_load_dwordx4 v231, s[20:23], s78 offen offset:1024 lds
v_cvt_pk_bf16_f32 v138, v8, v9
v_cvt_pk_bf16_f32 v139, v10, v11
v_accvgpr_write_b32 a48, v136
M v[120:123],v[176:179],a[24:27],v[120:123]
v_accvgpr_write_b32 a49, v137
v_accvgpr_write_b32 a50, v138
v_accvgpr_write_b32 a51, v139
v_pk_mul_f32 v[16:17], v[16:17], v[250:251] op_sel_hi:[1,0]
ds_read_b128 v[172:175], v217 offset:6400
buffer_load_dwordx4 v232, s[20:23], s78 offen offset:2048 lds
v_pk_mul_f32 v[18:19], v[18:19], v[250:251] op_sel_hi:[1,0]
v_pk_mul_f32 v[24:25], v[24:25], v[250:251] op_sel_hi:[1,0]
v_pk_mul_f32 v[26:27], v[26:27], v[250:251] op_sel_hi:[1,0]
M v[128:131],v[180:183],a[24:27],v[128:131]
v_cvt_pk_bf16_f32 v144, v16, v17
v_cvt_pk_bf16_f32 v145, v18, v19
v_cvt_pk_bf16_f32 v146, v24, v25
s_add_u32 s78, s67, 153600
ds_read_b128 v[176:179], v217 offset:12544
v_cvt_pk_bf16_f32 v147, v26, v27
v_accvgpr_write_b32 a56, v144
v_accvgpr_write_b32 a57, v145
v_accvgpr_write_b32 a58, v146
s_mov_b32 m0, 15360
M v[104:107],v[152:155],a[32:35],v[104:107]
buffer_load_dwordx4 v230, s[20:23], s78 offen lds
v_accvgpr_write_b32 a59, v147
v_pk_mul_f32 v[32:33], v[32:33], v[250:251] op_sel_hi:[1,0]
v_pk_mul_f32 v[34:35], v[34:35], v[250:251] op_sel_hi:[1,0]
ds_read_b128 v[180:183], v217 offset:18688
v_pk_mul_f32 v[40:41], v[40:41], v[250:251] op_sel_hi:[1,0]
v_pk_mul_f32 v[42:43], v[42:43], v[250:251] op_sel_hi:[1,0]
v_cvt_pk_bf16_f32 v136, v32, v33
W 3
M v[112:115],v[156:159],a[32:35],v[112:115]
buffer_load_dwordx4 v231, s[20:23], s78 offen offset:1024 lds
v_cvt_pk_bf16_f32 v137, v34, v35
v_cvt_pk_bf16_f32 v138, v40, v41
v_cvt_pk_bf16_f32 v139, v42, v43
v_accvgpr_write_b32 a64, v136
v_accvgpr_write_b32 a65, v137
v_accvgpr_write_b32 a66, v138
v_accvgpr_write_b32 a67, v139
M v[120:123],v[160:163],a[32:35],v[120:123]
buffer_load_dwordx4 v232, s[20:23], s78 offen offset:2048 lds
v_pk_mul_f32 v[48:49], v[48:49], v[250:251] op_sel_hi:[1,0]
v_pk_mul_f32 v[50:51], v[50:51], v[250:251] op_sel_hi:[1,0]
v_pk_mul_f32 v[56:57], v[56:57], v[250:251] op_sel_hi:[1,0]
v_pk_mul_f32 v[58:59], v[58:59], v[250:251] op_sel_hi:[1,0]
v_cvt_pk_bf16_f32 v144, v48, v49
v_cvt_pk_bf16_f32 v145, v50, v51
s_add_u32 s78, s67, 184320
M v[128:131],v[164:167],a[32:35],v[128:131]
v_cvt_pk_bf16_f32 v146, v56, v57
v_cvt_pk_bf16_f32 v147, v58, v59
v_accvgpr_write_b32 a72, v144
v_accvgpr_write_b32 a73, v145
s_mov_b32 m0, 18432
s_nop 0
buffer_load_dwordx4 v230, s[20:23], s78 offen lds
v_accvgpr_write_b32 a74, v146
v_accvgpr_write_b32 a75, v147
v_pk_mul_f32 v[64:65], v[64:65], v[250:251] op_sel_hi:[1,0]
M v[104:107],v[168:171],a[40:43],v[104:107]
v_add_u32 v220, 73728, v226
v_xor_b32 v221, 32, v220
v_xor_b32 v222, 64, v220
v_xor_b32 v223, 96, v220
s_waitcnt vmcnt(40)
s_barrier
T v[152:153],v220,0
T v[154:155],v220,2048
v_pk_mul_f32 v[66:67], v[66:67], v[250:251] op_sel_hi:[1,0]
v_pk_mul_f32 v[72:73], v[72:73], v[250:251] op_sel_hi:[1,0]
v_pk_mul_f32 v[74:75], v[74:75], v[250:251] op_sel_hi:[1,0]
T v[156:157],v220,4096
T v[158:159],v220,6144
buffer_load_dwordx4 v231, s[20:23], s78 offen offset:1024 lds
v_cvt_pk_bf16_f32 v136, v64, v65
v_cvt_pk_bf16_f32 v137, v66, v67
v_cvt_pk_bf16_f32 v138, v72, v73
v_cvt_pk_bf16_f32 v139, v74, v75
W 4
M v[112:115],v[172:175],a[40:43],v[112:115]
T v[160:161],v220,8192
T v[162:163],v220,10240
v_accvgpr_write_b32 a80, v136
v_accvgpr_write_b32 a81, v137
v_accvgpr_write_b32 a82, v138
T v[164:165],v220,12288
buffer_load_dwordx4 v232, s[20:23], s78 offen offset:2048 lds
v_accvgpr_write_b32 a83, v139
v_pk_mul_f32 v[80:81], v[80:81], v[250:251] op_sel_hi:[1,0]
v_pk_mul_f32 v[82:83], v[82:83], v[250:251] op_sel_hi:[1,0]
s_add_u32 s78, s67, 215040
s_mov_b32 m0, 21504
M v[120:123],v[176:179],a[40:43],v[120:123]
T v[166:167],v220,14336
v_pk_mul_f32 v[88:89], v[88:89], v[250:251] op_sel_hi:[1,0]
v_pk_mul_f32 v[90:91], v[90:91], v[250:251] op_sel_hi:[1,0]
v_cvt_pk_bf16_f32 v144, v80, v81
T v[168:169],v220,16384
buffer_load_dwordx4 v230, s[20:23], s78 offen lds
v_cvt_pk_bf16_f32 v145, v82, v83
v_cvt_pk_bf16_f32 v146, v88, v89
v_cvt_pk_bf16_f32 v147, v90, v91
v_accvgpr_write_b32 a88, v144
M v[128:131],v[180:183],a[40:43],v[128:131]
T v[170:171],v220,18432
v_accvgpr_write_b32 a89, v145
v_accvgpr_write_b32 a90, v146
v_accvgpr_write_b32 a91, v147
T v[172:173],v220,20480
buffer_load_dwordx4 v231, s[20:23], s78 offen offset:1024 lds
v_max3_f32 v248, v104, v105, v106
buffer_load_dwordx4 v232, s[20:23], s78 offen offset:2048 lds
W 5
M v[184:187],v[152:155],a[48:51],0
T v[174:175],v220,22528
s_add_u32 s79, s1, 181248
s_mov_b32 m0, 27648
s_nop 0
buffer_load_dwordx4 v227, s[12:15], s79 offen nt lds
M v[184:187],v[156:159],a[56:59],v[184:187]
T v[176:177],v221,0
v_max_f32 v248, v248, v107
M v[184:187],v[160:163],a[64:67],v[184:187]
T v[178:179],v221,2048
v_max3_f32 v248, v248, v112, v113
T v[180:181],v221,4096
v_max3_f32 v248, v248, v114, v115
v_max3_f32 v248, v248, v120, v121
v_max3_f32 v248, v248, v122, v123
T v[182:183],v221,6144
v_max3_f32 v248, v248, v128, v129
v_max3_f32 v248, v248, v130, v131
v_mov_b32 v250, v248
T v[152:153],v221,8192
buffer_load_dwordx4 v228, s[12:15], s79 offen offset:1024 nt lds
v_permlane32_swap_b32 v248, v250
v_max_f32 v248, v248, v250
T v[154:155],v221,10240
v_mov_b32 v250, v248
s_nop 1
v_permlane16_swap_b32 v248, v250
v_max_f32 v248, v248, v250
T v[156:157],v221,12288
v_sub_f32 v104, v104, v248
v_sub_f32 v105, v105, v248
v_sub_f32 v106, v106, v248
T v[158:159],v221,14336
v_sub_f32 v107, v107, v248
v_sub_f32 v112, v112, v248
W 5
M v[184:187],v[172:175],a[88:91],v[184:187]
T v[160:161],v221,16384
v_sub_f32 v113, v113, v248
M v[184:187],v[168:171],a[80:83],v[184:187]
T v[162:163],v221,18432
buffer_load_dwordx4 v229, s[12:15], s79 offen offset:2048 nt lds
M v[184:187],v[164:167],a[72:75],v[184:187]
T v[164:165],v221,20480
v_sub_f32 v114, v114, v248
M v[192:195],v[176:179],a[48:51],0
T v[166:167],v221,22528
W 5
M v[192:195],v[180:183],a[56:59],v[192:195]
T v[168:169],v222,0
s_add_u32 s79, s1, 187392
M v[192:195],v[152:155],a[64:67],v[192:195]
T v[170:171],v222,2048
s_mov_b32 m0, 33792
T v[172:173],v222,4096
buffer_load_dwordx4 v227, s[12:15], s79 offen nt lds
v_sub_f32 v115, v115, v248
W 5
T v[174:175],v222,6144
v_exp_f32 v200, v104
T v[176:177],v222,8192
v_cvt_pk_bf16_f32 v252, v184, v185
v_cvt_pk_bf16_f32 v253, v186, v187
v_accvgpr_write_b32 a96, v252
T v[178:179],v222,10240
v_accvgpr_write_b32 a97, v253
v_exp_f32 v201, v105
T v[180:181],v222,12288
v_exp_f32 v202, v106
v_cvt_pk_bf16_f32 v136, v200, v201
T v[182:183],v222,14336
buffer_load_dwordx4 v228, s[12:15], s79 offen offset:1024 nt lds
v_add_u32 v216, 122880, v224
W 5
M v[192:195],v[164:167],a[88:91],v[192:195]
T v[152:153],v222,16384
v_xor_b32 v217, 32, v216
M v[192:195],v[160:163],a[80:83],v[192:195]
T v[154:155],v222,18432
v_xor_b32 v218, 64, v216
M v[192:195],v[156:159],a[72:75],v[192:195]
T v[156:157],v222,20480
v_xor_b32 v219, 96, v216
M v[184:187],v[168:171],a[48:51],0
T v[158:159],v222,22528
W 5
M v[184:187],v[172:175],a[56:59],v[184:187]
T v[160:161],v223,0
buffer_load_dwordx4 v229, s[12:15], s79 offen offset:2048 nt lds
v_exp_f32 v203, v107
M v[184:187],v[176:179],a[64:67],v[184:187]
T v[162:163],v223,2048
v_cvt_pk_bf16_f32 v137, v202, v203
T v[164:165],v223,4096
v_exp_f32 v200, v112
W 5
T v[166:167],v223,6144
s_add_u32 s79, s1, 193536
s_mov_b32 m0, 39936
T v[168:169],v223,8192
v_cvt_pk_bf16_f32 v252, v192, v193
v_cvt_pk_bf16_f32 v253, v194, v195
v_accvgpr_write_b32 a98, v252
T v[170:171],v223,10240
buffer_load_dwordx4 v227, s[12:15], s79 offen nt lds
v_accvgpr_write_b32 a99, v253
W 5
T v[172:173],v223,12288
v_exp_f32 v201, v113
s_nop 0
v_cvt_pk_bf16_f32 v138, v200, v201
T v[174:175],v223,14336
v_exp_f32 v202, v114
v_sub_f32 v120, v120, v248
M v[184:187],v[156:159],a[88:91],v[184:187]
T v[176:177],v223,16384
v_sub_f32 v121, v121, v248
M v[184:187],v[152:155],a[80:83],v[184:187]
T v[178:179],v223,18432
v_sub_f32 v122, v122, v248
M v[184:187],v[180:183],a[72:75],v[184:187]
T v[180:181],v223,20480
W 5
M v[192:195],v[160:163],a[48:51],0
T v[182:183],v223,22528
buffer_load_dwordx4 v228, s[12:15], s79 offen offset:1024 nt lds
M v[192:195],v[164:167],a[56:59],v[192:195]
s_waitcnt vmcnt(38)
s_barrier
T v[152:153],v216,0
M v[192:195],v[168:171],a[64:67],v[192:195]
T v[154:155],v216,6144
v_sub_f32 v123, v123, v248
T v[156:157],v217,0
v_exp_f32 v203, v115
s_nop 0
v_cvt_pk_bf16_f32 v139, v202, v203
T v[158:159],v217,6144
v_sub_f32 v128, v128, v248
v_sub_f32 v129, v129, v248
v_sub_f32 v130, v130, v248
T v[160:161],v218,0
buffer_load_dwordx4 v229, s[12:15], s79 offen offset:2048 nt lds
v_sub_f32 v131, v131, v248
W 5
T v[162:163],v218,6144
v_exp_f32 v200, v120
T v[164:165],v219,0
v_exp_f32 v201, v121
s_nop 0
v_cvt_pk_bf16_f32 v144, v200, v201
T v[166:167],v219,6144
s_add_u32 s79, s1, 196608
s_mov_b32 m0, 43008
W 5
M v[192:195],v[180:183],a[88:91],v[192:195]
T v[168:169],v216,128
M v[192:195],v[176:179],a[80:83],v[192:195]
T v[170:171],v216,6272
buffer_load_dwordx4 v227, s[12:15], s79 offen nt lds
M v[192:195],v[172:175],a[72:75],v[192:195]
T v[172:173],v217,128
W 5
M v[0:3],v[152:155],v[136:139],0
T v[174:175],v217,6272
s_add_u32 s78, s68, 30720
T v[176:177],v218,128
v_exp_f32 v202, v122
s_mov_b32 m0, 52224
M v[96:99],v[244:247],v[136:139],0
T v[178:179],v218,6272
s_mov_b32 m0, 43008
T v[180:181],v219,128
buffer_load_dwordx4 v228, s[12:15], s79 offen offset:1024 nt lds
v_exp_f32 v203, v123
M v[8:11],v[156:159],v[136:139],0
T v[182:183],v219,6272
v_cvt_pk_bf16_f32 v145, v202, v203
T v[152:153],v216,256
v_exp_f32 v200, v128
s_mov_b32 m0, 52224
M v[16:19],v[160:163],v[136:139],0
T v[154:155],v216,6400
s_mov_b32 m0, 43008
T v[156:157],v217,256
s_mov_b32 m0, 52224
s_mov_b32 m0, 43008
W 5
M v[24:27],v[164:167],v[136:139],0
T v[158:159],v217,6400
buffer_load_dwordx4 v229, s[12:15], s79 offen offset:2048 nt lds
T v[160:161],v218,256
v_exp_f32 v201, v129
s_nop 0
v_cvt_pk_bf16_f32 v146, v200, v201
M v[32:35],v[168:171],v[136:139],0
T v[162:163],v218,6400
s_mov_b32 m0, 52224
T v[164:165],v219,256
s_add_u32 s79, s1, 199680
s_mov_b32 m0, 46080
s_mov_b32 m0, 52224
M v[40:43],v[172:175],v[136:139],0
T v[166:167],v219,6400
s_mov_b32 m0, 46080
T v[168:169],v216,12288
buffer_load_dwordx4 v227, s[12:15], s79 offen nt lds
v_exp_f32 v202, v130
M v[48:51],v[176:179],v[136:139],0
T v[170:171],v216,18432
s_mov_b32 m0, 52224
s_nop 0
buffer_load_dwordx4 v230, s[20:23], s78 offen lds
v_exp_f32 v203, v131
W 4
M v[56:59],v[180:183],v[136:139],0
s_mov_b32 m0, 46080
s_mov_b32 m0, 52224
T v[172:173],v217,12288
s_mov_b32 m0, 46080
s_mov_b32 m0, 52224
s_mov_b32 m0, 46080
M v[64:67],v[152:155],v[136:139],0
T v[174:175],v217,18432
buffer_load_dwordx4 v228, s[12:15], s79 offen offset:1024 nt lds
T v[176:177],v218,12288
v_cvt_pk_bf16_f32 v147, v202, v203
v_sub_f32 v204, 0, v248
v_sub_f32 v205, 0, v248
M v[72:75],v[156:159],v[136:139],0
T v[178:179],v218,18432
s_mov_b32 m0, 52224
T v[180:181],v219,12288
s_mov_b32 m0, 46080
s_mov_b32 m0, 52224
s_mov_b32 m0, 46080
M v[80:83],v[160:163],v[136:139],0
T v[182:183],v219,18432
s_mov_b32 m0, 52224
T v[152:153],v216,12416
s_mov_b32 m0, 46080
s_nop 0
buffer_load_dwordx4 v229, s[12:15], s79 offen offset:2048 nt lds
W 5
M v[88:91],v[164:167],v[136:139],0
T v[154:155],v216,18560
v_sub_f32 v206, 0, v248
T v[156:157],v217,12416
v_sub_f32 v207, 0, v248
v_cvt_pk_bf16_f32 v252, v184, v185
v_cvt_pk_bf16_f32 v253, v186, v187
M v[0:3],v[168:171],v[144:147],v[0:3]
T v[158:159],v217,18560
s_mov_b32 m0, 52224
T v[160:161],v218,12416
buffer_load_dwordx4 v231, s[20:23], s78 offen offset:1024 lds
v_accvgpr_write_b32 a104, v252
v_accvgpr_write_b32 a105, v253
M v[96:99],v[244:247],v[144:147],v[96:99]
T v[162:163],v218,18560
v_cvt_pk_bf16_f32 v252, v192, v193
T v[164:165],v219,12416
buffer_load_dwordx4 v232, s[20:23], s78 offen offset:2048 lds
s_add_u32 s78, s68, 92160
s_mov_b32 m0, 58368
M v[8:11],v[172:175],v[144:147],v[8:11]
T v[166:167],v219,18560
v_lshrrev_b32 v241, 4, v240
T v[168:169],v216,12544
buffer_load_dwordx4 v230, s[20:23], s78 offen lds
v_and_b32 v242, 15, v241
W 5
M v[16:19],v[176:179],v[144:147],v[16:19]
T v[170:171],v216,18688
v_mul_u32_u24 v233, 0x500, v242
T v[172:173],v217,12544
buffer_load_dwordx4 v231, s[20:23], s78 offen offset:1024 lds
v_lshrrev_b32 v234, 4, v241
v_and_b32 v235, 1, v234
M v[24:27],v[180:183],v[144:147],v[24:27]
T v[174:175],v217,18688
v_lshl_add_u32 v233, v235, 5, v233
T v[176:177],v218,12544
buffer_load_dwordx4 v232, s[20:23], s78 offen offset:2048 lds
s_add_u32 s78, s68, 153600
s_mov_b32 m0, 64512
M v[32:35],v[152:155],v[144:147],v[32:35]
T v[178:179],v218,18688
v_lshrrev_b32 v235, 1, v234
T v[180:181],v219,12544
buffer_load_dwordx4 v230, s[20:23], s78 offen lds
v_lshl_add_u32 v233, v235, 4, v233
v_lshrrev_b32 v242, 2, v242
M v[40:43],v[156:159],v[144:147],v[40:43]
T v[182:183],v219,18688
v_add_u32 v220, 98304, v225
buffer_load_dwordx4 v231, s[20:23], s78 offen offset:1024 lds
v_xor_b32 v221, 64, v220
v_cmp_eq_u32 s[74:75], v242, v234
W 4
M v[48:51],v[160:163],v[144:147],v[48:51]
s_waitcnt vmcnt(35)
s_barrier
ds_read_b128 v[152:155], v220 offset:0
ds_read_b128 v[156:159], v221 offset:0
buffer_load_dwordx4 v232, s[20:23], s78 offen offset:2048 lds
s_add_u32 s78, s68, 184320
s_mov_b32 m0, 67584
M v[56:59],v[164:167],v[144:147],v[56:59]
ds_read_b128 v[160:163], v220 offset:128
v_and_b32 v241, 3, v241
buffer_load_dwordx4 v230, s[20:23], s78 offen lds
v_lshlrev_b32 v241, 4, v241
v_mov_b32 v234, 0x3f80
v_mov_b32 v235, 0
M v[64:67],v[168:171],v[144:147],v[64:67]
v_cndmask_b32 v234, 0, v234, s[74:75]
v_lshlrev_b64 v[236:237], v241, v[234:235]
ds_read_b128 v[164:167], v221 offset:128
buffer_load_dwordx4 v231, s[20:23], s78 offen offset:1024 lds
v_mov_b32 v234, 0
v_mov_b32 v238, 0
M v[72:75],v[172:175],v[144:147],v[72:75]
ds_read_b128 v[168:171], v220 offset:256
v_mov_b32 v239, 0
ds_read_b128 v[172:175], v221 offset:256
buffer_load_dwordx4 v232, s[20:23], s78 offen offset:2048 lds
s_add_u32 s78, s68, 215040
s_mov_b32 m0, 70656
s_nop 0
buffer_load_dwordx4 v230, s[20:23], s78 offen lds
buffer_load_dwordx4 v231, s[20:23], s78 offen offset:1024 lds
buffer_load_dwordx4 v232, s[20:23], s78 offen offset:2048 lds
W 3
M v[80:83],v[176:179],v[144:147],v[80:83]
s_movk_i32 s85, 4096
s_movk_i32 s86, 8192
s_movk_i32 s87, 12288
v_cvt_pk_bf16_f32 v253, v194, v195
v_accvgpr_write_b32 a106, v252
v_accvgpr_write_b32 a107, v253
M v[88:91],v[180:183],v[144:147],v[88:91]
ds_read_b128 v[176:179], v220 offset:6144
W 2
M v[104:107],v[152:155],a[0:3],v[204:207]
ds_read_b128 v[180:183], v221 offset:6144
s_add_u32 s79, s2, 181248
s_mov_b32 m0, 76800
s_nop 0
buffer_load_dwordx4 v227, s[8:11], s79 offen nt lds
M v[104:107],v[156:159],a[8:11],v[104:107]
M v[104:107],v[160:163],a[16:19],v[104:107]
W 1
ds_read_b128 v[152:155], v220 offset:6272
buffer_load_dwordx4 v228, s[8:11], s79 offen offset:1024 nt lds
ds_read_b128 v[156:159], v221 offset:6272
ds_read_b128 v[160:163], v220 offset:6400
M v[104:107],v[172:175],a[40:43],v[104:107]
M v[104:107],v[168:171],a[32:35],v[104:107]
buffer_load_dwordx4 v229, s[8:11], s79 offen offset:2048 nt lds
W 2
M v[104:107],v[164:167],a[24:27],v[104:107]
ds_read_b128 v[164:167], v221 offset:6400
M v[112:115],v[176:179],a[0:3],v[204:207]
ds_read_b128 v[168:171], v220 offset:12288
W 2
M v[112:115],v[180:183],a[8:11],v[112:115]
ds_read_b128 v[172:175], v221 offset:12288
s_add_u32 s79, s2, 187392
M v[112:115],v[152:155],a[16:19],v[112:115]
s_mov_b32 m0, 82944
s_nop 0
buffer_load_dwordx4 v227, s[8:11], s79 offen nt lds
v_max3_f32 v248, v104, v105, v106
v_max_f32 v248, v248, v107
v_exp_f32 v200, v104
v_exp_f32 v201, v105
ds_read_b128 v[176:179], v220 offset:12416
v_exp_f32 v202, v106
v_cvt_pk_bf16_f32 v136, v200, v201
ds_read_b128 v[180:183], v221 offset:12416
v_exp_f32 v203, v107
s_nop 0
v_cvt_pk_bf16_f32 v137, v202, v203
ds_read_b128 v[152:155], v220 offset:12544
v_add_u32 v216, 0, v226
v_xor_b32 v217, 32, v216
v_xor_b32 v218, 64, v216
buffer_load_dwordx4 v228, s[8:11], s79 offen offset:1024 nt lds
v_xor_b32 v219, 96, v216
W 3
M v[112:115],v[164:167],a[40:43],v[112:115]
M v[112:115],v[160:163],a[32:35],v[112:115]
M v[112:115],v[156:159],a[24:27],v[112:115]
ds_read_b128 v[156:159], v221 offset:12544
W 2
M v[120:123],v[168:171],a[0:3],v[204:207]
ds_read_b128 v[160:163], v220 offset:18432
M v[120:123],v[172:175],a[8:11],v[120:123]
ds_read_b128 v[164:167], v221 offset:18432
buffer_load_dwordx4 v229, s[8:11], s79 offen offset:2048 nt lds
M v[120:123],v[176:179],a[16:19],v[120:123]
v_max3_f32 v248, v248, v112, v113
v_max3_f32 v248, v248, v114, v115
v_exp_f32 v200, v112
s_add_u32 s79, s2, 193536
s_mov_b32 m0, 89088
v_exp_f32 v201, v113
ds_read_b128 v[168:171], v220 offset:18560
v_exp_f32 v202, v114
v_cvt_pk_bf16_f32 v138, v200, v201
ds_read_b128 v[172:175], v221 offset:18560
buffer_load_dwordx4 v227, s[8:11], s79 offen nt lds
v_exp_f32 v203, v115
ds_read_b128 v[176:179], v220 offset:18688
v_cvt_pk_bf16_f32 v139, v202, v203
W 3
M v[120:123],v[156:159],a[40:43],v[120:123]
M v[120:123],v[152:155],a[32:35],v[120:123]
M v[120:123],v[180:183],a[24:27],v[120:123]
ds_read_b128 v[180:183], v221 offset:18688
buffer_load_dwordx4 v228, s[8:11], s79 offen offset:1024 nt lds
M v[128:131],v[160:163],a[0:3],v[204:207]
s_waitcnt vmcnt(38)
s_barrier
T v[152:153],v216,0
M v[128:131],v[164:167],a[8:11],v[128:131]
T v[154:155],v216,2048
W 3
M v[128:131],v[168:171],a[16:19],v[128:131]
T v[156:157],v216,4096
T v[158:159],v216,6144
v_max3_f32 v248, v248, v120, v121
v_max3_f32 v248, v248, v122, v123
buffer_load_dwordx4 v229, s[8:11], s79 offen offset:2048 nt lds
T v[160:161],v216,8192
T v[162:163],v216,10240
W 5
T v[164:165],v216,12288
T v[166:167],v216,14336
s_add_u32 s79, s2, 196608
s_mov_b32 m0, 92160
W 5
T v[168:169],v216,16384
M v[128:131],v[180:183],a[40:43],v[128:131]
T v[170:171],v216,18432
buffer_load_dwordx4 v227, s[8:11], s79 offen nt lds
M v[128:131],v[176:179],a[32:35],v[128:131]
W 4
M v[128:131],v[172:175],a[24:27],v[128:131]
T v[172:173],v216,20480
T v[174:175],v216,22528
M v[184:187],v[152:155],a[48:51],0
T v[176:177],v217,0
s_add_u32 s78, s69, 30720
M v[184:187],v[156:159],a[56:59],v[184:187]
T v[178:179],v217,2048
s_mov_b32 m0, 125952
M v[184:187],v[160:163],a[64:67],v[184:187]
T v[180:181],v217,4096
s_mov_b32 m0, 92160
T v[182:183],v217,6144
buffer_load_dwordx4 v228, s[8:11], s79 offen offset:1024 nt lds
s_mov_b32 m0, 125952
v_max3_f32 v248, v248, v128, v129
buffer_load_dwordx4 v230, s[20:23], s78 offen lds
v_max3_f32 v248, v248, v130, v131
v_cmp_lt_f32 vcc, s60, v248
s_nop 1
s_cbranch_vccnz LST1_1w_%=
LRT1_1w_%=:
T v[152:153],v217,8192
v_exp_f32 v200, v120
W 4
T v[154:155],v217,10240
s_mov_b32 m0, 92160
s_mov_b32 m0, 125952
s_mov_b32 m0, 92160
T v[156:157],v217,12288
s_mov_b32 m0, 125952
s_mov_b32 m0, 92160
s_mov_b32 m0, 125952
T v[158:159],v217,14336
s_mov_b32 m0, 92160
s_nop 0
buffer_load_dwordx4 v229, s[8:11], s79 offen offset:2048 nt lds
s_mov_b32 m0, 125952
M v[184:187],v[172:175],a[88:91],v[184:187]
T v[160:161],v217,16384
buffer_load_dwordx4 v231, s[20:23], s78 offen offset:1024 lds
M v[184:187],v[168:171],a[80:83],v[184:187]
T v[162:163],v217,18432
W 5
M v[184:187],v[164:167],a[72:75],v[184:187]
T v[164:165],v217,20480
s_add_u32 s79, s2, 199680
M v[192:195],v[176:179],a[48:51],0
T v[166:167],v217,22528
s_mov_b32 m0, 95232
M v[192:195],v[180:183],a[56:59],v[192:195]
T v[168:169],v218,0
W 5
M v[192:195],v[152:155],a[64:67],v[192:195]
T v[170:171],v218,2048
buffer_load_dwordx4 v227, s[8:11], s79 offen nt lds
T v[172:173],v218,4096
s_mov_b32 m0, 125952
s_nop 0
buffer_load_dwordx4 v232, s[20:23], s78 offen offset:2048 lds
s_add_u32 s78, s69, 92160
T v[174:175],v218,6144
s_mov_b32 m0, 132096
v_exp_f32 v201, v121
T v[176:177],v218,8192
s_mov_b32 m0, 95232
s_mov_b32 m0, 132096
s_mov_b32 m0, 95232
T v[178:179],v218,10240
v_cvt_pk_bf16_f32 v252, v184, v185
v_cvt_pk_bf16_f32 v253, v186, v187
v_accvgpr_write_b32 a112, v252
T v[180:181],v218,12288
buffer_load_dwordx4 v228, s[8:11], s79 offen offset:1024 nt lds
s_mov_b32 m0, 132096
v_accvgpr_write_b32 a113, v253
T v[182:183],v218,14336
buffer_load_dwordx4 v230, s[20:23], s78 offen lds
v_cvt_pk_bf16_f32 v144, v200, v201
W 5
M v[192:195],v[164:167],a[88:91],v[192:195]
T v[152:153],v218,16384
v_add_u32 v220, 24576, v224
M v[192:195],v[160:163],a[80:83],v[192:195]
T v[154:155],v218,18432
s_mov_b32 m0, 95232
M v[192:195],v[156:159],a[72:75],v[192:195]
T v[156:157],v218,20480
s_mov_b32 m0, 132096
M v[184:187],v[168:171],a[48:51],0
T v[158:159],v218,22528
W 5
M v[184:187],v[172:175],a[56:59],v[184:187]
T v[160:161],v219,0
s_mov_b32 m0, 95232
M v[184:187],v[176:179],a[64:67],v[184:187]
T v[162:163],v219,2048
buffer_load_dwordx4 v229, s[8:11], s79 offen offset:2048 nt lds
T v[164:165],v219,4096
s_mov_b32 m0, 132096
s_nop 0
buffer_load_dwordx4 v231, s[20:23], s78 offen offset:1024 lds
v_xor_b32 v221, 32, v220
T v[166:167],v219,6144
v_exp_f32 v202, v122
v_xor_b32 v222, 64, v220
T v[168:169],v219,8192
buffer_load_dwordx4 v232, s[20:23], s78 offen offset:2048 lds
s_add_u32 s78, s69, 153600
s_mov_b32 m0, 138240
T v[170:171],v219,10240
v_cvt_pk_bf16_f32 v252, v192, v193
v_cvt_pk_bf16_f32 v253, v194, v195
v_accvgpr_write_b32 a114, v252
T v[172:173],v219,12288
buffer_load_dwordx4 v230, s[20:23], s78 offen lds
v_accvgpr_write_b32 a115, v253
v_xor_b32 v223, 96, v220
T v[174:175],v219,14336
v_exp_f32 v203, v123
W 5
M v[184:187],v[156:159],a[88:91],v[184:187]
T v[176:177],v219,16384
buffer_load_dwordx4 v231, s[20:23], s78 offen offset:1024 lds
M v[184:187],v[152:155],a[80:83],v[184:187]
T v[178:179],v219,18432
v_cvt_pk_bf16_f32 v145, v202, v203
M v[184:187],v[180:183],a[72:75],v[184:187]
T v[180:181],v219,20480
buffer_load_dwordx4 v232, s[20:23], s78 offen offset:2048 lds
M v[192:195],v[160:163],a[48:51],0
T v[182:183],v219,22528
W 5
M v[192:195],v[164:167],a[56:59],v[192:195]
s_waitcnt vmcnt(38)
s_barrier
T v[152:153],v220,0
M v[192:195],v[168:171],a[64:67],v[192:195]
T v[154:155],v220,6144
s_add_u32 s78, s69, 184320
T v[156:157],v221,0
s_mov_b32 m0, 141312
s_nop 0
buffer_load_dwordx4 v230, s[20:23], s78 offen lds
W 5
T v[158:159],v221,6144
v_exp_f32 v200, v128
T v[160:161],v222,0
buffer_load_dwordx4 v231, s[20:23], s78 offen offset:1024 lds
v_exp_f32 v201, v129
T v[162:163],v222,6144
v_exp_f32 v202, v130
v_cvt_pk_bf16_f32 v146, v200, v201
T v[164:165],v223,0
buffer_load_dwordx4 v232, s[20:23], s78 offen offset:2048 lds
s_add_u32 s78, s69, 215040
s_mov_b32 m0, 144384
T v[166:167],v223,6144
v_exp_f32 v203, v131
W 5
M v[192:195],v[180:183],a[88:91],v[192:195]
T v[168:169],v220,128
buffer_load_dwordx4 v230, s[20:23], s78 offen lds
M v[192:195],v[176:179],a[80:83],v[192:195]
T v[170:171],v220,6272
v_cvt_pk_bf16_f32 v147, v202, v203
M v[192:195],v[172:175],a[72:75],v[192:195]
T v[172:173],v221,128
buffer_load_dwordx4 v231, s[20:23], s78 offen offset:1024 lds
buffer_load_dwordx4 v232, s[20:23], s78 offen offset:2048 lds
M v[0:3],v[152:155],v[136:139],v[0:3]
T v[174:175],v221,6272
s_add_u32 s79, s2, 181248
s_mov_b32 m0, 101376
s_nop 0
buffer_load_dwordx4 v227, s[12:15], s79 offen nt lds
T v[176:177],v222,128
v_cvt_pk_bf16_f32 v252, v184, v185
v_cvt_pk_bf16_f32 v253, v186, v187
v_accvgpr_write_b32 a120, v252
M v[96:99],v[244:247],v[136:139],v[96:99]
T v[178:179],v222,6272
v_accvgpr_write_b32 a121, v253
T v[180:181],v223,128
W 5
M v[8:11],v[156:159],v[136:139],v[8:11]
T v[182:183],v223,6272
T v[152:153],v220,256
buffer_load_dwordx4 v228, s[12:15], s79 offen offset:1024 nt lds
v_cvt_pk_bf16_f32 v252, v192, v193
v_cvt_pk_bf16_f32 v253, v194, v195
M v[16:19],v[160:163],v[136:139],v[16:19]
T v[154:155],v220,6400
v_accvgpr_write_b32 a122, v252
T v[156:157],v221,256
v_accvgpr_write_b32 a123, v253
W 5
M v[24:27],v[164:167],v[136:139],v[24:27]
T v[158:159],v221,6400
T v[160:161],v222,256
W 5
M v[32:35],v[168:171],v[136:139],v[32:35]
T v[162:163],v222,6400
buffer_load_dwordx4 v229, s[12:15], s79 offen offset:2048 nt lds
T v[164:165],v223,256
W 5
M v[40:43],v[172:175],v[136:139],v[40:43]
T v[166:167],v223,6400
T v[168:169],v220,12288
s_add_u32 s79, s2, 187392
s_mov_b32 m0, 107520
W 5
M v[48:51],v[176:179],v[136:139],v[48:51]
T v[170:171],v220,18432
buffer_load_dwordx4 v227, s[12:15], s79 offen nt lds
W 4
M v[56:59],v[180:183],v[136:139],v[56:59]
T v[172:173],v221,12288
W 3
M v[64:67],v[152:155],v[136:139],v[64:67]
T v[174:175],v221,18432
T v[176:177],v222,12288
W 3
M v[72:75],v[156:159],v[136:139],v[72:75]
T v[178:179],v222,18432
buffer_load_dwordx4 v228, s[12:15], s79 offen offset:1024 nt lds
T v[180:181],v223,12288
v_add_u32 v216, 49152, v226
v_xor_b32 v217, 32, v216
v_xor_b32 v218, 64, v216
M v[80:83],v[160:163],v[136:139],v[80:83]
T v[182:183],v223,18432
v_xor_b32 v219, 96, v216
T v[152:153],v220,12416
W 5
M v[88:91],v[164:167],v[136:139],v[88:91]
T v[154:155],v220,18560
T v[156:157],v221,12416
buffer_load_dwordx4 v229, s[12:15], s79 offen offset:2048 nt lds
W 5
M v[0:3],v[168:171],v[144:147],v[0:3]
T v[158:159],v221,18560
T v[160:161],v222,12416
W 5
M v[96:99],v[244:247],v[144:147],v[96:99]
T v[162:163],v222,18560
s_add_u32 s79, s2, 193536
T v[164:165],v223,12416
s_mov_b32 m0, 113664
W 5
M v[8:11],v[172:175],v[144:147],v[8:11]
T v[166:167],v223,18560
buffer_load_dwordx4 v227, s[12:15], s79 offen nt lds
T v[168:169],v220,12544
W 5
M v[16:19],v[176:179],v[144:147],v[16:19]
T v[170:171],v220,18688
T v[172:173],v221,12544
W 5
M v[24:27],v[180:183],v[144:147],v[24:27]
T v[174:175],v221,18688
T v[176:177],v222,12544
buffer_load_dwordx4 v228, s[12:15], s79 offen offset:1024 nt lds
W 5
M v[32:35],v[152:155],v[144:147],v[32:35]
T v[178:179],v222,18688
T v[180:181],v223,12544
W 5
M v[40:43],v[156:159],v[144:147],v[40:43]
T v[182:183],v223,18688
s_waitcnt vmcnt(38)
s_barrier
T v[152:153],v216,0
W 5
M v[48:51],v[160:163],v[144:147],v[48:51]
T v[154:155],v216,2048
buffer_load_dwordx4 v229, s[12:15], s79 offen offset:2048 nt lds
T v[156:157],v216,4096
W 5
M v[56:59],v[164:167],v[144:147],v[56:59]
T v[158:159],v216,6144
T v[160:161],v216,8192
s_add_u32 s79, s2, 196608
s_mov_b32 m0, 116736
W 5
M v[64:67],v[168:171],v[144:147],v[64:67]
T v[162:163],v216,10240
T v[164:165],v216,12288
buffer_load_dwordx4 v227, s[12:15], s79 offen nt lds
W 5
M v[72:75],v[172:175],v[144:147],v[72:75]
T v[166:167],v216,14336
T v[168:169],v216,16384
W 5
M v[80:83],v[176:179],v[144:147],v[80:83]
T v[170:171],v216,18432
T v[172:173],v216,20480
W 5
M v[88:91],v[180:183],v[144:147],v[88:91]
T v[174:175],v216,22528
buffer_load_dwordx4 v228, s[12:15], s79 offen offset:1024 nt lds
T v[176:177],v217,0
W 5
M v[184:187],v[152:155],a[48:51],0
T v[178:179],v217,2048
s_add_u32 s78, s70, 30720
M v[184:187],v[156:159],a[56:59],v[184:187]
T v[180:181],v217,4096
s_mov_b32 m0, 3072
M v[184:187],v[160:163],a[64:67],v[184:187]
T v[182:183],v217,6144
s_mov_b32 m0, 116736
s_nop 0
buffer_load_dwordx4 v229, s[12:15], s79 offen offset:2048 nt lds
s_mov_b32 m0, 3072
W 4
buffer_load_dwordx4 v230, s[20:23], s78 offen lds
T v[152:153],v217,8192
W 3
T v[154:155],v217,10240
s_add_u32 s79, s2, 199680
s_mov_b32 m0, 119808
s_mov_b32 m0, 3072
T v[156:157],v217,12288
s_mov_b32 m0, 119808
s_mov_b32 m0, 3072
s_mov_b32 m0, 119808
T v[158:159],v217,14336
buffer_load_dwordx4 v227, s[12:15], s79 offen nt lds
s_mov_b32 m0, 3072
W 4
M v[184:187],v[172:175],a[88:91],v[184:187]
T v[160:161],v217,16384
buffer_load_dwordx4 v231, s[20:23], s78 offen offset:1024 lds
M v[184:187],v[168:171],a[80:83],v[184:187]
T v[162:163],v217,18432
M v[184:187],v[164:167],a[72:75],v[184:187]
T v[164:165],v217,20480
s_mov_b32 m0, 119808
M v[192:195],v[176:179],a[48:51],0
T v[166:167],v217,22528
s_mov_b32 m0, 3072
M v[192:195],v[180:183],a[56:59],v[192:195]
T v[168:169],v218,0
W 5
M v[192:195],v[152:155],a[64:67],v[192:195]
T v[170:171],v218,2048
s_mov_b32 m0, 119808
T v[172:173],v218,4096
buffer_load_dwordx4 v228, s[12:15], s79 offen offset:1024 nt lds
s_mov_b32 m0, 3072
W 5
T v[174:175],v218,6144
buffer_load_dwordx4 v232, s[20:23], s78 offen offset:2048 lds
s_add_u32 s78, s70, 92160
s_mov_b32 m0, 9216
T v[176:177],v218,8192
v_cvt_pk_bf16_f32 v252, v184, v185
v_cvt_pk_bf16_f32 v253, v186, v187
v_accvgpr_write_b32 a128, v252
T v[178:179],v218,10240
s_mov_b32 m0, 119808
s_mov_b32 m0, 9216
s_mov_b32 m0, 119808
T v[180:181],v218,12288
s_mov_b32 m0, 9216
s_mov_b32 m0, 119808
s_mov_b32 m0, 9216
T v[182:183],v218,14336
s_mov_b32 m0, 119808
s_nop 0
buffer_load_dwordx4 v229, s[12:15], s79 offen offset:2048 nt lds
W 5
M v[192:195],v[164:167],a[88:91],v[192:195]
T v[152:153],v218,16384
s_mov_b32 m0, 9216
M v[192:195],v[160:163],a[80:83],v[192:195]
T v[154:155],v218,18432
v_accvgpr_write_b32 a129, v253
M v[192:195],v[156:159],a[72:75],v[192:195]
T v[156:157],v218,20480
buffer_load_dwordx4 v230, s[20:23], s78 offen lds
M v[184:187],v[168:171],a[48:51],0
T v[158:159],v218,22528
W 5
M v[184:187],v[172:175],a[56:59],v[184:187]
T v[160:161],v219,0
buffer_load_dwordx4 v231, s[20:23], s78 offen offset:1024 lds
M v[184:187],v[176:179],a[64:67],v[184:187]
T v[162:163],v219,2048
v_add_u32 v220, 73728, v225
T v[164:165],v219,4096
buffer_load_dwordx4 v232, s[20:23], s78 offen offset:2048 lds
v_xor_b32 v221, 64, v220
s_add_u32 s78, s70, 153600
T v[166:167],v219,6144
s_mov_b32 m0, 15360
W 5
T v[168:169],v219,8192
buffer_load_dwordx4 v230, s[20:23], s78 offen lds
v_cvt_pk_bf16_f32 v252, v192, v193
v_cvt_pk_bf16_f32 v253, v194, v195
T v[170:171],v219,10240
v_accvgpr_write_b32 a130, v252
v_accvgpr_write_b32 a131, v253
W 5
T v[172:173],v219,12288
buffer_load_dwordx4 v231, s[20:23], s78 offen offset:1024 lds
T v[174:175],v219,14336
W 5
M v[184:187],v[156:159],a[88:91],v[184:187]
T v[176:177],v219,16384
buffer_load_dwordx4 v232, s[20:23], s78 offen offset:2048 lds
M v[184:187],v[152:155],a[80:83],v[184:187]
T v[178:179],v219,18432
s_add_u32 s78, s70, 184320
M v[184:187],v[180:183],a[72:75],v[184:187]
T v[180:181],v219,20480
s_mov_b32 m0, 18432
M v[192:195],v[160:163],a[48:51],0
T v[182:183],v219,22528
W 5
M v[192:195],v[164:167],a[56:59],v[192:195]
s_waitcnt vmcnt(35)
s_barrier
ds_read_b128 v[152:155], v220 offset:0
M v[192:195],v[168:171],a[64:67],v[192:195]
ds_read_b128 v[156:159], v221 offset:0
buffer_load_dwordx4 v230, s[20:23], s78 offen lds
W 4
buffer_load_dwordx4 v231, s[20:23], s78 offen offset:1024 lds
ds_read_b128 v[160:163], v220 offset:128
v_cvt_pk_bf16_f32 v252, v184, v185
v_cvt_pk_bf16_f32 v253, v186, v187
v_accvgpr_write_b32 a136, v252
ds_read_b128 v[164:167], v221 offset:128
buffer_load_dwordx4 v232, s[20:23], s78 offen offset:2048 lds
s_add_u32 s78, s70, 215040
s_mov_b32 m0, 21504
ds_read_b128 v[168:171], v220 offset:256
v_accvgpr_write_b32 a137, v253
W 3
buffer_load_dwordx4 v230, s[20:23], s78 offen lds
M v[192:195],v[180:183],a[88:91],v[192:195]
M v[192:195],v[176:179],a[80:83],v[192:195]
buffer_load_dwordx4 v231, s[20:23], s78 offen offset:1024 lds
M v[192:195],v[172:175],a[72:75],v[192:195]
ds_read_b128 v[172:175], v221 offset:256
W 2
buffer_load_dwordx4 v232, s[20:23], s78 offen offset:2048 lds
M v[104:107],v[152:155],a[0:3],v[204:207]
ds_read_b128 v[176:179], v220 offset:6144
s_add_u32 s79, s3, 181248
s_mov_b32 m0, 27648
s_nop 0
buffer_load_dwordx4 v227, s[8:11], s79 offen nt lds
M v[104:107],v[156:159],a[8:11],v[104:107]
ds_read_b128 v[180:183], v221 offset:6144
M v[104:107],v[160:163],a[16:19],v[104:107]
W 2
ds_read_b128 v[152:155], v220 offset:6272
buffer_load_dwordx4 v228, s[8:11], s79 offen offset:1024 nt lds
v_cvt_pk_bf16_f32 v252, v192, v193
v_cvt_pk_bf16_f32 v253, v194, v195
ds_read_b128 v[156:159], v221 offset:6272
v_accvgpr_write_b32 a138, v252
v_accvgpr_write_b32 a139, v253
W 2
ds_read_b128 v[160:163], v220 offset:6400
M v[104:107],v[172:175],a[40:43],v[104:107]
M v[104:107],v[168:171],a[32:35],v[104:107]
buffer_load_dwordx4 v229, s[8:11], s79 offen offset:2048 nt lds
M v[104:107],v[164:167],a[24:27],v[104:107]
ds_read_b128 v[164:167], v221 offset:6400
W 2
M v[112:115],v[176:179],a[0:3],v[204:207]
ds_read_b128 v[168:171], v220 offset:12288
M v[112:115],v[180:183],a[8:11],v[112:115]
ds_read_b128 v[172:175], v221 offset:12288
s_add_u32 s79, s3, 187392
M v[112:115],v[152:155],a[16:19],v[112:115]
s_mov_b32 m0, 33792
s_nop 0
buffer_load_dwordx4 v227, s[8:11], s79 offen nt lds
v_max3_f32 v248, v104, v105, v106
v_max_f32 v248, v248, v107
v_exp_f32 v200, v104
v_exp_f32 v201, v105
ds_read_b128 v[176:179], v220 offset:12416
v_exp_f32 v202, v106
v_cvt_pk_bf16_f32 v136, v200, v201
ds_read_b128 v[180:183], v221 offset:12416
v_exp_f32 v203, v107
s_nop 0
v_cvt_pk_bf16_f32 v137, v202, v203
ds_read_b128 v[152:155], v220 offset:12544
v_add_u32 v216, 122880, v226
v_xor_b32 v217, 32, v216
v_xor_b32 v218, 64, v216
buffer_load_dwordx4 v228, s[8:11], s79 offen offset:1024 nt lds
v_xor_b32 v219, 96, v216
W 3
M v[112:115],v[164:167],a[40:43],v[112:115]
M v[112:115],v[160:163],a[32:35],v[112:115]
M v[112:115],v[156:159],a[24:27],v[112:115]
ds_read_b128 v[156:159], v221 offset:12544
W 2
M v[120:123],v[168:171],a[0:3],v[204:207]
ds_read_b128 v[160:163], v220 offset:18432
M v[120:123],v[172:175],a[8:11],v[120:123]
ds_read_b128 v[164:167], v221 offset:18432
buffer_load_dwordx4 v229, s[8:11], s79 offen offset:2048 nt lds
M v[120:123],v[176:179],a[16:19],v[120:123]
v_max3_f32 v248, v248, v112, v113
v_max3_f32 v248, v248, v114, v115
v_exp_f32 v200, v112
s_add_u32 s79, s3, 193536
s_mov_b32 m0, 39936
v_exp_f32 v201, v113
ds_read_b128 v[168:171], v220 offset:18560
v_exp_f32 v202, v114
v_cvt_pk_bf16_f32 v138, v200, v201
ds_read_b128 v[172:175], v221 offset:18560
buffer_load_dwordx4 v227, s[8:11], s79 offen nt lds
v_exp_f32 v203, v115
ds_read_b128 v[176:179], v220 offset:18688
v_cvt_pk_bf16_f32 v139, v202, v203
W 3
M v[120:123],v[156:159],a[40:43],v[120:123]
M v[120:123],v[152:155],a[32:35],v[120:123]
M v[120:123],v[180:183],a[24:27],v[120:123]
ds_read_b128 v[180:183], v221 offset:18688
buffer_load_dwordx4 v228, s[8:11], s79 offen offset:1024 nt lds
M v[128:131],v[160:163],a[0:3],v[204:207]
s_waitcnt vmcnt(38)
s_barrier
T v[152:153],v216,0
M v[128:131],v[164:167],a[8:11],v[128:131]
T v[154:155],v216,2048
W 3
M v[128:131],v[168:171],a[16:19],v[128:131]
T v[156:157],v216,4096
T v[158:159],v216,6144
v_max3_f32 v248, v248, v120, v121
v_max3_f32 v248, v248, v122, v123
buffer_load_dwordx4 v229, s[8:11], s79 offen offset:2048 nt lds
T v[160:161],v216,8192
T v[162:163],v216,10240
W 5
T v[164:165],v216,12288
T v[166:167],v216,14336
s_add_u32 s79, s3, 196608
s_mov_b32 m0, 43008
W 5
T v[168:169],v216,16384
M v[128:131],v[180:183],a[40:43],v[128:131]
T v[170:171],v216,18432
buffer_load_dwordx4 v227, s[8:11], s79 offen nt lds
M v[128:131],v[176:179],a[32:35],v[128:131]
W 4
M v[128:131],v[172:175],a[24:27],v[128:131]
T v[172:173],v216,20480
T v[174:175],v216,22528
M v[184:187],v[152:155],a[48:51],0
T v[176:177],v217,0
s_add_u32 s78, s71, 30720
M v[184:187],v[156:159],a[56:59],v[184:187]
T v[178:179],v217,2048
s_mov_b32 m0, 52224
M v[184:187],v[160:163],a[64:67],v[184:187]
T v[180:181],v217,4096
s_mov_b32 m0, 43008
T v[182:183],v217,6144
buffer_load_dwordx4 v228, s[8:11], s79 offen offset:1024 nt lds
s_mov_b32 m0, 52224
v_max3_f32 v248, v248, v128, v129
buffer_load_dwordx4 v230, s[20:23], s78 offen lds
v_max3_f32 v248, v248, v130, v131
v_cmp_lt_f32 vcc, s60, v248
s_nop 1
s_cbranch_vccnz LST1_2w_%=
LRT1_2w_%=:
T v[152:153],v217,8192
v_exp_f32 v200, v120
W 4
T v[154:155],v217,10240
s_mov_b32 m0, 43008
s_mov_b32 m0, 52224
s_mov_b32 m0, 43008
T v[156:157],v217,12288
s_mov_b32 m0, 52224
s_mov_b32 m0, 43008
s_mov_b32 m0, 52224
T v[158:159],v217,14336
s_mov_b32 m0, 43008
s_nop 0
buffer_load_dwordx4 v229, s[8:11], s79 offen offset:2048 nt lds
s_mov_b32 m0, 52224
M v[184:187],v[172:175],a[88:91],v[184:187]
T v[160:161],v217,16384
buffer_load_dwordx4 v231, s[20:23], s78 offen offset:1024 lds
M v[184:187],v[168:171],a[80:83],v[184:187]
T v[162:163],v217,18432
W 5
M v[184:187],v[164:167],a[72:75],v[184:187]
T v[164:165],v217,20480
s_add_u32 s79, s3, 199680
M v[192:195],v[176:179],a[48:51],0
T v[166:167],v217,22528
s_mov_b32 m0, 46080
M v[192:195],v[180:183],a[56:59],v[192:195]
T v[168:169],v218,0
W 5
M v[192:195],v[152:155],a[64:67],v[192:195]
T v[170:171],v218,2048
buffer_load_dwordx4 v227, s[8:11], s79 offen nt lds
T v[172:173],v218,4096
s_mov_b32 m0, 52224
s_nop 0
buffer_load_dwordx4 v232, s[20:23], s78 offen offset:2048 lds
s_add_u32 s78, s71, 92160
T v[174:175],v218,6144
s_mov_b32 m0, 58368
v_exp_f32 v201, v121
T v[176:177],v218,8192
s_mov_b32 m0, 46080
s_mov_b32 m0, 58368
s_mov_b32 m0, 46080
T v[178:179],v218,10240
v_cvt_pk_bf16_f32 v252, v184, v185
v_cvt_pk_bf16_f32 v253, v186, v187
v_accvgpr_write_b32 a144, v252
T v[180:181],v218,12288
buffer_load_dwordx4 v228, s[8:11], s79 offen offset:1024 nt lds
s_mov_b32 m0, 58368
v_accvgpr_write_b32 a145, v253
T v[182:183],v218,14336
buffer_load_dwordx4 v230, s[20:23], s78 offen lds
v_cvt_pk_bf16_f32 v144, v200, v201
W 5
M v[192:195],v[164:167],a[88:91],v[192:195]
T v[152:153],v218,16384
v_add_u32 v220, 98304, v224
M v[192:195],v[160:163],a[80:83],v[192:195]
T v[154:155],v218,18432
s_mov_b32 m0, 46080
M v[192:195],v[156:159],a[72:75],v[192:195]
T v[156:157],v218,20480
s_mov_b32 m0, 58368
M v[184:187],v[168:171],a[48:51],0
T v[158:159],v218,22528
W 5
M v[184:187],v[172:175],a[56:59],v[184:187]
T v[160:161],v219,0
s_mov_b32 m0, 46080
M v[184:187],v[176:179],a[64:67],v[184:187]
T v[162:163],v219,2048
buffer_load_dwordx4 v229, s[8:11], s79 offen offset:2048 nt lds
T v[164:165],v219,4096
s_mov_b32 m0, 58368
s_nop 0
buffer_load_dwordx4 v231, s[20:23], s78 offen offset:1024 lds
v_xor_b32 v221, 32, v220
T v[166:167],v219,6144
v_exp_f32 v202, v122
v_xor_b32 v222, 64, v220
T v[168:169],v219,8192
buffer_load_dwordx4 v232, s[20:23], s78 offen offset:2048 lds
s_add_u32 s78, s71, 153600
s_mov_b32 m0, 64512
T v[170:171],v219,10240
v_cvt_pk_bf16_f32 v252, v192, v193
v_cvt_pk_bf16_f32 v253, v194, v195
v_accvgpr_write_b32 a146, v252
T v[172:173],v219,12288
buffer_load_dwordx4 v230, s[20:23], s78 offen lds
v_accvgpr_write_b32 a147, v253
v_xor_b32 v223, 96, v220
T v[174:175],v219,14336
v_exp_f32 v203, v123
W 5
M v[184:187],v[156:159],a[88:91],v[184:187]
T v[176:177],v219,16384
buffer_load_dwordx4 v231, s[20:23], s78 offen offset:1024 lds
M v[184:187],v[152:155],a[80:83],v[184:187]
T v[178:179],v219,18432
v_cvt_pk_bf16_f32 v145, v202, v203
M v[184:187],v[180:183],a[72:75],v[184:187]
T v[180:181],v219,20480
buffer_load_dwordx4 v232, s[20:23], s78 offen offset:2048 lds
M v[192:195],v[160:163],a[48:51],0
T v[182:183],v219,22528
W 5
M v[192:195],v[164:167],a[56:59],v[192:195]
s_waitcnt vmcnt(36)
s_barrier
T v[152:153],v220,0
M v[192:195],v[168:171],a[64:67],v[192:195]
T v[154:155],v220,6144
s_add_u32 s78, s71, 184320
T v[156:157],v221,0
s_mov_b32 m0, 67584
s_nop 0
buffer_load_dwordx4 v230, s[20:23], s78 offen lds
W 5
T v[158:159],v221,6144
v_exp_f32 v200, v128
T v[160:161],v222,0
buffer_load_dwordx4 v231, s[20:23], s78 offen offset:1024 lds
v_exp_f32 v201, v129
T v[162:163],v222,6144
v_exp_f32 v202, v130
v_cvt_pk_bf16_f32 v146, v200, v201
T v[164:165],v223,0
buffer_load_dwordx4 v232, s[20:23], s78 offen offset:2048 lds
s_add_u32 s78, s71, 215040
s_mov_b32 m0, 70656
T v[166:167],v223,6144
v_exp_f32 v203, v131
W 5
M v[192:195],v[180:183],a[88:91],v[192:195]
T v[168:169],v220,128
buffer_load_dwordx4 v230, s[20:23], s78 offen lds
M v[192:195],v[176:179],a[80:83],v[192:195]
T v[170:171],v220,6272
v_cvt_pk_bf16_f32 v147, v202, v203
M v[192:195],v[172:175],a[72:75],v[192:195]
T v[172:173],v221,128
buffer_load_dwordx4 v231, s[20:23], s78 offen offset:1024 lds
buffer_load_dwordx4 v232, s[20:23], s78 offen offset:2048 lds
M v[0:3],v[152:155],v[136:139],v[0:3]
T v[174:175],v221,6272
s_add_u32 s79, s3, 181248
s_mov_b32 m0, 76800
s_nop 0
buffer_load_dwordx4 v227, s[12:15], s79 offen nt lds
T v[176:177],v222,128
v_cvt_pk_bf16_f32 v252, v184, v185
v_cvt_pk_bf16_f32 v253, v186, v187
v_accvgpr_write_b32 a152, v252
M v[96:99],v[244:247],v[136:139],v[96:99]
T v[178:179],v222,6272
v_accvgpr_write_b32 a153, v253
T v[180:181],v223,128
W 5
M v[8:11],v[156:159],v[136:139],v[8:11]
T v[182:183],v223,6272
T v[152:153],v220,256
buffer_load_dwordx4 v228, s[12:15], s79 offen offset:1024 nt lds
v_cvt_pk_bf16_f32 v252, v192, v193
v_cvt_pk_bf16_f32 v253, v194, v195
M v[16:19],v[160:163],v[136:139],v[16:19]
T v[154:155],v220,6400
v_accvgpr_write_b32 a154, v252
T v[156:157],v221,256
v_accvgpr_write_b32 a155, v253
W 5
M v[24:27],v[164:167],v[136:139],v[24:27]
T v[158:159],v221,6400
T v[160:161],v222,256
W 5
M v[32:35],v[168:171],v[136:139],v[32:35]
T v[162:163],v222,6400
buffer_load_dwordx4 v229, s[12:15], s79 offen offset:2048 nt lds
T v[164:165],v223,256
W 5
M v[40:43],v[172:175],v[136:139],v[40:43]
T v[166:167],v223,6400
T v[168:169],v220,12288
s_add_u32 s79, s3, 187392
s_mov_b32 m0, 82944
W 5
M v[48:51],v[176:179],v[136:139],v[48:51]
T v[170:171],v220,18432
buffer_load_dwordx4 v227, s[12:15], s79 offen nt lds
W 4
M v[56:59],v[180:183],v[136:139],v[56:59]
T v[172:173],v221,12288
W 3
M v[64:67],v[152:155],v[136:139],v[64:67]
T v[174:175],v221,18432
T v[176:177],v222,12288
W 3
M v[72:75],v[156:159],v[136:139],v[72:75]
T v[178:179],v222,18432
buffer_load_dwordx4 v228, s[12:15], s79 offen offset:1024 nt lds
T v[180:181],v223,12288
v_add_u32 v216, 0, v226
v_xor_b32 v217, 32, v216
v_xor_b32 v218, 64, v216
M v[80:83],v[160:163],v[136:139],v[80:83]
T v[182:183],v223,18432
v_xor_b32 v219, 96, v216
T v[152:153],v220,12416
W 5
M v[88:91],v[164:167],v[136:139],v[88:91]
T v[154:155],v220,18560
T v[156:157],v221,12416
buffer_load_dwordx4 v229, s[12:15], s79 offen offset:2048 nt lds
W 5
M v[0:3],v[168:171],v[144:147],v[0:3]
T v[158:159],v221,18560
T v[160:161],v222,12416
W 5
M v[96:99],v[244:247],v[144:147],v[96:99]
T v[162:163],v222,18560
s_add_u32 s79, s3, 193536
T v[164:165],v223,12416
s_mov_b32 m0, 89088
buffer_load_dword v215, v214, s[28:31], 0 offen sc1
W 5
M v[8:11],v[172:175],v[144:147],v[8:11]
T v[166:167],v223,18560
buffer_load_dwordx4 v227, s[12:15], s79 offen nt lds
T v[168:169],v220,12544
W 5
M v[16:19],v[176:179],v[144:147],v[16:19]
T v[170:171],v220,18688
T v[172:173],v221,12544
W 5
M v[24:27],v[180:183],v[144:147],v[24:27]
T v[174:175],v221,18688
T v[176:177],v222,12544
buffer_load_dwordx4 v228, s[12:15], s79 offen offset:1024 nt lds
W 5
M v[32:35],v[152:155],v[144:147],v[32:35]
T v[178:179],v222,18688
T v[180:181],v223,12544
W 5
M v[40:43],v[156:159],v[144:147],v[40:43]
T v[182:183],v223,18688
s_waitcnt vmcnt(39)
s_barrier
T v[152:153],v216,0
W 5
M v[48:51],v[160:163],v[144:147],v[48:51]
T v[154:155],v216,2048
buffer_load_dwordx4 v229, s[12:15], s79 offen offset:2048 nt lds
T v[156:157],v216,4096
W 5
M v[56:59],v[164:167],v[144:147],v[56:59]
T v[158:159],v216,6144
T v[160:161],v216,8192
s_add_u32 s79, s3, 196608
s_mov_b32 m0, 92160
W 5
M v[64:67],v[168:171],v[144:147],v[64:67]
T v[162:163],v216,10240
T v[164:165],v216,12288
buffer_load_dwordx4 v227, s[12:15], s79 offen nt lds
W 5
M v[72:75],v[172:175],v[144:147],v[72:75]
T v[166:167],v216,14336
T v[168:169],v216,16384
W 5
M v[80:83],v[176:179],v[144:147],v[80:83]
T v[170:171],v216,18432
T v[172:173],v216,20480
W 5
M v[88:91],v[180:183],v[144:147],v[88:91]
T v[174:175],v216,22528
buffer_load_dwordx4 v228, s[12:15], s79 offen offset:1024 nt lds
T v[176:177],v217,0
W 5
M v[184:187],v[152:155],a[48:51],0
T v[178:179],v217,2048
buffer_load_dwordx4 v229, s[12:15], s79 offen offset:2048 nt lds
M v[184:187],v[156:159],a[56:59],v[184:187]
T v[180:181],v217,4096
s_add_u32 s79, s3, 199680
M v[184:187],v[160:163],a[64:67],v[184:187]
T v[182:183],v217,6144
s_mov_b32 m0, 95232
s_nop 0
buffer_load_dwordx4 v227, s[12:15], s79 offen nt lds
W 4
T v[152:153],v217,8192
T v[154:155],v217,10240
buffer_load_dwordx4 v228, s[12:15], s79 offen offset:1024 nt lds
W 4
T v[156:157],v217,12288
T v[158:159],v217,14336
buffer_load_dwordx4 v229, s[12:15], s79 offen offset:2048 nt lds
W 4
T v[160:161],v217,16384
s_add_u32 s79, s4, 181248
s_mov_b32 m0, 125952
M v[184:187],v[172:175],a[88:91],v[184:187]
T v[162:163],v217,18432
buffer_load_dwordx4 v227, s[8:11], s79 offen nt lds
M v[184:187],v[168:171],a[80:83],v[184:187]
W 4
M v[184:187],v[164:167],a[72:75],v[184:187]
T v[164:165],v217,20480
T v[166:167],v217,22528
M v[192:195],v[176:179],a[48:51],0
T v[168:169],v218,0
buffer_load_dwordx4 v228, s[8:11], s79 offen offset:1024 nt lds
M v[192:195],v[180:183],a[56:59],v[192:195]
T v[170:171],v218,2048
W 5
M v[192:195],v[152:155],a[64:67],v[192:195]
T v[172:173],v218,4096
T v[174:175],v218,6144
buffer_load_dwordx4 v229, s[8:11], s79 offen offset:2048 nt lds
W 4
T v[176:177],v218,8192
v_cvt_pk_bf16_f32 v252, v184, v185
v_cvt_pk_bf16_f32 v253, v186, v187
v_accvgpr_write_b32 a160, v252
T v[178:179],v218,10240
v_accvgpr_write_b32 a161, v253
v_add_u32 v220, 24576, v225
v_xor_b32 v221, 64, v220
T v[180:181],v218,12288
s_add_u32 s79, s4, 187392
s_mov_b32 m0, 132096
W 4
T v[182:183],v218,14336
M v[192:195],v[164:167],a[88:91],v[192:195]
T v[152:153],v218,16384
T v[154:155],v218,18432
M v[192:195],v[160:163],a[80:83],v[192:195]
buffer_load_dwordx4 v227, s[8:11], s79 offen nt lds
W 5
M v[192:195],v[156:159],a[72:75],v[192:195]
T v[156:157],v218,20480
T v[158:159],v218,22528
M v[184:187],v[168:171],a[48:51],0
T v[160:161],v219,0
W 6
M v[184:187],v[172:175],a[56:59],v[184:187]
T v[162:163],v219,2048
M v[184:187],v[176:179],a[64:67],v[184:187]
T v[164:165],v219,4096
buffer_load_dwordx4 v228, s[8:11], s79 offen offset:1024 nt lds
T v[166:167],v219,6144
W 6
W 4
T v[168:169],v219,8192
v_cvt_pk_bf16_f32 v252, v192, v193
v_cvt_pk_bf16_f32 v253, v194, v195
v_accvgpr_write_b32 a162, v252
T v[170:171],v219,10240
v_accvgpr_write_b32 a163, v253
W 4
T v[172:173],v219,12288
buffer_load_dwordx4 v229, s[8:11], s79 offen offset:2048 nt lds
T v[174:175],v219,14336
W 4
M v[184:187],v[156:159],a[88:91],v[184:187]
T v[176:177],v219,16384
T v[178:179],v219,18432
M v[184:187],v[152:155],a[80:83],v[184:187]
s_add_u32 s79, s4, 193536
s_mov_b32 m0, 138240
M v[184:187],v[180:183],a[72:75],v[184:187]
T v[180:181],v219,20480
T v[182:183],v219,22528
M v[192:195],v[160:163],a[48:51],0
s_waitcnt vmcnt(33)
s_barrier
ds_read_b128 v[152:155], v220 offset:0
buffer_load_dwordx4 v227, s[8:11], s79 offen nt lds
M v[192:195],v[164:167],a[56:59],v[192:195]
ds_read_b128 v[156:159], v221 offset:0
W 6
M v[192:195],v[168:171],a[64:67],v[192:195]
W 4
W 2
ds_read_b128 v[160:163], v220 offset:128
buffer_load_dwordx4 v228, s[8:11], s79 offen offset:1024 nt lds
v_cvt_pk_bf16_f32 v252, v184, v185
v_cvt_pk_bf16_f32 v253, v186, v187
ds_read_b128 v[164:167], v221 offset:128
v_accvgpr_write_b32 a168, v252
v_accvgpr_write_b32 a169, v253
W 2
ds_read_b128 v[168:171], v220 offset:256
M v[192:195],v[180:183],a[88:91],v[192:195]
M v[192:195],v[176:179],a[80:83],v[192:195]
buffer_load_dwordx4 v229, s[8:11], s79 offen offset:2048 nt lds
M v[192:195],v[172:175],a[72:75],v[192:195]
ds_read_b128 v[172:175], v221 offset:256
W 2
M v[104:107],v[152:155],a[0:3],v[204:207]
ds_read_b128 v[176:179], v220 offset:6144
s_add_u32 s78, s96, 30720
M v[104:107],v[156:159],a[8:11],v[104:107]
ds_read_b128 v[180:183], v221 offset:6144
s_add_u32 s79, s4, 196608
M v[104:107],v[160:163],a[16:19],v[104:107]
s_mov_b32 m0, 141312
s_mov_b32 m0, 101376
s_mov_b32 m0, 141312
s_nop 0
buffer_load_dwordx4 v227, s[8:11], s79 offen nt lds
s_mov_b32 m0, 101376
s_nop 0
buffer_load_dwordx4 v230, s[20:23], s78 offen lds
W 2
ds_read_b128 v[152:155], v220 offset:6272
v_cvt_pk_bf16_f32 v252, v192, v193
v_cvt_pk_bf16_f32 v253, v194, v195
v_accvgpr_write_b32 a170, v252
ds_read_b128 v[156:159], v221 offset:6272
s_mov_b32 m0, 141312
s_mov_b32 m0, 101376
s_mov_b32 m0, 141312
ds_read_b128 v[160:163], v220 offset:6400
s_mov_b32 m0, 101376
s_mov_b32 m0, 141312
s_mov_b32 m0, 101376
s_mov_b32 m0, 141312
s_nop 0
buffer_load_dwordx4 v228, s[8:11], s79 offen offset:1024 nt lds
s_mov_b32 m0, 101376
v_accvgpr_write_b32 a171, v253
M v[104:107],v[172:175],a[40:43],v[104:107]
buffer_load_dwordx4 v231, s[20:23], s78 offen offset:1024 lds
W 3
M v[104:107],v[168:171],a[32:35],v[104:107]
M v[104:107],v[164:167],a[24:27],v[104:107]
ds_read_b128 v[164:167], v221 offset:6400
s_mov_b32 m0, 141312
M v[112:115],v[176:179],a[0:3],v[204:207]
ds_read_b128 v[168:171], v220 offset:12288
s_mov_b32 m0, 101376
M v[112:115],v[180:183],a[8:11],v[112:115]
ds_read_b128 v[172:175], v221 offset:12288
W 3
M v[112:115],v[152:155],a[16:19],v[112:115]
s_mov_b32 m0, 141312
s_nop 0
buffer_load_dwordx4 v229, s[8:11], s79 offen offset:2048 nt lds
s_mov_b32 m0, 101376
s_nop 0
buffer_load_dwordx4 v232, s[20:23], s78 offen offset:2048 lds
s_add_u32 s78, s96, 92160
s_mov_b32 m0, 107520
v_max3_f32 v248, v104, v105, v106
v_max_f32 v248, v248, v107
v_exp_f32 v200, v104
ds_read_b128 v[176:179], v220 offset:12416
s_add_u32 s79, s4, 199680
s_mov_b32 m0, 144384
s_mov_b32 m0, 107520
ds_read_b128 v[180:183], v221 offset:12416
s_mov_b32 m0, 144384
s_nop 0
buffer_load_dwordx4 v227, s[8:11], s79 offen nt lds
s_mov_b32 m0, 107520
ds_read_b128 v[152:155], v220 offset:12544
buffer_load_dwordx4 v230, s[20:23], s78 offen lds
v_exp_f32 v201, v105
v_exp_f32 v202, v106
v_cvt_pk_bf16_f32 v136, v200, v201
W 3
M v[112:115],v[164:167],a[40:43],v[112:115]
s_mov_b32 m0, 144384
s_mov_b32 m0, 107520
M v[112:115],v[160:163],a[32:35],v[112:115]
s_mov_b32 m0, 144384
s_mov_b32 m0, 107520
M v[112:115],v[156:159],a[24:27],v[112:115]
ds_read_b128 v[156:159], v221 offset:12544
s_mov_b32 m0, 144384
M v[120:123],v[168:171],a[0:3],v[204:207]
v_exp_f32 v203, v107
M v[120:123],v[172:175],a[8:11],v[120:123]
ds_read_b128 v[160:163], v220 offset:18432
buffer_load_dwordx4 v228, s[8:11], s79 offen offset:1024 nt lds
W 2
M v[120:123],v[176:179],a[16:19],v[120:123]
ds_read_b128 v[164:167], v221 offset:18432
s_mov_b32 m0, 107520
s_nop 0
buffer_load_dwordx4 v231, s[20:23], s78 offen offset:1024 lds
v_cvt_pk_bf16_f32 v137, v202, v203
v_max3_f32 v248, v248, v112, v113
v_max3_f32 v248, v248, v114, v115
s_mov_b32 m0, 144384
s_nop 0
buffer_load_dwordx4 v229, s[8:11], s79 offen offset:2048 nt lds
s_mov_b32 m0, 107520
v_add_u32 v216, 49152, v226
ds_read_b128 v[168:171], v220 offset:18560
buffer_load_dwordx4 v232, s[20:23], s78 offen offset:2048 lds
s_add_u32 s78, s96, 153600
s_mov_b32 m0, 113664
ds_read_b128 v[172:175], v221 offset:18560
v_exp_f32 v200, v112
v_xor_b32 v217, 32, v216
ds_read_b128 v[176:179], v220 offset:18688
buffer_load_dwordx4 v230, s[20:23], s78 offen lds
v_exp_f32 v201, v113
v_exp_f32 v202, v114
v_cvt_pk_bf16_f32 v138, v200, v201
W 3
M v[120:123],v[156:159],a[40:43],v[120:123]
buffer_load_dwordx4 v231, s[20:23], s78 offen offset:1024 lds
v_xor_b32 v218, 64, v216
M v[120:123],v[152:155],a[32:35],v[120:123]
v_exp_f32 v203, v115
M v[120:123],v[180:183],a[24:27],v[120:123]
ds_read_b128 v[180:183], v221 offset:18688
buffer_load_dwordx4 v232, s[20:23], s78 offen offset:2048 lds
M v[128:131],v[160:163],a[0:3],v[204:207]
v_xor_b32 v219, 96, v216
s_add_u32 s78, s96, 184320
M v[128:131],v[164:167],a[8:11],v[128:131]
s_waitcnt vmcnt(40)
s_barrier
T v[152:153],v216,0
W 2
M v[128:131],v[168:171],a[16:19],v[128:131]
T v[154:155],v216,2048
s_mov_b32 m0, 116736
T v[156:157],v216,4096
buffer_load_dwordx4 v230, s[20:23], s78 offen lds
v_cvt_pk_bf16_f32 v139, v202, v203
v_max3_f32 v248, v248, v120, v121
T v[158:159],v216,6144
v_max3_f32 v248, v248, v122, v123
T v[160:161],v216,8192
buffer_load_dwordx4 v231, s[20:23], s78 offen offset:1024 lds
T v[162:163],v216,10240
W 5
T v[164:165],v216,12288
buffer_load_dwordx4 v232, s[20:23], s78 offen offset:2048 lds
s_add_u32 s78, s96, 215040
s_mov_b32 m0, 119808
T v[166:167],v216,14336
W 5
M v[128:131],v[180:183],a[40:43],v[128:131]
T v[168:169],v216,16384
buffer_load_dwordx4 v230, s[20:23], s78 offen lds
M v[128:131],v[176:179],a[32:35],v[128:131]
T v[170:171],v216,18432
W 5
M v[128:131],v[172:175],a[24:27],v[128:131]
T v[172:173],v216,20480
buffer_load_dwordx4 v231, s[20:23], s78 offen offset:1024 lds
buffer_load_dwordx4 v232, s[20:23], s78 offen offset:2048 lds
M v[184:187],v[152:155],a[48:51],0
T v[174:175],v216,22528
s_add_u32 s79, s4, 181248
s_mov_b32 m0, 3072
s_nop 0
buffer_load_dwordx4 v227, s[12:15], s79 offen nt lds
M v[184:187],v[156:159],a[56:59],v[184:187]
T v[176:177],v217,0
W 5
M v[184:187],v[160:163],a[64:67],v[184:187]
T v[178:179],v217,2048
T v[180:181],v217,4096
v_max3_f32 v248, v248, v128, v129
v_max3_f32 v248, v248, v130, v131
v_cmp_lt_f32 vcc, s60, v248
T v[182:183],v217,6144
s_nop 0
s_cbranch_vccnz LST1_3w_%=
LRT1_3w_%=:
v_exp_f32 v200, v120
T v[152:153],v217,8192
buffer_load_dwordx4 v228, s[12:15], s79 offen offset:1024 nt lds
v_exp_f32 v201, v121
T v[154:155],v217,10240
v_exp_f32 v202, v122
v_cvt_pk_bf16_f32 v144, v200, v201
T v[156:157],v217,12288
v_exp_f32 v203, v123
s_nop 0
v_cvt_pk_bf16_f32 v145, v202, v203
T v[158:159],v217,14336
v_exp_f32 v200, v128
W 5
M v[184:187],v[172:175],a[88:91],v[184:187]
T v[160:161],v217,16384
M v[184:187],v[168:171],a[80:83],v[184:187]
T v[162:163],v217,18432
buffer_load_dwordx4 v229, s[12:15], s79 offen offset:2048 nt lds
M v[184:187],v[164:167],a[72:75],v[184:187]
T v[164:165],v217,20480
W 5
M v[192:195],v[176:179],a[48:51],0
T v[166:167],v217,22528
M v[192:195],v[180:183],a[56:59],v[192:195]
T v[168:169],v218,0
s_add_u32 s79, s4, 187392
M v[192:195],v[152:155],a[64:67],v[192:195]
T v[170:171],v218,2048
s_mov_b32 m0, 9216
T v[172:173],v218,4096
buffer_load_dwordx4 v227, s[12:15], s79 offen nt lds
v_exp_f32 v201, v129
T v[174:175],v218,6144
v_exp_f32 v202, v130
v_cvt_pk_bf16_f32 v146, v200, v201
T v[176:177],v218,8192
v_cvt_pk_bf16_f32 v252, v184, v185
v_cvt_pk_bf16_f32 v253, v186, v187
v_accvgpr_write_b32 a176, v252
T v[178:179],v218,10240
v_accvgpr_write_b32 a177, v253
v_exp_f32 v203, v131
T v[180:181],v218,12288
v_cvt_pk_bf16_f32 v147, v202, v203
v_add_u32 v220, 73728, v224
v_xor_b32 v221, 32, v220
T v[182:183],v218,14336
buffer_load_dwordx4 v228, s[12:15], s79 offen offset:1024 nt lds
v_xor_b32 v222, 64, v220
W 5
M v[192:195],v[164:167],a[88:91],v[192:195]
T v[152:153],v218,16384
T v[154:155],v218,18432
M v[192:195],v[160:163],a[80:83],v[192:195]
v_xor_b32 v223, 96, v220
W 5
M v[192:195],v[156:159],a[72:75],v[192:195]
T v[156:157],v218,20480
T v[158:159],v218,22528
M v[184:187],v[168:171],a[48:51],0
T v[160:161],v219,0
W 6
M v[184:187],v[172:175],a[56:59],v[184:187]
T v[162:163],v219,2048
buffer_load_dwordx4 v229, s[12:15], s79 offen offset:2048 nt lds
M v[184:187],v[176:179],a[64:67],v[184:187]
T v[164:165],v219,4096
W 5
T v[166:167],v219,6144
s_add_u32 s79, s4, 193536
s_mov_b32 m0, 15360
W 4
T v[168:169],v219,8192
v_cvt_pk_bf16_f32 v252, v192, v193
v_cvt_pk_bf16_f32 v253, v194, v195
v_accvgpr_write_b32 a178, v252
T v[170:171],v219,10240
buffer_load_dwordx4 v227, s[12:15], s79 offen nt lds
v_accvgpr_write_b32 a179, v253
W 4
T v[172:173],v219,12288
T v[174:175],v219,14336
W 4
M v[184:187],v[156:159],a[88:91],v[184:187]
T v[176:177],v219,16384
T v[178:179],v219,18432
M v[184:187],v[152:155],a[80:83],v[184:187]
M v[184:187],v[180:183],a[72:75],v[184:187]
T v[180:181],v219,20480
T v[182:183],v219,22528
M v[192:195],v[160:163],a[48:51],0
s_waitcnt vmcnt(37)
s_barrier
T v[152:153],v220,0
M v[192:195],v[164:167],a[56:59],v[192:195]
T v[154:155],v220,6144
buffer_load_dwordx4 v228, s[12:15], s79 offen offset:1024 nt lds
W 6
M v[192:195],v[168:171],a[64:67],v[192:195]
W 4
T v[156:157],v221,0
T v[158:159],v221,6144
W 4
T v[160:161],v222,0
v_cvt_pk_bf16_f32 v252, v184, v185
v_cvt_pk_bf16_f32 v253, v186, v187
v_accvgpr_write_b32 a184, v252
T v[162:163],v222,6144
buffer_load_dwordx4 v229, s[12:15], s79 offen offset:2048 nt lds
v_accvgpr_write_b32 a185, v253
W 4
T v[164:165],v223,0
T v[166:167],v223,6144
M v[192:195],v[180:183],a[88:91],v[192:195]
T v[168:169],v220,128
s_add_u32 s79, s4, 196608
M v[192:195],v[176:179],a[80:83],v[192:195]
T v[170:171],v220,6272
s_mov_b32 m0, 18432
M v[192:195],v[172:175],a[72:75],v[192:195]
T v[172:173],v221,128
buffer_load_dwordx4 v227, s[12:15], s79 offen nt lds
M v[0:3],v[152:155],v[136:139],v[0:3]
T v[174:175],v221,6272
W 5
T v[176:177],v222,128
buffer_load_dwordx4 v228, s[12:15], s79 offen offset:1024 nt lds
M v[96:99],v[244:247],v[136:139],v[96:99]
T v[178:179],v222,6272
W 5
T v[180:181],v223,128
buffer_load_dwordx4 v229, s[12:15], s79 offen offset:2048 nt lds
M v[8:11],v[156:159],v[136:139],v[8:11]
T v[182:183],v223,6272
s_add_u32 s79, s4, 199680
T v[152:153],v220,256
s_mov_b32 m0, 21504
s_nop 0
buffer_load_dwordx4 v227, s[12:15], s79 offen nt lds
v_cvt_pk_bf16_f32 v252, v192, v193
M v[16:19],v[160:163],v[136:139],v[16:19]
T v[154:155],v220,6400
v_cvt_pk_bf16_f32 v253, v194, v195
T v[156:157],v221,256
buffer_load_dwordx4 v228, s[12:15], s79 offen offset:1024 nt lds
v_accvgpr_write_b32 a186, v252
v_accvgpr_write_b32 a187, v253
M v[24:27],v[164:167],v[136:139],v[24:27]
T v[158:159],v221,6400
W 5
T v[160:161],v222,256
buffer_load_dwordx4 v229, s[12:15], s79 offen offset:2048 nt lds
M v[32:35],v[168:171],v[136:139],v[32:35]
T v[162:163],v222,6400
s_add_u32 s79, s5, 181248
T v[164:165],v223,256
s_mov_b32 m0, 27648
s_nop 0
buffer_load_dwordx4 v227, s[8:11], s79 offen nt lds
W 5
M v[40:43],v[172:175],v[136:139],v[40:43]
T v[166:167],v223,6400
T v[168:169],v220,12288
buffer_load_dwordx4 v228, s[8:11], s79 offen offset:1024 nt lds
W 5
M v[48:51],v[176:179],v[136:139],v[48:51]
T v[170:171],v220,18432
W 4
M v[56:59],v[180:183],v[136:139],v[56:59]
buffer_load_dwordx4 v229, s[8:11], s79 offen offset:2048 nt lds
T v[172:173],v221,12288
W 3
M v[64:67],v[152:155],v[136:139],v[64:67]
T v[174:175],v221,18432
T v[176:177],v222,12288
s_add_u32 s79, s5, 187392
s_mov_b32 m0, 33792
W 3
M v[72:75],v[156:159],v[136:139],v[72:75]
T v[178:179],v222,18432
v_add_u32 v216, 122880, v225
T v[180:181],v223,12288
buffer_load_dwordx4 v227, s[8:11], s79 offen nt lds
v_xor_b32 v217, 64, v216
M v[80:83],v[160:163],v[136:139],v[80:83]
T v[182:183],v223,18432
T v[152:153],v220,12416
W 5
M v[88:91],v[164:167],v[136:139],v[88:91]
T v[154:155],v220,18560
T v[156:157],v221,12416
W 5
M v[0:3],v[168:171],v[144:147],v[0:3]
T v[158:159],v221,18560
buffer_load_dwordx4 v228, s[8:11], s79 offen offset:1024 nt lds
T v[160:161],v222,12416
W 5
M v[96:99],v[244:247],v[144:147],v[96:99]
T v[162:163],v222,18560
T v[164:165],v223,12416
W 5
M v[8:11],v[172:175],v[144:147],v[8:11]
T v[166:167],v223,18560
T v[168:169],v220,12544
buffer_load_dwordx4 v229, s[8:11], s79 offen offset:2048 nt lds
W 5
M v[16:19],v[176:179],v[144:147],v[16:19]
T v[170:171],v220,18688
T v[172:173],v221,12544
W 5
M v[24:27],v[180:183],v[144:147],v[24:27]
T v[174:175],v221,18688
s_add_u32 s79, s5, 193536
T v[176:177],v222,12544
s_mov_b32 m0, 39936
W 5
M v[32:35],v[152:155],v[144:147],v[32:35]
T v[178:179],v222,18688
buffer_load_dwordx4 v227, s[8:11], s79 offen nt lds
T v[180:181],v223,12544
W 5
M v[40:43],v[156:159],v[144:147],v[40:43]
T v[182:183],v223,18688
s_waitcnt vmcnt(32)
s_barrier
ds_read_b128 v[152:155], v216 offset:0
W 5
M v[48:51],v[160:163],v[144:147],v[48:51]
ds_read_b128 v[156:159], v217 offset:0
ds_read_b128 v[160:163], v216 offset:128
buffer_load_dwordx4 v228, s[8:11], s79 offen offset:1024 nt lds
W 5
M v[56:59],v[164:167],v[144:147],v[56:59]
W 3
M v[64:67],v[168:171],v[144:147],v[64:67]
ds_read_b128 v[164:167], v217 offset:128
W 2
M v[72:75],v[172:175],v[144:147],v[72:75]
ds_read_b128 v[168:171], v216 offset:256
buffer_load_dwordx4 v229, s[8:11], s79 offen offset:2048 nt lds
ds_read_b128 v[172:175], v217 offset:256
M v[80:83],v[176:179],v[144:147],v[80:83]
s_add_u32 s79, s5, 196608
s_mov_b32 m0, 43008
M v[88:91],v[180:183],v[144:147],v[88:91]
W 2
ds_read_b128 v[176:179], v216 offset:6144
buffer_load_dwordx4 v227, s[8:11], s79 offen nt lds
M v[104:107],v[152:155],a[0:3],v[204:207]
ds_read_b128 v[180:183], v217 offset:6144
s_add_u32 s78, s97, 30720
M v[104:107],v[156:159],a[8:11],v[104:107]
s_mov_b32 m0, 52224
s_nop 0
buffer_load_dwordx4 v230, s[20:23], s78 offen lds
M v[104:107],v[160:163],a[16:19],v[104:107]
s_mov_b32 m0, 43008
s_mov_b32 m0, 52224
s_mov_b32 m0, 43008
s_mov_b32 m0, 52224
s_mov_b32 m0, 43008
s_mov_b32 m0, 52224
s_mov_b32 m0, 43008
s_nop 0
buffer_load_dwordx4 v228, s[8:11], s79 offen offset:1024 nt lds
s_mov_b32 m0, 52224
W 1
ds_read_b128 v[152:155], v216 offset:6272
buffer_load_dwordx4 v231, s[20:23], s78 offen offset:1024 lds
ds_read_b128 v[156:159], v217 offset:6272
ds_read_b128 v[160:163], v216 offset:6400
s_mov_b32 m0, 43008
s_mov_b32 m0, 52224
s_mov_b32 m0, 43008
s_mov_b32 m0, 52224
s_mov_b32 m0, 43008
s_mov_b32 m0, 52224
s_mov_b32 m0, 43008
M v[104:107],v[172:175],a[40:43],v[104:107]
buffer_load_dwordx4 v229, s[8:11], s79 offen offset:2048 nt lds
s_mov_b32 m0, 52224
M v[104:107],v[168:171],a[32:35],v[104:107]
buffer_load_dwordx4 v232, s[20:23], s78 offen offset:2048 lds
s_add_u32 s78, s97, 92160
M v[104:107],v[164:167],a[24:27],v[104:107]
ds_read_b128 v[164:167], v217 offset:6400
s_mov_b32 m0, 58368
M v[112:115],v[176:179],a[0:3],v[204:207]
ds_read_b128 v[168:171], v216 offset:12288
W 2
M v[112:115],v[180:183],a[8:11],v[112:115]
ds_read_b128 v[172:175], v217 offset:12288
s_add_u32 s79, s5, 199680
M v[112:115],v[152:155],a[16:19],v[112:115]
s_mov_b32 m0, 46080
s_nop 0
buffer_load_dwordx4 v227, s[8:11], s79 offen nt lds
s_mov_b32 m0, 58368
s_nop 0
buffer_load_dwordx4 v230, s[20:23], s78 offen lds
v_max3_f32 v248, v104, v105, v106
v_max_f32 v248, v248, v107
v_exp_f32 v200, v104
v_exp_f32 v201, v105
ds_read_b128 v[176:179], v216 offset:12416
s_mov_b32 m0, 46080
s_mov_b32 m0, 58368
s_mov_b32 m0, 46080
ds_read_b128 v[180:183], v217 offset:12416
s_mov_b32 m0, 58368
s_mov_b32 m0, 46080
s_mov_b32 m0, 58368
ds_read_b128 v[152:155], v216 offset:12544
s_mov_b32 m0, 46080
s_nop 0
buffer_load_dwordx4 v228, s[8:11], s79 offen offset:1024 nt lds
s_mov_b32 m0, 58368
s_nop 0
buffer_load_dwordx4 v231, s[20:23], s78 offen offset:1024 lds
v_exp_f32 v202, v106
W 3
M v[112:115],v[164:167],a[40:43],v[112:115]
v_cvt_pk_bf16_f32 v136, v200, v201
v_add_u32 v220, 98304, v226
M v[112:115],v[160:163],a[32:35],v[112:115]
s_mov_b32 m0, 46080
s_mov_b32 m0, 58368
M v[112:115],v[156:159],a[24:27],v[112:115]
ds_read_b128 v[156:159], v217 offset:12544
s_mov_b32 m0, 46080
M v[120:123],v[168:171],a[0:3],v[204:207]
v_exp_f32 v203, v107
M v[120:123],v[172:175],a[8:11],v[120:123]
ds_read_b128 v[160:163], v216 offset:18432
buffer_load_dwordx4 v229, s[8:11], s79 offen offset:2048 nt lds
W 2
M v[120:123],v[176:179],a[16:19],v[120:123]
ds_read_b128 v[164:167], v217 offset:18432
s_mov_b32 m0, 58368
s_nop 0
buffer_load_dwordx4 v232, s[20:23], s78 offen offset:2048 lds
s_add_u32 s78, s97, 153600
s_mov_b32 m0, 64512
v_cvt_pk_bf16_f32 v137, v202, v203
v_max3_f32 v248, v248, v112, v113
v_max3_f32 v248, v248, v114, v115
v_exp_f32 v200, v112
ds_read_b128 v[168:171], v216 offset:18560
buffer_load_dwordx4 v230, s[20:23], s78 offen lds
v_exp_f32 v201, v113
ds_read_b128 v[172:175], v217 offset:18560
v_exp_f32 v202, v114
v_cvt_pk_bf16_f32 v138, v200, v201
ds_read_b128 v[176:179], v216 offset:18688
buffer_load_dwordx4 v231, s[20:23], s78 offen offset:1024 lds
v_exp_f32 v203, v115
s_nop 0
v_cvt_pk_bf16_f32 v139, v202, v203
v_xor_b32 v221, 32, v220
v_xor_b32 v222, 64, v220
W 3
M v[120:123],v[156:159],a[40:43],v[120:123]
buffer_load_dwordx4 v232, s[20:23], s78 offen offset:2048 lds
v_xor_b32 v223, 96, v220
M v[120:123],v[152:155],a[32:35],v[120:123]
s_add_u32 s78, s97, 184320
s_mov_b32 m0, 67584
M v[120:123],v[180:183],a[24:27],v[120:123]
ds_read_b128 v[180:183], v217 offset:18688
buffer_load_dwordx4 v230, s[20:23], s78 offen lds
M v[128:131],v[160:163],a[0:3],v[204:207]
s_waitcnt vmcnt(40)
s_barrier
T v[152:153],v220,0
M v[128:131],v[164:167],a[8:11],v[128:131]
T v[154:155],v220,2048
W 3
M v[128:131],v[168:171],a[16:19],v[128:131]
T v[156:157],v220,4096
buffer_load_dwordx4 v231, s[20:23], s78 offen offset:1024 lds
T v[158:159],v220,6144
v_max3_f32 v248, v248, v120, v121
v_max3_f32 v248, v248, v122, v123
buffer_load_dwordx4 v232, s[20:23], s78 offen offset:2048 lds
s_add_u32 s78, s97, 215040
s_mov_b32 m0, 70656
T v[160:161],v220,8192
W 4
T v[162:163],v220,10240
buffer_load_dwordx4 v230, s[20:23], s78 offen lds
T v[164:165],v220,12288
W 4
T v[166:167],v220,14336
buffer_load_dwordx4 v231, s[20:23], s78 offen offset:1024 lds
M v[128:131],v[180:183],a[40:43],v[128:131]
T v[168:169],v220,16384
M v[128:131],v[176:179],a[32:35],v[128:131]
T v[170:171],v220,18432
buffer_load_dwordx4 v232, s[20:23], s78 offen offset:2048 lds
M v[128:131],v[172:175],a[24:27],v[128:131]
T v[172:173],v220,20480
T v[174:175],v220,22528
M v[184:187],v[152:155],a[48:51],0
T v[176:177],v221,0
s_add_u32 s79, s5, 181248
s_mov_b32 m0, 76800
s_nop 0
buffer_load_dwordx4 v227, s[12:15], s79 offen nt lds
W 6
M v[184:187],v[156:159],a[56:59],v[184:187]
T v[178:179],v221,2048
M v[184:187],v[160:163],a[64:67],v[184:187]
T v[180:181],v221,4096
W 6
T v[182:183],v221,6144
v_max3_f32 v248, v248, v128, v129
v_max3_f32 v248, v248, v130, v131
v_cmp_lt_f32 vcc, s60, v248
s_nop 1
s_cbranch_vccnz LST1_4w_%=
LRT1_4w_%=:
v_exp_f32 v200, v120
W 4
T v[152:153],v221,8192
buffer_load_dwordx4 v228, s[12:15], s79 offen offset:1024 nt lds
v_exp_f32 v201, v121
T v[154:155],v221,10240
v_exp_f32 v202, v122
v_cvt_pk_bf16_f32 v144, v200, v201
T v[156:157],v221,12288
v_exp_f32 v203, v123
s_nop 0
v_cvt_pk_bf16_f32 v145, v202, v203
T v[158:159],v221,14336
v_exp_f32 v200, v128
W 4
M v[184:187],v[172:175],a[88:91],v[184:187]
T v[160:161],v221,16384
M v[184:187],v[168:171],a[80:83],v[184:187]
T v[162:163],v221,18432
buffer_load_dwordx4 v229, s[12:15], s79 offen offset:2048 nt lds
M v[184:187],v[164:167],a[72:75],v[184:187]
T v[164:165],v221,20480
W 5
M v[192:195],v[176:179],a[48:51],0
T v[166:167],v221,22528
M v[192:195],v[180:183],a[56:59],v[192:195]
T v[168:169],v222,0
s_add_u32 s79, s5, 187392
M v[192:195],v[152:155],a[64:67],v[192:195]
T v[170:171],v222,2048
s_mov_b32 m0, 82944
T v[172:173],v222,4096
buffer_load_dwordx4 v227, s[12:15], s79 offen nt lds
v_exp_f32 v201, v129
T v[174:175],v222,6144
v_exp_f32 v202, v130
v_cvt_pk_bf16_f32 v146, v200, v201
T v[176:177],v222,8192
v_cvt_pk_bf16_f32 v252, v184, v185
v_cvt_pk_bf16_f32 v253, v186, v187
v_accvgpr_write_b32 a192, v252
T v[178:179],v222,10240
v_accvgpr_write_b32 a193, v253
v_exp_f32 v203, v131
T v[180:181],v222,12288
v_cvt_pk_bf16_f32 v147, v202, v203
v_add_u32 v216, 0, v224
v_xor_b32 v217, 32, v216
T v[182:183],v222,14336
buffer_load_dwordx4 v228, s[12:15], s79 offen offset:1024 nt lds
v_xor_b32 v218, 64, v216
W 5
M v[192:195],v[164:167],a[88:91],v[192:195]
T v[152:153],v222,16384
T v[154:155],v222,18432
M v[192:195],v[160:163],a[80:83],v[192:195]
v_xor_b32 v219, 96, v216
W 5
M v[192:195],v[156:159],a[72:75],v[192:195]
T v[156:157],v222,20480
T v[158:159],v222,22528
M v[184:187],v[168:171],a[48:51],0
T v[160:161],v223,0
W 6
M v[184:187],v[172:175],a[56:59],v[184:187]
T v[162:163],v223,2048
buffer_load_dwordx4 v229, s[12:15], s79 offen offset:2048 nt lds
M v[184:187],v[176:179],a[64:67],v[184:187]
T v[164:165],v223,4096
W 5
T v[166:167],v223,6144
s_add_u32 s79, s5, 193536
s_mov_b32 m0, 89088
W 4
T v[168:169],v223,8192
v_cvt_pk_bf16_f32 v252, v192, v193
v_cvt_pk_bf16_f32 v253, v194, v195
v_accvgpr_write_b32 a194, v252
T v[170:171],v223,10240
buffer_load_dwordx4 v227, s[12:15], s79 offen nt lds
v_accvgpr_write_b32 a195, v253
W 4
T v[172:173],v223,12288
T v[174:175],v223,14336
W 4
M v[184:187],v[156:159],a[88:91],v[184:187]
T v[176:177],v223,16384
T v[178:179],v223,18432
M v[184:187],v[152:155],a[80:83],v[184:187]
M v[184:187],v[180:183],a[72:75],v[184:187]
T v[180:181],v223,20480
T v[182:183],v223,22528
M v[192:195],v[160:163],a[48:51],0
s_waitcnt vmcnt(37)
s_barrier
T v[152:153],v216,0
M v[192:195],v[164:167],a[56:59],v[192:195]
T v[154:155],v216,6144
buffer_load_dwordx4 v228, s[12:15], s79 offen offset:1024 nt lds
W 6
M v[192:195],v[168:171],a[64:67],v[192:195]
W 4
T v[156:157],v217,0
T v[158:159],v217,6144
W 4
T v[160:161],v218,0
v_cvt_pk_bf16_f32 v252, v184, v185
v_cvt_pk_bf16_f32 v253, v186, v187
v_accvgpr_write_b32 a200, v252
T v[162:163],v218,6144
buffer_load_dwordx4 v229, s[12:15], s79 offen offset:2048 nt lds
v_accvgpr_write_b32 a201, v253
W 4
T v[164:165],v219,0
T v[166:167],v219,6144
M v[192:195],v[180:183],a[88:91],v[192:195]
T v[168:169],v216,128
s_add_u32 s79, s5, 196608
M v[192:195],v[176:179],a[80:83],v[192:195]
T v[170:171],v216,6272
s_mov_b32 m0, 92160
M v[192:195],v[172:175],a[72:75],v[192:195]
T v[172:173],v217,128
buffer_load_dwordx4 v227, s[12:15], s79 offen nt lds
M v[0:3],v[152:155],v[136:139],v[0:3]
T v[174:175],v217,6272
W 5
T v[176:177],v218,128
buffer_load_dwordx4 v228, s[12:15], s79 offen offset:1024 nt lds
M v[96:99],v[244:247],v[136:139],v[96:99]
T v[178:179],v218,6272
W 5
T v[180:181],v219,128
buffer_load_dwordx4 v229, s[12:15], s79 offen offset:2048 nt lds
M v[8:11],v[156:159],v[136:139],v[8:11]
T v[182:183],v219,6272
s_add_u32 s79, s5, 199680
T v[152:153],v216,256
s_mov_b32 m0, 95232
s_nop 0
buffer_load_dwordx4 v227, s[12:15], s79 offen nt lds
v_cvt_pk_bf16_f32 v252, v192, v193
M v[16:19],v[160:163],v[136:139],v[16:19]
T v[154:155],v216,6400
v_cvt_pk_bf16_f32 v253, v194, v195
T v[156:157],v217,256
buffer_load_dwordx4 v228, s[12:15], s79 offen offset:1024 nt lds
v_accvgpr_write_b32 a202, v252
v_accvgpr_write_b32 a203, v253
M v[24:27],v[164:167],v[136:139],v[24:27]
T v[158:159],v217,6400
W 5
T v[160:161],v218,256
buffer_load_dwordx4 v229, s[12:15], s79 offen offset:2048 nt lds
M v[32:35],v[168:171],v[136:139],v[32:35]
T v[162:163],v218,6400
s_add_u32 s79, s6, 181248
T v[164:165],v219,256
s_mov_b32 m0, 125952
s_nop 0
buffer_load_dwordx4 v227, s[8:11], s79 offen nt lds
W 5
M v[40:43],v[172:175],v[136:139],v[40:43]
T v[166:167],v219,6400
T v[168:169],v216,12288
buffer_load_dwordx4 v228, s[8:11], s79 offen offset:1024 nt lds
W 5
M v[48:51],v[176:179],v[136:139],v[48:51]
T v[170:171],v216,18432
W 4
M v[56:59],v[180:183],v[136:139],v[56:59]
buffer_load_dwordx4 v229, s[8:11], s79 offen offset:2048 nt lds
T v[172:173],v217,12288
W 3
M v[64:67],v[152:155],v[136:139],v[64:67]
T v[174:175],v217,18432
T v[176:177],v218,12288
s_add_u32 s79, s6, 187392
s_mov_b32 m0, 132096
W 3
M v[72:75],v[156:159],v[136:139],v[72:75]
T v[178:179],v218,18432
v_add_u32 v220, 24576, v225
T v[180:181],v219,12288
buffer_load_dwordx4 v227, s[8:11], s79 offen nt lds
v_xor_b32 v221, 64, v220
M v[80:83],v[160:163],v[136:139],v[80:83]
T v[182:183],v219,18432
T v[152:153],v216,12416
W 5
M v[88:91],v[164:167],v[136:139],v[88:91]
T v[154:155],v216,18560
T v[156:157],v217,12416
W 5
M v[0:3],v[168:171],v[144:147],v[0:3]
T v[158:159],v217,18560
buffer_load_dwordx4 v228, s[8:11], s79 offen offset:1024 nt lds
T v[160:161],v218,12416
W 5
M v[96:99],v[244:247],v[144:147],v[96:99]
T v[162:163],v218,18560
T v[164:165],v219,12416
W 5
M v[8:11],v[172:175],v[144:147],v[8:11]
T v[166:167],v219,18560
T v[168:169],v216,12544
buffer_load_dwordx4 v229, s[8:11], s79 offen offset:2048 nt lds
W 5
M v[16:19],v[176:179],v[144:147],v[16:19]
T v[170:171],v216,18688
T v[172:173],v217,12544
W 5
M v[24:27],v[180:183],v[144:147],v[24:27]
T v[174:175],v217,18688
s_add_u32 s79, s6, 193536
T v[176:177],v218,12544
s_mov_b32 m0, 138240
W 5
M v[32:35],v[152:155],v[144:147],v[32:35]
T v[178:179],v218,18688
buffer_load_dwordx4 v227, s[8:11], s79 offen nt lds
T v[180:181],v219,12544
W 5
M v[40:43],v[156:159],v[144:147],v[40:43]
T v[182:183],v219,18688
s_waitcnt vmcnt(32)
s_barrier
ds_read_b128 v[152:155], v220 offset:0
W 5
M v[48:51],v[160:163],v[144:147],v[48:51]
ds_read_b128 v[156:159], v221 offset:0
ds_read_b128 v[160:163], v220 offset:128
buffer_load_dwordx4 v228, s[8:11], s79 offen offset:1024 nt lds
W 5
M v[56:59],v[164:167],v[144:147],v[56:59]
W 3
M v[64:67],v[168:171],v[144:147],v[64:67]
ds_read_b128 v[164:167], v221 offset:128
W 2
M v[72:75],v[172:175],v[144:147],v[72:75]
ds_read_b128 v[168:171], v220 offset:256
buffer_load_dwordx4 v229, s[8:11], s79 offen offset:2048 nt lds
ds_read_b128 v[172:175], v221 offset:256
M v[80:83],v[176:179],v[144:147],v[80:83]
s_add_u32 s79, s6, 196608
s_mov_b32 m0, 141312
M v[88:91],v[180:183],v[144:147],v[88:91]
W 2
ds_read_b128 v[176:179], v220 offset:6144
buffer_load_dwordx4 v227, s[8:11], s79 offen nt lds
M v[104:107],v[152:155],a[0:3],v[204:207]
ds_read_b128 v[180:183], v221 offset:6144
s_add_u32 s78, s98, 30720
M v[104:107],v[156:159],a[8:11],v[104:107]
s_mov_b32 m0, 101376
s_nop 0
buffer_load_dwordx4 v230, s[20:23], s78 offen lds
M v[104:107],v[160:163],a[16:19],v[104:107]
s_mov_b32 m0, 141312
s_mov_b32 m0, 101376
s_mov_b32 m0, 141312
s_mov_b32 m0, 101376
s_mov_b32 m0, 141312
s_mov_b32 m0, 101376
s_mov_b32 m0, 141312
s_nop 0
buffer_load_dwordx4 v228, s[8:11], s79 offen offset:1024 nt lds
s_mov_b32 m0, 101376
W 1
ds_read_b128 v[152:155], v220 offset:6272
buffer_load_dwordx4 v231, s[20:23], s78 offen offset:1024 lds
ds_read_b128 v[156:159], v221 offset:6272
ds_read_b128 v[160:163], v220 offset:6400
s_mov_b32 m0, 141312
s_mov_b32 m0, 101376
s_mov_b32 m0, 141312
s_mov_b32 m0, 101376
s_mov_b32 m0, 141312
s_mov_b32 m0, 101376
s_mov_b32 m0, 141312
M v[104:107],v[172:175],a[40:43],v[104:107]
buffer_load_dwordx4 v229, s[8:11], s79 offen offset:2048 nt lds
s_mov_b32 m0, 101376
M v[104:107],v[168:171],a[32:35],v[104:107]
buffer_load_dwordx4 v232, s[20:23], s78 offen offset:2048 lds
s_add_u32 s78, s98, 92160
M v[104:107],v[164:167],a[24:27],v[104:107]
ds_read_b128 v[164:167], v221 offset:6400
s_mov_b32 m0, 107520
M v[112:115],v[176:179],a[0:3],v[204:207]
ds_read_b128 v[168:171], v220 offset:12288
W 2
M v[112:115],v[180:183],a[8:11],v[112:115]
ds_read_b128 v[172:175], v221 offset:12288
s_add_u32 s79, s6, 199680
M v[112:115],v[152:155],a[16:19],v[112:115]
s_mov_b32 m0, 144384
s_nop 0
buffer_load_dwordx4 v227, s[8:11], s79 offen nt lds
s_mov_b32 m0, 107520
s_nop 0
buffer_load_dwordx4 v230, s[20:23], s78 offen lds
v_max3_f32 v248, v104, v105, v106
v_max_f32 v248, v248, v107
v_exp_f32 v200, v104
v_exp_f32 v201, v105
ds_read_b128 v[176:179], v220 offset:12416
s_mov_b32 m0, 144384
s_mov_b32 m0, 107520
s_mov_b32 m0, 144384
ds_read_b128 v[180:183], v221 offset:12416
s_mov_b32 m0, 107520
s_mov_b32 m0, 144384
s_mov_b32 m0, 107520
ds_read_b128 v[152:155], v220 offset:12544
s_mov_b32 m0, 144384
s_nop 0
buffer_load_dwordx4 v228, s[8:11], s79 offen offset:1024 nt lds
s_mov_b32 m0, 107520
s_nop 0
buffer_load_dwordx4 v231, s[20:23], s78 offen offset:1024 lds
v_exp_f32 v202, v106
W 3
M v[112:115],v[164:167],a[40:43],v[112:115]
v_cvt_pk_bf16_f32 v136, v200, v201
v_add_u32 v216, 49152, v226
M v[112:115],v[160:163],a[32:35],v[112:115]
s_mov_b32 m0, 144384
s_mov_b32 m0, 107520
M v[112:115],v[156:159],a[24:27],v[112:115]
ds_read_b128 v[156:159], v221 offset:12544
s_mov_b32 m0, 144384
M v[120:123],v[168:171],a[0:3],v[204:207]
v_exp_f32 v203, v107
M v[120:123],v[172:175],a[8:11],v[120:123]
ds_read_b128 v[160:163], v220 offset:18432
buffer_load_dwordx4 v229, s[8:11], s79 offen offset:2048 nt lds
W 2
M v[120:123],v[176:179],a[16:19],v[120:123]
ds_read_b128 v[164:167], v221 offset:18432
s_mov_b32 m0, 107520
s_nop 0
buffer_load_dwordx4 v232, s[20:23], s78 offen offset:2048 lds
s_add_u32 s78, s98, 153600
s_mov_b32 m0, 113664
v_cvt_pk_bf16_f32 v137, v202, v203
v_max3_f32 v248, v248, v112, v113
v_max3_f32 v248, v248, v114, v115
v_exp_f32 v200, v112
ds_read_b128 v[168:171], v220 offset:18560
buffer_load_dwordx4 v230, s[20:23], s78 offen lds
v_exp_f32 v201, v113
ds_read_b128 v[172:175], v221 offset:18560
v_exp_f32 v202, v114
v_cvt_pk_bf16_f32 v138, v200, v201
ds_read_b128 v[176:179], v220 offset:18688
buffer_load_dwordx4 v231, s[20:23], s78 offen offset:1024 lds
v_exp_f32 v203, v115
s_nop 0
v_cvt_pk_bf16_f32 v139, v202, v203
v_xor_b32 v217, 32, v216
v_xor_b32 v218, 64, v216
W 3
M v[120:123],v[156:159],a[40:43],v[120:123]
buffer_load_dwordx4 v232, s[20:23], s78 offen offset:2048 lds
v_xor_b32 v219, 96, v216
M v[120:123],v[152:155],a[32:35],v[120:123]
s_add_u32 s78, s98, 184320
s_mov_b32 m0, 116736
M v[120:123],v[180:183],a[24:27],v[120:123]
ds_read_b128 v[180:183], v221 offset:18688
buffer_load_dwordx4 v230, s[20:23], s78 offen lds
M v[128:131],v[160:163],a[0:3],v[204:207]
s_waitcnt vmcnt(40)
s_barrier
T v[152:153],v216,0
M v[128:131],v[164:167],a[8:11],v[128:131]
T v[154:155],v216,2048
W 3
M v[128:131],v[168:171],a[16:19],v[128:131]
T v[156:157],v216,4096
buffer_load_dwordx4 v231, s[20:23], s78 offen offset:1024 lds
T v[158:159],v216,6144
v_max3_f32 v248, v248, v120, v121
v_max3_f32 v248, v248, v122, v123
buffer_load_dwordx4 v232, s[20:23], s78 offen offset:2048 lds
s_add_u32 s78, s98, 215040
s_mov_b32 m0, 119808
T v[160:161],v216,8192
W 4
T v[162:163],v216,10240
buffer_load_dwordx4 v230, s[20:23], s78 offen lds
T v[164:165],v216,12288
W 4
T v[166:167],v216,14336
buffer_load_dwordx4 v231, s[20:23], s78 offen offset:1024 lds
M v[128:131],v[180:183],a[40:43],v[128:131]
T v[168:169],v216,16384
M v[128:131],v[176:179],a[32:35],v[128:131]
T v[170:171],v216,18432
buffer_load_dwordx4 v232, s[20:23], s78 offen offset:2048 lds
M v[128:131],v[172:175],a[24:27],v[128:131]
T v[172:173],v216,20480
T v[174:175],v216,22528
M v[184:187],v[152:155],a[48:51],0
T v[176:177],v217,0
s_add_u32 s79, s6, 181248
s_mov_b32 m0, 3072
s_nop 0
buffer_load_dwordx4 v227, s[12:15], s79 offen nt lds
W 6
M v[184:187],v[156:159],a[56:59],v[184:187]
T v[178:179],v217,2048
M v[184:187],v[160:163],a[64:67],v[184:187]
T v[180:181],v217,4096
W 6
T v[182:183],v217,6144
v_max3_f32 v248, v248, v128, v129
v_max3_f32 v248, v248, v130, v131
v_cmp_lt_f32 vcc, s60, v248
s_nop 1
s_cbranch_vccnz LST1_5w_%=
LRT1_5w_%=:
v_exp_f32 v200, v120
W 4
T v[152:153],v217,8192
buffer_load_dwordx4 v228, s[12:15], s79 offen offset:1024 nt lds
v_exp_f32 v201, v121
T v[154:155],v217,10240
v_exp_f32 v202, v122
v_cvt_pk_bf16_f32 v144, v200, v201
T v[156:157],v217,12288
v_exp_f32 v203, v123
s_nop 0
v_cvt_pk_bf16_f32 v145, v202, v203
T v[158:159],v217,14336
v_exp_f32 v200, v128
W 4
M v[184:187],v[172:175],a[88:91],v[184:187]
T v[160:161],v217,16384
M v[184:187],v[168:171],a[80:83],v[184:187]
T v[162:163],v217,18432
buffer_load_dwordx4 v229, s[12:15], s79 offen offset:2048 nt lds
M v[184:187],v[164:167],a[72:75],v[184:187]
T v[164:165],v217,20480
W 5
M v[192:195],v[176:179],a[48:51],0
T v[166:167],v217,22528
M v[192:195],v[180:183],a[56:59],v[192:195]
T v[168:169],v218,0
s_add_u32 s79, s6, 187392
M v[192:195],v[152:155],a[64:67],v[192:195]
T v[170:171],v218,2048
s_mov_b32 m0, 9216
T v[172:173],v218,4096
buffer_load_dwordx4 v227, s[12:15], s79 offen nt lds
v_exp_f32 v201, v129
T v[174:175],v218,6144
v_exp_f32 v202, v130
v_cvt_pk_bf16_f32 v146, v200, v201
T v[176:177],v218,8192
v_cvt_pk_bf16_f32 v252, v184, v185
v_cvt_pk_bf16_f32 v253, v186, v187
v_accvgpr_write_b32 a208, v252
T v[178:179],v218,10240
v_accvgpr_write_b32 a209, v253
v_exp_f32 v203, v131
T v[180:181],v218,12288
v_cvt_pk_bf16_f32 v147, v202, v203
v_add_u32 v220, 73728, v224
v_xor_b32 v221, 32, v220
T v[182:183],v218,14336
buffer_load_dwordx4 v228, s[12:15], s79 offen offset:1024 nt lds
v_xor_b32 v222, 64, v220
W 5
M v[192:195],v[164:167],a[88:91],v[192:195]
T v[152:153],v218,16384
T v[154:155],v218,18432
M v[192:195],v[160:163],a[80:83],v[192:195]
v_xor_b32 v223, 96, v220
W 5
M v[192:195],v[156:159],a[72:75],v[192:195]
T v[156:157],v218,20480
T v[158:159],v218,22528
M v[184:187],v[168:171],a[48:51],0
T v[160:161],v219,0
W 6
M v[184:187],v[172:175],a[56:59],v[184:187]
T v[162:163],v219,2048
buffer_load_dwordx4 v229, s[12:15], s79 offen offset:2048 nt lds
M v[184:187],v[176:179],a[64:67],v[184:187]
T v[164:165],v219,4096
W 5
T v[166:167],v219,6144
s_add_u32 s79, s6, 193536
s_mov_b32 m0, 15360
W 4
T v[168:169],v219,8192
v_cvt_pk_bf16_f32 v252, v192, v193
v_cvt_pk_bf16_f32 v253, v194, v195
v_accvgpr_write_b32 a210, v252
T v[170:171],v219,10240
buffer_load_dwordx4 v227, s[12:15], s79 offen nt lds
v_accvgpr_write_b32 a211, v253
W 4
T v[172:173],v219,12288
T v[174:175],v219,14336
W 4
M v[184:187],v[156:159],a[88:91],v[184:187]
T v[176:177],v219,16384
T v[178:179],v219,18432
M v[184:187],v[152:155],a[80:83],v[184:187]
M v[184:187],v[180:183],a[72:75],v[184:187]
T v[180:181],v219,20480
T v[182:183],v219,22528
M v[192:195],v[160:163],a[48:51],0
s_waitcnt vmcnt(37)
s_barrier
T v[152:153],v220,0
M v[192:195],v[164:167],a[56:59],v[192:195]
T v[154:155],v220,6144
buffer_load_dwordx4 v228, s[12:15], s79 offen offset:1024 nt lds
W 6
M v[192:195],v[168:171],a[64:67],v[192:195]
W 4
T v[156:157],v221,0
T v[158:159],v221,6144
W 4
T v[160:161],v222,0
v_cvt_pk_bf16_f32 v252, v184, v185
v_cvt_pk_bf16_f32 v253, v186, v187
v_accvgpr_write_b32 a216, v252
T v[162:163],v222,6144
buffer_load_dwordx4 v229, s[12:15], s79 offen offset:2048 nt lds
v_accvgpr_write_b32 a217, v253
W 4
T v[164:165],v223,0
T v[166:167],v223,6144
M v[192:195],v[180:183],a[88:91],v[192:195]
T v[168:169],v220,128
s_add_u32 s79, s6, 196608
M v[192:195],v[176:179],a[80:83],v[192:195]
T v[170:171],v220,6272
s_mov_b32 m0, 18432
M v[192:195],v[172:175],a[72:75],v[192:195]
T v[172:173],v221,128
buffer_load_dwordx4 v227, s[12:15], s79 offen nt lds
M v[0:3],v[152:155],v[136:139],v[0:3]
T v[174:175],v221,6272
W 5
T v[176:177],v222,128
buffer_load_dwordx4 v228, s[12:15], s79 offen offset:1024 nt lds
M v[96:99],v[244:247],v[136:139],v[96:99]
T v[178:179],v222,6272
W 5
T v[180:181],v223,128
buffer_load_dwordx4 v229, s[12:15], s79 offen offset:2048 nt lds
M v[8:11],v[156:159],v[136:139],v[8:11]
T v[182:183],v223,6272
s_add_u32 s79, s6, 199680
T v[152:153],v220,256
s_mov_b32 m0, 21504
s_nop 0
buffer_load_dwordx4 v227, s[12:15], s79 offen nt lds
v_cvt_pk_bf16_f32 v252, v192, v193
M v[16:19],v[160:163],v[136:139],v[16:19]
T v[154:155],v220,6400
v_cvt_pk_bf16_f32 v253, v194, v195
T v[156:157],v221,256
buffer_load_dwordx4 v228, s[12:15], s79 offen offset:1024 nt lds
v_accvgpr_write_b32 a218, v252
v_accvgpr_write_b32 a219, v253
M v[24:27],v[164:167],v[136:139],v[24:27]
T v[158:159],v221,6400
W 5
T v[160:161],v222,256
buffer_load_dwordx4 v229, s[12:15], s79 offen offset:2048 nt lds
M v[32:35],v[168:171],v[136:139],v[32:35]
T v[162:163],v222,6400
s_add_u32 s79, s7, 181248
T v[164:165],v223,256
s_mov_b32 m0, 27648
s_nop 0
buffer_load_dwordx4 v227, s[8:11], s79 offen nt lds
W 5
M v[40:43],v[172:175],v[136:139],v[40:43]
T v[166:167],v223,6400
T v[168:169],v220,12288
buffer_load_dwordx4 v228, s[8:11], s79 offen offset:1024 nt lds
W 5
M v[48:51],v[176:179],v[136:139],v[48:51]
T v[170:171],v220,18432
W 4
M v[56:59],v[180:183],v[136:139],v[56:59]
buffer_load_dwordx4 v229, s[8:11], s79 offen offset:2048 nt lds
T v[172:173],v221,12288
W 3
M v[64:67],v[152:155],v[136:139],v[64:67]
T v[174:175],v221,18432
T v[176:177],v222,12288
s_add_u32 s79, s7, 187392
s_mov_b32 m0, 33792
W 3
M v[72:75],v[156:159],v[136:139],v[72:75]
T v[178:179],v222,18432
v_add_u32 v216, 122880, v225
T v[180:181],v223,12288
buffer_load_dwordx4 v227, s[8:11], s79 offen nt lds
v_xor_b32 v217, 64, v216
M v[80:83],v[160:163],v[136:139],v[80:83]
T v[182:183],v223,18432
T v[152:153],v220,12416
W 5
M v[88:91],v[164:167],v[136:139],v[88:91]
T v[154:155],v220,18560
T v[156:157],v221,12416
W 5
M v[0:3],v[168:171],v[144:147],v[0:3]
T v[158:159],v221,18560
buffer_load_dwordx4 v228, s[8:11], s79 offen offset:1024 nt lds
T v[160:161],v222,12416
W 5
M v[96:99],v[244:247],v[144:147],v[96:99]
T v[162:163],v222,18560
T v[164:165],v223,12416
W 5
M v[8:11],v[172:175],v[144:147],v[8:11]
T v[166:167],v223,18560
T v[168:169],v220,12544
buffer_load_dwordx4 v229, s[8:11], s79 offen offset:2048 nt lds
W 5
M v[16:19],v[176:179],v[144:147],v[16:19]
T v[170:171],v220,18688
T v[172:173],v221,12544
W 5
M v[24:27],v[180:183],v[144:147],v[24:27]
T v[174:175],v221,18688
s_add_u32 s79, s7, 193536
T v[176:177],v222,12544
s_mov_b32 m0, 39936
W 5
M v[32:35],v[152:155],v[144:147],v[32:35]
T v[178:179],v222,18688
buffer_load_dwordx4 v227, s[8:11], s79 offen nt lds
T v[180:181],v223,12544
W 5
M v[40:43],v[156:159],v[144:147],v[40:43]
T v[182:183],v223,18688
s_waitcnt vmcnt(32)
s_barrier
ds_read_b128 v[152:155], v216 offset:0
W 5
M v[48:51],v[160:163],v[144:147],v[48:51]
ds_read_b128 v[156:159], v217 offset:0
ds_read_b128 v[160:163], v216 offset:128
buffer_load_dwordx4 v228, s[8:11], s79 offen offset:1024 nt lds
W 5
M v[56:59],v[164:167],v[144:147],v[56:59]
W 3
M v[64:67],v[168:171],v[144:147],v[64:67]
ds_read_b128 v[164:167], v217 offset:128
W 2
M v[72:75],v[172:175],v[144:147],v[72:75]
ds_read_b128 v[168:171], v216 offset:256
buffer_load_dwordx4 v229, s[8:11], s79 offen offset:2048 nt lds
ds_read_b128 v[172:175], v217 offset:256
M v[80:83],v[176:179],v[144:147],v[80:83]
s_add_u32 s79, s7, 196608
s_mov_b32 m0, 43008
M v[88:91],v[180:183],v[144:147],v[88:91]
W 2
ds_read_b128 v[176:179], v216 offset:6144
buffer_load_dwordx4 v227, s[8:11], s79 offen nt lds
M v[104:107],v[152:155],a[0:3],v[204:207]
ds_read_b128 v[180:183], v217 offset:6144
s_add_u32 s78, s99, 30720
M v[104:107],v[156:159],a[8:11],v[104:107]
s_mov_b32 m0, 52224
s_nop 0
buffer_load_dwordx4 v230, s[20:23], s78 offen lds
M v[104:107],v[160:163],a[16:19],v[104:107]
s_mov_b32 m0, 43008
s_mov_b32 m0, 52224
s_mov_b32 m0, 43008
s_mov_b32 m0, 52224
s_mov_b32 m0, 43008
s_mov_b32 m0, 52224
s_mov_b32 m0, 43008
s_nop 0
buffer_load_dwordx4 v228, s[8:11], s79 offen offset:1024 nt lds
s_mov_b32 m0, 52224
W 1
ds_read_b128 v[152:155], v216 offset:6272
buffer_load_dwordx4 v231, s[20:23], s78 offen offset:1024 lds
ds_read_b128 v[156:159], v217 offset:6272
ds_read_b128 v[160:163], v216 offset:6400
s_mov_b32 m0, 43008
s_mov_b32 m0, 52224
s_mov_b32 m0, 43008
s_mov_b32 m0, 52224
s_mov_b32 m0, 43008
s_mov_b32 m0, 52224
s_mov_b32 m0, 43008
M v[104:107],v[172:175],a[40:43],v[104:107]
buffer_load_dwordx4 v229, s[8:11], s79 offen offset:2048 nt lds
s_mov_b32 m0, 52224
M v[104:107],v[168:171],a[32:35],v[104:107]
buffer_load_dwordx4 v232, s[20:23], s78 offen offset:2048 lds
s_add_u32 s78, s99, 92160
M v[104:107],v[164:167],a[24:27],v[104:107]
ds_read_b128 v[164:167], v217 offset:6400
s_mov_b32 m0, 58368
M v[112:115],v[176:179],a[0:3],v[204:207]
ds_read_b128 v[168:171], v216 offset:12288
W 2
M v[112:115],v[180:183],a[8:11],v[112:115]
ds_read_b128 v[172:175], v217 offset:12288
s_add_u32 s79, s7, 199680
M v[112:115],v[152:155],a[16:19],v[112:115]
s_mov_b32 m0, 46080
s_nop 0
buffer_load_dwordx4 v227, s[8:11], s79 offen nt lds
s_mov_b32 m0, 58368
s_nop 0
buffer_load_dwordx4 v230, s[20:23], s78 offen lds
v_max3_f32 v248, v104, v105, v106
v_max_f32 v248, v248, v107
v_exp_f32 v200, v104
v_exp_f32 v201, v105
ds_read_b128 v[176:179], v216 offset:12416
s_mov_b32 m0, 46080
s_mov_b32 m0, 58368
s_mov_b32 m0, 46080
ds_read_b128 v[180:183], v217 offset:12416
s_mov_b32 m0, 58368
s_mov_b32 m0, 46080
s_mov_b32 m0, 58368
ds_read_b128 v[152:155], v216 offset:12544
s_mov_b32 m0, 46080
s_nop 0
buffer_load_dwordx4 v228, s[8:11], s79 offen offset:1024 nt lds
s_mov_b32 m0, 58368
s_nop 0
buffer_load_dwordx4 v231, s[20:23], s78 offen offset:1024 lds
v_exp_f32 v202, v106
W 3
M v[112:115],v[164:167],a[40:43],v[112:115]
v_cvt_pk_bf16_f32 v136, v200, v201
v_add_u32 v220, 98304, v226
M v[112:115],v[160:163],a[32:35],v[112:115]
s_mov_b32 m0, 46080
s_mov_b32 m0, 58368
M v[112:115],v[156:159],a[24:27],v[112:115]
ds_read_b128 v[156:159], v217 offset:12544
s_mov_b32 m0, 46080
M v[120:123],v[168:171],a[0:3],v[204:207]
v_exp_f32 v203, v107
M v[120:123],v[172:175],a[8:11],v[120:123]
ds_read_b128 v[160:163], v216 offset:18432
buffer_load_dwordx4 v229, s[8:11], s79 offen offset:2048 nt lds
W 2
M v[120:123],v[176:179],a[16:19],v[120:123]
ds_read_b128 v[164:167], v217 offset:18432
s_mov_b32 m0, 58368
s_nop 0
buffer_load_dwordx4 v232, s[20:23], s78 offen offset:2048 lds
s_add_u32 s78, s99, 153600
s_mov_b32 m0, 64512
v_cvt_pk_bf16_f32 v137, v202, v203
v_max3_f32 v248, v248, v112, v113
v_max3_f32 v248, v248, v114, v115
v_exp_f32 v200, v112
ds_read_b128 v[168:171], v216 offset:18560
buffer_load_dwordx4 v230, s[20:23], s78 offen lds
v_exp_f32 v201, v113
ds_read_b128 v[172:175], v217 offset:18560
v_exp_f32 v202, v114
v_cvt_pk_bf16_f32 v138, v200, v201
ds_read_b128 v[176:179], v216 offset:18688
buffer_load_dwordx4 v231, s[20:23], s78 offen offset:1024 lds
v_exp_f32 v203, v115
s_nop 0
v_cvt_pk_bf16_f32 v139, v202, v203
v_xor_b32 v221, 32, v220
v_xor_b32 v222, 64, v220
W 3
M v[120:123],v[156:159],a[40:43],v[120:123]
buffer_load_dwordx4 v232, s[20:23], s78 offen offset:2048 lds
v_xor_b32 v223, 96, v220
M v[120:123],v[152:155],a[32:35],v[120:123]
s_add_u32 s78, s99, 184320
s_mov_b32 m0, 67584
M v[120:123],v[180:183],a[24:27],v[120:123]
ds_read_b128 v[180:183], v217 offset:18688
buffer_load_dwordx4 v230, s[20:23], s78 offen lds
M v[128:131],v[160:163],a[0:3],v[204:207]
s_waitcnt vmcnt(40)
s_barrier
T v[152:153],v220,0
M v[128:131],v[164:167],a[8:11],v[128:131]
T v[154:155],v220,2048
W 3
M v[128:131],v[168:171],a[16:19],v[128:131]
T v[156:157],v220,4096
buffer_load_dwordx4 v231, s[20:23], s78 offen offset:1024 lds
T v[158:159],v220,6144
v_max3_f32 v248, v248, v120, v121
v_max3_f32 v248, v248, v122, v123
buffer_load_dwordx4 v232, s[20:23], s78 offen offset:2048 lds
s_add_u32 s78, s99, 215040
s_mov_b32 m0, 70656
T v[160:161],v220,8192
W 4
T v[162:163],v220,10240
buffer_load_dwordx4 v230, s[20:23], s78 offen lds
T v[164:165],v220,12288
W 4
T v[166:167],v220,14336
buffer_load_dwordx4 v231, s[20:23], s78 offen offset:1024 lds
M v[128:131],v[180:183],a[40:43],v[128:131]
T v[168:169],v220,16384
M v[128:131],v[176:179],a[32:35],v[128:131]
T v[170:171],v220,18432
buffer_load_dwordx4 v232, s[20:23], s78 offen offset:2048 lds
M v[128:131],v[172:175],a[24:27],v[128:131]
T v[172:173],v220,20480
T v[174:175],v220,22528
M v[184:187],v[152:155],a[48:51],0
T v[176:177],v221,0
s_add_u32 s79, s7, 181248
s_mov_b32 m0, 76800
s_nop 0
buffer_load_dwordx4 v227, s[12:15], s79 offen nt lds
W 6
M v[184:187],v[156:159],a[56:59],v[184:187]
T v[178:179],v221,2048
M v[184:187],v[160:163],a[64:67],v[184:187]
T v[180:181],v221,4096
W 6
T v[182:183],v221,6144
v_max3_f32 v248, v248, v128, v129
v_max3_f32 v248, v248, v130, v131
v_cmp_lt_f32 vcc, s60, v248
s_nop 1
s_cbranch_vccnz LST1_6w_%=
LRT1_6w_%=:
v_exp_f32 v200, v120
W 4
T v[152:153],v221,8192
buffer_load_dwordx4 v228, s[12:15], s79 offen offset:1024 nt lds
v_exp_f32 v201, v121
T v[154:155],v221,10240
v_exp_f32 v202, v122
v_cvt_pk_bf16_f32 v144, v200, v201
T v[156:157],v221,12288
v_exp_f32 v203, v123
s_nop 0
v_cvt_pk_bf16_f32 v145, v202, v203
T v[158:159],v221,14336
v_exp_f32 v200, v128
W 4
M v[184:187],v[172:175],a[88:91],v[184:187]
T v[160:161],v221,16384
M v[184:187],v[168:171],a[80:83],v[184:187]
T v[162:163],v221,18432
buffer_load_dwordx4 v229, s[12:15], s79 offen offset:2048 nt lds
M v[184:187],v[164:167],a[72:75],v[184:187]
T v[164:165],v221,20480
W 5
M v[192:195],v[176:179],a[48:51],0
T v[166:167],v221,22528
M v[192:195],v[180:183],a[56:59],v[192:195]
T v[168:169],v222,0
s_add_u32 s79, s7, 187392
M v[192:195],v[152:155],a[64:67],v[192:195]
T v[170:171],v222,2048
s_mov_b32 m0, 82944
T v[172:173],v222,4096
buffer_load_dwordx4 v227, s[12:15], s79 offen nt lds
v_exp_f32 v201, v129
T v[174:175],v222,6144
v_exp_f32 v202, v130
v_cvt_pk_bf16_f32 v146, v200, v201
T v[176:177],v222,8192
v_cvt_pk_bf16_f32 v252, v184, v185
v_cvt_pk_bf16_f32 v253, v186, v187
v_accvgpr_write_b32 a224, v252
T v[178:179],v222,10240
v_accvgpr_write_b32 a225, v253
v_exp_f32 v203, v131
T v[180:181],v222,12288
v_cvt_pk_bf16_f32 v147, v202, v203
v_add_u32 v216, 0, v224
v_xor_b32 v217, 32, v216
T v[182:183],v222,14336
buffer_load_dwordx4 v228, s[12:15], s79 offen offset:1024 nt lds
v_xor_b32 v218, 64, v216
W 5
M v[192:195],v[164:167],a[88:91],v[192:195]
T v[152:153],v222,16384
T v[154:155],v222,18432
M v[192:195],v[160:163],a[80:83],v[192:195]
v_xor_b32 v219, 96, v216
W 5
M v[192:195],v[156:159],a[72:75],v[192:195]
T v[156:157],v222,20480
T v[158:159],v222,22528
M v[184:187],v[168:171],a[48:51],0
T v[160:161],v223,0
W 6
M v[184:187],v[172:175],a[56:59],v[184:187]
T v[162:163],v223,2048
buffer_load_dwordx4 v229, s[12:15], s79 offen offset:2048 nt lds
M v[184:187],v[176:179],a[64:67],v[184:187]
T v[164:165],v223,4096
W 5
T v[166:167],v223,6144
s_add_u32 s79, s7, 193536
s_mov_b32 m0, 89088
W 4
T v[168:169],v223,8192
v_cvt_pk_bf16_f32 v252, v192, v193
v_cvt_pk_bf16_f32 v253, v194, v195
v_accvgpr_write_b32 a226, v252
T v[170:171],v223,10240
buffer_load_dwordx4 v227, s[12:15], s79 offen nt lds
v_accvgpr_write_b32 a227, v253
W 4
T v[172:173],v223,12288
T v[174:175],v223,14336
W 4
M v[184:187],v[156:159],a[88:91],v[184:187]
T v[176:177],v223,16384
T v[178:179],v223,18432
M v[184:187],v[152:155],a[80:83],v[184:187]
M v[184:187],v[180:183],a[72:75],v[184:187]
T v[180:181],v223,20480
T v[182:183],v223,22528
M v[192:195],v[160:163],a[48:51],0
s_waitcnt vmcnt(37)
s_barrier
T v[152:153],v216,0
M v[192:195],v[164:167],a[56:59],v[192:195]
T v[154:155],v216,6144
buffer_load_dwordx4 v228, s[12:15], s79 offen offset:1024 nt lds
W 6
M v[192:195],v[168:171],a[64:67],v[192:195]
W 4
T v[156:157],v217,0
T v[158:159],v217,6144
W 4
T v[160:161],v218,0
v_cvt_pk_bf16_f32 v252, v184, v185
v_cvt_pk_bf16_f32 v253, v186, v187
v_accvgpr_write_b32 a232, v252
T v[162:163],v218,6144
buffer_load_dwordx4 v229, s[12:15], s79 offen offset:2048 nt lds
v_accvgpr_write_b32 a233, v253
W 4
T v[164:165],v219,0
T v[166:167],v219,6144
M v[192:195],v[180:183],a[88:91],v[192:195]
T v[168:169],v216,128
s_add_u32 s79, s7, 196608
M v[192:195],v[176:179],a[80:83],v[192:195]
T v[170:171],v216,6272
s_mov_b32 m0, 92160
M v[192:195],v[172:175],a[72:75],v[192:195]
T v[172:173],v217,128
buffer_load_dwordx4 v227, s[12:15], s79 offen nt lds
M v[0:3],v[152:155],v[136:139],v[0:3]
T v[174:175],v217,6272
s_add_u32 s78, s66, 276480
T v[176:177],v218,128
s_mov_b32 m0, 125952
s_nop 0
buffer_load_dwordx4 v230, s[20:23], s78 offen lds
W 5
M v[96:99],v[244:247],v[136:139],v[96:99]
T v[178:179],v218,6272
s_mov_b32 m0, 92160
T v[180:181],v219,128
s_mov_b32 m0, 125952
s_mov_b32 m0, 92160
s_mov_b32 m0, 125952
M v[8:11],v[156:159],v[136:139],v[8:11]
T v[182:183],v219,6272
s_mov_b32 m0, 92160
T v[152:153],v216,256
buffer_load_dwordx4 v228, s[12:15], s79 offen offset:1024 nt lds
s_mov_b32 m0, 125952
v_cvt_pk_bf16_f32 v252, v192, v193
M v[16:19],v[160:163],v[136:139],v[16:19]
T v[154:155],v216,6400
buffer_load_dwordx4 v231, s[20:23], s78 offen offset:1024 lds
T v[156:157],v217,256
v_cvt_pk_bf16_f32 v253, v194, v195
v_accvgpr_write_b32 a234, v252
v_accvgpr_write_b32 a235, v253
M v[24:27],v[164:167],v[136:139],v[24:27]
T v[158:159],v217,6400
s_mov_b32 m0, 92160
T v[160:161],v218,256
s_mov_b32 m0, 125952
s_mov_b32 m0, 92160
W 5
M v[32:35],v[168:171],v[136:139],v[32:35]
T v[162:163],v218,6400
buffer_load_dwordx4 v229, s[12:15], s79 offen offset:2048 nt lds
T v[164:165],v219,256
s_mov_b32 m0, 125952
s_nop 0
buffer_load_dwordx4 v232, s[20:23], s78 offen offset:2048 lds
s_add_u32 s78, s66, 337920
M v[40:43],v[172:175],v[136:139],v[40:43]
T v[166:167],v219,6400
s_mov_b32 m0, 132096
T v[168:169],v216,12288
s_add_u32 s79, s7, 199680
s_mov_b32 m0, 95232
s_mov_b32 m0, 132096
M v[48:51],v[176:179],v[136:139],v[48:51]
T v[170:171],v216,18432
s_mov_b32 m0, 95232
s_nop 0
buffer_load_dwordx4 v227, s[12:15], s79 offen nt lds
s_mov_b32 m0, 132096
W 4
M v[56:59],v[180:183],v[136:139],v[56:59]
buffer_load_dwordx4 v230, s[20:23], s78 offen lds
T v[172:173],v217,12288
W 3
M v[64:67],v[152:155],v[136:139],v[64:67]
T v[174:175],v217,18432
s_mov_b32 m0, 95232
T v[176:177],v218,12288
s_mov_b32 m0, 132096
s_mov_b32 m0, 95232
s_mov_b32 m0, 132096
M v[72:75],v[156:159],v[136:139],v[72:75]
T v[178:179],v218,18432
s_mov_b32 m0, 95232
T v[180:181],v219,12288
buffer_load_dwordx4 v228, s[12:15], s79 offen offset:1024 nt lds
v_add_u32 v220, 24576, v225
v_xor_b32 v221, 64, v220
M v[80:83],v[160:163],v[136:139],v[80:83]
T v[182:183],v219,18432
s_mov_b32 m0, 132096
T v[152:153],v216,12416
buffer_load_dwordx4 v231, s[20:23], s78 offen offset:1024 lds
W 5
M v[88:91],v[164:167],v[136:139],v[88:91]
T v[154:155],v216,18560
s_mov_b32 m0, 95232
T v[156:157],v217,12416
s_mov_b32 m0, 132096
s_mov_b32 m0, 95232
s_mov_b32 m0, 132096
M v[0:3],v[168:171],v[144:147],v[0:3]
T v[158:159],v217,18560
s_mov_b32 m0, 95232
T v[160:161],v218,12416
buffer_load_dwordx4 v229, s[12:15], s79 offen offset:2048 nt lds
s_mov_b32 m0, 132096
W 5
M v[96:99],v[244:247],v[144:147],v[96:99]
T v[162:163],v218,18560
buffer_load_dwordx4 v232, s[20:23], s78 offen offset:2048 lds
T v[164:165],v219,12416
s_add_u32 s78, s66, 399360
s_mov_b32 m0, 138240
W 5
M v[8:11],v[172:175],v[144:147],v[8:11]
T v[166:167],v219,18560
buffer_load_dwordx4 v230, s[20:23], s78 offen lds
T v[168:169],v216,12544
W 5
M v[16:19],v[176:179],v[144:147],v[16:19]
T v[170:171],v216,18688
buffer_load_dwordx4 v231, s[20:23], s78 offen offset:1024 lds
T v[172:173],v217,12544
W 5
M v[24:27],v[180:183],v[144:147],v[24:27]
T v[174:175],v217,18688
buffer_load_dwordx4 v232, s[20:23], s78 offen offset:2048 lds
T v[176:177],v218,12544
s_add_u32 s78, s66, 430080
s_mov_b32 m0, 141312
W 5
M v[32:35],v[152:155],v[144:147],v[32:35]
T v[178:179],v218,18688
buffer_load_dwordx4 v230, s[20:23], s78 offen lds
T v[180:181],v219,12544
W 5
M v[40:43],v[156:159],v[144:147],v[40:43]
T v[182:183],v219,18688
buffer_load_dwordx4 v231, s[20:23], s78 offen offset:1024 lds
s_waitcnt vmcnt(36)
s_barrier
ds_read_b128 v[152:155], v220 offset:0
W 5
M v[48:51],v[160:163],v[144:147],v[48:51]
ds_read_b128 v[156:159], v221 offset:0
buffer_load_dwordx4 v232, s[20:23], s78 offen offset:2048 lds
ds_read_b128 v[160:163], v220 offset:128
s_add_u32 s78, s66, 460800
s_mov_b32 m0, 144384
W 5
M v[56:59],v[164:167],v[144:147],v[56:59]
buffer_load_dwordx4 v230, s[20:23], s78 offen lds
W 3
M v[64:67],v[168:171],v[144:147],v[64:67]
buffer_load_dwordx4 v231, s[20:23], s78 offen offset:1024 lds
ds_read_b128 v[164:167], v221 offset:128
W 2
M v[72:75],v[172:175],v[144:147],v[72:75]
ds_read_b128 v[168:171], v220 offset:256
buffer_load_dwordx4 v232, s[20:23], s78 offen offset:2048 lds
ds_read_b128 v[172:175], v221 offset:256
M v[80:83],v[176:179],v[144:147],v[80:83]
M v[88:91],v[180:183],v[144:147],v[88:91]
W 2
ds_read_b128 v[176:179], v220 offset:6144
M v[104:107],v[152:155],a[0:3],v[204:207]
ds_read_b128 v[180:183], v221 offset:6144
s_add_u32 s78, s67, 276480
M v[104:107],v[156:159],a[8:11],v[104:107]
s_mov_b32 m0, 101376
s_nop 0
buffer_load_dwordx4 v230, s[20:23], s78 offen lds
M v[104:107],v[160:163],a[16:19],v[104:107]
W 2
buffer_load_dwordx4 v231, s[20:23], s78 offen offset:1024 lds
ds_read_b128 v[152:155], v220 offset:6272
buffer_load_dwordx4 v232, s[20:23], s78 offen offset:2048 lds
s_add_u32 s78, s67, 337920
s_mov_b32 m0, 107520
ds_read_b128 v[156:159], v221 offset:6272
W 2
ds_read_b128 v[160:163], v220 offset:6400
buffer_load_dwordx4 v230, s[20:23], s78 offen lds
M v[104:107],v[172:175],a[40:43],v[104:107]
buffer_load_dwordx4 v231, s[20:23], s78 offen offset:1024 lds
M v[104:107],v[168:171],a[32:35],v[104:107]
M v[104:107],v[164:167],a[24:27],v[104:107]
ds_read_b128 v[164:167], v221 offset:6400
buffer_load_dwordx4 v232, s[20:23], s78 offen offset:2048 lds
M v[112:115],v[176:179],a[0:3],v[204:207]
ds_read_b128 v[168:171], v220 offset:12288
s_add_u32 s78, s67, 399360
M v[112:115],v[180:183],a[8:11],v[112:115]
ds_read_b128 v[172:175], v221 offset:12288
W 3
M v[112:115],v[152:155],a[16:19],v[112:115]
s_mov_b32 m0, 113664
s_nop 0
buffer_load_dwordx4 v230, s[20:23], s78 offen lds
v_max3_f32 v248, v104, v105, v106
v_max_f32 v248, v248, v107
v_exp_f32 v200, v104
buffer_load_dwordx4 v231, s[20:23], s78 offen offset:1024 lds
v_exp_f32 v201, v105
s_nop 0
v_cvt_pk_bf16_f32 v136, v200, v201
ds_read_b128 v[176:179], v220 offset:12416
v_exp_f32 v202, v106
W 2
ds_read_b128 v[180:183], v221 offset:12416
buffer_load_dwordx4 v232, s[20:23], s78 offen offset:2048 lds
s_add_u32 s78, s67, 430080
s_mov_b32 m0, 116736
ds_read_b128 v[152:155], v220 offset:12544
v_exp_f32 v203, v107
s_nop 0
v_cvt_pk_bf16_f32 v137, v202, v203
buffer_load_dwordx4 v230, s[20:23], s78 offen lds
v_add_u32 v216, 49152, v226
v_xor_b32 v217, 32, v216
v_xor_b32 v218, 64, v216
M v[112:115],v[164:167],a[40:43],v[112:115]
v_xor_b32 v219, 96, v216
M v[112:115],v[160:163],a[32:35],v[112:115]
buffer_load_dwordx4 v231, s[20:23], s78 offen offset:1024 lds
W 2
M v[112:115],v[156:159],a[24:27],v[112:115]
ds_read_b128 v[156:159], v221 offset:12544
M v[120:123],v[168:171],a[0:3],v[204:207]
ds_read_b128 v[160:163], v220 offset:18432
buffer_load_dwordx4 v232, s[20:23], s78 offen offset:2048 lds
M v[120:123],v[172:175],a[8:11],v[120:123]
ds_read_b128 v[164:167], v221 offset:18432
s_add_u32 s78, s67, 460800
M v[120:123],v[176:179],a[16:19],v[120:123]
s_mov_b32 m0, 119808
s_nop 0
buffer_load_dwordx4 v230, s[20:23], s78 offen lds
v_max3_f32 v248, v248, v112, v113
v_max3_f32 v248, v248, v114, v115
v_exp_f32 v200, v112
buffer_load_dwordx4 v231, s[20:23], s78 offen offset:1024 lds
v_exp_f32 v201, v113
s_nop 0
v_cvt_pk_bf16_f32 v138, v200, v201
ds_read_b128 v[168:171], v220 offset:18560
v_exp_f32 v202, v114
W 2
ds_read_b128 v[172:175], v221 offset:18560
buffer_load_dwordx4 v232, s[20:23], s78 offen offset:2048 lds
v_exp_f32 v203, v115
ds_read_b128 v[176:179], v220 offset:18688
v_cvt_pk_bf16_f32 v139, v202, v203
M v[120:123],v[156:159],a[40:43],v[120:123]
M v[120:123],v[152:155],a[32:35],v[120:123]
W 2
M v[120:123],v[180:183],a[24:27],v[120:123]
ds_read_b128 v[180:183], v221 offset:18688
M v[128:131],v[160:163],a[0:3],v[204:207]
s_waitcnt vmcnt(45)
s_barrier
T v[152:153],v216,0
M v[128:131],v[164:167],a[8:11],v[128:131]
T v[154:155],v216,2048
W 3
M v[128:131],v[168:171],a[16:19],v[128:131]
T v[156:157],v216,4096
T v[158:159],v216,6144
v_max3_f32 v248, v248, v120, v121
v_max3_f32 v248, v248, v122, v123
T v[160:161],v216,8192
T v[162:163],v216,10240
W 5
T v[164:165],v216,12288
T v[166:167],v216,14336
W 5
T v[168:169],v216,16384
M v[128:131],v[180:183],a[40:43],v[128:131]
T v[170:171],v216,18432
M v[128:131],v[176:179],a[32:35],v[128:131]
W 4
M v[128:131],v[172:175],a[24:27],v[128:131]
T v[172:173],v216,20480
T v[174:175],v216,22528
M v[184:187],v[152:155],a[48:51],0
T v[176:177],v217,0
s_add_u32 s78, s68, 276480
M v[184:187],v[156:159],a[56:59],v[184:187]
T v[178:179],v217,2048
s_mov_b32 m0, 3072
M v[184:187],v[160:163],a[64:67],v[184:187]
T v[180:181],v217,4096
buffer_load_dwordx4 v230, s[20:23], s78 offen lds
T v[182:183],v217,6144
v_max3_f32 v248, v248, v128, v129
v_max3_f32 v248, v248, v130, v131
v_cmp_lt_f32 vcc, s60, v248
buffer_load_dwordx4 v231, s[20:23], s78 offen offset:1024 lds
s_nop 0
s_cbranch_vccnz LST1_7w_%=
LRT1_7w_%=:
v_exp_f32 v200, v120
T v[152:153],v217,8192
v_exp_f32 v201, v121
s_nop 0
v_cvt_pk_bf16_f32 v144, v200, v201
T v[154:155],v217,10240
buffer_load_dwordx4 v232, s[20:23], s78 offen offset:2048 lds
s_add_u32 s78, s68, 337920
s_mov_b32 m0, 9216
T v[156:157],v217,12288
v_exp_f32 v202, v122
W 4
T v[158:159],v217,14336
buffer_load_dwordx4 v230, s[20:23], s78 offen lds
v_exp_f32 v203, v123
M v[184:187],v[172:175],a[88:91],v[184:187]
T v[160:161],v217,16384
v_cvt_pk_bf16_f32 v145, v202, v203
M v[184:187],v[168:171],a[80:83],v[184:187]
T v[162:163],v217,18432
buffer_load_dwordx4 v231, s[20:23], s78 offen offset:1024 lds
M v[184:187],v[164:167],a[72:75],v[184:187]
T v[164:165],v217,20480
W 5
M v[192:195],v[176:179],a[48:51],0
T v[166:167],v217,22528
buffer_load_dwordx4 v232, s[20:23], s78 offen offset:2048 lds
M v[192:195],v[180:183],a[56:59],v[192:195]
T v[168:169],v218,0
s_add_u32 s78, s68, 399360
M v[192:195],v[152:155],a[64:67],v[192:195]
T v[170:171],v218,2048
s_mov_b32 m0, 15360
T v[172:173],v218,4096
buffer_load_dwordx4 v230, s[20:23], s78 offen lds
v_exp_f32 v200, v128
T v[174:175],v218,6144
v_exp_f32 v201, v129
s_nop 0
v_cvt_pk_bf16_f32 v146, v200, v201
T v[176:177],v218,8192
buffer_load_dwordx4 v231, s[20:23], s78 offen offset:1024 lds
v_cvt_pk_bf16_f32 v252, v184, v185
v_cvt_pk_bf16_f32 v253, v186, v187
T v[178:179],v218,10240
v_accvgpr_write_b32 a240, v252
v_accvgpr_write_b32 a241, v253
v_add_u32 v220, 73728, v224
T v[180:181],v218,12288
buffer_load_dwordx4 v232, s[20:23], s78 offen offset:2048 lds
s_add_u32 s78, s68, 430080
s_mov_b32 m0, 18432
T v[182:183],v218,14336
v_exp_f32 v202, v130
W 5
M v[192:195],v[164:167],a[88:91],v[192:195]
T v[152:153],v218,16384
buffer_load_dwordx4 v230, s[20:23], s78 offen lds
M v[192:195],v[160:163],a[80:83],v[192:195]
T v[154:155],v218,18432
v_xor_b32 v221, 32, v220
M v[192:195],v[156:159],a[72:75],v[192:195]
T v[156:157],v218,20480
buffer_load_dwordx4 v231, s[20:23], s78 offen offset:1024 lds
M v[184:187],v[168:171],a[48:51],0
T v[158:159],v218,22528
W 5
M v[184:187],v[172:175],a[56:59],v[184:187]
T v[160:161],v219,0
buffer_load_dwordx4 v232, s[20:23], s78 offen offset:2048 lds
M v[184:187],v[176:179],a[64:67],v[184:187]
T v[162:163],v219,2048
s_add_u32 s78, s68, 460800
T v[164:165],v219,4096
s_mov_b32 m0, 21504
s_nop 0
buffer_load_dwordx4 v230, s[20:23], s78 offen lds
v_xor_b32 v222, 64, v220
T v[166:167],v219,6144
v_exp_f32 v203, v131
s_nop 0
v_cvt_pk_bf16_f32 v147, v202, v203
T v[168:169],v219,8192
buffer_load_dwordx4 v231, s[20:23], s78 offen offset:1024 lds
v_cvt_pk_bf16_f32 v252, v192, v193
v_cvt_pk_bf16_f32 v253, v194, v195
T v[170:171],v219,10240
v_accvgpr_write_b32 a242, v252
v_accvgpr_write_b32 a243, v253
v_xor_b32 v223, 96, v220
T v[172:173],v219,12288
buffer_load_dwordx4 v232, s[20:23], s78 offen offset:2048 lds
W 5
T v[174:175],v219,14336
M v[184:187],v[156:159],a[88:91],v[184:187]
T v[176:177],v219,16384
T v[178:179],v219,18432
M v[184:187],v[152:155],a[80:83],v[184:187]
W 5
M v[184:187],v[180:183],a[72:75],v[184:187]
T v[180:181],v219,20480
T v[182:183],v219,22528
M v[192:195],v[160:163],a[48:51],0
s_waitcnt vmcnt(40)
s_barrier
T v[152:153],v220,0
M v[192:195],v[164:167],a[56:59],v[192:195]
T v[154:155],v220,6144
W 6
M v[192:195],v[168:171],a[64:67],v[192:195]
W 4
T v[156:157],v221,0
T v[158:159],v221,6144
W 4
T v[160:161],v222,0
v_cvt_pk_bf16_f32 v252, v184, v185
v_cvt_pk_bf16_f32 v253, v186, v187
v_accvgpr_write_b32 a248, v252
T v[162:163],v222,6144
v_accvgpr_write_b32 a249, v253
W 4
T v[164:165],v223,0
T v[166:167],v223,6144
M v[192:195],v[180:183],a[88:91],v[192:195]
T v[168:169],v220,128
W 5
M v[192:195],v[176:179],a[80:83],v[192:195]
T v[170:171],v220,6272
M v[192:195],v[172:175],a[72:75],v[192:195]
T v[172:173],v221,128
W 5
M v[0:3],v[152:155],v[136:139],v[0:3]
T v[174:175],v221,6272
s_add_u32 s78, s69, 276480
T v[176:177],v222,128
s_mov_b32 m0, 27648
s_nop 0
buffer_load_dwordx4 v230, s[20:23], s78 offen lds
W 5
M v[96:99],v[244:247],v[136:139],v[96:99]
T v[178:179],v222,6272
T v[180:181],v223,128
buffer_load_dwordx4 v231, s[20:23], s78 offen offset:1024 lds
W 5
M v[8:11],v[156:159],v[136:139],v[8:11]
T v[182:183],v223,6272
T v[152:153],v220,256
buffer_load_dwordx4 v232, s[20:23], s78 offen offset:2048 lds
s_add_u32 s78, s69, 337920
s_mov_b32 m0, 33792
M v[16:19],v[160:163],v[136:139],v[16:19]
T v[154:155],v220,6400
v_cvt_pk_bf16_f32 v252, v192, v193
T v[156:157],v221,256
buffer_load_dwordx4 v230, s[20:23], s78 offen lds
v_cvt_pk_bf16_f32 v253, v194, v195
v_accvgpr_write_b32 a250, v252
M v[24:27],v[164:167],v[136:139],v[24:27]
T v[158:159],v221,6400
v_accvgpr_write_b32 a251, v253
T v[160:161],v222,256
buffer_load_dwordx4 v231, s[20:23], s78 offen offset:1024 lds
W 5
M v[32:35],v[168:171],v[136:139],v[32:35]
T v[162:163],v222,6400
T v[164:165],v223,256
buffer_load_dwordx4 v232, s[20:23], s78 offen offset:2048 lds
s_add_u32 s78, s69, 399360
s_mov_b32 m0, 39936
M v[40:43],v[172:175],v[136:139],v[40:43]
T v[166:167],v223,6400
W 5
T v[168:169],v220,12288
buffer_load_dwordx4 v230, s[20:23], s78 offen lds
M v[48:51],v[176:179],v[136:139],v[48:51]
T v[170:171],v220,18432
W 5
buffer_load_dwordx4 v231, s[20:23], s78 offen offset:1024 lds
M v[56:59],v[180:183],v[136:139],v[56:59]
W 3
T v[172:173],v221,12288
buffer_load_dwordx4 v232, s[20:23], s78 offen offset:2048 lds
s_add_u32 s78, s69, 430080
s_mov_b32 m0, 43008
M v[64:67],v[152:155],v[136:139],v[64:67]
T v[174:175],v221,18432
W 3
T v[176:177],v222,12288
buffer_load_dwordx4 v230, s[20:23], s78 offen lds
M v[72:75],v[156:159],v[136:139],v[72:75]
T v[178:179],v222,18432
v_add_u32 v216, 122880, v226
T v[180:181],v223,12288
buffer_load_dwordx4 v231, s[20:23], s78 offen offset:1024 lds
v_xor_b32 v217, 32, v216
v_xor_b32 v218, 64, v216
M v[80:83],v[160:163],v[136:139],v[80:83]
T v[182:183],v223,18432
v_xor_b32 v219, 96, v216
T v[152:153],v220,12416
buffer_load_dwordx4 v232, s[20:23], s78 offen offset:2048 lds
s_add_u32 s78, s69, 460800
s_mov_b32 m0, 46080
M v[88:91],v[164:167],v[136:139],v[88:91]
T v[154:155],v220,18560
W 5
T v[156:157],v221,12416
buffer_load_dwordx4 v230, s[20:23], s78 offen lds
M v[0:3],v[168:171],v[144:147],v[0:3]
T v[158:159],v221,18560
W 5
T v[160:161],v222,12416
buffer_load_dwordx4 v231, s[20:23], s78 offen offset:1024 lds
M v[96:99],v[244:247],v[144:147],v[96:99]
T v[162:163],v222,18560
W 5
T v[164:165],v223,12416
buffer_load_dwordx4 v232, s[20:23], s78 offen offset:2048 lds
M v[8:11],v[172:175],v[144:147],v[8:11]
T v[166:167],v223,18560
W 5
T v[168:169],v220,12544
M v[16:19],v[176:179],v[144:147],v[16:19]
T v[170:171],v220,18688
W 5
T v[172:173],v221,12544
M v[24:27],v[180:183],v[144:147],v[24:27]
T v[174:175],v221,18688
W 5
T v[176:177],v222,12544
M v[32:35],v[152:155],v[144:147],v[32:35]
T v[178:179],v222,18688
W 5
T v[180:181],v223,12544
M v[40:43],v[156:159],v[144:147],v[40:43]
T v[182:183],v223,18688
W 5
M v[48:51],v[160:163],v[144:147],v[48:51]
s_waitcnt vmcnt(45)
s_barrier
T v[152:153],v216,0
T v[154:155],v216,2048
W 4
M v[56:59],v[164:167],v[144:147],v[56:59]
T v[156:157],v216,4096
T v[158:159],v216,6144
W 4
M v[64:67],v[168:171],v[144:147],v[64:67]
T v[160:161],v216,8192
T v[162:163],v216,10240
M v[72:75],v[172:175],v[144:147],v[72:75]
W 4
T v[164:165],v216,12288
M v[80:83],v[176:179],v[144:147],v[80:83]
T v[166:167],v216,14336
W 4
T v[168:169],v216,16384
M v[88:91],v[180:183],v[144:147],v[88:91]
T v[170:171],v216,18432
W 4
T v[172:173],v216,20480
v_rcp_f32 v250, v96
s_nop 0
v_pk_mul_f32 v[0:1], v[0:1], v[250:251] op_sel_hi:[1,0]
v_pk_mul_f32 v[2:3], v[2:3], v[250:251] op_sel_hi:[1,0]
v_pk_mul_f32 v[8:9], v[8:9], v[250:251] op_sel_hi:[1,0]
s_add_u32 s78, s70, 276480
s_mov_b32 m0, 52224
s_nop 0
buffer_load_dwordx4 v230, s[20:23], s78 offen lds
v_pk_mul_f32 v[10:11], v[10:11], v[250:251] op_sel_hi:[1,0]
v_cvt_pk_bf16_f32 v104, v0, v1
v_cvt_pk_bf16_f32 v105, v2, v3
v_cvt_pk_bf16_f32 v106, v8, v9
buffer_load_dwordx4 v231, s[20:23], s78 offen offset:1024 lds
v_cvt_pk_bf16_f32 v107, v10, v11
v_pk_mul_f32 v[16:17], v[16:17], v[250:251] op_sel_hi:[1,0]
v_pk_mul_f32 v[18:19], v[18:19], v[250:251] op_sel_hi:[1,0]
v_pk_mul_f32 v[24:25], v[24:25], v[250:251] op_sel_hi:[1,0]
buffer_load_dwordx4 v232, s[20:23], s78 offen offset:2048 lds
v_pk_mul_f32 v[26:27], v[26:27], v[250:251] op_sel_hi:[1,0]
v_cvt_pk_bf16_f32 v112, v16, v17
v_cvt_pk_bf16_f32 v113, v18, v19
v_cvt_pk_bf16_f32 v114, v24, v25
s_add_u32 s78, s70, 337920
s_mov_b32 m0, 58368
s_nop 0
buffer_load_dwordx4 v230, s[20:23], s78 offen lds
v_cvt_pk_bf16_f32 v115, v26, v27
v_pk_mul_f32 v[32:33], v[32:33], v[250:251] op_sel_hi:[1,0]
v_pk_mul_f32 v[34:35], v[34:35], v[250:251] op_sel_hi:[1,0]
v_pk_mul_f32 v[40:41], v[40:41], v[250:251] op_sel_hi:[1,0]
buffer_load_dwordx4 v231, s[20:23], s78 offen offset:1024 lds
v_pk_mul_f32 v[42:43], v[42:43], v[250:251] op_sel_hi:[1,0]
v_cvt_pk_bf16_f32 v120, v32, v33
v_cvt_pk_bf16_f32 v121, v34, v35
v_cvt_pk_bf16_f32 v122, v40, v41
buffer_load_dwordx4 v232, s[20:23], s78 offen offset:2048 lds
v_cvt_pk_bf16_f32 v123, v42, v43
v_pk_mul_f32 v[48:49], v[48:49], v[250:251] op_sel_hi:[1,0]
v_pk_mul_f32 v[50:51], v[50:51], v[250:251] op_sel_hi:[1,0]
v_pk_mul_f32 v[56:57], v[56:57], v[250:251] op_sel_hi:[1,0]
s_add_u32 s78, s70, 399360
s_mov_b32 m0, 64512
s_nop 0
buffer_load_dwordx4 v230, s[20:23], s78 offen lds
v_pk_mul_f32 v[58:59], v[58:59], v[250:251] op_sel_hi:[1,0]
v_cvt_pk_bf16_f32 v128, v48, v49
v_cvt_pk_bf16_f32 v129, v50, v51
v_cvt_pk_bf16_f32 v130, v56, v57
buffer_load_dwordx4 v231, s[20:23], s78 offen offset:1024 lds
v_cvt_pk_bf16_f32 v131, v58, v59
v_pk_mul_f32 v[64:65], v[64:65], v[250:251] op_sel_hi:[1,0]
v_pk_mul_f32 v[66:67], v[66:67], v[250:251] op_sel_hi:[1,0]
v_pk_mul_f32 v[72:73], v[72:73], v[250:251] op_sel_hi:[1,0]
buffer_load_dwordx4 v232, s[20:23], s78 offen offset:2048 lds
v_pk_mul_f32 v[74:75], v[74:75], v[250:251] op_sel_hi:[1,0]
v_cvt_pk_bf16_f32 v136, v64, v65
v_cvt_pk_bf16_f32 v137, v66, v67
v_cvt_pk_bf16_f32 v138, v72, v73
s_add_u32 s78, s70, 430080
s_mov_b32 m0, 67584
s_nop 0
buffer_load_dwordx4 v230, s[20:23], s78 offen lds
v_cvt_pk_bf16_f32 v139, v74, v75
v_pk_mul_f32 v[80:81], v[80:81], v[250:251] op_sel_hi:[1,0]
v_pk_mul_f32 v[82:83], v[82:83], v[250:251] op_sel_hi:[1,0]
v_pk_mul_f32 v[88:89], v[88:89], v[250:251] op_sel_hi:[1,0]
buffer_load_dwordx4 v231, s[20:23], s78 offen offset:1024 lds
v_pk_mul_f32 v[90:91], v[90:91], v[250:251] op_sel_hi:[1,0]
v_cvt_pk_bf16_f32 v144, v80, v81
v_cvt_pk_bf16_f32 v145, v82, v83
v_cvt_pk_bf16_f32 v146, v88, v89
buffer_load_dwordx4 v232, s[20:23], s78 offen offset:2048 lds
v_cvt_pk_bf16_f32 v147, v90, v91
M v[184:187],v[236:239],a[96:99],0
T v[174:175],v216,22528
s_add_u32 s78, s70, 460800
M v[184:187],v[152:155],v[104:107],v[184:187]
s_mov_b32 m0, 70656
s_nop 0
buffer_load_dwordx4 v230, s[20:23], s78 offen lds
M v[184:187],v[156:159],v[112:115],v[184:187]
T v[176:177],v217,0
W 4
M v[184:187],v[160:163],v[120:123],v[184:187]
T v[178:179],v217,2048
buffer_load_dwordx4 v231, s[20:23], s78 offen offset:1024 lds
T v[180:181],v217,4096
W 4
T v[182:183],v217,6144
buffer_load_dwordx4 v232, s[20:23], s78 offen offset:2048 lds
T v[152:153],v217,8192
T v[154:155],v217,10240
W 5
T v[156:157],v217,12288
W 4
T v[158:159],v217,14336
M v[184:187],v[172:175],v[144:147],v[184:187]
T v[160:161],v217,16384
M v[184:187],v[168:171],v[136:139],v[184:187]
T v[162:163],v217,18432
W 4
M v[184:187],v[164:167],v[128:131],v[184:187]
T v[164:165],v217,20480
M v[192:195],v[234:237],a[96:99],0
T v[166:167],v217,22528
M v[192:195],v[176:179],v[104:107],v[192:195]
W 4
M v[192:195],v[180:183],v[112:115],v[192:195]
T v[168:169],v218,0
M v[192:195],v[152:155],v[120:123],v[192:195]
T v[170:171],v218,2048
W 4
T v[172:173],v218,4096
T v[174:175],v218,6144
v_cvt_pk_bf16_f32 v0, v184, v185
v_cvt_pk_bf16_f32 v1, v186, v187
W 4
T v[176:177],v218,8192
T v[178:179],v218,10240
v_add_u32 v220, 98304, v226
v_xor_b32 v221, 32, v220
v_xor_b32 v222, 64, v220
v_xor_b32 v223, 96, v220
T v[180:181],v218,12288
W 4
T v[182:183],v218,14336
M v[192:195],v[164:167],v[144:147],v[192:195]
T v[152:153],v218,16384
M v[192:195],v[160:163],v[136:139],v[192:195]
T v[154:155],v218,18432
W 4
M v[192:195],v[156:159],v[128:131],v[192:195]
T v[156:157],v218,20480
M v[184:187],v[236:239],a[104:107],0
T v[158:159],v218,22528
M v[184:187],v[168:171],v[104:107],v[184:187]
W 4
M v[184:187],v[172:175],v[112:115],v[184:187]
T v[160:161],v219,0
M v[184:187],v[176:179],v[120:123],v[184:187]
T v[162:163],v219,2048
W 4
T v[164:165],v219,4096
T v[166:167],v219,6144
v_cvt_pk_bf16_f32 v2, v192, v193
v_cvt_pk_bf16_f32 v3, v194, v195
W 4
s_mov_b32 s76, 0
buffer_store_dwordx4 v[0:3], v240, s[32:35], s76 offen sc1
T v[168:169],v219,8192
T v[170:171],v219,10240
W 4
T v[172:173],v219,12288
T v[174:175],v219,14336
W 4
M v[184:187],v[156:159],v[144:147],v[184:187]
T v[176:177],v219,16384
M v[184:187],v[152:155],v[136:139],v[184:187]
T v[178:179],v219,18432
W 4
M v[184:187],v[180:183],v[128:131],v[184:187]
T v[180:181],v219,20480
M v[192:195],v[234:237],a[104:107],0
T v[182:183],v219,22528
M v[192:195],v[160:163],v[104:107],v[192:195]
W 4
M v[192:195],v[164:167],v[112:115],v[192:195]
s_waitcnt vmcnt(46)
s_barrier
T v[152:153],v220,0
M v[192:195],v[168:171],v[120:123],v[192:195]
T v[154:155],v220,2048
W 4
T v[156:157],v220,4096
T v[158:159],v220,6144
v_cvt_pk_bf16_f32 v8, v184, v185
v_cvt_pk_bf16_f32 v9, v186, v187
W 4
T v[160:161],v220,8192
T v[162:163],v220,10240
W 4
T v[164:165],v220,12288
T v[166:167],v220,14336
W 4
M v[192:195],v[180:183],v[144:147],v[192:195]
T v[168:169],v220,16384
M v[192:195],v[176:179],v[136:139],v[192:195]
T v[170:171],v220,18432
W 4
M v[192:195],v[172:175],v[128:131],v[192:195]
T v[172:173],v220,20480
M v[184:187],v[236:239],a[112:115],0
T v[174:175],v220,22528
s_add_u32 s78, s71, 276480
M v[184:187],v[152:155],v[104:107],v[184:187]
s_mov_b32 m0, 76800
s_nop 0
buffer_load_dwordx4 v230, s[20:23], s78 offen lds
M v[184:187],v[156:159],v[112:115],v[184:187]
T v[176:177],v221,0
v_add_u32 v200, s54, v240
M v[184:187],v[160:163],v[120:123],v[184:187]
T v[178:179],v221,2048
buffer_load_dwordx4 v231, s[20:23], s78 offen offset:1024 lds
T v[180:181],v221,4096
v_add_u32 v201, s55, v240
v_add_u32 v202, s56, v240
W 4
T v[182:183],v221,6144
buffer_load_dwordx4 v232, s[20:23], s78 offen offset:2048 lds
s_add_u32 s78, s71, 337920
s_mov_b32 m0, 82944
v_cvt_pk_bf16_f32 v10, v192, v193
v_cvt_pk_bf16_f32 v11, v194, v195
T v[152:153],v221,8192
s_mov_b32 s76, 16384
buffer_store_dwordx4 v[8:11], v240, s[32:35], s76 offen sc1
buffer_load_dwordx4 v230, s[20:23], s78 offen lds
T v[154:155],v221,10240
W 4
T v[156:157],v221,12288
buffer_load_dwordx4 v231, s[20:23], s78 offen offset:1024 lds
T v[158:159],v221,14336
W 4
M v[184:187],v[172:175],v[144:147],v[184:187]
T v[160:161],v221,16384
buffer_load_dwordx4 v232, s[20:23], s78 offen offset:2048 lds
M v[184:187],v[168:171],v[136:139],v[184:187]
T v[162:163],v221,18432
s_add_u32 s78, s71, 399360
M v[184:187],v[164:167],v[128:131],v[184:187]
T v[164:165],v221,20480
s_mov_b32 m0, 89088
M v[192:195],v[234:237],a[112:115],0
T v[166:167],v221,22528
buffer_load_dwordx4 v230, s[20:23], s78 offen lds
M v[192:195],v[176:179],v[104:107],v[192:195]
W 4
M v[192:195],v[180:183],v[112:115],v[192:195]
T v[168:169],v222,0
buffer_load_dwordx4 v231, s[20:23], s78 offen offset:1024 lds
M v[192:195],v[152:155],v[120:123],v[192:195]
T v[170:171],v222,2048
W 4
T v[172:173],v222,4096
buffer_load_dwordx4 v232, s[20:23], s78 offen offset:2048 lds
s_add_u32 s78, s71, 430080
s_mov_b32 m0, 92160
T v[174:175],v222,6144
v_cvt_pk_bf16_f32 v16, v184, v185
v_cvt_pk_bf16_f32 v17, v186, v187
W 4
buffer_load_dwordx4 v230, s[20:23], s78 offen lds
T v[176:177],v222,8192
T v[178:179],v222,10240
buffer_load_dwordx4 v231, s[20:23], s78 offen offset:1024 lds
v_add_u32 v216, 0, v226
v_xor_b32 v217, 32, v216
T v[180:181],v222,12288
v_xor_b32 v218, 64, v216
v_xor_b32 v219, 96, v216
W 4
T v[182:183],v222,14336
buffer_load_dwordx4 v232, s[20:23], s78 offen offset:2048 lds
s_add_u32 s78, s71, 460800
s_mov_b32 m0, 95232
M v[192:195],v[164:167],v[144:147],v[192:195]
T v[152:153],v222,16384
M v[192:195],v[160:163],v[136:139],v[192:195]
T v[154:155],v222,18432
buffer_load_dwordx4 v230, s[20:23], s78 offen lds
M v[192:195],v[156:159],v[128:131],v[192:195]
T v[156:157],v222,20480
W 5
M v[184:187],v[236:239],a[120:123],0
T v[158:159],v222,22528
buffer_load_dwordx4 v231, s[20:23], s78 offen offset:1024 lds
M v[184:187],v[168:171],v[104:107],v[184:187]
W 4
M v[184:187],v[172:175],v[112:115],v[184:187]
T v[160:161],v223,0
buffer_load_dwordx4 v232, s[20:23], s78 offen offset:2048 lds
M v[184:187],v[176:179],v[120:123],v[184:187]
T v[162:163],v223,2048
W 4
T v[164:165],v223,4096
T v[166:167],v223,6144
v_cvt_pk_bf16_f32 v18, v192, v193
v_cvt_pk_bf16_f32 v19, v194, v195
W 4
s_mov_b32 s76, 229376
buffer_store_dwordx4 v[16:19], v240, s[32:35], s76 offen sc1
T v[168:169],v223,8192
T v[170:171],v223,10240
W 4
T v[172:173],v223,12288
T v[174:175],v223,14336
W 4
M v[184:187],v[156:159],v[144:147],v[184:187]
T v[176:177],v223,16384
M v[184:187],v[152:155],v[136:139],v[184:187]
T v[178:179],v223,18432
W 4
M v[184:187],v[180:183],v[128:131],v[184:187]
T v[180:181],v223,20480
M v[192:195],v[234:237],a[120:123],0
T v[182:183],v223,22528
M v[192:195],v[160:163],v[104:107],v[192:195]
W 4
M v[192:195],v[164:167],v[112:115],v[192:195]
s_waitcnt vmcnt(48)
s_barrier
T v[152:153],v216,0
M v[192:195],v[168:171],v[120:123],v[192:195]
T v[154:155],v216,2048
W 4
T v[156:157],v216,4096
T v[158:159],v216,6144
v_cvt_pk_bf16_f32 v24, v184, v185
v_cvt_pk_bf16_f32 v25, v186, v187
W 4
T v[160:161],v216,8192
T v[162:163],v216,10240
W 4
T v[164:165],v216,12288
T v[166:167],v216,14336
W 4
M v[192:195],v[180:183],v[144:147],v[192:195]
T v[168:169],v216,16384
M v[192:195],v[176:179],v[136:139],v[192:195]
T v[170:171],v216,18432
W 4
M v[192:195],v[172:175],v[128:131],v[192:195]
T v[172:173],v216,20480
M v[184:187],v[236:239],a[128:131],0
T v[174:175],v216,22528
s_add_u32 s78, s96, 276480
M v[184:187],v[152:155],v[104:107],v[184:187]
s_mov_b32 m0, 125952
s_nop 0
buffer_load_dwordx4 v230, s[20:23], s78 offen lds
M v[184:187],v[156:159],v[112:115],v[184:187]
T v[176:177],v217,0
W 4
M v[184:187],v[160:163],v[120:123],v[184:187]
T v[178:179],v217,2048
buffer_load_dwordx4 v231, s[20:23], s78 offen offset:1024 lds
T v[180:181],v217,4096
W 4
T v[182:183],v217,6144
buffer_load_dwordx4 v232, s[20:23], s78 offen offset:2048 lds
s_add_u32 s78, s96, 337920
s_mov_b32 m0, 132096
v_cvt_pk_bf16_f32 v26, v192, v193
v_cvt_pk_bf16_f32 v27, v194, v195
T v[152:153],v217,8192
s_mov_b32 s76, 245760
buffer_store_dwordx4 v[24:27], v240, s[32:35], s76 offen sc1
buffer_load_dwordx4 v230, s[20:23], s78 offen lds
T v[154:155],v217,10240
W 4
T v[156:157],v217,12288
buffer_load_dwordx4 v231, s[20:23], s78 offen offset:1024 lds
T v[158:159],v217,14336
W 4
M v[184:187],v[172:175],v[144:147],v[184:187]
T v[160:161],v217,16384
buffer_load_dwordx4 v232, s[20:23], s78 offen offset:2048 lds
M v[184:187],v[168:171],v[136:139],v[184:187]
T v[162:163],v217,18432
s_add_u32 s78, s96, 399360
M v[184:187],v[164:167],v[128:131],v[184:187]
T v[164:165],v217,20480
s_mov_b32 m0, 138240
M v[192:195],v[234:237],a[128:131],0
T v[166:167],v217,22528
buffer_load_dwordx4 v230, s[20:23], s78 offen lds
M v[192:195],v[176:179],v[104:107],v[192:195]
W 4
M v[192:195],v[180:183],v[112:115],v[192:195]
T v[168:169],v218,0
buffer_load_dwordx4 v231, s[20:23], s78 offen offset:1024 lds
M v[192:195],v[152:155],v[120:123],v[192:195]
T v[170:171],v218,2048
W 4
T v[172:173],v218,4096
buffer_load_dwordx4 v232, s[20:23], s78 offen offset:2048 lds
s_add_u32 s78, s96, 430080
s_mov_b32 m0, 141312
T v[174:175],v218,6144
v_cvt_pk_bf16_f32 v32, v184, v185
v_cvt_pk_bf16_f32 v33, v186, v187
W 4
buffer_load_dwordx4 v230, s[20:23], s78 offen lds
T v[176:177],v218,8192
T v[178:179],v218,10240
buffer_load_dwordx4 v231, s[20:23], s78 offen offset:1024 lds
v_add_u32 v220, 24576, v226
v_xor_b32 v221, 32, v220
T v[180:181],v218,12288
v_xor_b32 v222, 64, v220
v_xor_b32 v223, 96, v220
W 4
T v[182:183],v218,14336
buffer_load_dwordx4 v232, s[20:23], s78 offen offset:2048 lds
s_add_u32 s78, s96, 460800
s_mov_b32 m0, 144384
M v[192:195],v[164:167],v[144:147],v[192:195]
T v[152:153],v218,16384
M v[192:195],v[160:163],v[136:139],v[192:195]
T v[154:155],v218,18432
buffer_load_dwordx4 v230, s[20:23], s78 offen lds
M v[192:195],v[156:159],v[128:131],v[192:195]
T v[156:157],v218,20480
W 5
M v[184:187],v[236:239],a[136:139],0
T v[158:159],v218,22528
buffer_load_dwordx4 v231, s[20:23], s78 offen offset:1024 lds
M v[184:187],v[168:171],v[104:107],v[184:187]
W 4
M v[184:187],v[172:175],v[112:115],v[184:187]
T v[160:161],v219,0
buffer_load_dwordx4 v232, s[20:23], s78 offen offset:2048 lds
M v[184:187],v[176:179],v[120:123],v[184:187]
T v[162:163],v219,2048
W 4
T v[164:165],v219,4096
T v[166:167],v219,6144
v_cvt_pk_bf16_f32 v34, v192, v193
v_cvt_pk_bf16_f32 v35, v194, v195
W 4
s_mov_b32 s76, 458752
buffer_store_dwordx4 v[32:35], v240, s[32:35], s76 offen sc1
T v[168:169],v219,8192
T v[170:171],v219,10240
W 4
T v[172:173],v219,12288
T v[174:175],v219,14336
W 4
M v[184:187],v[156:159],v[144:147],v[184:187]
T v[176:177],v219,16384
M v[184:187],v[152:155],v[136:139],v[184:187]
T v[178:179],v219,18432
W 4
M v[184:187],v[180:183],v[128:131],v[184:187]
T v[180:181],v219,20480
M v[192:195],v[234:237],a[136:139],0
T v[182:183],v219,22528
M v[192:195],v[160:163],v[104:107],v[192:195]
W 4
M v[192:195],v[164:167],v[112:115],v[192:195]
s_waitcnt vmcnt(50)
s_barrier
T v[152:153],v220,0
M v[192:195],v[168:171],v[120:123],v[192:195]
T v[154:155],v220,2048
W 4
T v[156:157],v220,4096
T v[158:159],v220,6144
v_cvt_pk_bf16_f32 v40, v184, v185
v_cvt_pk_bf16_f32 v41, v186, v187
W 4
T v[160:161],v220,8192
T v[162:163],v220,10240
W 4
T v[164:165],v220,12288
T v[166:167],v220,14336
W 4
M v[192:195],v[180:183],v[144:147],v[192:195]
T v[168:169],v220,16384
M v[192:195],v[176:179],v[136:139],v[192:195]
T v[170:171],v220,18432
W 4
M v[192:195],v[172:175],v[128:131],v[192:195]
T v[172:173],v220,20480
M v[184:187],v[236:239],a[144:147],0
T v[174:175],v220,22528
s_add_u32 s78, s97, 276480
M v[184:187],v[152:155],v[104:107],v[184:187]
s_mov_b32 m0, 101376
s_nop 0
buffer_load_dwordx4 v230, s[20:23], s78 offen lds
M v[184:187],v[156:159],v[112:115],v[184:187]
T v[176:177],v221,0
W 4
M v[184:187],v[160:163],v[120:123],v[184:187]
T v[178:179],v221,2048
buffer_load_dwordx4 v231, s[20:23], s78 offen offset:1024 lds
T v[180:181],v221,4096
W 4
T v[182:183],v221,6144
buffer_load_dwordx4 v232, s[20:23], s78 offen offset:2048 lds
s_add_u32 s78, s97, 337920
s_mov_b32 m0, 107520
v_cvt_pk_bf16_f32 v42, v192, v193
v_cvt_pk_bf16_f32 v43, v194, v195
T v[152:153],v221,8192
s_mov_b32 s76, 475136
buffer_store_dwordx4 v[40:43], v240, s[32:35], s76 offen sc1
buffer_load_dwordx4 v230, s[20:23], s78 offen lds
T v[154:155],v221,10240
W 4
T v[156:157],v221,12288
buffer_load_dwordx4 v231, s[20:23], s78 offen offset:1024 lds
T v[158:159],v221,14336
W 4
M v[184:187],v[172:175],v[144:147],v[184:187]
T v[160:161],v221,16384
buffer_load_dwordx4 v232, s[20:23], s78 offen offset:2048 lds
M v[184:187],v[168:171],v[136:139],v[184:187]
T v[162:163],v221,18432
s_add_u32 s78, s97, 399360
M v[184:187],v[164:167],v[128:131],v[184:187]
T v[164:165],v221,20480
s_mov_b32 m0, 113664
M v[192:195],v[234:237],a[144:147],0
T v[166:167],v221,22528
buffer_load_dwordx4 v230, s[20:23], s78 offen lds
M v[192:195],v[176:179],v[104:107],v[192:195]
W 4
M v[192:195],v[180:183],v[112:115],v[192:195]
T v[168:169],v222,0
buffer_load_dwordx4 v231, s[20:23], s78 offen offset:1024 lds
M v[192:195],v[152:155],v[120:123],v[192:195]
T v[170:171],v222,2048
W 4
T v[172:173],v222,4096
buffer_load_dwordx4 v232, s[20:23], s78 offen offset:2048 lds
s_add_u32 s78, s97, 430080
s_mov_b32 m0, 116736
T v[174:175],v222,6144
v_cvt_pk_bf16_f32 v48, v184, v185
v_cvt_pk_bf16_f32 v49, v186, v187
W 4
buffer_load_dwordx4 v230, s[20:23], s78 offen lds
T v[176:177],v222,8192
T v[178:179],v222,10240
buffer_load_dwordx4 v231, s[20:23], s78 offen offset:1024 lds
v_add_u32 v216, 49152, v226
v_xor_b32 v217, 32, v216
T v[180:181],v222,12288
v_xor_b32 v218, 64, v216
v_xor_b32 v219, 96, v216
W 4
T v[182:183],v222,14336
buffer_load_dwordx4 v232, s[20:23], s78 offen offset:2048 lds
s_add_u32 s78, s97, 460800
s_mov_b32 m0, 119808
M v[192:195],v[164:167],v[144:147],v[192:195]
T v[152:153],v222,16384
M v[192:195],v[160:163],v[136:139],v[192:195]
T v[154:155],v222,18432
buffer_load_dwordx4 v230, s[20:23], s78 offen lds
M v[192:195],v[156:159],v[128:131],v[192:195]
T v[156:157],v222,20480
W 5
M v[184:187],v[236:239],a[152:155],0
T v[158:159],v222,22528
buffer_load_dwordx4 v231, s[20:23], s78 offen offset:1024 lds
M v[184:187],v[168:171],v[104:107],v[184:187]
W 4
M v[184:187],v[172:175],v[112:115],v[184:187]
T v[160:161],v223,0
buffer_load_dwordx4 v232, s[20:23], s78 offen offset:2048 lds
M v[184:187],v[176:179],v[120:123],v[184:187]
T v[162:163],v223,2048
W 4
T v[164:165],v223,4096
T v[166:167],v223,6144
v_cvt_pk_bf16_f32 v50, v192, v193
v_cvt_pk_bf16_f32 v51, v194, v195
W 4
s_mov_b32 s76, 688128
buffer_store_dwordx4 v[48:51], v240, s[32:35], s76 offen sc1
T v[168:169],v223,8192
T v[170:171],v223,10240
W 4
T v[172:173],v223,12288
T v[174:175],v223,14336
W 4
M v[184:187],v[156:159],v[144:147],v[184:187]
T v[176:177],v223,16384
M v[184:187],v[152:155],v[136:139],v[184:187]
T v[178:179],v223,18432
W 4
M v[184:187],v[180:183],v[128:131],v[184:187]
T v[180:181],v223,20480
M v[192:195],v[234:237],a[152:155],0
T v[182:183],v223,22528
M v[192:195],v[160:163],v[104:107],v[192:195]
W 4
M v[192:195],v[164:167],v[112:115],v[192:195]
s_waitcnt vmcnt(52)
s_barrier
T v[152:153],v216,0
M v[192:195],v[168:171],v[120:123],v[192:195]
T v[154:155],v216,2048
W 4
T v[156:157],v216,4096
T v[158:159],v216,6144
v_cvt_pk_bf16_f32 v56, v184, v185
v_cvt_pk_bf16_f32 v57, v186, v187
W 4
T v[160:161],v216,8192
T v[162:163],v216,10240
W 4
T v[164:165],v216,12288
T v[166:167],v216,14336
W 4
M v[192:195],v[180:183],v[144:147],v[192:195]
T v[168:169],v216,16384
M v[192:195],v[176:179],v[136:139],v[192:195]
T v[170:171],v216,18432
W 4
M v[192:195],v[172:175],v[128:131],v[192:195]
T v[172:173],v216,20480
M v[184:187],v[236:239],a[160:163],0
T v[174:175],v216,22528
s_add_u32 s78, s98, 276480
M v[184:187],v[152:155],v[104:107],v[184:187]
s_mov_b32 m0, 3072
s_nop 0
buffer_load_dwordx4 v230, s[20:23], s78 offen lds
M v[184:187],v[156:159],v[112:115],v[184:187]
T v[176:177],v217,0
W 4
M v[184:187],v[160:163],v[120:123],v[184:187]
T v[178:179],v217,2048
buffer_load_dwordx4 v231, s[20:23], s78 offen offset:1024 lds
T v[180:181],v217,4096
W 4
T v[182:183],v217,6144
buffer_load_dwordx4 v232, s[20:23], s78 offen offset:2048 lds
s_add_u32 s78, s98, 337920
s_mov_b32 m0, 9216
v_cvt_pk_bf16_f32 v58, v192, v193
v_cvt_pk_bf16_f32 v59, v194, v195
T v[152:153],v217,8192
s_mov_b32 s76, 704512
buffer_store_dwordx4 v[56:59], v240, s[32:35], s76 offen sc1
buffer_load_dwordx4 v230, s[20:23], s78 offen lds
T v[154:155],v217,10240
W 4
T v[156:157],v217,12288
buffer_load_dwordx4 v231, s[20:23], s78 offen offset:1024 lds
T v[158:159],v217,14336
W 4
M v[184:187],v[172:175],v[144:147],v[184:187]
T v[160:161],v217,16384
buffer_load_dwordx4 v232, s[20:23], s78 offen offset:2048 lds
M v[184:187],v[168:171],v[136:139],v[184:187]
T v[162:163],v217,18432
s_add_u32 s78, s98, 399360
M v[184:187],v[164:167],v[128:131],v[184:187]
T v[164:165],v217,20480
s_mov_b32 m0, 15360
M v[192:195],v[234:237],a[160:163],0
T v[166:167],v217,22528
buffer_load_dwordx4 v230, s[20:23], s78 offen lds
M v[192:195],v[176:179],v[104:107],v[192:195]
W 4
M v[192:195],v[180:183],v[112:115],v[192:195]
T v[168:169],v218,0
buffer_load_dwordx4 v231, s[20:23], s78 offen offset:1024 lds
M v[192:195],v[152:155],v[120:123],v[192:195]
T v[170:171],v218,2048
W 4
T v[172:173],v218,4096
buffer_load_dwordx4 v232, s[20:23], s78 offen offset:2048 lds
s_add_u32 s78, s98, 430080
s_mov_b32 m0, 18432
T v[174:175],v218,6144
v_cvt_pk_bf16_f32 v0, v184, v185
v_cvt_pk_bf16_f32 v1, v186, v187
W 4
buffer_load_dwordx4 v230, s[20:23], s78 offen lds
T v[176:177],v218,8192
T v[178:179],v218,10240
buffer_load_dwordx4 v231, s[20:23], s78 offen offset:1024 lds
v_add_u32 v220, 73728, v226
v_xor_b32 v221, 32, v220
T v[180:181],v218,12288
v_xor_b32 v222, 64, v220
v_xor_b32 v223, 96, v220
W 4
T v[182:183],v218,14336
buffer_load_dwordx4 v232, s[20:23], s78 offen offset:2048 lds
s_add_u32 s78, s98, 460800
s_mov_b32 m0, 21504
M v[192:195],v[164:167],v[144:147],v[192:195]
T v[152:153],v218,16384
M v[192:195],v[160:163],v[136:139],v[192:195]
T v[154:155],v218,18432
buffer_load_dwordx4 v230, s[20:23], s78 offen lds
M v[192:195],v[156:159],v[128:131],v[192:195]
T v[156:157],v218,20480
W 5
M v[184:187],v[236:239],a[168:171],0
T v[158:159],v218,22528
buffer_load_dwordx4 v231, s[20:23], s78 offen offset:1024 lds
M v[184:187],v[168:171],v[104:107],v[184:187]
W 4
M v[184:187],v[172:175],v[112:115],v[184:187]
T v[160:161],v219,0
buffer_load_dwordx4 v232, s[20:23], s78 offen offset:2048 lds
M v[184:187],v[176:179],v[120:123],v[184:187]
T v[162:163],v219,2048
W 4
T v[164:165],v219,4096
T v[166:167],v219,6144
v_cvt_pk_bf16_f32 v2, v192, v193
v_cvt_pk_bf16_f32 v3, v194, v195
W 4
s_mov_b32 s76, 917504
buffer_store_dwordx4 v[0:3], v240, s[32:35], s76 offen sc1
T v[168:169],v219,8192
T v[170:171],v219,10240
W 4
T v[172:173],v219,12288
T v[174:175],v219,14336
W 4
M v[184:187],v[156:159],v[144:147],v[184:187]
T v[176:177],v219,16384
M v[184:187],v[152:155],v[136:139],v[184:187]
T v[178:179],v219,18432
W 4
M v[184:187],v[180:183],v[128:131],v[184:187]
T v[180:181],v219,20480
M v[192:195],v[234:237],a[168:171],0
T v[182:183],v219,22528
M v[192:195],v[160:163],v[104:107],v[192:195]
W 4
M v[192:195],v[164:167],v[112:115],v[192:195]
s_waitcnt vmcnt(52)
s_barrier
T v[152:153],v220,0
M v[192:195],v[168:171],v[120:123],v[192:195]
T v[154:155],v220,2048
W 4
T v[156:157],v220,4096
T v[158:159],v220,6144
v_cvt_pk_bf16_f32 v8, v184, v185
v_cvt_pk_bf16_f32 v9, v186, v187
W 4
T v[160:161],v220,8192
T v[162:163],v220,10240
W 4
T v[164:165],v220,12288
T v[166:167],v220,14336
W 4
M v[192:195],v[180:183],v[144:147],v[192:195]
T v[168:169],v220,16384
M v[192:195],v[176:179],v[136:139],v[192:195]
T v[170:171],v220,18432
W 4
M v[192:195],v[172:175],v[128:131],v[192:195]
T v[172:173],v220,20480
M v[184:187],v[236:239],a[176:179],0
T v[174:175],v220,22528
s_add_u32 s78, s99, 276480
M v[184:187],v[152:155],v[104:107],v[184:187]
s_mov_b32 m0, 27648
s_nop 0
buffer_load_dwordx4 v230, s[20:23], s78 offen lds
M v[184:187],v[156:159],v[112:115],v[184:187]
T v[176:177],v221,0
W 4
M v[184:187],v[160:163],v[120:123],v[184:187]
T v[178:179],v221,2048
buffer_load_dwordx4 v231, s[20:23], s78 offen offset:1024 lds
T v[180:181],v221,4096
W 4
T v[182:183],v221,6144
buffer_load_dwordx4 v232, s[20:23], s78 offen offset:2048 lds
s_add_u32 s78, s99, 337920
s_mov_b32 m0, 33792
v_cvt_pk_bf16_f32 v10, v192, v193
v_cvt_pk_bf16_f32 v11, v194, v195
T v[152:153],v221,8192
s_mov_b32 s76, 933888
buffer_store_dwordx4 v[8:11], v240, s[32:35], s76 offen sc1
buffer_load_dwordx4 v230, s[20:23], s78 offen lds
T v[154:155],v221,10240
W 4
T v[156:157],v221,12288
buffer_load_dwordx4 v231, s[20:23], s78 offen offset:1024 lds
T v[158:159],v221,14336
W 4
M v[184:187],v[172:175],v[144:147],v[184:187]
T v[160:161],v221,16384
buffer_load_dwordx4 v232, s[20:23], s78 offen offset:2048 lds
M v[184:187],v[168:171],v[136:139],v[184:187]
T v[162:163],v221,18432
s_add_u32 s78, s99, 399360
M v[184:187],v[164:167],v[128:131],v[184:187]
T v[164:165],v221,20480
s_mov_b32 m0, 39936
M v[192:195],v[234:237],a[176:179],0
T v[166:167],v221,22528
buffer_load_dwordx4 v230, s[20:23], s78 offen lds
M v[192:195],v[176:179],v[104:107],v[192:195]
W 4
M v[192:195],v[180:183],v[112:115],v[192:195]
T v[168:169],v222,0
buffer_load_dwordx4 v231, s[20:23], s78 offen offset:1024 lds
M v[192:195],v[152:155],v[120:123],v[192:195]
T v[170:171],v222,2048
W 4
T v[172:173],v222,4096
buffer_load_dwordx4 v232, s[20:23], s78 offen offset:2048 lds
s_add_u32 s78, s99, 430080
s_mov_b32 m0, 43008
T v[174:175],v222,6144
v_cvt_pk_bf16_f32 v16, v184, v185
v_cvt_pk_bf16_f32 v17, v186, v187
W 4
buffer_load_dwordx4 v230, s[20:23], s78 offen lds
T v[176:177],v222,8192
T v[178:179],v222,10240
buffer_load_dwordx4 v231, s[20:23], s78 offen offset:1024 lds
v_add_u32 v216, 122880, v226
v_xor_b32 v217, 32, v216
T v[180:181],v222,12288
v_xor_b32 v218, 64, v216
v_xor_b32 v219, 96, v216
W 4
T v[182:183],v222,14336
buffer_load_dwordx4 v232, s[20:23], s78 offen offset:2048 lds
s_add_u32 s78, s99, 460800
s_mov_b32 m0, 46080
M v[192:195],v[164:167],v[144:147],v[192:195]
T v[152:153],v222,16384
M v[192:195],v[160:163],v[136:139],v[192:195]
T v[154:155],v222,18432
buffer_load_dwordx4 v230, s[20:23], s78 offen lds
M v[192:195],v[156:159],v[128:131],v[192:195]
T v[156:157],v222,20480
W 5
M v[184:187],v[236:239],a[184:187],0
T v[158:159],v222,22528
buffer_load_dwordx4 v231, s[20:23], s78 offen offset:1024 lds
M v[184:187],v[168:171],v[104:107],v[184:187]
W 4
M v[184:187],v[172:175],v[112:115],v[184:187]
T v[160:161],v223,0
buffer_load_dwordx4 v232, s[20:23], s78 offen offset:2048 lds
M v[184:187],v[176:179],v[120:123],v[184:187]
T v[162:163],v223,2048
W 4
T v[164:165],v223,4096
T v[166:167],v223,6144
v_cvt_pk_bf16_f32 v18, v192, v193
v_cvt_pk_bf16_f32 v19, v194, v195
W 4
s_mov_b32 s76, 1146880
buffer_store_dwordx4 v[16:19], v240, s[32:35], s76 offen sc1
T v[168:169],v223,8192
T v[170:171],v223,10240
W 4
T v[172:173],v223,12288
T v[174:175],v223,14336
W 4
M v[184:187],v[156:159],v[144:147],v[184:187]
T v[176:177],v223,16384
M v[184:187],v[152:155],v[136:139],v[184:187]
T v[178:179],v223,18432
W 4
M v[184:187],v[180:183],v[128:131],v[184:187]
T v[180:181],v223,20480
M v[192:195],v[234:237],a[184:187],0
T v[182:183],v223,22528
M v[192:195],v[160:163],v[104:107],v[192:195]
W 4
M v[192:195],v[164:167],v[112:115],v[192:195]
s_waitcnt vmcnt(52)
s_barrier
T v[152:153],v216,0
M v[192:195],v[168:171],v[120:123],v[192:195]
T v[154:155],v216,2048
W 4
T v[156:157],v216,4096
T v[158:159],v216,6144
v_cvt_pk_bf16_f32 v24, v184, v185
v_cvt_pk_bf16_f32 v25, v186, v187
W 4
T v[160:161],v216,8192
T v[162:163],v216,10240
W 4
T v[164:165],v216,12288
T v[166:167],v216,14336
W 4
M v[192:195],v[180:183],v[144:147],v[192:195]
T v[168:169],v216,16384
M v[192:195],v[176:179],v[136:139],v[192:195]
T v[170:171],v216,18432
W 4
M v[192:195],v[172:175],v[128:131],v[192:195]
T v[172:173],v216,20480
M v[184:187],v[236:239],a[192:195],0
T v[174:175],v216,22528
M v[184:187],v[152:155],v[104:107],v[184:187]
W 4
M v[184:187],v[156:159],v[112:115],v[184:187]
T v[176:177],v217,0
M v[184:187],v[160:163],v[120:123],v[184:187]
T v[178:179],v217,2048
W 4
T v[180:181],v217,4096
T v[182:183],v217,6144
v_cvt_pk_bf16_f32 v26, v192, v193
v_cvt_pk_bf16_f32 v27, v194, v195
W 4
s_mov_b32 s76, 1163264
buffer_store_dwordx4 v[24:27], v240, s[32:35], s76 offen sc1
T v[152:153],v217,8192
T v[154:155],v217,10240
W 4
T v[156:157],v217,12288
T v[158:159],v217,14336
W 4
M v[184:187],v[172:175],v[144:147],v[184:187]
T v[160:161],v217,16384
M v[184:187],v[168:171],v[136:139],v[184:187]
T v[162:163],v217,18432
W 4
M v[184:187],v[164:167],v[128:131],v[184:187]
T v[164:165],v217,20480
M v[192:195],v[234:237],a[192:195],0
T v[166:167],v217,22528
M v[192:195],v[176:179],v[104:107],v[192:195]
W 4
M v[192:195],v[180:183],v[112:115],v[192:195]
T v[168:169],v218,0
M v[192:195],v[152:155],v[120:123],v[192:195]
T v[170:171],v218,2048
W 4
T v[172:173],v218,4096
T v[174:175],v218,6144
v_cvt_pk_bf16_f32 v32, v184, v185
v_cvt_pk_bf16_f32 v33, v186, v187
W 4
T v[176:177],v218,8192
T v[178:179],v218,10240
v_add_u32 v220, 98304, v226
v_xor_b32 v221, 32, v220
v_xor_b32 v222, 64, v220
v_xor_b32 v223, 96, v220
T v[180:181],v218,12288
W 4
T v[182:183],v218,14336
M v[192:195],v[164:167],v[144:147],v[192:195]
T v[152:153],v218,16384
M v[192:195],v[160:163],v[136:139],v[192:195]
T v[154:155],v218,18432
W 4
M v[192:195],v[156:159],v[128:131],v[192:195]
T v[156:157],v218,20480
M v[184:187],v[236:239],a[200:203],0
T v[158:159],v218,22528
M v[184:187],v[168:171],v[104:107],v[184:187]
W 4
M v[184:187],v[172:175],v[112:115],v[184:187]
T v[160:161],v219,0
M v[184:187],v[176:179],v[120:123],v[184:187]
T v[162:163],v219,2048
W 4
T v[164:165],v219,4096
T v[166:167],v219,6144
v_cvt_pk_bf16_f32 v34, v192, v193
v_cvt_pk_bf16_f32 v35, v194, v195
W 4
s_mov_b32 s76, 1376256
buffer_store_dwordx4 v[32:35], v240, s[32:35], s76 offen sc1
T v[168:169],v219,8192
T v[170:171],v219,10240
W 4
T v[172:173],v219,12288
T v[174:175],v219,14336
W 4
M v[184:187],v[156:159],v[144:147],v[184:187]
T v[176:177],v219,16384
M v[184:187],v[152:155],v[136:139],v[184:187]
T v[178:179],v219,18432
W 4
M v[184:187],v[180:183],v[128:131],v[184:187]
T v[180:181],v219,20480
M v[192:195],v[234:237],a[200:203],0
T v[182:183],v219,22528
M v[192:195],v[160:163],v[104:107],v[192:195]
W 4
M v[192:195],v[164:167],v[112:115],v[192:195]
s_waitcnt vmcnt(37)
s_barrier
T v[152:153],v220,0
M v[192:195],v[168:171],v[120:123],v[192:195]
T v[154:155],v220,2048
W 4
T v[156:157],v220,4096
T v[158:159],v220,6144
v_cvt_pk_bf16_f32 v40, v184, v185
v_cvt_pk_bf16_f32 v41, v186, v187
W 4
T v[160:161],v220,8192
T v[162:163],v220,10240
W 4
T v[164:165],v220,12288
T v[166:167],v220,14336
W 4
M v[192:195],v[180:183],v[144:147],v[192:195]
T v[168:169],v220,16384
M v[192:195],v[176:179],v[136:139],v[192:195]
T v[170:171],v220,18432
W 4
M v[192:195],v[172:175],v[128:131],v[192:195]
T v[172:173],v220,20480
M v[184:187],v[236:239],a[208:211],0
T v[174:175],v220,22528
M v[184:187],v[152:155],v[104:107],v[184:187]
W 4
M v[184:187],v[156:159],v[112:115],v[184:187]
T v[176:177],v221,0
M v[184:187],v[160:163],v[120:123],v[184:187]
T v[178:179],v221,2048
W 4
T v[180:181],v221,4096
T v[182:183],v221,6144
v_cvt_pk_bf16_f32 v42, v192, v193
v_cvt_pk_bf16_f32 v43, v194, v195
W 4
s_mov_b32 s76, 1392640
buffer_store_dwordx4 v[40:43], v240, s[32:35], s76 offen sc1
T v[152:153],v221,8192
T v[154:155],v221,10240
W 4
T v[156:157],v221,12288
T v[158:159],v221,14336
W 4
M v[184:187],v[172:175],v[144:147],v[184:187]
T v[160:161],v221,16384
M v[184:187],v[168:171],v[136:139],v[184:187]
T v[162:163],v221,18432
W 4
M v[184:187],v[164:167],v[128:131],v[184:187]
T v[164:165],v221,20480
M v[192:195],v[234:237],a[208:211],0
T v[166:167],v221,22528
M v[192:195],v[176:179],v[104:107],v[192:195]
W 4
M v[192:195],v[180:183],v[112:115],v[192:195]
T v[168:169],v222,0
M v[192:195],v[152:155],v[120:123],v[192:195]
T v[170:171],v222,2048
W 4
T v[172:173],v222,4096
T v[174:175],v222,6144
v_cvt_pk_bf16_f32 v48, v184, v185
v_cvt_pk_bf16_f32 v49, v186, v187
W 4
T v[176:177],v222,8192
T v[178:179],v222,10240
v_add_u32 v216, 0, v226
v_xor_b32 v217, 32, v216
v_xor_b32 v218, 64, v216
v_xor_b32 v219, 96, v216
T v[180:181],v222,12288
W 4
T v[182:183],v222,14336
M v[192:195],v[164:167],v[144:147],v[192:195]
T v[152:153],v222,16384
M v[192:195],v[160:163],v[136:139],v[192:195]
T v[154:155],v222,18432
W 4
M v[192:195],v[156:159],v[128:131],v[192:195]
T v[156:157],v222,20480
M v[184:187],v[236:239],a[216:219],0
T v[158:159],v222,22528
M v[184:187],v[168:171],v[104:107],v[184:187]
W 4
M v[184:187],v[172:175],v[112:115],v[184:187]
T v[160:161],v223,0
M v[184:187],v[176:179],v[120:123],v[184:187]
T v[162:163],v223,2048
W 4
T v[164:165],v223,4096
T v[166:167],v223,6144
v_cvt_pk_bf16_f32 v50, v192, v193
v_cvt_pk_bf16_f32 v51, v194, v195
W 4
s_mov_b32 s76, 1605632
buffer_store_dwordx4 v[48:51], v240, s[32:35], s76 offen sc1
T v[168:169],v223,8192
T v[170:171],v223,10240
W 4
T v[172:173],v223,12288
T v[174:175],v223,14336
W 4
M v[184:187],v[156:159],v[144:147],v[184:187]
T v[176:177],v223,16384
M v[184:187],v[152:155],v[136:139],v[184:187]
T v[178:179],v223,18432
W 4
M v[184:187],v[180:183],v[128:131],v[184:187]
T v[180:181],v223,20480
M v[192:195],v[234:237],a[216:219],0
T v[182:183],v223,22528
M v[192:195],v[160:163],v[104:107],v[192:195]
W 4
M v[192:195],v[164:167],v[112:115],v[192:195]
s_waitcnt vmcnt(22)
s_barrier
T v[152:153],v216,0
M v[192:195],v[168:171],v[120:123],v[192:195]
T v[154:155],v216,2048
W 4
T v[156:157],v216,4096
T v[158:159],v216,6144
v_cvt_pk_bf16_f32 v56, v184, v185
v_cvt_pk_bf16_f32 v57, v186, v187
W 4
T v[160:161],v216,8192
T v[162:163],v216,10240
W 4
T v[164:165],v216,12288
T v[166:167],v216,14336
W 4
M v[192:195],v[180:183],v[144:147],v[192:195]
T v[168:169],v216,16384
M v[192:195],v[176:179],v[136:139],v[192:195]
T v[170:171],v216,18432
W 4
M v[192:195],v[172:175],v[128:131],v[192:195]
T v[172:173],v216,20480
M v[184:187],v[236:239],a[224:227],0
T v[174:175],v216,22528
M v[184:187],v[152:155],v[104:107],v[184:187]
W 4
M v[184:187],v[156:159],v[112:115],v[184:187]
T v[176:177],v217,0
M v[184:187],v[160:163],v[120:123],v[184:187]
T v[178:179],v217,2048
W 4
T v[180:181],v217,4096
T v[182:183],v217,6144
v_cvt_pk_bf16_f32 v58, v192, v193
v_cvt_pk_bf16_f32 v59, v194, v195
W 4
s_mov_b32 s76, 1622016
buffer_store_dwordx4 v[56:59], v240, s[32:35], s76 offen sc1
T v[152:153],v217,8192
T v[154:155],v217,10240
W 4
T v[156:157],v217,12288
T v[158:159],v217,14336
W 4
M v[184:187],v[172:175],v[144:147],v[184:187]
T v[160:161],v217,16384
M v[184:187],v[168:171],v[136:139],v[184:187]
T v[162:163],v217,18432
W 4
M v[184:187],v[164:167],v[128:131],v[184:187]
T v[164:165],v217,20480
M v[192:195],v[234:237],a[224:227],0
T v[166:167],v217,22528
M v[192:195],v[176:179],v[104:107],v[192:195]
W 4
M v[192:195],v[180:183],v[112:115],v[192:195]
T v[168:169],v218,0
M v[192:195],v[152:155],v[120:123],v[192:195]
T v[170:171],v218,2048
W 4
T v[172:173],v218,4096
T v[174:175],v218,6144
v_cvt_pk_bf16_f32 v0, v184, v185
v_cvt_pk_bf16_f32 v1, v186, v187
W 4
T v[176:177],v218,8192
T v[178:179],v218,10240
v_add_u32 v220, 24576, v226
v_xor_b32 v221, 32, v220
v_xor_b32 v222, 64, v220
v_xor_b32 v223, 96, v220
T v[180:181],v218,12288
W 4
T v[182:183],v218,14336
M v[192:195],v[164:167],v[144:147],v[192:195]
T v[152:153],v218,16384
M v[192:195],v[160:163],v[136:139],v[192:195]
T v[154:155],v218,18432
W 4
M v[192:195],v[156:159],v[128:131],v[192:195]
T v[156:157],v218,20480
M v[184:187],v[236:239],a[232:235],0
T v[158:159],v218,22528
M v[184:187],v[168:171],v[104:107],v[184:187]
W 4
M v[184:187],v[172:175],v[112:115],v[184:187]
T v[160:161],v219,0
M v[184:187],v[176:179],v[120:123],v[184:187]
T v[162:163],v219,2048
W 4
T v[164:165],v219,4096
T v[166:167],v219,6144
v_cvt_pk_bf16_f32 v2, v192, v193
v_cvt_pk_bf16_f32 v3, v194, v195
W 4
s_mov_b32 s76, 1835008
buffer_store_dwordx4 v[0:3], v240, s[32:35], s76 offen sc1
T v[168:169],v219,8192
T v[170:171],v219,10240
W 4
T v[172:173],v219,12288
T v[174:175],v219,14336
W 4
M v[184:187],v[156:159],v[144:147],v[184:187]
T v[176:177],v219,16384
M v[184:187],v[152:155],v[136:139],v[184:187]
T v[178:179],v219,18432
W 4
M v[184:187],v[180:183],v[128:131],v[184:187]
T v[180:181],v219,20480
M v[192:195],v[234:237],a[232:235],0
T v[182:183],v219,22528
M v[192:195],v[160:163],v[104:107],v[192:195]
W 4
M v[192:195],v[164:167],v[112:115],v[192:195]
s_waitcnt vmcnt(7)
s_barrier
T v[152:153],v220,0
M v[192:195],v[168:171],v[120:123],v[192:195]
T v[154:155],v220,2048
W 4
T v[156:157],v220,4096
T v[158:159],v220,6144
v_cvt_pk_bf16_f32 v8, v184, v185
v_cvt_pk_bf16_f32 v9, v186, v187
W 4
T v[160:161],v220,8192
T v[162:163],v220,10240
W 4
T v[164:165],v220,12288
T v[166:167],v220,14336
W 4
M v[192:195],v[180:183],v[144:147],v[192:195]
T v[168:169],v220,16384
M v[192:195],v[176:179],v[136:139],v[192:195]
T v[170:171],v220,18432
W 4
M v[192:195],v[172:175],v[128:131],v[192:195]
T v[172:173],v220,20480
M v[184:187],v[236:239],a[240:243],0
T v[174:175],v220,22528
M v[184:187],v[152:155],v[104:107],v[184:187]
W 4
M v[184:187],v[156:159],v[112:115],v[184:187]
T v[176:177],v221,0
M v[184:187],v[160:163],v[120:123],v[184:187]
T v[178:179],v221,2048
W 4
T v[180:181],v221,4096
T v[182:183],v221,6144
v_cvt_pk_bf16_f32 v10, v192, v193
v_cvt_pk_bf16_f32 v11, v194, v195
W 4
s_mov_b32 s76, 1851392
buffer_store_dwordx4 v[8:11], v240, s[32:35], s76 offen sc1
T v[152:153],v221,8192
T v[154:155],v221,10240
W 4
T v[156:157],v221,12288
s_mov_b32 exec_lo, 0xffff
s_mov_b32 exec_hi, 0
buffer_store_dword v215, v243, s[28:31], 0 offen sc1
s_mov_b64 exec, -1
T v[158:159],v221,14336
W 4
M v[184:187],v[172:175],v[144:147],v[184:187]
T v[160:161],v221,16384
M v[184:187],v[168:171],v[136:139],v[184:187]
T v[162:163],v221,18432
W 4
M v[184:187],v[164:167],v[128:131],v[184:187]
T v[164:165],v221,20480
M v[192:195],v[234:237],a[240:243],0
T v[166:167],v221,22528
M v[192:195],v[176:179],v[104:107],v[192:195]
W 4
M v[192:195],v[180:183],v[112:115],v[192:195]
T v[168:169],v222,0
M v[192:195],v[152:155],v[120:123],v[192:195]
T v[170:171],v222,2048
W 4
T v[172:173],v222,4096
T v[174:175],v222,6144
v_cvt_pk_bf16_f32 v16, v184, v185
v_cvt_pk_bf16_f32 v17, v186, v187
W 4
T v[176:177],v222,8192
T v[178:179],v222,10240
W 4
T v[180:181],v222,12288
T v[182:183],v222,14336
W 4
M v[192:195],v[164:167],v[144:147],v[192:195]
T v[152:153],v222,16384
M v[192:195],v[160:163],v[136:139],v[192:195]
T v[154:155],v222,18432
W 4
M v[192:195],v[156:159],v[128:131],v[192:195]
T v[156:157],v222,20480
M v[184:187],v[236:239],a[248:251],0
T v[158:159],v222,22528
M v[184:187],v[168:171],v[104:107],v[184:187]
W 4
M v[184:187],v[172:175],v[112:115],v[184:187]
T v[160:161],v223,0
M v[184:187],v[176:179],v[120:123],v[184:187]
T v[162:163],v223,2048
W 4
T v[164:165],v223,4096
T v[166:167],v223,6144
v_cvt_pk_bf16_f32 v18, v192, v193
v_cvt_pk_bf16_f32 v19, v194, v195
W 4
s_mov_b32 s76, 2064384
buffer_store_dwordx4 v[16:19], v240, s[32:35], s76 offen sc1
T v[168:169],v223,8192
T v[170:171],v223,10240
W 4
T v[172:173],v223,12288
T v[174:175],v223,14336
W 4
M v[184:187],v[156:159],v[144:147],v[184:187]
T v[176:177],v223,16384
M v[184:187],v[152:155],v[136:139],v[184:187]
T v[178:179],v223,18432
W 4
M v[184:187],v[180:183],v[128:131],v[184:187]
T v[180:181],v223,20480
M v[192:195],v[234:237],a[248:251],0
T v[182:183],v223,22528
M v[192:195],v[160:163],v[104:107],v[192:195]
W 4
M v[192:195],v[164:167],v[112:115],v[192:195]
M v[192:195],v[168:171],v[120:123],v[192:195]
W 2
v_cvt_pk_bf16_f32 v24, v184, v185
v_cvt_pk_bf16_f32 v25, v186, v187
W 0
M v[192:195],v[180:183],v[144:147],v[192:195]
M v[192:195],v[176:179],v[136:139],v[192:195]
M v[192:195],v[172:175],v[128:131],v[192:195]
s_nop 7
v_cvt_pk_bf16_f32 v26, v192, v193
v_cvt_pk_bf16_f32 v27, v194, v195
s_mov_b32 s76, 2080768
buffer_store_dwordx4 v[24:27], v240, s[32:35], s76 offen sc1
s_nop 7
s_nop 7
s_branch LJOIN_%=
LST0_1w_%=:
s_getpc_b64 s[80:81]
s_branch LRARE_%=
s_branch LRT0_1w_%=
LST0_2w_%=:
s_getpc_b64 s[80:81]
s_branch LRARE_%=
s_branch LRT0_2w_%=
LST0_3w_%=:
s_getpc_b64 s[80:81]
s_branch LRARE_%=
s_branch LRT0_3w_%=
LST0_4w_%=:
s_getpc_b64 s[80:81]
s_branch LRARE_%=
s_branch LRT0_4w_%=
LST0_5w_%=:
s_getpc_b64 s[80:81]
s_branch LRARE_%=
s_branch LRT0_5w_%=
LST0_6w_%=:
s_getpc_b64 s[80:81]
s_branch LRARE_%=
s_branch LRT0_6w_%=
LST0_7w_%=:
s_getpc_b64 s[80:81]
s_branch LRARE_%=
s_branch LRT0_7w_%=
LST1_1w_%=:
s_getpc_b64 s[80:81]
s_branch LRARE_%=
s_branch LRT1_1w_%=
LST1_2w_%=:
s_getpc_b64 s[80:81]
s_branch LRARE_%=
s_branch LRT1_2w_%=
LST1_3w_%=:
s_getpc_b64 s[80:81]
s_branch LRARE_%=
s_branch LRT1_3w_%=
LST1_4w_%=:
s_getpc_b64 s[80:81]
s_branch LRARE_%=
s_branch LRT1_4w_%=
LST1_5w_%=:
s_getpc_b64 s[80:81]
s_branch LRARE_%=
s_branch LRT1_5w_%=
LST1_6w_%=:
s_getpc_b64 s[80:81]
s_branch LRARE_%=
s_branch LRT1_6w_%=
LST1_7w_%=:
s_getpc_b64 s[80:81]
s_branch LRARE_%=
s_branch LRT1_7w_%=
LEND_%=:
Lmha_end:
)ASM"
    : "+{v[104:111]}"(ve), "+{v[224:239]}"(vc), "+{v[240:243]}"(vd), "+{s[88:91]}"(tq), "+{v253}"(wm)
    : "{s[8:11]}"(sK), "{s[12:15]}"(sV), "{s[16:19]}"(sWQ), "{s[20:23]}"(sWO), "{s[24:27]}"(sX), "{s[28:31]}"(sWS), "{s[32:35]}"(sP0), "{s[36:39]}"(sP1),
       "{s[40:55]}"(sa), "{s[56:71]}"(sb), "{s[0:7]}"(st), "{s[92:95]}"(sOut), "{s[96:99]}"(sc)
    : "memory", "vcc", "scc", "v0", "v1", "v2", "v3", "v4", "v5", "v6", "v7", "v8", "v9", "v10", "v11", "v12", "v13", "v14", "v15", "v16", "v17", "v18", "v19", "v20", "v21", "v22", "v23", "v24", "v25", "v26", "v27", "v28", "v29", "v30", "v31", "v32", "v33", "v34", "v35", "v36", "v37", "v38", "v39", "v40", "v41", "v42", "v43", "v44", "v45", "v46", "v47", "v48", "v49", "v50", "v51", "v52", "v53", "v54", "v55", "v56", "v57", "v58", "v59", "v60", "v61", "v62", "v63", "v64", "v65", "v66", "v67", "v68", "v69", "v70", "v71", "v72", "v73", "v74", "v75", "v76", "v77", "v78", "v79", "v80", "v81", "v82", "v83", "v84", "v85", "v86", "v87", "v88", "v89", "v90", "v91", "v92", "v93", "v94", "v95", "v96", "v97", "v98", "v99", "v100", "v101", "v102", "v103", "v112", "v113", "v114", "v115", "v116", "v117", "v118", "v119", "v120", "v121", "v122", "v123", "v124", "v125", "v126", "v127", "v128", "v129", "v130", "v131", "v132", "v133", "v134", "v135", "v136", "v137", "v138", "v139", "v140", "v141", "v142", "v143", "v144", "v145", "v146", "v147", "v148", "v149", "v150", "v151", "v152", "v153", "v154", "v155", "v156", "v157", "v158", "v159", "v160", "v161", "v162", "v163", "v164", "v165", "v166", "v167", "v168", "v169", "v170", "v171", "v172", "v173", "v174", "v175", "v176", "v177", "v178", "v179", "v180", "v181", "v182", "v183", "v184", "v185", "v186", "v187", "v188", "v189", "v190", "v191", "v192", "v193", "v194", "v195", "v196", "v197", "v198", "v199", "v200", "v201", "v202", "v203", "v204", "v205", "v206", "v207", "v208", "v209", "v210", "v211", "v212", "v213", "v214", "v215", "v216", "v217", "v218", "v219", "v220", "v221", "v222", "v223", "v244", "v245", "v246", "v247", "v248", "v249", "v250", "v251", "v252", "v254", "v255", "a0", "a1", "a2", "a3", "a4", "a5", "a6", "a7", "a8", "a9", "a10", "a11", "a12", "a13", "a14", "a15", "a16", "a17", "a18", "a19", "a20", "a21", "a22", "a23", "a24", "a25", "a26", "a27", "a28", "a29", "a30", "a31", "a32", "a33", "a34", "a35", "a36", "a37", "a38", "a39", "a40", "a41", "a42", "a43", "a44", "a45", "a46", "a47", "a48", "a49", "a50", "a51", "a52", "a53", "a54", "a55", "a56", "a57", "a58", "a59", "a60", "a61", "a62", "a63", "a64", "a65", "a66", "a67", "a68", "a69", "a70", "a71", "a72", "a73", "a74", "a75", "a76", "a77", "a78", "a79", "a80", "a81", "a82", "a83", "a84", "a85", "a86", "a87", "a88", "a89", "a90", "a91", "a92", "a93", "a94", "a95", "a96", "a97", "a98", "a99", "a100", "a101", "a102", "a103", "a104", "a105", "a106", "a107", "a108", "a109", "a110", "a111", "a112", "a113", "a114", "a115", "a116", "a117", "a118", "a119", "a120", "a121", "a122", "a123", "a124", "a125", "a126", "a127", "a128", "a129", "a130", "a131", "a132", "a133", "a134", "a135", "a136", "a137", "a138", "a139", "a140", "a141", "a142", "a143", "a144", "a145", "a146", "a147", "a148", "a149", "a150", "a151", "a152", "a153", "a154", "a155", "a156", "a157", "a158", "a159", "a160", "a161", "a162", "a163", "a164", "a165", "a166", "a167", "a168", "a169", "a170", "a171", "a172", "a173", "a174", "a175", "a176", "a177", "a178", "a179", "a180", "a181", "a182", "a183", "a184", "a185", "a186", "a187", "a188", "a189", "a190", "a191", "a192", "a193", "a194", "a195", "a196", "a197", "a198", "a199", "a200", "a201", "a202", "a203", "a204", "a205", "a206", "a207", "a208", "a209", "a210", "a211", "a212", "a213", "a214", "a215", "a216", "a217", "a218", "a219", "a220", "a221", "a222", "a223", "a224", "a225", "a226", "a227", "a228", "a229", "a230", "a231", "a232", "a233", "a234", "a235", "a236", "a237", "a238", "a239", "a240", "a241", "a242", "a243", "a244", "a245", "a246", "a247", "a248", "a249", "a250", "a251", "a252", "a253", "a254", "a255", "s72", "s73", "s74", "s75", "s76", "s77", "s78", "s79", "s80", "s81", "s82", "s83", "s84", "s85", "s86", "s87");
}

extern "C" void kernel_launch(void* const* d_in, const int* in_sizes, int n_in, void* d_out, int out_size, void* d_ws, size_t ws_size, hipStream_t stream) {
  mha_fused<<<256, 256, 0, stream>>>((const char*)d_in[0], (const char*)d_in[1], (const char*)d_in[2], (const char*)d_in[3], (const char*)d_in[4], (char*)d_ws, (char*)d_out);
}
```
